```python
import torch

def _softmax(x, axis=-1):
    return torch.softmax(x, dim=axis)
def _gelu(x):
    return torch.nn.functional.gelu(x, approximate="tanh")
def _silu(x):
    return torch.nn.functional.silu(x)
def _sigmoid(x):
    return torch.sigmoid(x)
def _layernorm(x, axis=-1, eps=1e-5):
    m = x.mean(dim=axis, keepdim=True)
    v = x.var(dim=axis, keepdim=True, unbiased=False)
    return (x - m) / torch.sqrt(v + eps)
def _scatter_add(tgt, idx, src, axis=0):
    return tgt.clone().index_add_(axis, idx, src)
def _segment_sum(src, seg, n):
    out = torch.zeros((n,) + src.shape[1:], dtype=src.dtype)
    return out.index_add_(0, seg, src)
def _pad(x, pw):
    flat = tuple(v for pair in reversed(pw) for v in pair)
    return torch.nn.functional.pad(x, flat)

kernel_name = 'parallel_sum__softmax_full_mha__glu_gated_glu__prompt'

def setup_inputs():

    _w = (lambda a: (lambda t: t.to(torch.bfloat16) if t.is_floating_point() else t)(torch.as_tensor(a)))
    def _A_setup():
        B, S, D = 1, 2048, 576
        return {
            "x": _w((lambda *s: torch.randn(*s, dtype=torch.bfloat16) * 0.05)(B, S, D)),
            "wqkv": _w((lambda *s: torch.randn(*s, dtype=torch.bfloat16) * 0.2)(D, 3 * D)),
            "wo": _w((lambda *s: torch.randn(*s, dtype=torch.bfloat16) * 0.1)(D, D)),
        }
    _A_in = _A_setup()

    def _B_setup():
        B, S, D, F = 1, 2048, 576, 4608
        return {
            "x": _w((lambda *s: torch.randn(*s, dtype=torch.bfloat16) * 0.05)(B, S, D)),
            "w_gate": _w((lambda *s: torch.randn(*s, dtype=torch.bfloat16) * 0.8)(D, F)),
            "w_up": _w((lambda *s: torch.randn(*s, dtype=torch.bfloat16) * 0.1)(D, F)),
            "w_down": _w((lambda *s: torch.randn(*s, dtype=torch.bfloat16) * 0.1)(F, D)),
        }
    _B_in = _B_setup()

    return {
        "A_x": _A_in["x"],
        "A_wqkv": _A_in["wqkv"],
        "A_wo": _A_in["wo"],
        "B_x": _B_in["x"],
        "B_w_gate": _B_in["w_gate"],
        "B_w_up": _B_in["w_up"],
        "B_w_down": _B_in["w_down"],
    }

def reference(A_x, A_wqkv, A_wo, B_x, B_w_gate, B_w_up, B_w_down):
    def _A_ref(x, wqkv, wo):
        B, S, D = x.shape
        H = 32; Dh = D // H
        qkv = (lambda x, *d: x.permute(*d))((x @ wqkv).reshape(B, S, 3, H, Dh), 2, 0, 3, 1, 4)
        q, k, v = qkv[0], qkv[1], qkv[2]
        scale = Dh ** -0.5
        scores = torch.einsum("bhqd,bhkd->bhqk", q, k) * scale
        p = _softmax(scores, axis=-1)
        o = torch.einsum("bhqk,bhkd->bhqd", p, v)
        return (lambda x, *d: x.permute(*d))(o, 0, 2, 1, 3).reshape(B, S, D) @ wo

    def _B_ref(x, w_gate, w_up, w_down):
        g = torch.einsum("bsd,df->bsf", x, w_gate)
        u = torch.einsum("bsd,df->bsf", x, w_up)
        h = _sigmoid(g) * u
        return torch.einsum("bsf,fd->bsd", h, w_down)

    x = A_x
    xn = _layernorm(x, axis=-1)
    return x + _A_ref(xn, A_wqkv, A_wo) + _B_ref(xn, B_w_gate, B_w_up, B_w_down)
```

```cpp
#include <hip/hip_runtime.h>
#include <hip/hip_bf16.h>
typedef unsigned short u16;
static __device__ __forceinline__ float b2f(u16 v){ return __uint_as_float(((unsigned)v)<<16); }
static __device__ __forceinline__ u16 f2b(float f){ unsigned u=__float_as_uint(f); u += 0x7fffu + ((u>>16)&1u); return (u16)(u>>16); }
constexpr int S=2048,D=576,H=32,DH=18,F=4608;
__global__ void n_ln(const u16* x, u16* xn){ int r=blockIdx.x; __shared__ float red[2][4]; float s=0,q=0; for(int i=threadIdx.x;i<D;i+=256){float v=b2f(x[r*D+i]); s+=v;}
  for(int o=32;o;o>>=1) s+=__shfl_xor(s,o); if((threadIdx.x&63)==0) red[0][threadIdx.x>>6]=s; __syncthreads(); float m=(red[0][0]+red[0][1]+red[0][2]+red[0][3])/D;
  for(int i=threadIdx.x;i<D;i+=256){float v=b2f(x[r*D+i])-m; q+=v*v;} for(int o=32;o;o>>=1) q+=__shfl_xor(q,o); if((threadIdx.x&63)==0) red[1][threadIdx.x>>6]=q; __syncthreads();
  float rs=rsqrtf((red[1][0]+red[1][1]+red[1][2]+red[1][3])/D+1e-5f); for(int i=threadIdx.x;i<D;i+=256) xn[r*D+i]=f2b((b2f(x[r*D+i])-m)*rs); }
__global__ void n_gemm(const u16* A,const u16* B,float* C,int M,int N,int K){ __shared__ float As[16][17],Bs[16][17]; int tx=threadIdx.x&15,ty=threadIdx.x>>4; int r=blockIdx.y*16+ty,c=blockIdx.x*16+tx; float acc=0;
  for(int k=0;k<K;k+=16){ As[ty][tx]=b2f(A[(size_t)r*K+k+tx]); Bs[ty][tx]=b2f(B[(size_t)(k+ty)*N+c]); __syncthreads(); for(int j=0;j<16;j++) acc+=As[ty][j]*Bs[j][tx]; __syncthreads(); } C[(size_t)r*N+c]=acc; }
__global__ void n_round(const float* a,u16* b,int n){ int i=blockIdx.x*256+threadIdx.x; if(i<n) b[i]=f2b(a[i]); }
__global__ void n_glu(const float* g,const float* u,u16* h,int n){ int i=blockIdx.x*256+threadIdx.x; if(i<n) h[i]=f2b(u[i]/(1.f+__expf(-g[i]))); }
__global__ void n_attn(const u16* qkv,u16* o){ int h=blockIdx.y; int q=blockIdx.x*64+threadIdx.x; float qv[DH],acc[DH]; for(int d=0;d<DH;d++){qv[d]=b2f(qkv[q*3*D+h*DH+d])*0.23570226f; acc[d]=0;} float m=-1e30f,l=0;
  for(int k=0;k<S;k++){ float s=0; for(int d=0;d<DH;d++) s+=qv[d]*b2f(qkv[k*3*D+D+h*DH+d]); float mn=fmaxf(m,s); float a=__expf(m-mn),p=__expf(s-mn); l=l*a+p; for(int d=0;d<DH;d++) acc[d]=acc[d]*a+p*b2f(qkv[k*3*D+2*D+h*DH+d]); m=mn; }
  for(int d=0;d<DH;d++) o[q*D+h*DH+d]=f2b(acc[d]/l); }
__global__ void n_final(const u16* x,const float* a,const float* b,u16* out,int n){ int i=blockIdx.x*256+threadIdx.x; if(i<n) out[i]=f2b(b2f(x[i])+a[i]+b[i]); }
extern "C" void kernel_launch(void* const* d_in, const int* in_sizes, int n_in, void* d_out, int out_size, void* d_ws, size_t ws_size, hipStream_t st) {
  const u16* x=(const u16*)d_in[0]; const u16* wqkv=(const u16*)d_in[1]; const u16* wo=(const u16*)d_in[2]; const u16* wg=(const u16*)d_in[4]; const u16* wu=(const u16*)d_in[5]; const u16* wd=(const u16*)d_in[6];
  char* w=(char*)d_ws; u16* xn=(u16*)w; w+=S*D*2; float* t0=(float*)w; w+=(size_t)S*F*4; float* t1=(float*)w; w+=(size_t)S*F*4; u16* qkv=(u16*)w; w+=S*3*D*2; u16* o=(u16*)w; w+=S*D*2; u16* h=(u16*)w; w+=(size_t)S*F*2; float* a=(float*)w; w+=S*D*4; float* b=(float*)w;
  n_ln<<<S,256,0,st>>>(x,xn);
  n_gemm<<<dim3(3*D/16,S/16),256,0,st>>>(xn,wqkv,t0,S,3*D,D);
  n_round<<<S*3*D/256,256,0,st>>>(t0,qkv,S*3*D);
  n_attn<<<dim3(S/64,H),64,0,st>>>(qkv,o);
  n_gemm<<<dim3(D/16,S/16),256,0,st>>>(o,wo,a,S,D,D);
  n_gemm<<<dim3(F/16,S/16),256,0,st>>>(xn,wg,t0,S,F,D);
  n_gemm<<<dim3(F/16,S/16),256,0,st>>>(xn,wu,t1,S,F,D);
  n_glu<<<S*F/256,256,0,st>>>(t0,t1,h,S*F);
  n_gemm<<<dim3(D/16,S/16),256,0,st>>>(h,wd,b,S,D,F);
  n_final<<<S*D/256,256,0,st>>>(x,a,b,(u16*)d_out,S*D);
}
```

```cpp
#include <hip/hip_runtime.h>
typedef unsigned short u16;

__global__ __launch_bounds__(256, 1) void k_main(const u16* x, const u16* wqkv, const u16* wo, u16* out, const u16* wg, const u16* wu, const u16* wd, char* ws) {
  extern __shared__ __attribute__((aligned(128))) char smem[];
  const int b0 = blockIdx.x;
  const int b = b0;
  const int wid = __builtin_amdgcn_readfirstlane(threadIdx.x >> 6);
  const int xc = b & 7, jj = (b >> 3) & 15;
  const int t = 8 * (xc & 1) + (jj & 7), s = 2 * (xc >> 1) + (jj >> 3);
  const int tf = 2 * xc + (jj >> 3), sf = jj & 7;
  const int rot = 4 * (b >> 7) + (s >> 1);
  const unsigned ldsb = (unsigned)(size_t)smem;
  if (b < 128) {
    asm volatile(
    "s_lshl_b32 s67, %5, 12\n"
    "s_add_u32 s67, s67, 65536\n"
    "s_mov_b64 s[30:31], %2\n"
    "s_add_u32 s30, s30, s67\n"
    "s_addc_u32 s31, s31, 0\n"
    "s_lshl_b32 s67, %12, 12\n"
    "s_add_u32 s67, s67, 65536\n"
    "s_mov_b64 s[32:33], %2\n"
    "s_add_u32 s32, s32, s67\n"
    "s_addc_u32 s33, s33, 0\n"
    "v_mov_b32 v243, 0\n"
    "global_load_dword v244, v243, s[30:31] sc1\n"
    "global_load_dword v245, v243, s[32:33] sc1\n"
    "s_lshl_b32 s67, %6, 12\n"
    "s_mov_b64 s[34:35], %2\n"
    "s_add_u32 s34, s34, s67\n"
    "s_addc_u32 s35, s35, 0\n"
    "global_load_dword v246, v243, s[34:35] sc1\n"
    "v_mbcnt_lo_u32_b32 v255, -1, 0\n"
    "v_mbcnt_hi_u32_b32 v255, -1, v255\n"
    "s_mov_b64 s[36:37], %0\n"
    "s_and_b32 s37, s37, 0xffff\n"
    "s_mov_b32 s38, 2359296\n"
    "s_mov_b32 s39, 0x20000\n"
    "s_lshl_b32 s64, %5, 2\n"
    "s_add_u32 s64, s64, %7\n"
    "s_mul_i32 s64, s64, 36864\n"
    "v_lshrrev_b32 v254, 3, v255\n"
    "v_mul_u32_u24 v254, 0x480, v254\n"
    "v_and_b32 v252, 7, v255\n"
    "v_lshrrev_b32 v251, 4, v255\n"
    "v_xor_b32 v252, v252, v251\n"
    "v_lshl_add_u32 v250, v252, 4, v254\n"
    "v_xor_b32 v252, 4, v252\n"
    "v_lshl_add_u32 v249, v252, 4, v254\n"
    "s_mul_i32 s63, %7, 36864\n"
    "s_add_u32 s63, s63, %8\n"
    "s_add_u32 s52, s64, 1152\n"
    "s_lshl_b32 s65, %15, 7\n"
    "s_add_u32 s64, s64, s65\n"
    "s_add_u32 m0, s63, 0\n"
    "s_add_u32 s65, s64, 0\n"
    "buffer_load_dwordx4 v250, s[36:39], s65 offen lds\n"
    "s_add_u32 m0, s63, 1024\n"
    "s_add_u32 s65, s64, 9216\n"
    "buffer_load_dwordx4 v249, s[36:39], s65 offen lds\n"
    "s_add_u32 m0, s63, 2048\n"
    "s_add_u32 s65, s64, 18432\n"
    "buffer_load_dwordx4 v250, s[36:39], s65 offen lds\n"
    "s_add_u32 m0, s63, 3072\n"
    "s_add_u32 s65, s64, 27648\n"
    "buffer_load_dwordx4 v249, s[36:39], s65 offen lds\n"
    "s_add_u32 s64, s64, 0x80\n"
    "s_cmp_eq_u32 s64, s52\n"
    "s_cselect_b32 s65, 1152, 0\n"
    "s_sub_u32 s64, s64, s65\n"
    "s_add_u32 m0, s63, 4096\n"
    "s_add_u32 s65, s64, 0\n"
    "buffer_load_dwordx4 v250, s[36:39], s65 offen lds\n"
    "s_add_u32 m0, s63, 5120\n"
    "s_add_u32 s65, s64, 9216\n"
    "buffer_load_dwordx4 v249, s[36:39], s65 offen lds\n"
    "s_add_u32 m0, s63, 6144\n"
    "s_add_u32 s65, s64, 18432\n"
    "buffer_load_dwordx4 v250, s[36:39], s65 offen lds\n"
    "s_add_u32 m0, s63, 7168\n"
    "s_add_u32 s65, s64, 27648\n"
    "buffer_load_dwordx4 v249, s[36:39], s65 offen lds\n"
    "v_and_b32 v254, 15, v255\n"
    "v_lshrrev_b32 v253, 4, v255\n"
    "v_bfe_u32 v252, v255, 1, 3\n"
    "v_xor_b32 v252, v252, v253\n"
    "v_lshlrev_b32 v252, 4, v252\n"
    "v_lshl_add_u32 v252, v254, 7, v252\n"
    "v_add_u32 v251, s63, v252\n"
    "v_xor_b32 v252, 64, v251\n"
    "v_mov_b32 v144, 0\n"
    "v_mov_b32 v145, 0\n"
    "v_mov_b32 v146, 0\n"
    "v_mov_b32 v147, 0\n"
    "v_mov_b32 v148, 0\n"
    "v_mov_b32 v149, 0\n"
    "v_mov_b32 v150, 0\n"
    "v_mov_b32 v151, 0\n"
    "s_mov_b64 s[40:41], %1\n"
    "s_and_b32 s41, s41, 0xffff\n"
    "s_mov_b32 s42, 1990656\n"
    "s_mov_b32 s43, 0x20000\n"
    "s_and_b32 s67, %5, 7\n"
    "s_lshl_b32 s67, s67, 2\n"
    "s_add_u32 s67, s67, %7\n"
    "s_mul_i32 s68, s67, 165888\n"
    "s_mul_i32 s69, %6, 1152\n"
    "s_add_u32 s68, s68, s69\n"
    "s_add_u32 s68, s68, 384\n"
    "s_mul_i32 s69, %6, 576\n"
    "s_add_u32 s69, s69, s67\n"
    "s_add_u32 s69, s69, 192\n"
    "s_mul_i32 s69, s69, 1152\n"
    "s_mov_b64 s[70:71], %9\n"
    "s_mov_b64 s[72:73], %10\n"
    "s_mov_b64 s[74:75], %11\n"
    "s_add_u32 s70, s70, s68\n"
    "s_addc_u32 s71, s71, 0\n"
    "s_add_u32 s72, s72, s68\n"
    "s_addc_u32 s73, s73, 0\n"
    "s_add_u32 s74, s74, s69\n"
    "s_addc_u32 s75, s75, 0\n"
    "v_cmp_gt_u32 vcc, 18, v255\n"
    "v_cmp_gt_u32 s[76:77], 36, v255\n"
    "v_subrev_u32 v200, 18, v255\n"
    "v_subrev_u32 v201, 36, v255\n"
    "v_min_u32 v201, 17, v201\n"
    "v_cndmask_b32 v200, v200, v255, vcc\n"
    "v_mul_u32_u24 v200, 0x2400, v200\n"
    "v_lshlrev_b32 v202, 7, v201\n"
    "v_cmp_lt_u32 s[68:69], 8, v201\n"
    "v_mov_b32 v203, 35712\n"
    "s_nop 0\n"
    "v_cndmask_b32 v203, 0, v203, s[68:69]\n"
    "v_add_u32 v201, v202, v203\n"
    "v_cndmask_b32 v200, v201, v200, s[76:77]\n"
    "v_mov_b32 v201, s72\n"
    "v_mov_b32 v202, s70\n"
    "v_cndmask_b32 v201, v201, v202, vcc\n"
    "v_mov_b32 v202, s74\n"
    "v_cndmask_b32 v236, v202, v201, s[76:77]\n"
    "v_mov_b32 v201, s73\n"
    "v_mov_b32 v202, s71\n"
    "v_cndmask_b32 v201, v201, v202, vcc\n"
    "v_mov_b32 v202, s75\n"
    "v_cndmask_b32 v237, v202, v201, s[76:77]\n"
    "v_mov_b32 v201, 73728\n"
    "v_mov_b32 v202, 0x80\n"
    "v_cndmask_b32 v238, v201, v202, s[76:77]\n"
    "v_add_co_u32 v236, vcc, v200, v236\n"
    "s_nop 1\n"
    "v_addc_co_u32 v237, vcc, 0, v237, vcc\n"
    "s_and_b32 s67, %5, 7\n"
    "s_lshl_b32 s67, s67, 2\n"
    "s_add_u32 s67, s67, %7\n"
    "s_mul_i32 s59, %6, 144\n"
    "s_mul_i32 s69, s67, 10\n"
    "s_lshl_b32 s70, s67, 3\n"
    "s_add_u32 s70, s70, 320\n"
    "s_lshl_b32 s68, %15, 6\n"
    "s_add_u32 s69, s69, s68\n"
    "s_add_u32 s70, s70, s68\n"
    "v_mul_u32_u24 v200, 43, v255\n"
    "v_lshrrev_b32 v200, 8, v200\n"
    "v_mul_u32_u24 v201, 6, v200\n"
    "v_sub_u32 v201, v255, v201\n"
    "v_lshrrev_b32 v202, 1, v201\n"
    "v_mul_u32_u24 v202, 0x480, v202\n"
    "v_and_b32 v201, 1, v201\n"
    "v_lshl_add_u32 v202, v201, 7, v202\n"
    "v_mov_b32 v204, 0x7fffffff\n"
    "v_add_u32 v201, s69, v200\n"
    "v_subrev_u32 v203, 0x240, v201\n"
    "v_min_u32 v201, v201, v203\n"
    "v_mul_u32_u24 v201, 0xd80, v201\n"
    "v_cmp_gt_u32 vcc, 60, v255\n"
    "v_add_u32 v201, v201, v202\n"
    "s_nop 0\n"
    "v_cndmask_b32 v232, v204, v201, vcc\n"
    "v_add_u32 v201, s70, v200\n"
    "v_subrev_u32 v203, 0x240, v201\n"
    "v_min_u32 v201, v201, v203\n"
    "v_mul_u32_u24 v201, 0xd80, v201\n"
    "v_cmp_gt_u32 vcc, 48, v255\n"
    "v_add_u32 v201, v201, v202\n"
    "s_nop 0\n"
    "v_cndmask_b32 v233, v204, v201, vcc\n"
    "s_lshl_b32 s67, %7, 9\n"
    "v_add_u32 v200, 0, v255\n"
    "v_add_u32 v200, s67, v200\n"
    "v_mul_u32_u24 v201, 0x889, v200\n"
    "v_lshrrev_b32 v201, 16, v201\n"
    "v_mul_u32_u24 v202, 30, v201\n"
    "v_sub_u32 v202, v200, v202\n"
    "v_min_u32 v202, 26, v202\n"
    "v_min_u32 v201, 63, v201\n"
    "v_cmp_lt_u32 vcc, 8, v202\n"
    "v_cmp_lt_u32 s[76:77], 17, v202\n"
    "v_and_b32 v203, 4, v201\n"
    "v_lshlrev_b32 v203, 1, v203\n"
    "v_and_b32 v204, 8, v201\n"
    "v_lshrrev_b32 v204, 1, v204\n"
    "v_and_b32 v201, 0x33, v201\n"
    "v_or3_b32 v201, v201, v203, v204\n"
    "v_mul_u32_u24 v201, 0xd80, v201\n"
    "v_lshl_add_u32 v201, v202, 4, v201\n"
    "v_mov_b32 v203, 1008\n"
    "v_cndmask_b32 v203, 0, v203, vcc\n"
    "v_mov_b32 v204, 2016\n"
    "v_cndmask_b32 v203, v203, v204, s[76:77]\n"
    "v_add_u32 v216, v201, v203\n"
    "v_add_u32 v200, 64, v255\n"
    "v_add_u32 v200, s67, v200\n"
    "v_mul_u32_u24 v201, 0x889, v200\n"
    "v_lshrrev_b32 v201, 16, v201\n"
    "v_mul_u32_u24 v202, 30, v201\n"
    "v_sub_u32 v202, v200, v202\n"
    "v_min_u32 v202, 26, v202\n"
    "v_min_u32 v201, 63, v201\n"
    "v_cmp_lt_u32 vcc, 8, v202\n"
    "v_cmp_lt_u32 s[76:77], 17, v202\n"
    "v_and_b32 v203, 4, v201\n"
    "v_lshlrev_b32 v203, 1, v203\n"
    "v_and_b32 v204, 8, v201\n"
    "v_lshrrev_b32 v204, 1, v204\n"
    "v_and_b32 v201, 0x33, v201\n"
    "v_or3_b32 v201, v201, v203, v204\n"
    "v_mul_u32_u24 v201, 0xd80, v201\n"
    "v_lshl_add_u32 v201, v202, 4, v201\n"
    "v_mov_b32 v203, 1008\n"
    "v_cndmask_b32 v203, 0, v203, vcc\n"
    "v_mov_b32 v204, 2016\n"
    "v_cndmask_b32 v203, v203, v204, s[76:77]\n"
    "v_add_u32 v217, v201, v203\n"
    "v_add_u32 v200, 128, v255\n"
    "v_add_u32 v200, s67, v200\n"
    "v_mul_u32_u24 v201, 0x889, v200\n"
    "v_lshrrev_b32 v201, 16, v201\n"
    "v_mul_u32_u24 v202, 30, v201\n"
    "v_sub_u32 v202, v200, v202\n"
    "v_min_u32 v202, 26, v202\n"
    "v_min_u32 v201, 63, v201\n"
    "v_cmp_lt_u32 vcc, 8, v202\n"
    "v_cmp_lt_u32 s[76:77], 17, v202\n"
    "v_and_b32 v203, 4, v201\n"
    "v_lshlrev_b32 v203, 1, v203\n"
    "v_and_b32 v204, 8, v201\n"
    "v_lshrrev_b32 v204, 1, v204\n"
    "v_and_b32 v201, 0x33, v201\n"
    "v_or3_b32 v201, v201, v203, v204\n"
    "v_mul_u32_u24 v201, 0xd80, v201\n"
    "v_lshl_add_u32 v201, v202, 4, v201\n"
    "v_mov_b32 v203, 1008\n"
    "v_cndmask_b32 v203, 0, v203, vcc\n"
    "v_mov_b32 v204, 2016\n"
    "v_cndmask_b32 v203, v203, v204, s[76:77]\n"
    "v_add_u32 v218, v201, v203\n"
    "v_add_u32 v200, 192, v255\n"
    "v_add_u32 v200, s67, v200\n"
    "v_mul_u32_u24 v201, 0x889, v200\n"
    "v_lshrrev_b32 v201, 16, v201\n"
    "v_mul_u32_u24 v202, 30, v201\n"
    "v_sub_u32 v202, v200, v202\n"
    "v_min_u32 v202, 26, v202\n"
    "v_min_u32 v201, 63, v201\n"
    "v_cmp_lt_u32 vcc, 8, v202\n"
    "v_cmp_lt_u32 s[76:77], 17, v202\n"
    "v_and_b32 v203, 4, v201\n"
    "v_lshlrev_b32 v203, 1, v203\n"
    "v_and_b32 v204, 8, v201\n"
    "v_lshrrev_b32 v204, 1, v204\n"
    "v_and_b32 v201, 0x33, v201\n"
    "v_or3_b32 v201, v201, v203, v204\n"
    "v_mul_u32_u24 v201, 0xd80, v201\n"
    "v_lshl_add_u32 v201, v202, 4, v201\n"
    "v_mov_b32 v203, 1008\n"
    "v_cndmask_b32 v203, 0, v203, vcc\n"
    "v_mov_b32 v204, 2016\n"
    "v_cndmask_b32 v203, v203, v204, s[76:77]\n"
    "v_add_u32 v219, v201, v203\n"
    "v_add_u32 v200, 256, v255\n"
    "v_add_u32 v200, s67, v200\n"
    "v_mul_u32_u24 v201, 0x889, v200\n"
    "v_lshrrev_b32 v201, 16, v201\n"
    "v_mul_u32_u24 v202, 30, v201\n"
    "v_sub_u32 v202, v200, v202\n"
    "v_min_u32 v202, 26, v202\n"
    "v_min_u32 v201, 63, v201\n"
    "v_cmp_lt_u32 vcc, 8, v202\n"
    "v_cmp_lt_u32 s[76:77], 17, v202\n"
    "v_and_b32 v203, 4, v201\n"
    "v_lshlrev_b32 v203, 1, v203\n"
    "v_and_b32 v204, 8, v201\n"
    "v_lshrrev_b32 v204, 1, v204\n"
    "v_and_b32 v201, 0x33, v201\n"
    "v_or3_b32 v201, v201, v203, v204\n"
    "v_mul_u32_u24 v201, 0xd80, v201\n"
    "v_lshl_add_u32 v201, v202, 4, v201\n"
    "v_mov_b32 v203, 1008\n"
    "v_cndmask_b32 v203, 0, v203, vcc\n"
    "v_mov_b32 v204, 2016\n"
    "v_cndmask_b32 v203, v203, v204, s[76:77]\n"
    "v_add_u32 v220, v201, v203\n"
    "v_add_u32 v200, 320, v255\n"
    "v_add_u32 v200, s67, v200\n"
    "v_mul_u32_u24 v201, 0x889, v200\n"
    "v_lshrrev_b32 v201, 16, v201\n"
    "v_mul_u32_u24 v202, 30, v201\n"
    "v_sub_u32 v202, v200, v202\n"
    "v_min_u32 v202, 26, v202\n"
    "v_min_u32 v201, 63, v201\n"
    "v_cmp_lt_u32 vcc, 8, v202\n"
    "v_cmp_lt_u32 s[76:77], 17, v202\n"
    "v_and_b32 v203, 4, v201\n"
    "v_lshlrev_b32 v203, 1, v203\n"
    "v_and_b32 v204, 8, v201\n"
    "v_lshrrev_b32 v204, 1, v204\n"
    "v_and_b32 v201, 0x33, v201\n"
    "v_or3_b32 v201, v201, v203, v204\n"
    "v_mul_u32_u24 v201, 0xd80, v201\n"
    "v_lshl_add_u32 v201, v202, 4, v201\n"
    "v_mov_b32 v203, 1008\n"
    "v_cndmask_b32 v203, 0, v203, vcc\n"
    "v_mov_b32 v204, 2016\n"
    "v_cndmask_b32 v203, v203, v204, s[76:77]\n"
    "v_add_u32 v221, v201, v203\n"
    "v_add_u32 v200, 384, v255\n"
    "v_add_u32 v200, s67, v200\n"
    "v_mul_u32_u24 v201, 0x889, v200\n"
    "v_lshrrev_b32 v201, 16, v201\n"
    "v_mul_u32_u24 v202, 30, v201\n"
    "v_sub_u32 v202, v200, v202\n"
    "v_min_u32 v202, 26, v202\n"
    "v_min_u32 v201, 63, v201\n"
    "v_cmp_lt_u32 vcc, 8, v202\n"
    "v_cmp_lt_u32 s[76:77], 17, v202\n"
    "v_and_b32 v203, 4, v201\n"
    "v_lshlrev_b32 v203, 1, v203\n"
    "v_and_b32 v204, 8, v201\n"
    "v_lshrrev_b32 v204, 1, v204\n"
    "v_and_b32 v201, 0x33, v201\n"
    "v_or3_b32 v201, v201, v203, v204\n"
    "v_mul_u32_u24 v201, 0xd80, v201\n"
    "v_lshl_add_u32 v201, v202, 4, v201\n"
    "v_mov_b32 v203, 1008\n"
    "v_cndmask_b32 v203, 0, v203, vcc\n"
    "v_mov_b32 v204, 2016\n"
    "v_cndmask_b32 v203, v203, v204, s[76:77]\n"
    "v_add_u32 v222, v201, v203\n"
    "v_add_u32 v200, 448, v255\n"
    "v_add_u32 v200, s67, v200\n"
    "v_mul_u32_u24 v201, 0x889, v200\n"
    "v_lshrrev_b32 v201, 16, v201\n"
    "v_mul_u32_u24 v202, 30, v201\n"
    "v_sub_u32 v202, v200, v202\n"
    "v_min_u32 v202, 26, v202\n"
    "v_min_u32 v201, 63, v201\n"
    "v_cmp_lt_u32 vcc, 8, v202\n"
    "v_cmp_lt_u32 s[76:77], 17, v202\n"
    "v_and_b32 v203, 4, v201\n"
    "v_lshlrev_b32 v203, 1, v203\n"
    "v_and_b32 v204, 8, v201\n"
    "v_lshrrev_b32 v204, 1, v204\n"
    "v_and_b32 v201, 0x33, v201\n"
    "v_or3_b32 v201, v201, v203, v204\n"
    "v_mul_u32_u24 v201, 0xd80, v201\n"
    "v_lshl_add_u32 v201, v202, 4, v201\n"
    "v_mov_b32 v203, 1008\n"
    "v_cndmask_b32 v203, 0, v203, vcc\n"
    "v_mov_b32 v204, 2016\n"
    "v_cndmask_b32 v203, v203, v204, s[76:77]\n"
    "v_add_u32 v223, v201, v203\n"
    "v_lshrrev_b32 v200, 5, v255\n"
    "v_bfe_u32 v201, v255, 4, 1\n"
    "v_bfe_u32 v202, v255, 2, 2\n"
    "v_lshl_add_u32 v202, v201, 2, v202\n"
    "v_lshl_add_u32 v202, v200, 4, v202\n"
    "v_mul_u32_u24 v200, 480, v202\n"
    "v_add_u32 v200, %8, v200\n"
    "v_and_b32 v201, 3, v255\n"
    "v_lshl_add_u32 v224, v201, 3, v200\n"
    "v_and_b32 v202, 1, v201\n"
    "v_mul_u32_u24 v202, 72, v202\n"
    "v_lshrrev_b32 v201, 1, v201\n"
    "v_mul_u32_u24 v201, 0x90, v201\n"
    "v_add3_u32 v201, v201, v202, 32\n"
    "v_add_u32 v225, v200, v201\n"
    "s_lshl_b32 s60, %7, 13\n"
    "s_add_u32 s60, s60, %8\n"
    "s_mul_i32 s58, %6, 144\n"
    "s_add_u32 s64, s64, 0x80\n"
    "s_cmp_eq_u32 s64, s52\n"
    "s_cselect_b32 s65, 1152, 0\n"
    "s_sub_u32 s64, s64, s65\n"
    "s_add_u32 m0, s63, 8192\n"
    "s_add_u32 s65, s64, 0\n"
    "buffer_load_dwordx4 v250, s[36:39], s65 offen lds\n"
    "s_add_u32 m0, s63, 9216\n"
    "s_add_u32 s65, s64, 9216\n"
    "buffer_load_dwordx4 v249, s[36:39], s65 offen lds\n"
    "s_add_u32 m0, s63, 10240\n"
    "s_add_u32 s65, s64, 18432\n"
    "buffer_load_dwordx4 v250, s[36:39], s65 offen lds\n"
    "s_add_u32 m0, s63, 11264\n"
    "s_add_u32 s65, s64, 27648\n"
    "buffer_load_dwordx4 v249, s[36:39], s65 offen lds\n"
    "s_waitcnt vmcnt(8)\n"
    "ds_read_b128 v[0:3], v251 offset:0\n"
    "ds_read_b128 v[4:7], v251 offset:2048\n"
    "ds_read_b128 v[8:11], v252 offset:0\n"
    "ds_read_b128 v[12:15], v252 offset:2048\n"
    "s_mov_b32 s53, 0x3f803f80\n"
    "s_add_u32 s64, s64, 0x80\n"
    "s_cmp_eq_u32 s64, s52\n"
    "s_cselect_b32 s65, 1152, 0\n"
    "s_sub_u32 s64, s64, s65\n"
    "s_add_u32 m0, s63, 12288\n"
    "s_add_u32 s65, s64, 0\n"
    "buffer_load_dwordx4 v250, s[36:39], s65 offen lds\n"
    "s_add_u32 m0, s63, 13312\n"
    "s_add_u32 s65, s64, 9216\n"
    "buffer_load_dwordx4 v249, s[36:39], s65 offen lds\n"
    "s_add_u32 m0, s63, 14336\n"
    "s_add_u32 s65, s64, 18432\n"
    "buffer_load_dwordx4 v250, s[36:39], s65 offen lds\n"
    "s_add_u32 m0, s63, 15360\n"
    "s_add_u32 s65, s64, 27648\n"
    "buffer_load_dwordx4 v249, s[36:39], s65 offen lds\n"
    "s_waitcnt vmcnt(8)\n"
    "ds_read_b128 v[16:19], v251 offset:4096\n"
    "ds_read_b128 v[20:23], v251 offset:6144\n"
    "ds_read_b128 v[24:27], v252 offset:4096\n"
    "ds_read_b128 v[28:31], v252 offset:6144\n"
    "s_waitcnt lgkmcnt(4)\n"
    "v_dot2c_f32_bf16 v144, s53, v0\n"
    "v_dot2c_f32_bf16 v148, v0, v0\n"
    "v_dot2c_f32_bf16 v145, s53, v1\n"
    "v_dot2c_f32_bf16 v149, v1, v1\n"
    "v_dot2c_f32_bf16 v144, s53, v2\n"
    "v_dot2c_f32_bf16 v148, v2, v2\n"
    "v_dot2c_f32_bf16 v145, s53, v3\n"
    "v_dot2c_f32_bf16 v149, v3, v3\n"
    "v_dot2c_f32_bf16 v146, s53, v4\n"
    "v_dot2c_f32_bf16 v150, v4, v4\n"
    "v_dot2c_f32_bf16 v147, s53, v5\n"
    "v_dot2c_f32_bf16 v151, v5, v5\n"
    "v_dot2c_f32_bf16 v146, s53, v6\n"
    "v_dot2c_f32_bf16 v150, v6, v6\n"
    "v_dot2c_f32_bf16 v147, s53, v7\n"
    "v_dot2c_f32_bf16 v151, v7, v7\n"
    "v_dot2c_f32_bf16 v144, s53, v8\n"
    "v_dot2c_f32_bf16 v148, v8, v8\n"
    "v_dot2c_f32_bf16 v145, s53, v9\n"
    "v_dot2c_f32_bf16 v149, v9, v9\n"
    "v_dot2c_f32_bf16 v144, s53, v10\n"
    "v_dot2c_f32_bf16 v148, v10, v10\n"
    "v_dot2c_f32_bf16 v145, s53, v11\n"
    "v_dot2c_f32_bf16 v149, v11, v11\n"
    "v_dot2c_f32_bf16 v146, s53, v12\n"
    "v_dot2c_f32_bf16 v150, v12, v12\n"
    "v_dot2c_f32_bf16 v147, s53, v13\n"
    "v_dot2c_f32_bf16 v151, v13, v13\n"
    "v_dot2c_f32_bf16 v146, s53, v14\n"
    "v_dot2c_f32_bf16 v150, v14, v14\n"
    "v_dot2c_f32_bf16 v147, s53, v15\n"
    "v_dot2c_f32_bf16 v151, v15, v15\n"
    "s_add_u32 s64, s64, 0x80\n"
    "s_cmp_eq_u32 s64, s52\n"
    "s_cselect_b32 s65, 1152, 0\n"
    "s_sub_u32 s64, s64, s65\n"
    "s_add_u32 m0, s63, 16384\n"
    "s_add_u32 s65, s64, 0\n"
    "buffer_load_dwordx4 v250, s[36:39], s65 offen lds\n"
    "s_add_u32 m0, s63, 17408\n"
    "s_add_u32 s65, s64, 9216\n"
    "buffer_load_dwordx4 v249, s[36:39], s65 offen lds\n"
    "s_add_u32 m0, s63, 18432\n"
    "s_add_u32 s65, s64, 18432\n"
    "buffer_load_dwordx4 v250, s[36:39], s65 offen lds\n"
    "s_add_u32 m0, s63, 19456\n"
    "s_add_u32 s65, s64, 27648\n"
    "buffer_load_dwordx4 v249, s[36:39], s65 offen lds\n"
    "s_waitcnt vmcnt(8)\n"
    "ds_read_b128 v[32:35], v251 offset:8192\n"
    "ds_read_b128 v[36:39], v251 offset:10240\n"
    "ds_read_b128 v[40:43], v252 offset:8192\n"
    "ds_read_b128 v[44:47], v252 offset:10240\n"
    "s_waitcnt lgkmcnt(4)\n"
    "v_dot2c_f32_bf16 v144, s53, v16\n"
    "v_dot2c_f32_bf16 v148, v16, v16\n"
    "v_dot2c_f32_bf16 v145, s53, v17\n"
    "v_dot2c_f32_bf16 v149, v17, v17\n"
    "v_dot2c_f32_bf16 v144, s53, v18\n"
    "v_dot2c_f32_bf16 v148, v18, v18\n"
    "v_dot2c_f32_bf16 v145, s53, v19\n"
    "v_dot2c_f32_bf16 v149, v19, v19\n"
    "v_dot2c_f32_bf16 v146, s53, v20\n"
    "v_dot2c_f32_bf16 v150, v20, v20\n"
    "v_dot2c_f32_bf16 v147, s53, v21\n"
    "v_dot2c_f32_bf16 v151, v21, v21\n"
    "v_dot2c_f32_bf16 v146, s53, v22\n"
    "v_dot2c_f32_bf16 v150, v22, v22\n"
    "v_dot2c_f32_bf16 v147, s53, v23\n"
    "v_dot2c_f32_bf16 v151, v23, v23\n"
    "v_dot2c_f32_bf16 v144, s53, v24\n"
    "v_dot2c_f32_bf16 v148, v24, v24\n"
    "v_dot2c_f32_bf16 v145, s53, v25\n"
    "v_dot2c_f32_bf16 v149, v25, v25\n"
    "v_dot2c_f32_bf16 v144, s53, v26\n"
    "v_dot2c_f32_bf16 v148, v26, v26\n"
    "v_dot2c_f32_bf16 v145, s53, v27\n"
    "v_dot2c_f32_bf16 v149, v27, v27\n"
    "v_dot2c_f32_bf16 v146, s53, v28\n"
    "v_dot2c_f32_bf16 v150, v28, v28\n"
    "v_dot2c_f32_bf16 v147, s53, v29\n"
    "v_dot2c_f32_bf16 v151, v29, v29\n"
    "v_dot2c_f32_bf16 v146, s53, v30\n"
    "v_dot2c_f32_bf16 v150, v30, v30\n"
    "v_dot2c_f32_bf16 v147, s53, v31\n"
    "v_dot2c_f32_bf16 v151, v31, v31\n"
    "s_add_u32 s64, s64, 0x80\n"
    "s_cmp_eq_u32 s64, s52\n"
    "s_cselect_b32 s65, 1152, 0\n"
    "s_sub_u32 s64, s64, s65\n"
    "s_add_u32 m0, s63, 20480\n"
    "s_add_u32 s65, s64, 0\n"
    "buffer_load_dwordx4 v250, s[36:39], s65 offen lds\n"
    "s_add_u32 m0, s63, 21504\n"
    "s_add_u32 s65, s64, 9216\n"
    "buffer_load_dwordx4 v249, s[36:39], s65 offen lds\n"
    "s_add_u32 m0, s63, 22528\n"
    "s_add_u32 s65, s64, 18432\n"
    "buffer_load_dwordx4 v250, s[36:39], s65 offen lds\n"
    "s_add_u32 m0, s63, 23552\n"
    "s_add_u32 s65, s64, 27648\n"
    "buffer_load_dwordx4 v249, s[36:39], s65 offen lds\n"
    "s_waitcnt vmcnt(8)\n"
    "ds_read_b128 v[48:51], v251 offset:12288\n"
    "ds_read_b128 v[52:55], v251 offset:14336\n"
    "ds_read_b128 v[56:59], v252 offset:12288\n"
    "ds_read_b128 v[60:63], v252 offset:14336\n"
    "s_waitcnt lgkmcnt(4)\n"
    "v_dot2c_f32_bf16 v144, s53, v32\n"
    "v_dot2c_f32_bf16 v148, v32, v32\n"
    "v_dot2c_f32_bf16 v145, s53, v33\n"
    "v_dot2c_f32_bf16 v149, v33, v33\n"
    "v_dot2c_f32_bf16 v144, s53, v34\n"
    "v_dot2c_f32_bf16 v148, v34, v34\n"
    "v_dot2c_f32_bf16 v145, s53, v35\n"
    "v_dot2c_f32_bf16 v149, v35, v35\n"
    "v_dot2c_f32_bf16 v146, s53, v36\n"
    "v_dot2c_f32_bf16 v150, v36, v36\n"
    "v_dot2c_f32_bf16 v147, s53, v37\n"
    "v_dot2c_f32_bf16 v151, v37, v37\n"
    "v_dot2c_f32_bf16 v146, s53, v38\n"
    "v_dot2c_f32_bf16 v150, v38, v38\n"
    "v_dot2c_f32_bf16 v147, s53, v39\n"
    "v_dot2c_f32_bf16 v151, v39, v39\n"
    "v_dot2c_f32_bf16 v144, s53, v40\n"
    "v_dot2c_f32_bf16 v148, v40, v40\n"
    "v_dot2c_f32_bf16 v145, s53, v41\n"
    "v_dot2c_f32_bf16 v149, v41, v41\n"
    "v_dot2c_f32_bf16 v144, s53, v42\n"
    "v_dot2c_f32_bf16 v148, v42, v42\n"
    "v_dot2c_f32_bf16 v145, s53, v43\n"
    "v_dot2c_f32_bf16 v149, v43, v43\n"
    "v_dot2c_f32_bf16 v146, s53, v44\n"
    "v_dot2c_f32_bf16 v150, v44, v44\n"
    "v_dot2c_f32_bf16 v147, s53, v45\n"
    "v_dot2c_f32_bf16 v151, v45, v45\n"
    "v_dot2c_f32_bf16 v146, s53, v46\n"
    "v_dot2c_f32_bf16 v150, v46, v46\n"
    "v_dot2c_f32_bf16 v147, s53, v47\n"
    "v_dot2c_f32_bf16 v151, v47, v47\n"
    "s_add_u32 s64, s64, 0x80\n"
    "s_cmp_eq_u32 s64, s52\n"
    "s_cselect_b32 s65, 1152, 0\n"
    "s_sub_u32 s64, s64, s65\n"
    "s_add_u32 m0, s63, 24576\n"
    "s_add_u32 s65, s64, 0\n"
    "buffer_load_dwordx4 v250, s[36:39], s65 offen lds\n"
    "s_add_u32 m0, s63, 25600\n"
    "s_add_u32 s65, s64, 9216\n"
    "buffer_load_dwordx4 v249, s[36:39], s65 offen lds\n"
    "s_add_u32 m0, s63, 26624\n"
    "s_add_u32 s65, s64, 18432\n"
    "buffer_load_dwordx4 v250, s[36:39], s65 offen lds\n"
    "s_add_u32 m0, s63, 27648\n"
    "s_add_u32 s65, s64, 27648\n"
    "buffer_load_dwordx4 v249, s[36:39], s65 offen lds\n"
    "s_waitcnt vmcnt(8)\n"
    "ds_read_b128 v[64:67], v251 offset:16384\n"
    "ds_read_b128 v[68:71], v251 offset:18432\n"
    "ds_read_b128 v[72:75], v252 offset:16384\n"
    "ds_read_b128 v[76:79], v252 offset:18432\n"
    "s_waitcnt lgkmcnt(4)\n"
    "v_dot2c_f32_bf16 v144, s53, v48\n"
    "v_dot2c_f32_bf16 v148, v48, v48\n"
    "v_dot2c_f32_bf16 v145, s53, v49\n"
    "v_dot2c_f32_bf16 v149, v49, v49\n"
    "v_dot2c_f32_bf16 v144, s53, v50\n"
    "v_dot2c_f32_bf16 v148, v50, v50\n"
    "v_dot2c_f32_bf16 v145, s53, v51\n"
    "v_dot2c_f32_bf16 v149, v51, v51\n"
    "v_dot2c_f32_bf16 v146, s53, v52\n"
    "v_dot2c_f32_bf16 v150, v52, v52\n"
    "v_dot2c_f32_bf16 v147, s53, v53\n"
    "v_dot2c_f32_bf16 v151, v53, v53\n"
    "v_dot2c_f32_bf16 v146, s53, v54\n"
    "v_dot2c_f32_bf16 v150, v54, v54\n"
    "v_dot2c_f32_bf16 v147, s53, v55\n"
    "v_dot2c_f32_bf16 v151, v55, v55\n"
    "v_dot2c_f32_bf16 v144, s53, v56\n"
    "v_dot2c_f32_bf16 v148, v56, v56\n"
    "v_dot2c_f32_bf16 v145, s53, v57\n"
    "v_dot2c_f32_bf16 v149, v57, v57\n"
    "v_dot2c_f32_bf16 v144, s53, v58\n"
    "v_dot2c_f32_bf16 v148, v58, v58\n"
    "v_dot2c_f32_bf16 v145, s53, v59\n"
    "v_dot2c_f32_bf16 v149, v59, v59\n"
    "v_dot2c_f32_bf16 v146, s53, v60\n"
    "v_dot2c_f32_bf16 v150, v60, v60\n"
    "v_dot2c_f32_bf16 v147, s53, v61\n"
    "v_dot2c_f32_bf16 v151, v61, v61\n"
    "v_dot2c_f32_bf16 v146, s53, v62\n"
    "v_dot2c_f32_bf16 v150, v62, v62\n"
    "v_dot2c_f32_bf16 v147, s53, v63\n"
    "v_dot2c_f32_bf16 v151, v63, v63\n"
    "s_add_u32 s64, s64, 0x80\n"
    "s_cmp_eq_u32 s64, s52\n"
    "s_cselect_b32 s65, 1152, 0\n"
    "s_sub_u32 s64, s64, s65\n"
    "s_add_u32 m0, s63, 28672\n"
    "s_add_u32 s65, s64, 0\n"
    "buffer_load_dwordx4 v250, s[36:39], s65 offen lds\n"
    "s_add_u32 m0, s63, 29696\n"
    "s_add_u32 s65, s64, 9216\n"
    "buffer_load_dwordx4 v249, s[36:39], s65 offen lds\n"
    "s_add_u32 m0, s63, 30720\n"
    "s_add_u32 s65, s64, 18432\n"
    "buffer_load_dwordx4 v250, s[36:39], s65 offen lds\n"
    "s_add_u32 m0, s63, 31744\n"
    "s_add_u32 s65, s64, 27648\n"
    "buffer_load_dwordx4 v249, s[36:39], s65 offen lds\n"
    "s_waitcnt vmcnt(8)\n"
    "ds_read_b128 v[80:83], v251 offset:20480\n"
    "ds_read_b128 v[84:87], v251 offset:22528\n"
    "ds_read_b128 v[88:91], v252 offset:20480\n"
    "ds_read_b128 v[92:95], v252 offset:22528\n"
    "s_waitcnt lgkmcnt(4)\n"
    "v_dot2c_f32_bf16 v144, s53, v64\n"
    "v_dot2c_f32_bf16 v148, v64, v64\n"
    "v_dot2c_f32_bf16 v145, s53, v65\n"
    "v_dot2c_f32_bf16 v149, v65, v65\n"
    "v_dot2c_f32_bf16 v144, s53, v66\n"
    "v_dot2c_f32_bf16 v148, v66, v66\n"
    "v_dot2c_f32_bf16 v145, s53, v67\n"
    "v_dot2c_f32_bf16 v149, v67, v67\n"
    "v_dot2c_f32_bf16 v146, s53, v68\n"
    "v_dot2c_f32_bf16 v150, v68, v68\n"
    "v_dot2c_f32_bf16 v147, s53, v69\n"
    "v_dot2c_f32_bf16 v151, v69, v69\n"
    "v_dot2c_f32_bf16 v146, s53, v70\n"
    "v_dot2c_f32_bf16 v150, v70, v70\n"
    "v_dot2c_f32_bf16 v147, s53, v71\n"
    "v_dot2c_f32_bf16 v151, v71, v71\n"
    "v_dot2c_f32_bf16 v144, s53, v72\n"
    "v_dot2c_f32_bf16 v148, v72, v72\n"
    "v_dot2c_f32_bf16 v145, s53, v73\n"
    "v_dot2c_f32_bf16 v149, v73, v73\n"
    "v_dot2c_f32_bf16 v144, s53, v74\n"
    "v_dot2c_f32_bf16 v148, v74, v74\n"
    "v_dot2c_f32_bf16 v145, s53, v75\n"
    "v_dot2c_f32_bf16 v149, v75, v75\n"
    "v_dot2c_f32_bf16 v146, s53, v76\n"
    "v_dot2c_f32_bf16 v150, v76, v76\n"
    "v_dot2c_f32_bf16 v147, s53, v77\n"
    "v_dot2c_f32_bf16 v151, v77, v77\n"
    "v_dot2c_f32_bf16 v146, s53, v78\n"
    "v_dot2c_f32_bf16 v150, v78, v78\n"
    "v_dot2c_f32_bf16 v147, s53, v79\n"
    "v_dot2c_f32_bf16 v151, v79, v79\n"
    "s_add_u32 s64, s64, 0x80\n"
    "s_cmp_eq_u32 s64, s52\n"
    "s_cselect_b32 s65, 1152, 0\n"
    "s_sub_u32 s64, s64, s65\n"
    "s_add_u32 m0, s63, 32768\n"
    "s_add_u32 s65, s64, 0\n"
    "buffer_load_dwordx4 v250, s[36:39], s65 offen lds\n"
    "s_add_u32 m0, s63, 33792\n"
    "s_add_u32 s65, s64, 9216\n"
    "buffer_load_dwordx4 v249, s[36:39], s65 offen lds\n"
    "s_add_u32 m0, s63, 34816\n"
    "s_add_u32 s65, s64, 18432\n"
    "buffer_load_dwordx4 v250, s[36:39], s65 offen lds\n"
    "s_add_u32 m0, s63, 35840\n"
    "s_add_u32 s65, s64, 27648\n"
    "buffer_load_dwordx4 v249, s[36:39], s65 offen lds\n"
    "s_getpc_b64 s[96:97]\n"
    "98:\n"
    "s_lshr_b32 s67, %14, 3\n"
    "s_and_b32 s67, s67, 15\n"
    "s_lshl_b32 s67, s67, 2\n"
    "s_add_u32 s67, s67, %7\n"
    "s_lshl_b32 s67, s67, 7\n"
    "v_and_b32 v200, 7, v255\n"
    "v_lshlrev_b32 v200, 13, v200\n"
    "v_add_u32 v200, s67, v200\n"
    "v_min_u32 v200, 99f-98b-4, v200\n"
    "s_add_u32 m0, %8, 147456\n"
    "s_mov_b64 exec, 0xff\n"
    "global_load_lds_dword v200, s[96:97]\n"
    "s_mov_b64 exec, -1\n"
    "s_lshl_b32 s69, %5, 29\n"
    "s_lshr_b32 s69, s69, 27\n"
    "s_add_u32 s69, s69, %7\n"
    "s_lshl_b32 s69, s69, 2\n"
    "s_lshl_b32 s68, %15, 6\n"
    "s_add_u32 s69, s69, s68\n"
    "s_cmp_ge_u32 s69, 576\n"
    "s_cselect_b32 s68, 576, 0\n"
    "s_sub_u32 s69, s69, s68\n"
    "s_mul_i32 s69, s69, 3456\n"
    "s_add_u32 s69, s69, s59\n"
    "v_mul_u32_u24 v200, 43, v255\n"
    "v_lshrrev_b32 v200, 8, v200\n"
    "v_mul_u32_u24 v201, 6, v200\n"
    "v_sub_u32 v201, v255, v201\n"
    "v_lshrrev_b32 v202, 1, v201\n"
    "v_mul_u32_u24 v202, 0x480, v202\n"
    "v_and_b32 v201, 1, v201\n"
    "v_lshl_add_u32 v202, v201, 7, v202\n"
    "v_mul_u32_u24 v200, 0xd80, v200\n"
    "v_add_u32 v200, v200, v202\n"
    "s_add_u32 m0, %8, 147456\n"
    "s_mov_b64 exec, 0xffffff\n"
    "buffer_load_dword v200, s[40:43], s69 offen lds\n"
    "s_mov_b64 exec, -1\n"
    "s_waitcnt vmcnt(10)\n"
    "ds_read_b128 v[96:99], v251 offset:24576\n"
    "ds_read_b128 v[100:103], v251 offset:26624\n"
    "ds_read_b128 v[104:107], v252 offset:24576\n"
    "ds_read_b128 v[108:111], v252 offset:26624\n"
    "s_waitcnt lgkmcnt(4)\n"
    "v_dot2c_f32_bf16 v144, s53, v80\n"
    "v_dot2c_f32_bf16 v148, v80, v80\n"
    "v_dot2c_f32_bf16 v145, s53, v81\n"
    "v_dot2c_f32_bf16 v149, v81, v81\n"
    "v_dot2c_f32_bf16 v144, s53, v82\n"
    "v_dot2c_f32_bf16 v148, v82, v82\n"
    "v_dot2c_f32_bf16 v145, s53, v83\n"
    "v_dot2c_f32_bf16 v149, v83, v83\n"
    "v_dot2c_f32_bf16 v146, s53, v84\n"
    "v_dot2c_f32_bf16 v150, v84, v84\n"
    "v_dot2c_f32_bf16 v147, s53, v85\n"
    "v_dot2c_f32_bf16 v151, v85, v85\n"
    "v_dot2c_f32_bf16 v146, s53, v86\n"
    "v_dot2c_f32_bf16 v150, v86, v86\n"
    "v_dot2c_f32_bf16 v147, s53, v87\n"
    "v_dot2c_f32_bf16 v151, v87, v87\n"
    "v_dot2c_f32_bf16 v144, s53, v88\n"
    "v_dot2c_f32_bf16 v148, v88, v88\n"
    "v_dot2c_f32_bf16 v145, s53, v89\n"
    "v_dot2c_f32_bf16 v149, v89, v89\n"
    "v_dot2c_f32_bf16 v144, s53, v90\n"
    "v_dot2c_f32_bf16 v148, v90, v90\n"
    "v_dot2c_f32_bf16 v145, s53, v91\n"
    "v_dot2c_f32_bf16 v149, v91, v91\n"
    "v_dot2c_f32_bf16 v146, s53, v92\n"
    "v_dot2c_f32_bf16 v150, v92, v92\n"
    "v_dot2c_f32_bf16 v147, s53, v93\n"
    "v_dot2c_f32_bf16 v151, v93, v93\n"
    "v_dot2c_f32_bf16 v146, s53, v94\n"
    "v_dot2c_f32_bf16 v150, v94, v94\n"
    "v_dot2c_f32_bf16 v147, s53, v95\n"
    "v_dot2c_f32_bf16 v151, v95, v95\n"
    "s_waitcnt vmcnt(6)\n"
    "ds_read_b128 v[112:115], v251 offset:28672\n"
    "ds_read_b128 v[116:119], v251 offset:30720\n"
    "ds_read_b128 v[120:123], v252 offset:28672\n"
    "ds_read_b128 v[124:127], v252 offset:30720\n"
    "s_waitcnt lgkmcnt(4)\n"
    "v_dot2c_f32_bf16 v144, s53, v96\n"
    "v_dot2c_f32_bf16 v148, v96, v96\n"
    "v_dot2c_f32_bf16 v145, s53, v97\n"
    "v_dot2c_f32_bf16 v149, v97, v97\n"
    "v_dot2c_f32_bf16 v144, s53, v98\n"
    "v_dot2c_f32_bf16 v148, v98, v98\n"
    "v_dot2c_f32_bf16 v145, s53, v99\n"
    "v_dot2c_f32_bf16 v149, v99, v99\n"
    "v_dot2c_f32_bf16 v146, s53, v100\n"
    "v_dot2c_f32_bf16 v150, v100, v100\n"
    "v_dot2c_f32_bf16 v147, s53, v101\n"
    "v_dot2c_f32_bf16 v151, v101, v101\n"
    "v_dot2c_f32_bf16 v146, s53, v102\n"
    "v_dot2c_f32_bf16 v150, v102, v102\n"
    "v_dot2c_f32_bf16 v147, s53, v103\n"
    "v_dot2c_f32_bf16 v151, v103, v103\n"
    "v_dot2c_f32_bf16 v144, s53, v104\n"
    "v_dot2c_f32_bf16 v148, v104, v104\n"
    "v_dot2c_f32_bf16 v145, s53, v105\n"
    "v_dot2c_f32_bf16 v149, v105, v105\n"
    "v_dot2c_f32_bf16 v144, s53, v106\n"
    "v_dot2c_f32_bf16 v148, v106, v106\n"
    "v_dot2c_f32_bf16 v145, s53, v107\n"
    "v_dot2c_f32_bf16 v149, v107, v107\n"
    "v_dot2c_f32_bf16 v146, s53, v108\n"
    "v_dot2c_f32_bf16 v150, v108, v108\n"
    "v_dot2c_f32_bf16 v147, s53, v109\n"
    "v_dot2c_f32_bf16 v151, v109, v109\n"
    "v_dot2c_f32_bf16 v146, s53, v110\n"
    "v_dot2c_f32_bf16 v150, v110, v110\n"
    "v_dot2c_f32_bf16 v147, s53, v111\n"
    "v_dot2c_f32_bf16 v151, v111, v111\n"
    "s_waitcnt vmcnt(2)\n"
    "ds_read_b128 v[128:131], v251 offset:32768\n"
    "ds_read_b128 v[132:135], v251 offset:34816\n"
    "ds_read_b128 v[136:139], v252 offset:32768\n"
    "ds_read_b128 v[140:143], v252 offset:34816\n"
    "s_waitcnt lgkmcnt(4)\n"
    "v_dot2c_f32_bf16 v144, s53, v112\n"
    "v_dot2c_f32_bf16 v148, v112, v112\n"
    "v_dot2c_f32_bf16 v145, s53, v113\n"
    "v_dot2c_f32_bf16 v149, v113, v113\n"
    "v_dot2c_f32_bf16 v144, s53, v114\n"
    "v_dot2c_f32_bf16 v148, v114, v114\n"
    "v_dot2c_f32_bf16 v145, s53, v115\n"
    "v_dot2c_f32_bf16 v149, v115, v115\n"
    "v_dot2c_f32_bf16 v146, s53, v116\n"
    "v_dot2c_f32_bf16 v150, v116, v116\n"
    "v_dot2c_f32_bf16 v147, s53, v117\n"
    "v_dot2c_f32_bf16 v151, v117, v117\n"
    "v_dot2c_f32_bf16 v146, s53, v118\n"
    "v_dot2c_f32_bf16 v150, v118, v118\n"
    "v_dot2c_f32_bf16 v147, s53, v119\n"
    "v_dot2c_f32_bf16 v151, v119, v119\n"
    "v_dot2c_f32_bf16 v144, s53, v120\n"
    "v_dot2c_f32_bf16 v148, v120, v120\n"
    "v_dot2c_f32_bf16 v145, s53, v121\n"
    "v_dot2c_f32_bf16 v149, v121, v121\n"
    "v_dot2c_f32_bf16 v144, s53, v122\n"
    "v_dot2c_f32_bf16 v148, v122, v122\n"
    "v_dot2c_f32_bf16 v145, s53, v123\n"
    "v_dot2c_f32_bf16 v149, v123, v123\n"
    "v_dot2c_f32_bf16 v146, s53, v124\n"
    "v_dot2c_f32_bf16 v150, v124, v124\n"
    "v_dot2c_f32_bf16 v147, s53, v125\n"
    "v_dot2c_f32_bf16 v151, v125, v125\n"
    "v_dot2c_f32_bf16 v146, s53, v126\n"
    "v_dot2c_f32_bf16 v150, v126, v126\n"
    "v_dot2c_f32_bf16 v147, s53, v127\n"
    "v_dot2c_f32_bf16 v151, v127, v127\n"
    "s_waitcnt lgkmcnt(0)\n"
    "s_add_u32 m0, %8, 147456\n"
    "s_nop 0\n"
    "buffer_load_dword v232, s[40:43], s59 offen lds\n"
    "v_readfirstlane_b32 s78, v244\n"
    "v_readfirstlane_b32 s62, v245\n"
    "v_readfirstlane_b32 s79, v246\n"
    "s_nop 1\n"
    "s_add_u32 s78, s78, 1\n"
    "s_add_u32 s62, s62, 1\n"
    "s_add_u32 s79, s79, 1\n"
    "s_mov_b32 s67, %15\n"
    "s_mul_i32 s58, s67, 221184\n"
    "s_add_u32 s58, s58, s59\n"
    "s_add_u32 s67, s67, 1\n"
    "s_cmp_eq_u32 s67, 9\n"
    "s_cselect_b32 s67, 0, s67\n"
    "s_mul_i32 s66, s67, 221184\n"
    "s_add_u32 s66, s66, s59\n"
    "s_add_u32 s67, s67, 1\n"
    "s_cmp_eq_u32 s67, 9\n"
    "s_cselect_b32 s67, 0, s67\n"
    "s_mul_i32 s69, s67, 221184\n"
    "s_add_u32 s69, s69, s59\n"
    "s_add_u32 s67, s67, 1\n"
    "s_cmp_eq_u32 s67, 9\n"
    "s_cselect_b32 s67, 0, s67\n"
    "s_mul_i32 s70, s67, 221184\n"
    "s_add_u32 s70, s70, s59\n"
    "s_add_u32 s67, s67, 1\n"
    "s_cmp_eq_u32 s67, 9\n"
    "s_cselect_b32 s67, 0, s67\n"
    "s_mul_i32 s71, s67, 221184\n"
    "s_add_u32 s71, s71, s59\n"
    "s_add_u32 s67, s67, 1\n"
    "s_cmp_eq_u32 s67, 9\n"
    "s_cselect_b32 s67, 0, s67\n"
    "s_mul_i32 s72, s67, 221184\n"
    "s_add_u32 s72, s72, s59\n"
    "s_add_u32 s67, s67, 1\n"
    "s_cmp_eq_u32 s67, 9\n"
    "s_cselect_b32 s67, 0, s67\n"
    "s_mul_i32 s73, s67, 221184\n"
    "s_add_u32 s73, s73, s59\n"
    "s_add_u32 s67, s67, 1\n"
    "s_cmp_eq_u32 s67, 9\n"
    "s_cselect_b32 s67, 0, s67\n"
    "s_mul_i32 s76, s67, 221184\n"
    "s_add_u32 s76, s76, s59\n"
    "s_add_u32 s67, s67, 1\n"
    "s_cmp_eq_u32 s67, 9\n"
    "s_cselect_b32 s67, 0, s67\n"
    "s_mul_i32 s77, s67, 221184\n"
    "s_add_u32 s77, s77, s59\n"
    "s_add_u32 s67, s67, 1\n"
    "s_cmp_eq_u32 s67, 9\n"
    "s_cselect_b32 s67, 0, s67\n"
    "s_barrier\n"
    "v_dot2c_f32_bf16 v144, s53, v128\n"
    "v_dot2c_f32_bf16 v148, v128, v128\n"
    "v_dot2c_f32_bf16 v145, s53, v129\n"
    "v_dot2c_f32_bf16 v149, v129, v129\n"
    "v_dot2c_f32_bf16 v144, s53, v130\n"
    "v_dot2c_f32_bf16 v148, v130, v130\n"
    "v_dot2c_f32_bf16 v145, s53, v131\n"
    "v_dot2c_f32_bf16 v149, v131, v131\n"
    "v_dot2c_f32_bf16 v146, s53, v132\n"
    "v_dot2c_f32_bf16 v150, v132, v132\n"
    "v_dot2c_f32_bf16 v147, s53, v133\n"
    "v_dot2c_f32_bf16 v151, v133, v133\n"
    "v_dot2c_f32_bf16 v146, s53, v134\n"
    "v_dot2c_f32_bf16 v150, v134, v134\n"
    "v_dot2c_f32_bf16 v147, s53, v135\n"
    "v_dot2c_f32_bf16 v151, v135, v135\n"
    "v_dot2c_f32_bf16 v144, s53, v136\n"
    "v_dot2c_f32_bf16 v148, v136, v136\n"
    "v_dot2c_f32_bf16 v145, s53, v137\n"
    "v_dot2c_f32_bf16 v149, v137, v137\n"
    "v_dot2c_f32_bf16 v144, s53, v138\n"
    "v_dot2c_f32_bf16 v148, v138, v138\n"
    "v_dot2c_f32_bf16 v145, s53, v139\n"
    "v_dot2c_f32_bf16 v149, v139, v139\n"
    "v_dot2c_f32_bf16 v146, s53, v140\n"
    "v_dot2c_f32_bf16 v150, v140, v140\n"
    "v_dot2c_f32_bf16 v147, s53, v141\n"
    "v_dot2c_f32_bf16 v151, v141, v141\n"
    "v_dot2c_f32_bf16 v146, s53, v142\n"
    "v_dot2c_f32_bf16 v150, v142, v142\n"
    "v_dot2c_f32_bf16 v147, s53, v143\n"
    "v_dot2c_f32_bf16 v151, v143, v143\n"
    "s_nop 2\n"
    "v_add_f32 v144, v144, v145\n"
    "v_add_f32 v146, v146, v147\n"
    "v_add_f32 v148, v148, v149\n"
    "v_add_f32 v150, v150, v151\n"
    "v_mov_b32 v145, v144\n"
    "v_mov_b32 v147, v146\n"
    "v_mov_b32 v149, v148\n"
    "v_mov_b32 v151, v150\n"
    "s_nop 1\n"
    "v_permlane16_swap_b32 v144, v145\n"
    "v_permlane16_swap_b32 v146, v147\n"
    "v_permlane16_swap_b32 v148, v149\n"
    "v_permlane16_swap_b32 v150, v151\n"
    "s_nop 1\n"
    "v_add_f32 v144, v144, v145\n"
    "v_add_f32 v146, v146, v147\n"
    "v_add_f32 v148, v148, v149\n"
    "v_add_f32 v150, v150, v151\n"
    "v_mov_b32 v145, v144\n"
    "v_mov_b32 v147, v146\n"
    "v_mov_b32 v149, v148\n"
    "v_mov_b32 v151, v150\n"
    "s_nop 1\n"
    "v_permlane32_swap_b32 v144, v145\n"
    "v_permlane32_swap_b32 v146, v147\n"
    "v_permlane32_swap_b32 v148, v149\n"
    "v_permlane32_swap_b32 v150, v151\n"
    "s_nop 1\n"
    "v_add_f32 v144, v144, v145\n"
    "v_add_f32 v146, v146, v147\n"
    "v_add_f32 v148, v148, v149\n"
    "v_add_f32 v150, v150, v151\n"
    "v_mul_f32 v144, 0x3ae38e39, v144\n"
    "v_mul_f32 v146, 0x3ae38e39, v146\n"
    "v_mul_f32 v148, 0x3ae38e39, v148\n"
    "v_mul_f32 v150, 0x3ae38e39, v150\n"
    "v_fma_f32 v148, -v144, v144, v148\n"
    "v_max_f32 v148, 0, v148\n"
    "v_add_f32 v148, 0x3727c5ac, v148\n"
    "v_rsq_f32 v145, v148\n"
    "v_sqrt_f32 v148, v148\n"
    "v_mov_b32 v149, 0\n"
    "v_mul_f32 v145, 1.0, v145\n"
    "v_cvt_pk_bf16_f32 v152, v145, v149\n"
    "v_lshlrev_b32 v153, 16, v152\n"
    "v_mul_f32_e64 v154, -v144, v153\n"
    "v_mul_f32 v228, v148, v153\n"
    "v_fma_f32 v150, -v146, v146, v150\n"
    "v_max_f32 v150, 0, v150\n"
    "v_add_f32 v150, 0x3727c5ac, v150\n"
    "v_rsq_f32 v147, v150\n"
    "v_sqrt_f32 v150, v150\n"
    "v_mov_b32 v151, 0\n"
    "v_mul_f32 v147, 1.0, v147\n"
    "v_cvt_pk_bf16_f32 v155, v147, v151\n"
    "v_lshlrev_b32 v156, 16, v155\n"
    "v_mul_f32_e64 v157, -v146, v156\n"
    "v_mul_f32 v229, v150, v156\n"
    "v_rcp_f32 v228, v228\n"
    "v_rcp_f32 v229, v229\n"
    "s_nop 0\n"
    "v_mul_f32 v230, 0x3eae1a93, v228\n"
    "v_mul_f32 v231, 0x3eae1a93, v229\n"
    "v_dot2_f32_bf16 v158, v0, v152, v154\n"
    "v_dot2_f32_bf16 v159, v0, v153, v154\n"
    "v_dot2_f32_bf16 v160, v1, v152, v154\n"
    "v_dot2_f32_bf16 v161, v1, v153, v154\n"
    "v_dot2_f32_bf16 v162, v2, v152, v154\n"
    "v_dot2_f32_bf16 v163, v2, v153, v154\n"
    "v_cvt_pk_bf16_f32 v0, v158, v159\n"
    "v_dot2_f32_bf16 v164, v3, v152, v154\n"
    "v_dot2_f32_bf16 v165, v3, v153, v154\n"
    "v_cvt_pk_bf16_f32 v1, v160, v161\n"
    "v_dot2_f32_bf16 v158, v4, v155, v157\n"
    "v_dot2_f32_bf16 v159, v4, v156, v157\n"
    "v_cvt_pk_bf16_f32 v2, v162, v163\n"
    "v_dot2_f32_bf16 v160, v5, v155, v157\n"
    "v_dot2_f32_bf16 v161, v5, v156, v157\n"
    "v_cvt_pk_bf16_f32 v3, v164, v165\n"
    "v_dot2_f32_bf16 v162, v6, v155, v157\n"
    "v_dot2_f32_bf16 v163, v6, v156, v157\n"
    "v_cvt_pk_bf16_f32 v4, v158, v159\n"
    "v_dot2_f32_bf16 v164, v7, v155, v157\n"
    "v_dot2_f32_bf16 v165, v7, v156, v157\n"
    "v_cvt_pk_bf16_f32 v5, v160, v161\n"
    "v_dot2_f32_bf16 v158, v8, v152, v154\n"
    "v_dot2_f32_bf16 v159, v8, v153, v154\n"
    "v_cvt_pk_bf16_f32 v6, v162, v163\n"
    "v_dot2_f32_bf16 v160, v9, v152, v154\n"
    "v_dot2_f32_bf16 v161, v9, v153, v154\n"
    "v_cvt_pk_bf16_f32 v7, v164, v165\n"
    "v_dot2_f32_bf16 v162, v10, v152, v154\n"
    "v_dot2_f32_bf16 v163, v10, v153, v154\n"
    "v_cvt_pk_bf16_f32 v8, v158, v159\n"
    "v_dot2_f32_bf16 v164, v11, v152, v154\n"
    "v_dot2_f32_bf16 v165, v11, v153, v154\n"
    "v_cvt_pk_bf16_f32 v9, v160, v161\n"
    "v_dot2_f32_bf16 v158, v12, v155, v157\n"
    "v_dot2_f32_bf16 v159, v12, v156, v157\n"
    "v_cvt_pk_bf16_f32 v10, v162, v163\n"
    "v_dot2_f32_bf16 v160, v13, v155, v157\n"
    "v_dot2_f32_bf16 v161, v13, v156, v157\n"
    "v_cvt_pk_bf16_f32 v11, v164, v165\n"
    "v_dot2_f32_bf16 v162, v14, v155, v157\n"
    "v_dot2_f32_bf16 v163, v14, v156, v157\n"
    "v_cvt_pk_bf16_f32 v12, v158, v159\n"
    "v_dot2_f32_bf16 v164, v15, v155, v157\n"
    "v_dot2_f32_bf16 v165, v15, v156, v157\n"
    "v_cvt_pk_bf16_f32 v13, v160, v161\n"
    "v_dot2_f32_bf16 v158, v16, v152, v154\n"
    "v_dot2_f32_bf16 v159, v16, v153, v154\n"
    "v_cvt_pk_bf16_f32 v14, v162, v163\n"
    "v_dot2_f32_bf16 v160, v17, v152, v154\n"
    "v_dot2_f32_bf16 v161, v17, v153, v154\n"
    "v_cvt_pk_bf16_f32 v15, v164, v165\n"
    "v_dot2_f32_bf16 v162, v18, v152, v154\n"
    "v_dot2_f32_bf16 v163, v18, v153, v154\n"
    "v_cvt_pk_bf16_f32 v16, v158, v159\n"
    "v_dot2_f32_bf16 v164, v19, v152, v154\n"
    "v_dot2_f32_bf16 v165, v19, v153, v154\n"
    "v_cvt_pk_bf16_f32 v17, v160, v161\n"
    "v_dot2_f32_bf16 v158, v20, v155, v157\n"
    "v_dot2_f32_bf16 v159, v20, v156, v157\n"
    "v_cvt_pk_bf16_f32 v18, v162, v163\n"
    "v_dot2_f32_bf16 v160, v21, v155, v157\n"
    "v_dot2_f32_bf16 v161, v21, v156, v157\n"
    "v_cvt_pk_bf16_f32 v19, v164, v165\n"
    "v_dot2_f32_bf16 v162, v22, v155, v157\n"
    "v_dot2_f32_bf16 v163, v22, v156, v157\n"
    "v_cvt_pk_bf16_f32 v20, v158, v159\n"
    "v_dot2_f32_bf16 v164, v23, v155, v157\n"
    "v_dot2_f32_bf16 v165, v23, v156, v157\n"
    "v_cvt_pk_bf16_f32 v21, v160, v161\n"
    "v_dot2_f32_bf16 v158, v24, v152, v154\n"
    "v_dot2_f32_bf16 v159, v24, v153, v154\n"
    "v_cvt_pk_bf16_f32 v22, v162, v163\n"
    "v_dot2_f32_bf16 v160, v25, v152, v154\n"
    "v_dot2_f32_bf16 v161, v25, v153, v154\n"
    "v_cvt_pk_bf16_f32 v23, v164, v165\n"
    "v_dot2_f32_bf16 v162, v26, v152, v154\n"
    "v_dot2_f32_bf16 v163, v26, v153, v154\n"
    "v_cvt_pk_bf16_f32 v24, v158, v159\n"
    "v_dot2_f32_bf16 v164, v27, v152, v154\n"
    "v_dot2_f32_bf16 v165, v27, v153, v154\n"
    "v_cvt_pk_bf16_f32 v25, v160, v161\n"
    "v_dot2_f32_bf16 v158, v28, v155, v157\n"
    "v_dot2_f32_bf16 v159, v28, v156, v157\n"
    "v_cvt_pk_bf16_f32 v26, v162, v163\n"
    "v_dot2_f32_bf16 v160, v29, v155, v157\n"
    "v_dot2_f32_bf16 v161, v29, v156, v157\n"
    "v_cvt_pk_bf16_f32 v27, v164, v165\n"
    "v_dot2_f32_bf16 v162, v30, v155, v157\n"
    "v_dot2_f32_bf16 v163, v30, v156, v157\n"
    "v_cvt_pk_bf16_f32 v28, v158, v159\n"
    "v_dot2_f32_bf16 v164, v31, v155, v157\n"
    "v_dot2_f32_bf16 v165, v31, v156, v157\n"
    "v_cvt_pk_bf16_f32 v29, v160, v161\n"
    "v_dot2_f32_bf16 v158, v32, v152, v154\n"
    "v_dot2_f32_bf16 v159, v32, v153, v154\n"
    "v_cvt_pk_bf16_f32 v30, v162, v163\n"
    "v_dot2_f32_bf16 v160, v33, v152, v154\n"
    "v_dot2_f32_bf16 v161, v33, v153, v154\n"
    "v_cvt_pk_bf16_f32 v31, v164, v165\n"
    "v_dot2_f32_bf16 v162, v34, v152, v154\n"
    "v_dot2_f32_bf16 v163, v34, v153, v154\n"
    "v_cvt_pk_bf16_f32 v32, v158, v159\n"
    "v_dot2_f32_bf16 v164, v35, v152, v154\n"
    "v_dot2_f32_bf16 v165, v35, v153, v154\n"
    "v_cvt_pk_bf16_f32 v33, v160, v161\n"
    "v_dot2_f32_bf16 v158, v36, v155, v157\n"
    "v_dot2_f32_bf16 v159, v36, v156, v157\n"
    "v_cvt_pk_bf16_f32 v34, v162, v163\n"
    "v_dot2_f32_bf16 v160, v37, v155, v157\n"
    "v_dot2_f32_bf16 v161, v37, v156, v157\n"
    "v_cvt_pk_bf16_f32 v35, v164, v165\n"
    "v_dot2_f32_bf16 v162, v38, v155, v157\n"
    "v_dot2_f32_bf16 v163, v38, v156, v157\n"
    "v_cvt_pk_bf16_f32 v36, v158, v159\n"
    "v_dot2_f32_bf16 v164, v39, v155, v157\n"
    "v_dot2_f32_bf16 v165, v39, v156, v157\n"
    "v_cvt_pk_bf16_f32 v37, v160, v161\n"
    "s_mov_b32 s65, s58\n"
    "s_add_u32 m0, s60, 0\n"
    "v_dot2_f32_bf16 v158, v40, v152, v154\n"
    "buffer_load_dwordx4 v216, s[40:43], s65 offen lds\n"
    "v_dot2_f32_bf16 v159, v40, v153, v154\n"
    "v_cvt_pk_bf16_f32 v38, v162, v163\n"
    "v_dot2_f32_bf16 v160, v41, v152, v154\n"
    "v_dot2_f32_bf16 v161, v41, v153, v154\n"
    "v_cvt_pk_bf16_f32 v39, v164, v165\n"
    "v_dot2_f32_bf16 v162, v42, v152, v154\n"
    "v_dot2_f32_bf16 v163, v42, v153, v154\n"
    "v_cvt_pk_bf16_f32 v40, v158, v159\n"
    "v_dot2_f32_bf16 v164, v43, v152, v154\n"
    "v_dot2_f32_bf16 v165, v43, v153, v154\n"
    "v_cvt_pk_bf16_f32 v41, v160, v161\n"
    "s_add_u32 m0, s60, 1024\n"
    "v_dot2_f32_bf16 v158, v44, v155, v157\n"
    "buffer_load_dwordx4 v217, s[40:43], s65 offen lds\n"
    "v_dot2_f32_bf16 v159, v44, v156, v157\n"
    "v_cvt_pk_bf16_f32 v42, v162, v163\n"
    "v_dot2_f32_bf16 v160, v45, v155, v157\n"
    "v_dot2_f32_bf16 v161, v45, v156, v157\n"
    "v_cvt_pk_bf16_f32 v43, v164, v165\n"
    "v_dot2_f32_bf16 v162, v46, v155, v157\n"
    "v_dot2_f32_bf16 v163, v46, v156, v157\n"
    "v_cvt_pk_bf16_f32 v44, v158, v159\n"
    "v_dot2_f32_bf16 v164, v47, v155, v157\n"
    "v_dot2_f32_bf16 v165, v47, v156, v157\n"
    "v_cvt_pk_bf16_f32 v45, v160, v161\n"
    "s_add_u32 m0, s60, 2048\n"
    "v_dot2_f32_bf16 v158, v48, v152, v154\n"
    "buffer_load_dwordx4 v218, s[40:43], s65 offen lds\n"
    "v_dot2_f32_bf16 v159, v48, v153, v154\n"
    "v_cvt_pk_bf16_f32 v46, v162, v163\n"
    "v_dot2_f32_bf16 v160, v49, v152, v154\n"
    "v_dot2_f32_bf16 v161, v49, v153, v154\n"
    "v_cvt_pk_bf16_f32 v47, v164, v165\n"
    "v_dot2_f32_bf16 v162, v50, v152, v154\n"
    "v_dot2_f32_bf16 v163, v50, v153, v154\n"
    "v_cvt_pk_bf16_f32 v48, v158, v159\n"
    "v_dot2_f32_bf16 v164, v51, v152, v154\n"
    "v_dot2_f32_bf16 v165, v51, v153, v154\n"
    "v_cvt_pk_bf16_f32 v49, v160, v161\n"
    "s_add_u32 m0, s60, 3072\n"
    "v_dot2_f32_bf16 v158, v52, v155, v157\n"
    "buffer_load_dwordx4 v219, s[40:43], s65 offen lds\n"
    "v_dot2_f32_bf16 v159, v52, v156, v157\n"
    "v_cvt_pk_bf16_f32 v50, v162, v163\n"
    "v_dot2_f32_bf16 v160, v53, v155, v157\n"
    "v_dot2_f32_bf16 v161, v53, v156, v157\n"
    "v_cvt_pk_bf16_f32 v51, v164, v165\n"
    "v_dot2_f32_bf16 v162, v54, v155, v157\n"
    "v_dot2_f32_bf16 v163, v54, v156, v157\n"
    "v_cvt_pk_bf16_f32 v52, v158, v159\n"
    "v_dot2_f32_bf16 v164, v55, v155, v157\n"
    "v_dot2_f32_bf16 v165, v55, v156, v157\n"
    "v_cvt_pk_bf16_f32 v53, v160, v161\n"
    "s_add_u32 m0, s60, 4096\n"
    "v_dot2_f32_bf16 v158, v56, v152, v154\n"
    "buffer_load_dwordx4 v220, s[40:43], s65 offen lds\n"
    "v_dot2_f32_bf16 v159, v56, v153, v154\n"
    "v_cvt_pk_bf16_f32 v54, v162, v163\n"
    "v_dot2_f32_bf16 v160, v57, v152, v154\n"
    "v_dot2_f32_bf16 v161, v57, v153, v154\n"
    "v_cvt_pk_bf16_f32 v55, v164, v165\n"
    "v_dot2_f32_bf16 v162, v58, v152, v154\n"
    "v_dot2_f32_bf16 v163, v58, v153, v154\n"
    "v_cvt_pk_bf16_f32 v56, v158, v159\n"
    "v_dot2_f32_bf16 v164, v59, v152, v154\n"
    "v_dot2_f32_bf16 v165, v59, v153, v154\n"
    "v_cvt_pk_bf16_f32 v57, v160, v161\n"
    "s_add_u32 m0, s60, 5120\n"
    "v_dot2_f32_bf16 v158, v60, v155, v157\n"
    "buffer_load_dwordx4 v221, s[40:43], s65 offen lds\n"
    "v_dot2_f32_bf16 v159, v60, v156, v157\n"
    "v_cvt_pk_bf16_f32 v58, v162, v163\n"
    "v_dot2_f32_bf16 v160, v61, v155, v157\n"
    "v_dot2_f32_bf16 v161, v61, v156, v157\n"
    "v_cvt_pk_bf16_f32 v59, v164, v165\n"
    "v_dot2_f32_bf16 v162, v62, v155, v157\n"
    "v_dot2_f32_bf16 v163, v62, v156, v157\n"
    "v_cvt_pk_bf16_f32 v60, v158, v159\n"
    "v_dot2_f32_bf16 v164, v63, v155, v157\n"
    "v_dot2_f32_bf16 v165, v63, v156, v157\n"
    "v_cvt_pk_bf16_f32 v61, v160, v161\n"
    "s_add_u32 m0, s60, 6144\n"
    "v_dot2_f32_bf16 v158, v64, v152, v154\n"
    "buffer_load_dwordx4 v222, s[40:43], s65 offen lds\n"
    "v_dot2_f32_bf16 v159, v64, v153, v154\n"
    "v_cvt_pk_bf16_f32 v62, v162, v163\n"
    "v_dot2_f32_bf16 v160, v65, v152, v154\n"
    "v_dot2_f32_bf16 v161, v65, v153, v154\n"
    "v_cvt_pk_bf16_f32 v63, v164, v165\n"
    "v_dot2_f32_bf16 v162, v66, v152, v154\n"
    "v_dot2_f32_bf16 v163, v66, v153, v154\n"
    "v_cvt_pk_bf16_f32 v64, v158, v159\n"
    "v_dot2_f32_bf16 v164, v67, v152, v154\n"
    "v_dot2_f32_bf16 v165, v67, v153, v154\n"
    "v_cvt_pk_bf16_f32 v65, v160, v161\n"
    "s_add_u32 m0, s60, 7168\n"
    "v_dot2_f32_bf16 v158, v68, v155, v157\n"
    "buffer_load_dwordx4 v223, s[40:43], s65 offen lds\n"
    "v_dot2_f32_bf16 v159, v68, v156, v157\n"
    "v_cvt_pk_bf16_f32 v66, v162, v163\n"
    "v_dot2_f32_bf16 v160, v69, v155, v157\n"
    "v_dot2_f32_bf16 v161, v69, v156, v157\n"
    "v_cvt_pk_bf16_f32 v67, v164, v165\n"
    "v_dot2_f32_bf16 v162, v70, v155, v157\n"
    "v_dot2_f32_bf16 v163, v70, v156, v157\n"
    "v_cvt_pk_bf16_f32 v68, v158, v159\n"
    "v_dot2_f32_bf16 v164, v71, v155, v157\n"
    "v_dot2_f32_bf16 v165, v71, v156, v157\n"
    "v_cvt_pk_bf16_f32 v69, v160, v161\n"
    "s_mov_b32 s65, s66\n"
    "s_add_u32 m0, s60, 32768\n"
    "v_dot2_f32_bf16 v158, v72, v152, v154\n"
    "buffer_load_dwordx4 v216, s[40:43], s65 offen lds\n"
    "v_dot2_f32_bf16 v159, v72, v153, v154\n"
    "v_cvt_pk_bf16_f32 v70, v162, v163\n"
    "v_dot2_f32_bf16 v160, v73, v152, v154\n"
    "v_dot2_f32_bf16 v161, v73, v153, v154\n"
    "v_cvt_pk_bf16_f32 v71, v164, v165\n"
    "v_dot2_f32_bf16 v162, v74, v152, v154\n"
    "v_dot2_f32_bf16 v163, v74, v153, v154\n"
    "v_cvt_pk_bf16_f32 v72, v158, v159\n"
    "v_dot2_f32_bf16 v164, v75, v152, v154\n"
    "v_dot2_f32_bf16 v165, v75, v153, v154\n"
    "v_cvt_pk_bf16_f32 v73, v160, v161\n"
    "s_add_u32 m0, s60, 33792\n"
    "v_dot2_f32_bf16 v158, v76, v155, v157\n"
    "buffer_load_dwordx4 v217, s[40:43], s65 offen lds\n"
    "v_dot2_f32_bf16 v159, v76, v156, v157\n"
    "v_cvt_pk_bf16_f32 v74, v162, v163\n"
    "v_dot2_f32_bf16 v160, v77, v155, v157\n"
    "v_dot2_f32_bf16 v161, v77, v156, v157\n"
    "v_cvt_pk_bf16_f32 v75, v164, v165\n"
    "v_dot2_f32_bf16 v162, v78, v155, v157\n"
    "v_dot2_f32_bf16 v163, v78, v156, v157\n"
    "v_cvt_pk_bf16_f32 v76, v158, v159\n"
    "v_dot2_f32_bf16 v164, v79, v155, v157\n"
    "v_dot2_f32_bf16 v165, v79, v156, v157\n"
    "v_cvt_pk_bf16_f32 v77, v160, v161\n"
    "s_add_u32 m0, s60, 34816\n"
    "v_dot2_f32_bf16 v158, v80, v152, v154\n"
    "buffer_load_dwordx4 v218, s[40:43], s65 offen lds\n"
    "v_dot2_f32_bf16 v159, v80, v153, v154\n"
    "v_cvt_pk_bf16_f32 v78, v162, v163\n"
    "v_dot2_f32_bf16 v160, v81, v152, v154\n"
    "v_dot2_f32_bf16 v161, v81, v153, v154\n"
    "v_cvt_pk_bf16_f32 v79, v164, v165\n"
    "v_dot2_f32_bf16 v162, v82, v152, v154\n"
    "v_dot2_f32_bf16 v163, v82, v153, v154\n"
    "v_cvt_pk_bf16_f32 v80, v158, v159\n"
    "v_dot2_f32_bf16 v164, v83, v152, v154\n"
    "v_dot2_f32_bf16 v165, v83, v153, v154\n"
    "v_cvt_pk_bf16_f32 v81, v160, v161\n"
    "s_add_u32 m0, s60, 35840\n"
    "v_dot2_f32_bf16 v158, v84, v155, v157\n"
    "buffer_load_dwordx4 v219, s[40:43], s65 offen lds\n"
    "v_dot2_f32_bf16 v159, v84, v156, v157\n"
    "v_cvt_pk_bf16_f32 v82, v162, v163\n"
    "v_dot2_f32_bf16 v160, v85, v155, v157\n"
    "v_dot2_f32_bf16 v161, v85, v156, v157\n"
    "v_cvt_pk_bf16_f32 v83, v164, v165\n"
    "v_dot2_f32_bf16 v162, v86, v155, v157\n"
    "v_dot2_f32_bf16 v163, v86, v156, v157\n"
    "v_cvt_pk_bf16_f32 v84, v158, v159\n"
    "v_dot2_f32_bf16 v164, v87, v155, v157\n"
    "v_dot2_f32_bf16 v165, v87, v156, v157\n"
    "v_cvt_pk_bf16_f32 v85, v160, v161\n"
    "s_add_u32 m0, s60, 36864\n"
    "v_dot2_f32_bf16 v158, v88, v152, v154\n"
    "buffer_load_dwordx4 v220, s[40:43], s65 offen lds\n"
    "v_dot2_f32_bf16 v159, v88, v153, v154\n"
    "v_cvt_pk_bf16_f32 v86, v162, v163\n"
    "v_dot2_f32_bf16 v160, v89, v152, v154\n"
    "v_dot2_f32_bf16 v161, v89, v153, v154\n"
    "v_cvt_pk_bf16_f32 v87, v164, v165\n"
    "v_dot2_f32_bf16 v162, v90, v152, v154\n"
    "v_dot2_f32_bf16 v163, v90, v153, v154\n"
    "v_cvt_pk_bf16_f32 v88, v158, v159\n"
    "v_dot2_f32_bf16 v164, v91, v152, v154\n"
    "v_dot2_f32_bf16 v165, v91, v153, v154\n"
    "v_cvt_pk_bf16_f32 v89, v160, v161\n"
    "s_add_u32 m0, s60, 37888\n"
    "v_dot2_f32_bf16 v158, v92, v155, v157\n"
    "buffer_load_dwordx4 v221, s[40:43], s65 offen lds\n"
    "v_dot2_f32_bf16 v159, v92, v156, v157\n"
    "v_cvt_pk_bf16_f32 v90, v162, v163\n"
    "v_dot2_f32_bf16 v160, v93, v155, v157\n"
    "v_dot2_f32_bf16 v161, v93, v156, v157\n"
    "v_cvt_pk_bf16_f32 v91, v164, v165\n"
    "v_dot2_f32_bf16 v162, v94, v155, v157\n"
    "v_dot2_f32_bf16 v163, v94, v156, v157\n"
    "v_cvt_pk_bf16_f32 v92, v158, v159\n"
    "v_dot2_f32_bf16 v164, v95, v155, v157\n"
    "v_dot2_f32_bf16 v165, v95, v156, v157\n"
    "v_cvt_pk_bf16_f32 v93, v160, v161\n"
    "s_add_u32 m0, s60, 38912\n"
    "v_dot2_f32_bf16 v158, v96, v152, v154\n"
    "buffer_load_dwordx4 v222, s[40:43], s65 offen lds\n"
    "v_dot2_f32_bf16 v159, v96, v153, v154\n"
    "v_cvt_pk_bf16_f32 v94, v162, v163\n"
    "v_dot2_f32_bf16 v160, v97, v152, v154\n"
    "v_dot2_f32_bf16 v161, v97, v153, v154\n"
    "v_cvt_pk_bf16_f32 v95, v164, v165\n"
    "v_dot2_f32_bf16 v162, v98, v152, v154\n"
    "v_dot2_f32_bf16 v163, v98, v153, v154\n"
    "v_cvt_pk_bf16_f32 v96, v158, v159\n"
    "v_dot2_f32_bf16 v164, v99, v152, v154\n"
    "v_dot2_f32_bf16 v165, v99, v153, v154\n"
    "v_cvt_pk_bf16_f32 v97, v160, v161\n"
    "s_add_u32 m0, s60, 39936\n"
    "v_dot2_f32_bf16 v158, v100, v155, v157\n"
    "buffer_load_dwordx4 v223, s[40:43], s65 offen lds\n"
    "v_dot2_f32_bf16 v159, v100, v156, v157\n"
    "v_cvt_pk_bf16_f32 v98, v162, v163\n"
    "v_dot2_f32_bf16 v160, v101, v155, v157\n"
    "v_dot2_f32_bf16 v161, v101, v156, v157\n"
    "v_cvt_pk_bf16_f32 v99, v164, v165\n"
    "v_dot2_f32_bf16 v162, v102, v155, v157\n"
    "v_dot2_f32_bf16 v163, v102, v156, v157\n"
    "v_cvt_pk_bf16_f32 v100, v158, v159\n"
    "v_dot2_f32_bf16 v164, v103, v155, v157\n"
    "v_dot2_f32_bf16 v165, v103, v156, v157\n"
    "v_cvt_pk_bf16_f32 v101, v160, v161\n"
    "v_dot2_f32_bf16 v158, v104, v152, v154\n"
    "v_dot2_f32_bf16 v159, v104, v153, v154\n"
    "v_cvt_pk_bf16_f32 v102, v162, v163\n"
    "v_dot2_f32_bf16 v160, v105, v152, v154\n"
    "v_dot2_f32_bf16 v161, v105, v153, v154\n"
    "v_cvt_pk_bf16_f32 v103, v164, v165\n"
    "v_dot2_f32_bf16 v162, v106, v152, v154\n"
    "v_dot2_f32_bf16 v163, v106, v153, v154\n"
    "v_cvt_pk_bf16_f32 v104, v158, v159\n"
    "v_dot2_f32_bf16 v164, v107, v152, v154\n"
    "v_dot2_f32_bf16 v165, v107, v153, v154\n"
    "v_cvt_pk_bf16_f32 v105, v160, v161\n"
    "v_dot2_f32_bf16 v158, v108, v155, v157\n"
    "v_dot2_f32_bf16 v159, v108, v156, v157\n"
    "v_cvt_pk_bf16_f32 v106, v162, v163\n"
    "v_dot2_f32_bf16 v160, v109, v155, v157\n"
    "v_dot2_f32_bf16 v161, v109, v156, v157\n"
    "v_cvt_pk_bf16_f32 v107, v164, v165\n"
    "v_dot2_f32_bf16 v162, v110, v155, v157\n"
    "v_dot2_f32_bf16 v163, v110, v156, v157\n"
    "v_cvt_pk_bf16_f32 v108, v158, v159\n"
    "v_dot2_f32_bf16 v164, v111, v155, v157\n"
    "v_dot2_f32_bf16 v165, v111, v156, v157\n"
    "v_cvt_pk_bf16_f32 v109, v160, v161\n"
    "v_dot2_f32_bf16 v158, v112, v152, v154\n"
    "v_dot2_f32_bf16 v159, v112, v153, v154\n"
    "v_cvt_pk_bf16_f32 v110, v162, v163\n"
    "v_dot2_f32_bf16 v160, v113, v152, v154\n"
    "v_dot2_f32_bf16 v161, v113, v153, v154\n"
    "v_cvt_pk_bf16_f32 v111, v164, v165\n"
    "v_dot2_f32_bf16 v162, v114, v152, v154\n"
    "v_dot2_f32_bf16 v163, v114, v153, v154\n"
    "v_cvt_pk_bf16_f32 v112, v158, v159\n"
    "v_dot2_f32_bf16 v164, v115, v152, v154\n"
    "v_dot2_f32_bf16 v165, v115, v153, v154\n"
    "v_cvt_pk_bf16_f32 v113, v160, v161\n"
    "v_dot2_f32_bf16 v158, v116, v155, v157\n"
    "v_dot2_f32_bf16 v159, v116, v156, v157\n"
    "v_cvt_pk_bf16_f32 v114, v162, v163\n"
    "v_dot2_f32_bf16 v160, v117, v155, v157\n"
    "v_dot2_f32_bf16 v161, v117, v156, v157\n"
    "v_cvt_pk_bf16_f32 v115, v164, v165\n"
    "v_dot2_f32_bf16 v162, v118, v155, v157\n"
    "v_dot2_f32_bf16 v163, v118, v156, v157\n"
    "v_cvt_pk_bf16_f32 v116, v158, v159\n"
    "v_dot2_f32_bf16 v164, v119, v155, v157\n"
    "v_dot2_f32_bf16 v165, v119, v156, v157\n"
    "v_cvt_pk_bf16_f32 v117, v160, v161\n"
    "s_add_u32 m0, %8, 147456\n"
    "s_nop 0\n"
    "buffer_load_dword v233, s[40:43], s59 offen lds\n"
    "v_dot2_f32_bf16 v158, v120, v152, v154\n"
    "v_dot2_f32_bf16 v159, v120, v153, v154\n"
    "v_cvt_pk_bf16_f32 v118, v162, v163\n"
    "v_dot2_f32_bf16 v160, v121, v152, v154\n"
    "v_dot2_f32_bf16 v161, v121, v153, v154\n"
    "v_cvt_pk_bf16_f32 v119, v164, v165\n"
    "v_dot2_f32_bf16 v162, v122, v152, v154\n"
    "v_dot2_f32_bf16 v163, v122, v153, v154\n"
    "v_cvt_pk_bf16_f32 v120, v158, v159\n"
    "v_dot2_f32_bf16 v164, v123, v152, v154\n"
    "v_dot2_f32_bf16 v165, v123, v153, v154\n"
    "v_cvt_pk_bf16_f32 v121, v160, v161\n"
    "v_dot2_f32_bf16 v158, v124, v155, v157\n"
    "v_dot2_f32_bf16 v159, v124, v156, v157\n"
    "v_cvt_pk_bf16_f32 v122, v162, v163\n"
    "v_dot2_f32_bf16 v160, v125, v155, v157\n"
    "v_dot2_f32_bf16 v161, v125, v156, v157\n"
    "v_cvt_pk_bf16_f32 v123, v164, v165\n"
    "v_dot2_f32_bf16 v162, v126, v155, v157\n"
    "v_dot2_f32_bf16 v163, v126, v156, v157\n"
    "v_cvt_pk_bf16_f32 v124, v158, v159\n"
    "v_dot2_f32_bf16 v164, v127, v155, v157\n"
    "v_dot2_f32_bf16 v165, v127, v156, v157\n"
    "v_cvt_pk_bf16_f32 v125, v160, v161\n"
    "v_dot2_f32_bf16 v158, v128, v152, v154\n"
    "v_dot2_f32_bf16 v159, v128, v153, v154\n"
    "v_cvt_pk_bf16_f32 v126, v162, v163\n"
    "v_dot2_f32_bf16 v160, v129, v152, v154\n"
    "v_dot2_f32_bf16 v161, v129, v153, v154\n"
    "v_cvt_pk_bf16_f32 v127, v164, v165\n"
    "v_dot2_f32_bf16 v162, v130, v152, v154\n"
    "v_dot2_f32_bf16 v163, v130, v153, v154\n"
    "v_cvt_pk_bf16_f32 v128, v158, v159\n"
    "v_dot2_f32_bf16 v164, v131, v152, v154\n"
    "v_dot2_f32_bf16 v165, v131, v153, v154\n"
    "v_cvt_pk_bf16_f32 v129, v160, v161\n"
    "v_dot2_f32_bf16 v158, v132, v155, v157\n"
    "v_dot2_f32_bf16 v159, v132, v156, v157\n"
    "v_cvt_pk_bf16_f32 v130, v162, v163\n"
    "v_dot2_f32_bf16 v160, v133, v155, v157\n"
    "v_dot2_f32_bf16 v161, v133, v156, v157\n"
    "v_cvt_pk_bf16_f32 v131, v164, v165\n"
    "v_dot2_f32_bf16 v162, v134, v155, v157\n"
    "v_dot2_f32_bf16 v163, v134, v156, v157\n"
    "v_cvt_pk_bf16_f32 v132, v158, v159\n"
    "v_dot2_f32_bf16 v164, v135, v155, v157\n"
    "v_dot2_f32_bf16 v165, v135, v156, v157\n"
    "v_cvt_pk_bf16_f32 v133, v160, v161\n"
    "v_dot2_f32_bf16 v158, v136, v152, v154\n"
    "v_dot2_f32_bf16 v159, v136, v153, v154\n"
    "v_cvt_pk_bf16_f32 v134, v162, v163\n"
    "v_dot2_f32_bf16 v160, v137, v152, v154\n"
    "v_dot2_f32_bf16 v161, v137, v153, v154\n"
    "v_cvt_pk_bf16_f32 v135, v164, v165\n"
    "v_dot2_f32_bf16 v162, v138, v152, v154\n"
    "v_dot2_f32_bf16 v163, v138, v153, v154\n"
    "v_cvt_pk_bf16_f32 v136, v158, v159\n"
    "v_dot2_f32_bf16 v164, v139, v152, v154\n"
    "v_dot2_f32_bf16 v165, v139, v153, v154\n"
    "v_cvt_pk_bf16_f32 v137, v160, v161\n"
    "v_dot2_f32_bf16 v158, v140, v155, v157\n"
    "v_dot2_f32_bf16 v159, v140, v156, v157\n"
    "v_cvt_pk_bf16_f32 v138, v162, v163\n"
    "v_dot2_f32_bf16 v160, v141, v155, v157\n"
    "v_dot2_f32_bf16 v161, v141, v156, v157\n"
    "v_cvt_pk_bf16_f32 v139, v164, v165\n"
    "v_dot2_f32_bf16 v162, v142, v155, v157\n"
    "v_dot2_f32_bf16 v163, v142, v156, v157\n"
    "v_cvt_pk_bf16_f32 v140, v158, v159\n"
    "v_dot2_f32_bf16 v164, v143, v155, v157\n"
    "v_dot2_f32_bf16 v165, v143, v156, v157\n"
    "v_cvt_pk_bf16_f32 v141, v160, v161\n"
    "s_nop 2\n"
    "v_cvt_pk_bf16_f32 v142, v162, v163\n"
    "v_cvt_pk_bf16_f32 v143, v164, v165\n"
    "s_waitcnt vmcnt(9)\n"
    "s_barrier\n"
    "ds_read_b64_tr_b16 a[64:65], v224 offset:0\n"
    "ds_read_b64_tr_b16 a[66:67], v224 offset:3840\n"
    "ds_read_b64_tr_b16 a[68:69], v224 offset:15360\n"
    "ds_read_b64_tr_b16 a[70:71], v224 offset:19200\n"
    "ds_read_b64_tr_b16 a[72:73], v224 offset:40\n"
    "ds_read_b64_tr_b16 a[74:75], v224 offset:3880\n"
    "ds_read_b64_tr_b16 a[76:77], v224 offset:15400\n"
    "ds_read_b64_tr_b16 a[78:79], v224 offset:19240\n"
    "ds_read_b64_tr_b16 a[80:81], v224 offset:72\n"
    "ds_read_b64_tr_b16 a[82:83], v224 offset:3912\n"
    "ds_read_b64_tr_b16 a[84:85], v224 offset:15432\n"
    "ds_read_b64_tr_b16 a[86:87], v224 offset:19272\n"
    "s_waitcnt lgkmcnt(8)\n"
    "v_mfma_f32_16x16x32_bf16 v[144:147], a[64:67], v[0:3], 0\n"
    "s_mov_b32 s65, s69\n"
    "ds_read_b64_tr_b16 a[88:89], v224 offset:112\n"
    "v_mfma_f32_16x16x32_bf16 v[144:147], a[68:71], v[8:11], v[144:147]\n"
    "ds_read_b64_tr_b16 a[90:91], v224 offset:3952\n"
    "v_mfma_f32_16x16x32_bf16 v[148:151], a[64:67], v[4:7], 0\n"
    "s_add_u32 m0, s60, 65536\n"
    "ds_read_b64_tr_b16 a[92:93], v224 offset:15472\n"
    "ds_read_b64_tr_b16 a[94:95], v224 offset:19312\n"
    "v_mfma_f32_16x16x32_bf16 v[148:151], a[68:71], v[12:15], v[148:151]\n"
    "s_waitcnt lgkmcnt(8)\n"
    "buffer_load_dwordx4 v216, s[40:43], s65 offen lds\n"
    "v_mfma_f32_16x16x32_bf16 v[152:155], a[72:75], v[0:3], 0\n"
    "s_mul_i32 s53, %7, 5120\n"
    "ds_read_b64_tr_b16 a[64:65], v224 offset:144\n"
    "v_mfma_f32_16x16x32_bf16 v[152:155], a[76:79], v[8:11], v[152:155]\n"
    "ds_read_b64_tr_b16 a[66:67], v224 offset:3984\n"
    "v_mfma_f32_16x16x32_bf16 v[156:159], a[72:75], v[4:7], 0\n"
    "s_add_u32 m0, s60, 66560\n"
    "ds_read_b64_tr_b16 a[68:69], v224 offset:15504\n"
    "ds_read_b64_tr_b16 a[70:71], v224 offset:19344\n"
    "v_mfma_f32_16x16x32_bf16 v[156:159], a[76:79], v[12:15], v[156:159]\n"
    "s_waitcnt lgkmcnt(8)\n"
    "buffer_load_dwordx4 v217, s[40:43], s65 offen lds\n"
    "v_mfma_f32_16x16x32_bf16 v[160:163], a[80:83], v[0:3], 0\n"
    "s_add_u32 s53, s53, 65536\n"
    "ds_read_b64_tr_b16 a[72:73], v224 offset:184\n"
    "v_mfma_f32_16x16x32_bf16 v[160:163], a[84:87], v[8:11], v[160:163]\n"
    "ds_read_b64_tr_b16 a[74:75], v224 offset:4024\n"
    "v_mfma_f32_16x16x32_bf16 v[164:167], a[80:83], v[4:7], 0\n"
    "s_add_u32 m0, s60, 67584\n"
    "ds_read_b64_tr_b16 a[76:77], v224 offset:15544\n"
    "ds_read_b64_tr_b16 a[78:79], v224 offset:19384\n"
    "v_mfma_f32_16x16x32_bf16 v[164:167], a[84:87], v[12:15], v[164:167]\n"
    "s_waitcnt lgkmcnt(8)\n"
    "buffer_load_dwordx4 v218, s[40:43], s65 offen lds\n"
    "v_mfma_f32_16x16x32_bf16 v[168:171], a[88:91], v[0:3], 0\n"
    "s_add_u32 s53, s53, %8\n"
    "ds_read_b64_tr_b16 a[80:81], v224 offset:216\n"
    "v_mfma_f32_16x16x32_bf16 v[168:171], a[92:95], v[8:11], v[168:171]\n"
    "ds_read_b64_tr_b16 a[82:83], v224 offset:4056\n"
    "v_mfma_f32_16x16x32_bf16 v[172:175], a[88:91], v[4:7], 0\n"
    "s_add_u32 m0, s60, 68608\n"
    "ds_read_b64_tr_b16 a[84:85], v224 offset:15576\n"
    "ds_read_b64_tr_b16 a[86:87], v224 offset:19416\n"
    "v_mfma_f32_16x16x32_bf16 v[172:175], a[92:95], v[12:15], v[172:175]\n"
    "s_waitcnt lgkmcnt(8)\n"
    "buffer_load_dwordx4 v219, s[40:43], s65 offen lds\n"
    "v_mfma_f32_16x16x32_bf16 v[176:179], a[64:67], v[0:3], 0\n"
    "s_mul_i32 s61, %7, 1280\n"
    "ds_read_b64_tr_b16 a[88:89], v224 offset:256\n"
    "v_mfma_f32_16x16x32_bf16 v[176:179], a[68:71], v[8:11], v[176:179]\n"
    "ds_read_b64_tr_b16 a[90:91], v224 offset:4096\n"
    "v_mfma_f32_16x16x32_bf16 v[180:183], a[64:67], v[4:7], 0\n"
    "s_add_u32 m0, s60, 69632\n"
    "ds_read_b64_tr_b16 a[92:93], v224 offset:15616\n"
    "ds_read_b64_tr_b16 a[94:95], v224 offset:19456\n"
    "v_mfma_f32_16x16x32_bf16 v[180:183], a[68:71], v[12:15], v[180:183]\n"
    "s_waitcnt lgkmcnt(8)\n"
    "buffer_load_dwordx4 v220, s[40:43], s65 offen lds\n"
    "v_mfma_f32_16x16x32_bf16 v[184:187], a[72:75], v[0:3], 0\n"
    "s_add_u32 s61, s61, 32768\n"
    "ds_read_b64_tr_b16 a[64:65], v225 offset:0\n"
    "v_mfma_f32_16x16x32_bf16 v[184:187], a[76:79], v[8:11], v[184:187]\n"
    "ds_read_b64_tr_b16 a[66:67], v225 offset:3840\n"
    "v_mfma_f32_16x16x32_bf16 v[188:191], a[72:75], v[4:7], 0\n"
    "s_add_u32 m0, s60, 70656\n"
    "ds_read_b64_tr_b16 a[68:69], v225 offset:15360\n"
    "ds_read_b64_tr_b16 a[70:71], v225 offset:19200\n"
    "v_mfma_f32_16x16x32_bf16 v[188:191], a[76:79], v[12:15], v[188:191]\n"
    "s_waitcnt lgkmcnt(8)\n"
    "buffer_load_dwordx4 v221, s[40:43], s65 offen lds\n"
    "v_mfma_f32_16x16x32_bf16 v[192:195], a[80:83], v[0:3], 0\n"
    "s_add_u32 s61, s61, %8\n"
    "ds_read_b64_tr_b16 a[72:73], v224 offset:288\n"
    "v_mfma_f32_16x16x32_bf16 v[192:195], a[84:87], v[8:11], v[192:195]\n"
    "ds_read_b64_tr_b16 a[74:75], v224 offset:4128\n"
    "v_mfma_f32_16x16x32_bf16 v[196:199], a[80:83], v[4:7], 0\n"
    "s_add_u32 m0, s60, 71680\n"
    "ds_read_b64_tr_b16 a[76:77], v224 offset:15648\n"
    "ds_read_b64_tr_b16 a[78:79], v224 offset:19488\n"
    "v_mfma_f32_16x16x32_bf16 v[196:199], a[84:87], v[12:15], v[196:199]\n"
    "s_waitcnt lgkmcnt(8)\n"
    "buffer_load_dwordx4 v222, s[40:43], s65 offen lds\n"
    "v_mfma_f32_16x16x32_bf16 v[200:203], a[88:91], v[0:3], 0\n"
    "s_lshl_b32 s67, %6, 8\n"
    "ds_read_b64_tr_b16 a[80:81], v224 offset:320\n"
    "v_mfma_f32_16x16x32_bf16 v[200:203], a[92:95], v[8:11], v[200:203]\n"
    "ds_read_b64_tr_b16 a[82:83], v224 offset:4160\n"
    "v_mfma_f32_16x16x32_bf16 v[204:207], a[88:91], v[4:7], 0\n"
    "s_add_u32 m0, s60, 72704\n"
    "ds_read_b64_tr_b16 a[84:85], v224 offset:15680\n"
    "ds_read_b64_tr_b16 a[86:87], v224 offset:19520\n"
    "v_mfma_f32_16x16x32_bf16 v[204:207], a[92:95], v[12:15], v[204:207]\n"
    "s_waitcnt lgkmcnt(8)\n"
    "buffer_load_dwordx4 v223, s[40:43], s65 offen lds\n"
    "v_mfma_f32_16x16x32_bf16 v[208:211], a[64:67], v[0:3], 0\n"
    "s_lshl_b32 s68, %5, 2\n"
    "ds_read_b64_tr_b16 a[88:89], v224 offset:352\n"
    "v_mfma_f32_16x16x32_bf16 v[208:211], a[68:71], v[8:11], v[208:211]\n"
    "ds_read_b64_tr_b16 a[90:91], v224 offset:4192\n"
    "v_mfma_f32_16x16x32_bf16 v[212:215], a[64:67], v[4:7], 0\n"
    "s_add_u32 s67, s67, s68\n"
    "ds_read_b64_tr_b16 a[92:93], v224 offset:15712\n"
    "v_mfma_f32_16x16x32_bf16 v[212:215], a[68:71], v[12:15], v[212:215]\n"
    "s_waitcnt lgkmcnt(7)\n"
    "ds_read_b64_tr_b16 a[94:95], v224 offset:19552\n"
    "v_mfma_f32_16x16x32_bf16 a[0:3], v[0:3], a[72:75], 0\n"
    "s_add_u32 s67, s67, %7\n"
    "ds_read_b64_tr_b16 a[64:65], v224 offset:384\n"
    "v_add_u32 v226, 0x8000, v224\n"
    "v_mfma_f32_16x16x32_bf16 a[0:3], v[8:11], a[76:79], a[0:3]\n"
    "ds_read_b64_tr_b16 a[66:67], v224 offset:4224\n"
    "v_add_u32 v227, 0x8000, v225\n"
    "v_mfma_f32_16x16x32_bf16 a[4:7], v[4:7], a[72:75], 0\n"
    "s_mul_i32 s67, s67, 2432\n"
    "ds_read_b64_tr_b16 a[68:69], v224 offset:15744\n"
    "v_mfma_f32_16x16x32_bf16 a[4:7], v[12:15], a[76:79], a[4:7]\n"
    "s_waitcnt lgkmcnt(7)\n"
    "ds_read_b64_tr_b16 a[70:71], v224 offset:19584\n"
    "v_mfma_f32_16x16x32_bf16 a[8:11], v[0:3], a[80:83], 0\n"
    "s_add_u32 s67, s67, 25165824\n"
    "ds_read_b64_tr_b16 a[72:73], v224 offset:416\n"
    "v_lshlrev_b32 v239, 4, v255\n"
    "v_mfma_f32_16x16x32_bf16 a[8:11], v[8:11], a[84:87], a[8:11]\n"
    "ds_read_b64_tr_b16 a[74:75], v224 offset:4256\n"
    "v_mfma_f32_16x16x32_bf16 a[12:15], v[4:7], a[80:83], 0\n"
    "s_mov_b64 s[44:45], %2\n"
    "ds_read_b64_tr_b16 a[76:77], v224 offset:15776\n"
    "v_and_b32 v254, 31, v255\n"
    "v_mfma_f32_16x16x32_bf16 a[12:15], v[12:15], a[84:87], a[12:15]\n"
    "s_waitcnt vmcnt(9) lgkmcnt(7)\n"
    "s_barrier\n"
    "ds_read_b64_tr_b16 a[78:79], v224 offset:19616\n"
    "v_mfma_f32_16x16x32_bf16 a[16:19], v[0:3], a[88:91], 0\n"
    "s_add_u32 s44, s44, s67\n"
    "s_addc_u32 s45, s45, 0\n"
    "ds_read_b64_tr_b16 a[80:81], v226 offset:0\n"
    "v_lshlrev_b32 v240, 2, v254\n"
    "v_mfma_f32_16x16x32_bf16 a[16:19], v[8:11], a[92:95], a[16:19]\n"
    "ds_read_b64_tr_b16 a[82:83], v226 offset:3840\n"
    "v_mfma_f32_16x16x32_bf16 a[20:23], v[4:7], a[88:91], 0\n"
    "s_and_b32 s45, s45, 0xffff\n"
    "ds_read_b64_tr_b16 a[84:85], v226 offset:15360\n"
    "v_add_u32 v243, 0x900, v240\n"
    "v_mfma_f32_16x16x32_bf16 a[20:23], v[12:15], a[92:95], a[20:23]\n"
    "s_waitcnt lgkmcnt(7)\n"
    "ds_read_b64_tr_b16 a[86:87], v226 offset:19200\n"
    "v_mfma_f32_16x16x32_bf16 a[24:27], v[0:3], a[64:67], 0\n"
    "s_mov_b32 s46, 469376\n"
    "ds_read_b64_tr_b16 a[88:89], v226 offset:40\n"
    "v_min_u32 v254, 19, v254\n"
    "v_mfma_f32_16x16x32_bf16 a[24:27], v[8:11], a[68:71], a[24:27]\n"
    "ds_read_b64_tr_b16 a[90:91], v226 offset:3880\n"
    "v_mfma_f32_16x16x32_bf16 a[28:31], v[4:7], a[64:67], 0\n"
    "s_mov_b32 s47, 0x20000\n"
    "ds_read_b64_tr_b16 a[92:93], v226 offset:15400\n"
    "v_lshrrev_b32 v241, 5, v255\n"
    "v_mfma_f32_16x16x32_bf16 a[28:31], v[12:15], a[68:71], a[28:31]\n"
    "s_waitcnt lgkmcnt(7)\n"
    "ds_read_b64_tr_b16 a[94:95], v226 offset:19240\n"
    "v_mfma_f32_16x16x32_bf16 a[32:35], v[0:3], a[72:75], 0\n"
    "s_mov_b64 s[48:49], %3\n"
    "ds_read_b64_tr_b16 a[64:65], v226 offset:72\n"
    "v_mul_u32_u24 v241, 0x140, v241\n"
    "v_mfma_f32_16x16x32_bf16 a[32:35], v[8:11], a[76:79], a[32:35]\n"
    "ds_read_b64_tr_b16 a[66:67], v226 offset:3912\n"
    "v_mfma_f32_16x16x32_bf16 a[36:39], v[4:7], a[72:75], 0\n"
    "s_and_b32 s49, s49, 0xffff\n"
    "ds_read_b64_tr_b16 a[68:69], v226 offset:15432\n"
    "v_lshl_add_u32 v254, v254, 4, v241\n"
    "v_mfma_f32_16x16x32_bf16 a[36:39], v[12:15], a[76:79], a[36:39]\n"
    "s_waitcnt lgkmcnt(7)\n"
    "ds_read_b64_tr_b16 a[70:71], v226 offset:19272\n"
    "v_mfma_f32_16x16x32_bf16 v[144:147], a[80:83], v[16:19], v[144:147]\n"
    "s_mov_b32 s65, s70\n"
    "ds_read_b64_tr_b16 a[72:73], v226 offset:112\n"
    "v_add_u32 v244, 0x400, v254\n"
    "v_mfma_f32_16x16x32_bf16 v[144:147], a[84:87], v[24:27], v[144:147]\n"
    "ds_read_b64_tr_b16 a[74:75], v226 offset:3952\n"
    "v_mfma_f32_16x16x32_bf16 v[148:151], a[80:83], v[20:23], v[148:151]\n"
    "s_add_u32 m0, s60, 98304\n"
    "ds_read_b64_tr_b16 a[76:77], v226 offset:15472\n"
    "ds_read_b64_tr_b16 a[78:79], v226 offset:19312\n"
    "v_mfma_f32_16x16x32_bf16 v[148:151], a[84:87], v[28:31], v[148:151]\n"
    "s_waitcnt lgkmcnt(8)\n"
    "buffer_load_dwordx4 v216, s[40:43], s65 offen lds\n"
    "v_mfma_f32_16x16x32_bf16 v[152:155], a[88:91], v[16:19], v[152:155]\n"
    "s_mov_b32 s50, 663552\n"
    "ds_read_b64_tr_b16 a[80:81], v226 offset:144\n"
    "v_add_u32 v224, 0x8000, v226\n"
    "v_mfma_f32_16x16x32_bf16 v[152:155], a[92:95], v[24:27], v[152:155]\n"
    "ds_read_b64_tr_b16 a[82:83], v226 offset:3984\n"
    "v_add_u32 v225, 0x8000, v227\n"
    "v_mfma_f32_16x16x32_bf16 v[156:159], a[88:91], v[20:23], v[156:159]\n"
    "s_mov_b32 s51, 0x20000\n"
    "ds_read_b64_tr_b16 a[84:85], v226 offset:15504\n"
    "v_lshrrev_b32 v254, 2, v255\n"
    "v_mfma_f32_16x16x32_bf16 v[156:159], a[92:95], v[28:31], v[156:159]\n"
    "s_waitcnt lgkmcnt(7)\n"
    "ds_read_b64_tr_b16 a[86:87], v226 offset:19344\n"
    "v_mfma_f32_16x16x32_bf16 v[160:163], a[64:67], v[16:19], v[160:163]\n"
    "s_add_u32 m0, s60, 99328\n"
    "ds_read_b64_tr_b16 a[88:89], v226 offset:184\n"
    "ds_read_b64_tr_b16 a[90:91], v226 offset:4024\n"
    "v_mfma_f32_16x16x32_bf16 v[160:163], a[68:71], v[24:27], v[160:163]\n"
    "buffer_load_dwordx4 v217, s[40:43], s65 offen lds\n"
    "v_mfma_f32_16x16x32_bf16 v[164:167], a[64:67], v[20:23], v[164:167]\n"
    "s_lshl_b32 s54, %13, 1\n"
    "ds_read_b64_tr_b16 a[92:93], v226 offset:15544\n"
    "v_mul_u32_u24 v254, 0x480, v254\n"
    "v_mfma_f32_16x16x32_bf16 v[164:167], a[68:71], v[28:31], v[164:167]\n"
    "s_waitcnt lgkmcnt(7)\n"
    "ds_read_b64_tr_b16 a[94:95], v226 offset:19384\n"
    "v_mfma_f32_16x16x32_bf16 v[168:171], a[72:75], v[16:19], v[168:171]\n"
    "s_add_u32 s55, s54, 1\n"
    "ds_read_b64_tr_b16 a[64:65], v226 offset:216\n"
    "v_and_b32 v241, 3, v255\n"
    "v_mfma_f32_16x16x32_bf16 v[168:171], a[76:79], v[24:27], v[168:171]\n"
    "ds_read_b64_tr_b16 a[66:67], v226 offset:4056\n"
    "v_mfma_f32_16x16x32_bf16 v[172:175], a[72:75], v[20:23], v[172:175]\n"
    "s_lshr_b32 s56, %13, 2\n"
    "ds_read_b64_tr_b16 a[68:69], v226 offset:15576\n"
    "v_bfe_u32 v242, v255, 4, 1\n"
    "v_mfma_f32_16x16x32_bf16 v[172:175], a[76:79], v[28:31], v[172:175]\n"
    "s_waitcnt lgkmcnt(7)\n"
    "ds_read_b64_tr_b16 a[70:71], v226 offset:19416\n"
    "v_mfma_f32_16x16x32_bf16 v[176:179], a[80:83], v[16:19], v[176:179]\n"
    "s_add_u32 m0, s60, 100352\n"
    "ds_read_b64_tr_b16 a[72:73], v226 offset:256\n"
    "ds_read_b64_tr_b16 a[74:75], v226 offset:4096\n"
    "v_mfma_f32_16x16x32_bf16 v[176:179], a[84:87], v[24:27], v[176:179]\n"
    "buffer_load_dwordx4 v218, s[40:43], s65 offen lds\n"
    "v_mfma_f32_16x16x32_bf16 v[180:183], a[80:83], v[20:23], v[180:183]\n"
    "s_add_u32 s56, s56, 16\n"
    "ds_read_b64_tr_b16 a[76:77], v226 offset:15616\n"
    "v_lshlrev_b32 v242, 1, v242\n"
    "v_mfma_f32_16x16x32_bf16 v[180:183], a[84:87], v[28:31], v[180:183]\n"
    "s_waitcnt lgkmcnt(7)\n"
    "ds_read_b64_tr_b16 a[78:79], v226 offset:19456\n"
    "v_mfma_f32_16x16x32_bf16 v[184:187], a[88:91], v[16:19], v[184:187]\n"
    "s_mul_i32 s57, %7, 9216\n"
    "ds_read_b64_tr_b16 a[80:81], v227 offset:0\n"
    "v_xor_b32 v241, v241, v242\n"
    "v_mfma_f32_16x16x32_bf16 v[184:187], a[92:95], v[24:27], v[184:187]\n"
    "ds_read_b64_tr_b16 a[82:83], v227 offset:3840\n"
    "v_mfma_f32_16x16x32_bf16 v[188:191], a[88:91], v[20:23], v[188:191]\n"
    "s_add_u32 m0, s60, 101376\n"
    "ds_read_b64_tr_b16 a[84:85], v227 offset:15360\n"
    "ds_read_b64_tr_b16 a[86:87], v227 offset:19200\n"
    "v_mfma_f32_16x16x32_bf16 v[188:191], a[92:95], v[28:31], v[188:191]\n"
    "s_waitcnt lgkmcnt(8)\n"
    "buffer_load_dwordx4 v219, s[40:43], s65 offen lds\n"
    "v_mfma_f32_16x16x32_bf16 v[192:195], a[64:67], v[16:19], v[192:195]\n"
    "s_add_u32 s57, s57, %8\n"
    "ds_read_b64_tr_b16 a[88:89], v226 offset:288\n"
    "v_lshl_add_u32 v245, v241, 4, v254\n"
    "v_mfma_f32_16x16x32_bf16 v[192:195], a[68:71], v[24:27], v[192:195]\n"
    "ds_read_b64_tr_b16 a[90:91], v226 offset:4128\n"
    "v_mfma_f32_16x16x32_bf16 v[196:199], a[64:67], v[20:23], v[196:199]\n"
    "s_add_u32 s57, s57, 32768\n"
    "ds_read_b64_tr_b16 a[92:93], v226 offset:15648\n"
    "v_and_b32 v234, 15, v255\n"
    "v_mfma_f32_16x16x32_bf16 v[196:199], a[68:71], v[28:31], v[196:199]\n"
    "s_waitcnt lgkmcnt(7)\n"
    "ds_read_b64_tr_b16 a[94:95], v226 offset:19488\n"
    "v_mfma_f32_16x16x32_bf16 v[200:203], a[72:75], v[16:19], v[200:203]\n"
    "ds_read_b64_tr_b16 a[64:65], v226 offset:320\n"
    "v_lshrrev_b32 v235, 4, v255\n"
    "v_mfma_f32_16x16x32_bf16 v[200:203], a[76:79], v[24:27], v[200:203]\n"
    "ds_read_b64_tr_b16 a[66:67], v226 offset:4160\n"
    "v_mfma_f32_16x16x32_bf16 v[204:207], a[72:75], v[20:23], v[204:207]\n"
    "s_add_u32 m0, s60, 102400\n"
    "ds_read_b64_tr_b16 a[68:69], v226 offset:15680\n"
    "ds_read_b64_tr_b16 a[70:71], v226 offset:19520\n"
    "v_mfma_f32_16x16x32_bf16 v[204:207], a[76:79], v[28:31], v[204:207]\n"
    "s_waitcnt lgkmcnt(8)\n"
    "buffer_load_dwordx4 v220, s[40:43], s65 offen lds\n"
    "v_mfma_f32_16x16x32_bf16 v[208:211], a[80:83], v[16:19], v[208:211]\n"
    "ds_read_b64_tr_b16 a[72:73], v226 offset:352\n"
    "v_mul_u32_u24 v235, 0x140, v235\n"
    "v_mfma_f32_16x16x32_bf16 v[208:211], a[84:87], v[24:27], v[208:211]\n"
    "ds_read_b64_tr_b16 a[74:75], v226 offset:4192\n"
    "v_mfma_f32_16x16x32_bf16 v[212:215], a[80:83], v[20:23], v[212:215]\n"
    "ds_read_b64_tr_b16 a[76:77], v226 offset:15712\n"
    "v_add_u32 v235, s53, v235\n"
    "v_mfma_f32_16x16x32_bf16 v[212:215], a[84:87], v[28:31], v[212:215]\n"
    "s_waitcnt lgkmcnt(7)\n"
    "ds_read_b64_tr_b16 a[78:79], v226 offset:19552\n"
    "v_mfma_f32_16x16x32_bf16 a[0:3], v[16:19], a[88:91], a[0:3]\n"
    "s_add_u32 m0, s60, 103424\n"
    "ds_read_b64_tr_b16 a[80:81], v226 offset:384\n"
    "ds_read_b64_tr_b16 a[82:83], v226 offset:4224\n"
    "v_mfma_f32_16x16x32_bf16 a[0:3], v[24:27], a[92:95], a[0:3]\n"
    "buffer_load_dwordx4 v221, s[40:43], s65 offen lds\n"
    "v_mfma_f32_16x16x32_bf16 a[4:7], v[20:23], a[88:91], a[4:7]\n"
    "ds_read_b64_tr_b16 a[84:85], v226 offset:15744\n"
    "v_add_u32 v254, 0, v234\n"
    "v_mfma_f32_16x16x32_bf16 a[4:7], v[28:31], a[92:95], a[4:7]\n"
    "s_waitcnt lgkmcnt(7)\n"
    "ds_read_b64_tr_b16 a[86:87], v226 offset:19584\n"
    "v_mfma_f32_16x16x32_bf16 a[8:11], v[16:19], a[64:67], a[8:11]\n"
    "ds_read_b64_tr_b16 a[88:89], v226 offset:416\n"
    "v_mul_u32_u24 v241, 57, v254\n"
    "v_mfma_f32_16x16x32_bf16 a[8:11], v[24:27], a[68:71], a[8:11]\n"
    "ds_read_b64_tr_b16 a[90:91], v226 offset:4256\n"
    "v_mfma_f32_16x16x32_bf16 a[12:15], v[20:23], a[64:67], a[12:15]\n"
    "s_add_u32 m0, s60, 104448\n"
    "ds_read_b64_tr_b16 a[92:93], v226 offset:15776\n"
    "ds_read_b64_tr_b16 a[94:95], v226 offset:19616\n"
    "v_mfma_f32_16x16x32_bf16 a[12:15], v[28:31], a[68:71], a[12:15]\n"
    "s_waitcnt vmcnt(6) lgkmcnt(8)\n"
    "s_barrier\n"
    "buffer_load_dwordx4 v222, s[40:43], s65 offen lds\n"
    "v_mfma_f32_16x16x32_bf16 a[16:19], v[16:19], a[72:75], a[16:19]\n"
    "ds_read_b64_tr_b16 a[64:65], v224 offset:0\n"
    "v_lshrrev_b32 v241, 10, v241\n"
    "v_mfma_f32_16x16x32_bf16 a[16:19], v[24:27], a[76:79], a[16:19]\n"
    "ds_read_b64_tr_b16 a[66:67], v224 offset:3840\n"
    "v_mfma_f32_16x16x32_bf16 a[20:23], v[20:23], a[72:75], a[20:23]\n"
    "ds_read_b64_tr_b16 a[68:69], v224 offset:15360\n"
    "v_mul_u32_u24 v242, 18, v241\n"
    "v_mfma_f32_16x16x32_bf16 a[20:23], v[28:31], a[76:79], a[20:23]\n"
    "s_waitcnt lgkmcnt(7)\n"
    "ds_read_b64_tr_b16 a[70:71], v224 offset:19200\n"
    "v_mfma_f32_16x16x32_bf16 a[24:27], v[16:19], a[80:83], a[24:27]\n"
    "ds_read_b64_tr_b16 a[72:73], v224 offset:40\n"
    "v_sub_u32 v242, v254, v242\n"
    "v_mfma_f32_16x16x32_bf16 a[24:27], v[24:27], a[84:87], a[24:27]\n"
    "ds_read_b64_tr_b16 a[74:75], v224 offset:3880\n"
    "v_mfma_f32_16x16x32_bf16 a[28:31], v[20:23], a[80:83], a[28:31]\n"
    "s_add_u32 m0, s60, 105472\n"
    "ds_read_b64_tr_b16 a[76:77], v224 offset:15400\n"
    "ds_read_b64_tr_b16 a[78:79], v224 offset:19240\n"
    "v_mfma_f32_16x16x32_bf16 a[28:31], v[28:31], a[84:87], a[28:31]\n"
    "s_waitcnt lgkmcnt(8)\n"
    "buffer_load_dwordx4 v223, s[40:43], s65 offen lds\n"
    "v_mfma_f32_16x16x32_bf16 a[32:35], v[16:19], a[88:91], a[32:35]\n"
    "ds_read_b64_tr_b16 a[80:81], v224 offset:72\n"
    "v_mul_u32_u24 v241, 0x500, v241\n"
    "v_mfma_f32_16x16x32_bf16 a[32:35], v[24:27], a[92:95], a[32:35]\n"
    "ds_read_b64_tr_b16 a[82:83], v224 offset:3912\n"
    "v_mfma_f32_16x16x32_bf16 a[36:39], v[20:23], a[88:91], a[36:39]\n"
    "ds_read_b64_tr_b16 a[84:85], v224 offset:15432\n"
    "v_lshl_add_u32 v241, v242, 4, v241\n"
    "v_mfma_f32_16x16x32_bf16 a[36:39], v[28:31], a[92:95], a[36:39]\n"
    "s_waitcnt lgkmcnt(7)\n"
    "ds_read_b64_tr_b16 a[86:87], v224 offset:19272\n"
    "v_mfma_f32_16x16x32_bf16 v[144:147], a[64:67], v[32:35], v[144:147]\n"
    "s_mov_b32 s65, s71\n"
    "ds_read_b64_tr_b16 a[88:89], v224 offset:112\n"
    "v_add_u32 v246, v241, v235\n"
    "v_mfma_f32_16x16x32_bf16 v[144:147], a[68:71], v[40:43], v[144:147]\n"
    "ds_read_b64_tr_b16 a[90:91], v224 offset:3952\n"
    "v_mfma_f32_16x16x32_bf16 v[148:151], a[64:67], v[36:39], v[148:151]\n"
    "s_add_u32 m0, s60, 0\n"
    "ds_read_b64_tr_b16 a[92:93], v224 offset:15472\n"
    "ds_read_b64_tr_b16 a[94:95], v224 offset:19312\n"
    "v_mfma_f32_16x16x32_bf16 v[148:151], a[68:71], v[44:47], v[148:151]\n"
    "s_waitcnt lgkmcnt(8)\n"
    "buffer_load_dwordx4 v216, s[40:43], s65 offen lds\n"
    "v_mfma_f32_16x16x32_bf16 v[152:155], a[72:75], v[32:35], v[152:155]\n"
    "ds_read_b64_tr_b16 a[64:65], v224 offset:144\n"
    "v_add_u32 v226, 0x8000, v224\n"
    "v_mfma_f32_16x16x32_bf16 v[152:155], a[76:79], v[40:43], v[152:155]\n"
    "ds_read_b64_tr_b16 a[66:67], v224 offset:3984\n"
    "v_add_u32 v227, 0x8000, v225\n"
    "v_mfma_f32_16x16x32_bf16 v[156:159], a[72:75], v[36:39], v[156:159]\n"
    "ds_read_b64_tr_b16 a[68:69], v224 offset:15504\n"
    "v_add_u32 v254, 16, v234\n"
    "v_mfma_f32_16x16x32_bf16 v[156:159], a[76:79], v[44:47], v[156:159]\n"
    "s_waitcnt lgkmcnt(7)\n"
    "ds_read_b64_tr_b16 a[70:71], v224 offset:19344\n"
    "v_mfma_f32_16x16x32_bf16 v[160:163], a[80:83], v[32:35], v[160:163]\n"
    "s_add_u32 m0, s60, 1024\n"
    "ds_read_b64_tr_b16 a[72:73], v224 offset:184\n"
    "ds_read_b64_tr_b16 a[74:75], v224 offset:4024\n"
    "v_mfma_f32_16x16x32_bf16 v[160:163], a[84:87], v[40:43], v[160:163]\n"
    "buffer_load_dwordx4 v217, s[40:43], s65 offen lds\n"
    "v_mfma_f32_16x16x32_bf16 v[164:167], a[80:83], v[36:39], v[164:167]\n"
    "ds_read_b64_tr_b16 a[76:77], v224 offset:15544\n"
    "v_mul_u32_u24 v241, 57, v254\n"
    "v_mfma_f32_16x16x32_bf16 v[164:167], a[84:87], v[44:47], v[164:167]\n"
    "s_waitcnt lgkmcnt(7)\n"
    "ds_read_b64_tr_b16 a[78:79], v224 offset:19384\n"
    "v_mfma_f32_16x16x32_bf16 v[168:171], a[88:91], v[32:35], v[168:171]\n"
    "ds_read_b64_tr_b16 a[80:81], v224 offset:216\n"
    "v_lshrrev_b32 v241, 10, v241\n"
    "v_mfma_f32_16x16x32_bf16 v[168:171], a[92:95], v[40:43], v[168:171]\n"
    "ds_read_b64_tr_b16 a[82:83], v224 offset:4056\n"
    "v_mfma_f32_16x16x32_bf16 v[172:175], a[88:91], v[36:39], v[172:175]\n"
    "ds_read_b64_tr_b16 a[84:85], v224 offset:15576\n"
    "v_mul_u32_u24 v242, 18, v241\n"
    "v_mfma_f32_16x16x32_bf16 v[172:175], a[92:95], v[44:47], v[172:175]\n"
    "s_waitcnt lgkmcnt(7)\n"
    "ds_read_b64_tr_b16 a[86:87], v224 offset:19416\n"
    "v_mfma_f32_16x16x32_bf16 v[176:179], a[64:67], v[32:35], v[176:179]\n"
    "s_add_u32 m0, s60, 2048\n"
    "ds_read_b64_tr_b16 a[88:89], v224 offset:256\n"
    "ds_read_b64_tr_b16 a[90:91], v224 offset:4096\n"
    "v_mfma_f32_16x16x32_bf16 v[176:179], a[68:71], v[40:43], v[176:179]\n"
    "buffer_load_dwordx4 v218, s[40:43], s65 offen lds\n"
    "v_mfma_f32_16x16x32_bf16 v[180:183], a[64:67], v[36:39], v[180:183]\n"
    "ds_read_b64_tr_b16 a[92:93], v224 offset:15616\n"
    "v_sub_u32 v242, v254, v242\n"
    "v_mfma_f32_16x16x32_bf16 v[180:183], a[68:71], v[44:47], v[180:183]\n"
    "s_waitcnt lgkmcnt(7)\n"
    "ds_read_b64_tr_b16 a[94:95], v224 offset:19456\n"
    "v_mfma_f32_16x16x32_bf16 v[184:187], a[72:75], v[32:35], v[184:187]\n"
    "ds_read_b64_tr_b16 a[64:65], v225 offset:0\n"
    "v_mul_u32_u24 v241, 0x500, v241\n"
    "v_mfma_f32_16x16x32_bf16 v[184:187], a[76:79], v[40:43], v[184:187]\n"
    "ds_read_b64_tr_b16 a[66:67], v225 offset:3840\n"
    "v_mfma_f32_16x16x32_bf16 v[188:191], a[72:75], v[36:39], v[188:191]\n"
    "s_add_u32 m0, s60, 3072\n"
    "ds_read_b64_tr_b16 a[68:69], v225 offset:15360\n"
    "ds_read_b64_tr_b16 a[70:71], v225 offset:19200\n"
    "v_mfma_f32_16x16x32_bf16 v[188:191], a[76:79], v[44:47], v[188:191]\n"
    "s_waitcnt lgkmcnt(8)\n"
    "buffer_load_dwordx4 v219, s[40:43], s65 offen lds\n"
    "v_mfma_f32_16x16x32_bf16 v[192:195], a[80:83], v[32:35], v[192:195]\n"
    "ds_read_b64_tr_b16 a[72:73], v224 offset:288\n"
    "v_lshl_add_u32 v241, v242, 4, v241\n"
    "v_mfma_f32_16x16x32_bf16 v[192:195], a[84:87], v[40:43], v[192:195]\n"
    "ds_read_b64_tr_b16 a[74:75], v224 offset:4128\n"
    "v_mfma_f32_16x16x32_bf16 v[196:199], a[80:83], v[36:39], v[196:199]\n"
    "ds_read_b64_tr_b16 a[76:77], v224 offset:15648\n"
    "v_add_u32 v247, v241, v235\n"
    "v_mfma_f32_16x16x32_bf16 v[196:199], a[84:87], v[44:47], v[196:199]\n"
    "s_waitcnt lgkmcnt(7)\n"
    "ds_read_b64_tr_b16 a[78:79], v224 offset:19488\n"
    "v_mfma_f32_16x16x32_bf16 v[200:203], a[88:91], v[32:35], v[200:203]\n"
    "ds_read_b64_tr_b16 a[80:81], v224 offset:320\n"
    "v_add_u32 v254, 32, v234\n"
    "v_mfma_f32_16x16x32_bf16 v[200:203], a[92:95], v[40:43], v[200:203]\n"
    "ds_read_b64_tr_b16 a[82:83], v224 offset:4160\n"
    "v_mfma_f32_16x16x32_bf16 v[204:207], a[88:91], v[36:39], v[204:207]\n"
    "s_add_u32 m0, s60, 4096\n"
    "ds_read_b64_tr_b16 a[84:85], v224 offset:15680\n"
    "ds_read_b64_tr_b16 a[86:87], v224 offset:19520\n"
    "v_mfma_f32_16x16x32_bf16 v[204:207], a[92:95], v[44:47], v[204:207]\n"
    "s_waitcnt lgkmcnt(8)\n"
    "buffer_load_dwordx4 v220, s[40:43], s65 offen lds\n"
    "v_mfma_f32_16x16x32_bf16 v[208:211], a[64:67], v[32:35], v[208:211]\n"
    "ds_read_b64_tr_b16 a[88:89], v224 offset:352\n"
    "v_mul_u32_u24 v241, 57, v254\n"
    "v_mfma_f32_16x16x32_bf16 v[208:211], a[68:71], v[40:43], v[208:211]\n"
    "ds_read_b64_tr_b16 a[90:91], v224 offset:4192\n"
    "v_mfma_f32_16x16x32_bf16 v[212:215], a[64:67], v[36:39], v[212:215]\n"
    "ds_read_b64_tr_b16 a[92:93], v224 offset:15712\n"
    "v_lshrrev_b32 v241, 10, v241\n"
    "v_mfma_f32_16x16x32_bf16 v[212:215], a[68:71], v[44:47], v[212:215]\n"
    "s_waitcnt lgkmcnt(7)\n"
    "ds_read_b64_tr_b16 a[94:95], v224 offset:19552\n"
    "v_mfma_f32_16x16x32_bf16 a[0:3], v[32:35], a[72:75], a[0:3]\n"
    "s_add_u32 m0, s60, 5120\n"
    "ds_read_b64_tr_b16 a[64:65], v224 offset:384\n"
    "ds_read_b64_tr_b16 a[66:67], v224 offset:4224\n"
    "v_mfma_f32_16x16x32_bf16 a[0:3], v[40:43], a[76:79], a[0:3]\n"
    "buffer_load_dwordx4 v221, s[40:43], s65 offen lds\n"
    "v_mfma_f32_16x16x32_bf16 a[4:7], v[36:39], a[72:75], a[4:7]\n"
    "ds_read_b64_tr_b16 a[68:69], v224 offset:15744\n"
    "v_mul_u32_u24 v242, 18, v241\n"
    "v_mfma_f32_16x16x32_bf16 a[4:7], v[44:47], a[76:79], a[4:7]\n"
    "s_waitcnt lgkmcnt(7)\n"
    "ds_read_b64_tr_b16 a[70:71], v224 offset:19584\n"
    "v_mfma_f32_16x16x32_bf16 a[8:11], v[32:35], a[80:83], a[8:11]\n"
    "ds_read_b64_tr_b16 a[72:73], v224 offset:416\n"
    "v_sub_u32 v242, v254, v242\n"
    "v_mfma_f32_16x16x32_bf16 a[8:11], v[40:43], a[84:87], a[8:11]\n"
    "ds_read_b64_tr_b16 a[74:75], v224 offset:4256\n"
    "v_mfma_f32_16x16x32_bf16 a[12:15], v[36:39], a[80:83], a[12:15]\n"
    "s_add_u32 m0, s60, 6144\n"
    "ds_read_b64_tr_b16 a[76:77], v224 offset:15776\n"
    "ds_read_b64_tr_b16 a[78:79], v224 offset:19616\n"
    "v_mfma_f32_16x16x32_bf16 a[12:15], v[44:47], a[84:87], a[12:15]\n"
    "s_waitcnt vmcnt(6) lgkmcnt(8)\n"
    "s_barrier\n"
    "buffer_load_dwordx4 v222, s[40:43], s65 offen lds\n"
    "v_mfma_f32_16x16x32_bf16 a[16:19], v[32:35], a[88:91], a[16:19]\n"
    "ds_read_b64_tr_b16 a[80:81], v226 offset:0\n"
    "v_mul_u32_u24 v241, 0x500, v241\n"
    "v_mfma_f32_16x16x32_bf16 a[16:19], v[40:43], a[92:95], a[16:19]\n"
    "ds_read_b64_tr_b16 a[82:83], v226 offset:3840\n"
    "v_mfma_f32_16x16x32_bf16 a[20:23], v[36:39], a[88:91], a[20:23]\n"
    "ds_read_b64_tr_b16 a[84:85], v226 offset:15360\n"
    "v_lshl_add_u32 v241, v242, 4, v241\n"
    "v_mfma_f32_16x16x32_bf16 a[20:23], v[44:47], a[92:95], a[20:23]\n"
    "s_waitcnt lgkmcnt(7)\n"
    "ds_read_b64_tr_b16 a[86:87], v226 offset:19200\n"
    "v_mfma_f32_16x16x32_bf16 a[24:27], v[32:35], a[64:67], a[24:27]\n"
    "ds_read_b64_tr_b16 a[88:89], v226 offset:40\n"
    "v_add_u32 v248, v241, v235\n"
    "v_mfma_f32_16x16x32_bf16 a[24:27], v[40:43], a[68:71], a[24:27]\n"
    "ds_read_b64_tr_b16 a[90:91], v226 offset:3880\n"
    "v_mfma_f32_16x16x32_bf16 a[28:31], v[36:39], a[64:67], a[28:31]\n"
    "s_add_u32 m0, s60, 7168\n"
    "ds_read_b64_tr_b16 a[92:93], v226 offset:15400\n"
    "ds_read_b64_tr_b16 a[94:95], v226 offset:19240\n"
    "v_mfma_f32_16x16x32_bf16 a[28:31], v[44:47], a[68:71], a[28:31]\n"
    "s_waitcnt lgkmcnt(8)\n"
    "buffer_load_dwordx4 v223, s[40:43], s65 offen lds\n"
    "v_mfma_f32_16x16x32_bf16 a[32:35], v[32:35], a[72:75], a[32:35]\n"
    "ds_read_b64_tr_b16 a[64:65], v226 offset:72\n"
    "v_add_u32 v254, 48, v234\n"
    "v_mfma_f32_16x16x32_bf16 a[32:35], v[40:43], a[76:79], a[32:35]\n"
    "ds_read_b64_tr_b16 a[66:67], v226 offset:3912\n"
    "v_mfma_f32_16x16x32_bf16 a[36:39], v[36:39], a[72:75], a[36:39]\n"
    "ds_read_b64_tr_b16 a[68:69], v226 offset:15432\n"
    "v_mul_u32_u24 v241, 57, v254\n"
    "v_mfma_f32_16x16x32_bf16 a[36:39], v[44:47], a[76:79], a[36:39]\n"
    "s_waitcnt lgkmcnt(7)\n"
    "ds_read_b64_tr_b16 a[70:71], v226 offset:19272\n"
    "v_mfma_f32_16x16x32_bf16 v[144:147], a[80:83], v[48:51], v[144:147]\n"
    "s_mov_b32 s65, s72\n"
    "ds_read_b64_tr_b16 a[72:73], v226 offset:112\n"
    "v_lshrrev_b32 v241, 10, v241\n"
    "v_mfma_f32_16x16x32_bf16 v[144:147], a[84:87], v[56:59], v[144:147]\n"
    "ds_read_b64_tr_b16 a[74:75], v226 offset:3952\n"
    "v_mfma_f32_16x16x32_bf16 v[148:151], a[80:83], v[52:55], v[148:151]\n"
    "s_add_u32 m0, s60, 32768\n"
    "ds_read_b64_tr_b16 a[76:77], v226 offset:15472\n"
    "ds_read_b64_tr_b16 a[78:79], v226 offset:19312\n"
    "v_mfma_f32_16x16x32_bf16 v[148:151], a[84:87], v[60:63], v[148:151]\n"
    "s_waitcnt lgkmcnt(8)\n"
    "buffer_load_dwordx4 v216, s[40:43], s65 offen lds\n"
    "v_mfma_f32_16x16x32_bf16 v[152:155], a[88:91], v[48:51], v[152:155]\n"
    "ds_read_b64_tr_b16 a[80:81], v226 offset:144\n"
    "v_add_u32 v224, 0xfffe8000, v226\n"
    "v_mfma_f32_16x16x32_bf16 v[152:155], a[92:95], v[56:59], v[152:155]\n"
    "ds_read_b64_tr_b16 a[82:83], v226 offset:3984\n"
    "v_add_u32 v225, 0xfffe8000, v227\n"
    "v_mfma_f32_16x16x32_bf16 v[156:159], a[88:91], v[52:55], v[156:159]\n"
    "ds_read_b64_tr_b16 a[84:85], v226 offset:15504\n"
    "v_mul_u32_u24 v242, 18, v241\n"
    "v_mfma_f32_16x16x32_bf16 v[156:159], a[92:95], v[60:63], v[156:159]\n"
    "s_waitcnt lgkmcnt(7)\n"
    "ds_read_b64_tr_b16 a[86:87], v226 offset:19344\n"
    "v_mfma_f32_16x16x32_bf16 v[160:163], a[64:67], v[48:51], v[160:163]\n"
    "s_add_u32 m0, s60, 33792\n"
    "ds_read_b64_tr_b16 a[88:89], v226 offset:184\n"
    "ds_read_b64_tr_b16 a[90:91], v226 offset:4024\n"
    "v_mfma_f32_16x16x32_bf16 v[160:163], a[68:71], v[56:59], v[160:163]\n"
    "buffer_load_dwordx4 v217, s[40:43], s65 offen lds\n"
    "v_mfma_f32_16x16x32_bf16 v[164:167], a[64:67], v[52:55], v[164:167]\n"
    "ds_read_b64_tr_b16 a[92:93], v226 offset:15544\n"
    "v_sub_u32 v242, v254, v242\n"
    "v_mfma_f32_16x16x32_bf16 v[164:167], a[68:71], v[60:63], v[164:167]\n"
    "s_waitcnt lgkmcnt(7)\n"
    "ds_read_b64_tr_b16 a[94:95], v226 offset:19384\n"
    "v_mfma_f32_16x16x32_bf16 v[168:171], a[72:75], v[48:51], v[168:171]\n"
    "ds_read_b64_tr_b16 a[64:65], v226 offset:216\n"
    "v_mul_u32_u24 v241, 0x500, v241\n"
    "v_mfma_f32_16x16x32_bf16 v[168:171], a[76:79], v[56:59], v[168:171]\n"
    "ds_read_b64_tr_b16 a[66:67], v226 offset:4056\n"
    "v_mfma_f32_16x16x32_bf16 v[172:175], a[72:75], v[52:55], v[172:175]\n"
    "ds_read_b64_tr_b16 a[68:69], v226 offset:15576\n"
    "v_lshl_add_u32 v241, v242, 4, v241\n"
    "v_mfma_f32_16x16x32_bf16 v[172:175], a[76:79], v[60:63], v[172:175]\n"
    "s_waitcnt lgkmcnt(7)\n"
    "ds_read_b64_tr_b16 a[70:71], v226 offset:19416\n"
    "v_mfma_f32_16x16x32_bf16 v[176:179], a[80:83], v[48:51], v[176:179]\n"
    "s_add_u32 m0, s60, 34816\n"
    "ds_read_b64_tr_b16 a[72:73], v226 offset:256\n"
    "ds_read_b64_tr_b16 a[74:75], v226 offset:4096\n"
    "v_mfma_f32_16x16x32_bf16 v[176:179], a[84:87], v[56:59], v[176:179]\n"
    "buffer_load_dwordx4 v218, s[40:43], s65 offen lds\n"
    "v_mfma_f32_16x16x32_bf16 v[180:183], a[80:83], v[52:55], v[180:183]\n"
    "ds_read_b64_tr_b16 a[76:77], v226 offset:15616\n"
    "v_add_u32 v249, v241, v235\n"
    "v_mfma_f32_16x16x32_bf16 v[180:183], a[84:87], v[60:63], v[180:183]\n"
    "s_waitcnt lgkmcnt(7)\n"
    "ds_read_b64_tr_b16 a[78:79], v226 offset:19456\n"
    "v_mfma_f32_16x16x32_bf16 v[184:187], a[88:91], v[48:51], v[184:187]\n"
    "ds_read_b64_tr_b16 a[80:81], v227 offset:0\n"
    "v_add_u32 v254, 64, v234\n"
    "v_mfma_f32_16x16x32_bf16 v[184:187], a[92:95], v[56:59], v[184:187]\n"
    "ds_read_b64_tr_b16 a[82:83], v227 offset:3840\n"
    "v_mfma_f32_16x16x32_bf16 v[188:191], a[88:91], v[52:55], v[188:191]\n"
    "s_add_u32 m0, s60, 35840\n"
    "ds_read_b64_tr_b16 a[84:85], v227 offset:15360\n"
    "ds_read_b64_tr_b16 a[86:87], v227 offset:19200\n"
    "v_mfma_f32_16x16x32_bf16 v[188:191], a[92:95], v[60:63], v[188:191]\n"
    "s_waitcnt lgkmcnt(8)\n"
    "buffer_load_dwordx4 v219, s[40:43], s65 offen lds\n"
    "v_mfma_f32_16x16x32_bf16 v[192:195], a[64:67], v[48:51], v[192:195]\n"
    "ds_read_b64_tr_b16 a[88:89], v226 offset:288\n"
    "v_mul_u32_u24 v241, 57, v254\n"
    "v_mfma_f32_16x16x32_bf16 v[192:195], a[68:71], v[56:59], v[192:195]\n"
    "ds_read_b64_tr_b16 a[90:91], v226 offset:4128\n"
    "v_mfma_f32_16x16x32_bf16 v[196:199], a[64:67], v[52:55], v[196:199]\n"
    "ds_read_b64_tr_b16 a[92:93], v226 offset:15648\n"
    "v_lshrrev_b32 v241, 10, v241\n"
    "v_mfma_f32_16x16x32_bf16 v[196:199], a[68:71], v[60:63], v[196:199]\n"
    "s_waitcnt lgkmcnt(7)\n"
    "ds_read_b64_tr_b16 a[94:95], v226 offset:19488\n"
    "v_mfma_f32_16x16x32_bf16 v[200:203], a[72:75], v[48:51], v[200:203]\n"
    "ds_read_b64_tr_b16 a[64:65], v226 offset:320\n"
    "v_mul_u32_u24 v242, 18, v241\n"
    "v_mfma_f32_16x16x32_bf16 v[200:203], a[76:79], v[56:59], v[200:203]\n"
    "ds_read_b64_tr_b16 a[66:67], v226 offset:4160\n"
    "v_mfma_f32_16x16x32_bf16 v[204:207], a[72:75], v[52:55], v[204:207]\n"
    "s_add_u32 m0, s60, 36864\n"
    "ds_read_b64_tr_b16 a[68:69], v226 offset:15680\n"
    "ds_read_b64_tr_b16 a[70:71], v226 offset:19520\n"
    "v_mfma_f32_16x16x32_bf16 v[204:207], a[76:79], v[60:63], v[204:207]\n"
    "s_waitcnt lgkmcnt(8)\n"
    "buffer_load_dwordx4 v220, s[40:43], s65 offen lds\n"
    "v_mfma_f32_16x16x32_bf16 v[208:211], a[80:83], v[48:51], v[208:211]\n"
    "ds_read_b64_tr_b16 a[72:73], v226 offset:352\n"
    "v_sub_u32 v242, v254, v242\n"
    "v_mfma_f32_16x16x32_bf16 v[208:211], a[84:87], v[56:59], v[208:211]\n"
    "ds_read_b64_tr_b16 a[74:75], v226 offset:4192\n"
    "v_mfma_f32_16x16x32_bf16 v[212:215], a[80:83], v[52:55], v[212:215]\n"
    "ds_read_b64_tr_b16 a[76:77], v226 offset:15712\n"
    "v_mul_u32_u24 v241, 0x500, v241\n"
    "v_mfma_f32_16x16x32_bf16 v[212:215], a[84:87], v[60:63], v[212:215]\n"
    "s_waitcnt lgkmcnt(7)\n"
    "ds_read_b64_tr_b16 a[78:79], v226 offset:19552\n"
    "v_mfma_f32_16x16x32_bf16 a[0:3], v[48:51], a[88:91], a[0:3]\n"
    "s_add_u32 m0, s60, 37888\n"
    "ds_read_b64_tr_b16 a[80:81], v226 offset:384\n"
    "ds_read_b64_tr_b16 a[82:83], v226 offset:4224\n"
    "v_mfma_f32_16x16x32_bf16 a[0:3], v[56:59], a[92:95], a[0:3]\n"
    "buffer_load_dwordx4 v221, s[40:43], s65 offen lds\n"
    "v_mfma_f32_16x16x32_bf16 a[4:7], v[52:55], a[88:91], a[4:7]\n"
    "ds_read_b64_tr_b16 a[84:85], v226 offset:15744\n"
    "v_lshl_add_u32 v241, v242, 4, v241\n"
    "v_mfma_f32_16x16x32_bf16 a[4:7], v[60:63], a[92:95], a[4:7]\n"
    "s_waitcnt lgkmcnt(7)\n"
    "ds_read_b64_tr_b16 a[86:87], v226 offset:19584\n"
    "v_mfma_f32_16x16x32_bf16 a[8:11], v[48:51], a[64:67], a[8:11]\n"
    "ds_read_b64_tr_b16 a[88:89], v226 offset:416\n"
    "v_add_u32 v250, v241, v235\n"
    "v_mfma_f32_16x16x32_bf16 a[8:11], v[56:59], a[68:71], a[8:11]\n"
    "ds_read_b64_tr_b16 a[90:91], v226 offset:4256\n"
    "v_mfma_f32_16x16x32_bf16 a[12:15], v[52:55], a[64:67], a[12:15]\n"
    "s_add_u32 m0, s60, 38912\n"
    "ds_read_b64_tr_b16 a[92:93], v226 offset:15776\n"
    "ds_read_b64_tr_b16 a[94:95], v226 offset:19616\n"
    "v_mfma_f32_16x16x32_bf16 a[12:15], v[60:63], a[68:71], a[12:15]\n"
    "s_waitcnt vmcnt(6) lgkmcnt(8)\n"
    "s_barrier\n"
    "buffer_load_dwordx4 v222, s[40:43], s65 offen lds\n"
    "v_mfma_f32_16x16x32_bf16 a[16:19], v[48:51], a[72:75], a[16:19]\n"
    "ds_read_b64_tr_b16 a[64:65], v224 offset:0\n"
    "v_bfe_u32 v241, v255, 2, 2\n"
    "v_mfma_f32_16x16x32_bf16 a[16:19], v[56:59], a[76:79], a[16:19]\n"
    "ds_read_b64_tr_b16 a[66:67], v224 offset:3840\n"
    "v_mfma_f32_16x16x32_bf16 a[20:23], v[52:55], a[72:75], a[20:23]\n"
    "ds_read_b64_tr_b16 a[68:69], v224 offset:15360\n"
    "v_mul_u32_u24 v241, 0x500, v241\n"
    "v_mfma_f32_16x16x32_bf16 a[20:23], v[60:63], a[76:79], a[20:23]\n"
    "s_waitcnt lgkmcnt(7)\n"
    "ds_read_b64_tr_b16 a[70:71], v224 offset:19200\n"
    "v_mfma_f32_16x16x32_bf16 a[24:27], v[48:51], a[80:83], a[24:27]\n"
    "ds_read_b64_tr_b16 a[72:73], v224 offset:40\n"
    "v_and_b32 v242, 3, v255\n"
    "v_mfma_f32_16x16x32_bf16 a[24:27], v[56:59], a[84:87], a[24:27]\n"
    "ds_read_b64_tr_b16 a[74:75], v224 offset:3880\n"
    "v_mfma_f32_16x16x32_bf16 a[28:31], v[52:55], a[80:83], a[28:31]\n"
    "s_add_u32 m0, s60, 39936\n"
    "ds_read_b64_tr_b16 a[76:77], v224 offset:15400\n"
    "ds_read_b64_tr_b16 a[78:79], v224 offset:19240\n"
    "v_mfma_f32_16x16x32_bf16 a[28:31], v[60:63], a[84:87], a[28:31]\n"
    "s_waitcnt lgkmcnt(8)\n"
    "buffer_load_dwordx4 v223, s[40:43], s65 offen lds\n"
    "v_mfma_f32_16x16x32_bf16 a[32:35], v[48:51], a[88:91], a[32:35]\n"
    "ds_read_b64_tr_b16 a[80:81], v224 offset:72\n"
    "v_mul_u32_u24 v242, 0x140, v242\n"
    "v_mfma_f32_16x16x32_bf16 a[32:35], v[56:59], a[92:95], a[32:35]\n"
    "ds_read_b64_tr_b16 a[82:83], v224 offset:3912\n"
    "v_mfma_f32_16x16x32_bf16 a[36:39], v[52:55], a[88:91], a[36:39]\n"
    "ds_read_b64_tr_b16 a[84:85], v224 offset:15432\n"
    "v_add_u32 v241, v241, v242\n"
    "v_mfma_f32_16x16x32_bf16 a[36:39], v[60:63], a[92:95], a[36:39]\n"
    "s_waitcnt lgkmcnt(7)\n"
    "ds_read_b64_tr_b16 a[86:87], v224 offset:19272\n"
    "v_mfma_f32_16x16x32_bf16 v[144:147], a[64:67], v[64:67], v[144:147]\n"
    "s_mov_b32 s65, s73\n"
    "ds_read_b64_tr_b16 a[88:89], v224 offset:112\n"
    "v_and_b32 v242, 16, v255\n"
    "v_mfma_f32_16x16x32_bf16 v[144:147], a[68:71], v[72:75], v[144:147]\n"
    "ds_read_b64_tr_b16 a[90:91], v224 offset:3952\n"
    "v_mfma_f32_16x16x32_bf16 v[148:151], a[64:67], v[68:71], v[148:151]\n"
    "s_add_u32 m0, s60, 65536\n"
    "ds_read_b64_tr_b16 a[92:93], v224 offset:15472\n"
    "ds_read_b64_tr_b16 a[94:95], v224 offset:19312\n"
    "v_mfma_f32_16x16x32_bf16 v[148:151], a[68:71], v[76:79], v[148:151]\n"
    "s_waitcnt lgkmcnt(8)\n"
    "buffer_load_dwordx4 v216, s[40:43], s65 offen lds\n"
    "v_mfma_f32_16x16x32_bf16 v[152:155], a[72:75], v[64:67], v[152:155]\n"
    "ds_read_b64_tr_b16 a[64:65], v224 offset:144\n"
    "v_add_u32 v226, 0x8000, v224\n"
    "v_mfma_f32_16x16x32_bf16 v[152:155], a[76:79], v[72:75], v[152:155]\n"
    "ds_read_b64_tr_b16 a[66:67], v224 offset:3984\n"
    "v_add_u32 v227, 0x8000, v225\n"
    "v_mfma_f32_16x16x32_bf16 v[156:159], a[72:75], v[68:71], v[156:159]\n"
    "ds_read_b64_tr_b16 a[68:69], v224 offset:15504\n"
    "v_add_u32 v241, v241, v242\n"
    "v_mfma_f32_16x16x32_bf16 v[156:159], a[76:79], v[76:79], v[156:159]\n"
    "s_waitcnt lgkmcnt(7)\n"
    "ds_read_b64_tr_b16 a[70:71], v224 offset:19344\n"
    "v_mfma_f32_16x16x32_bf16 v[160:163], a[80:83], v[64:67], v[160:163]\n"
    "s_add_u32 m0, s60, 66560\n"
    "ds_read_b64_tr_b16 a[72:73], v224 offset:184\n"
    "ds_read_b64_tr_b16 a[74:75], v224 offset:4024\n"
    "v_mfma_f32_16x16x32_bf16 v[160:163], a[84:87], v[72:75], v[160:163]\n"
    "buffer_load_dwordx4 v217, s[40:43], s65 offen lds\n"
    "v_mfma_f32_16x16x32_bf16 v[164:167], a[80:83], v[68:71], v[164:167]\n"
    "ds_read_b64_tr_b16 a[76:77], v224 offset:15544\n"
    "v_add_u32 v241, 288, v241\n"
    "v_mfma_f32_16x16x32_bf16 v[164:167], a[84:87], v[76:79], v[164:167]\n"
    "s_waitcnt lgkmcnt(7)\n"
    "ds_read_b64_tr_b16 a[78:79], v224 offset:19384\n"
    "v_mfma_f32_16x16x32_bf16 v[168:171], a[88:91], v[64:67], v[168:171]\n"
    "ds_read_b64_tr_b16 a[80:81], v224 offset:216\n"
    "v_add_u32 v251, s53, v241\n"
    "v_mfma_f32_16x16x32_bf16 v[168:171], a[92:95], v[72:75], v[168:171]\n"
    "ds_read_b64_tr_b16 a[82:83], v224 offset:4056\n"
    "v_mfma_f32_16x16x32_bf16 v[172:175], a[88:91], v[68:71], v[172:175]\n"
    "ds_read_b64_tr_b16 a[84:85], v224 offset:15576\n"
    "v_cmp_gt_u32 s[74:75], 16, v255\n"
    "v_mfma_f32_16x16x32_bf16 v[172:175], a[92:95], v[76:79], v[172:175]\n"
    "s_waitcnt lgkmcnt(7)\n"
    "ds_read_b64_tr_b16 a[86:87], v224 offset:19416\n"
    "v_mfma_f32_16x16x32_bf16 v[176:179], a[64:67], v[64:67], v[176:179]\n"
    "s_add_u32 m0, s60, 67584\n"
    "ds_read_b64_tr_b16 a[88:89], v224 offset:256\n"
    "ds_read_b64_tr_b16 a[90:91], v224 offset:4096\n"
    "v_mfma_f32_16x16x32_bf16 v[176:179], a[68:71], v[72:75], v[176:179]\n"
    "buffer_load_dwordx4 v218, s[40:43], s65 offen lds\n"
    "v_mfma_f32_16x16x32_bf16 v[180:183], a[64:67], v[68:71], v[180:183]\n"
    "ds_read_b64_tr_b16 a[92:93], v224 offset:15616\n"
    "v_mov_b32 v242, 0x3f803f80\n"
    "v_mfma_f32_16x16x32_bf16 v[180:183], a[68:71], v[76:79], v[180:183]\n"
    "s_waitcnt lgkmcnt(7)\n"
    "ds_read_b64_tr_b16 a[94:95], v224 offset:19456\n"
    "v_mfma_f32_16x16x32_bf16 v[184:187], a[72:75], v[64:67], v[184:187]\n"
    "ds_read_b64_tr_b16 a[64:65], v225 offset:0\n"
    "v_cndmask_b32 v232, 0, v242, s[74:75]\n"
    "v_mfma_f32_16x16x32_bf16 v[184:187], a[76:79], v[72:75], v[184:187]\n"
    "ds_read_b64_tr_b16 a[66:67], v225 offset:3840\n"
    "v_mfma_f32_16x16x32_bf16 v[188:191], a[72:75], v[68:71], v[188:191]\n"
    "s_add_u32 m0, s60, 68608\n"
    "ds_read_b64_tr_b16 a[68:69], v225 offset:15360\n"
    "ds_read_b64_tr_b16 a[70:71], v225 offset:19200\n"
    "v_mfma_f32_16x16x32_bf16 v[188:191], a[76:79], v[76:79], v[188:191]\n"
    "s_waitcnt lgkmcnt(8)\n"
    "buffer_load_dwordx4 v219, s[40:43], s65 offen lds\n"
    "v_mfma_f32_16x16x32_bf16 v[192:195], a[80:83], v[64:67], v[192:195]\n"
    "ds_read_b64_tr_b16 a[72:73], v224 offset:288\n"
    "v_cmp_gt_u32 s[74:75], 32, v255\n"
    "v_mfma_f32_16x16x32_bf16 v[192:195], a[84:87], v[72:75], v[192:195]\n"
    "ds_read_b64_tr_b16 a[74:75], v224 offset:4128\n"
    "v_mfma_f32_16x16x32_bf16 v[196:199], a[80:83], v[68:71], v[196:199]\n"
    "ds_read_b64_tr_b16 a[76:77], v224 offset:15648\n"
    "v_mov_b32 v233, v232\n"
    "v_mfma_f32_16x16x32_bf16 v[196:199], a[84:87], v[76:79], v[196:199]\n"
    "s_waitcnt lgkmcnt(7)\n"
    "ds_read_b64_tr_b16 a[78:79], v224 offset:19488\n"
    "v_mfma_f32_16x16x32_bf16 v[200:203], a[88:91], v[64:67], v[200:203]\n"
    "ds_read_b64_tr_b16 a[80:81], v224 offset:320\n"
    "v_mov_b32 v234, v232\n"
    "v_mfma_f32_16x16x32_bf16 v[200:203], a[92:95], v[72:75], v[200:203]\n"
    "ds_read_b64_tr_b16 a[82:83], v224 offset:4160\n"
    "v_mfma_f32_16x16x32_bf16 v[204:207], a[88:91], v[68:71], v[204:207]\n"
    "s_add_u32 m0, s60, 69632\n"
    "ds_read_b64_tr_b16 a[84:85], v224 offset:15680\n"
    "ds_read_b64_tr_b16 a[86:87], v224 offset:19520\n"
    "v_mfma_f32_16x16x32_bf16 v[204:207], a[92:95], v[76:79], v[204:207]\n"
    "s_waitcnt lgkmcnt(8)\n"
    "buffer_load_dwordx4 v220, s[40:43], s65 offen lds\n"
    "v_mfma_f32_16x16x32_bf16 v[208:211], a[64:67], v[64:67], v[208:211]\n"
    "ds_read_b64_tr_b16 a[88:89], v224 offset:352\n"
    "v_mov_b32 v235, v232\n"
    "v_mfma_f32_16x16x32_bf16 v[208:211], a[68:71], v[72:75], v[208:211]\n"
    "ds_read_b64_tr_b16 a[90:91], v224 offset:4192\n"
    "v_mfma_f32_16x16x32_bf16 v[212:215], a[64:67], v[68:71], v[212:215]\n"
    "ds_read_b64_tr_b16 a[92:93], v224 offset:15712\n"
    "v_cndmask_b32 v252, v228, v230, s[74:75]\n"
    "v_mfma_f32_16x16x32_bf16 v[212:215], a[68:71], v[76:79], v[212:215]\n"
    "s_waitcnt lgkmcnt(7)\n"
    "ds_read_b64_tr_b16 a[94:95], v224 offset:19552\n"
    "v_mfma_f32_16x16x32_bf16 a[0:3], v[64:67], a[72:75], a[0:3]\n"
    "s_add_u32 m0, s60, 70656\n"
    "ds_read_b64_tr_b16 a[64:65], v224 offset:384\n"
    "ds_read_b64_tr_b16 a[66:67], v224 offset:4224\n"
    "v_mfma_f32_16x16x32_bf16 a[0:3], v[72:75], a[76:79], a[0:3]\n"
    "buffer_load_dwordx4 v221, s[40:43], s65 offen lds\n"
    "v_mfma_f32_16x16x32_bf16 a[4:7], v[68:71], a[72:75], a[4:7]\n"
    "ds_read_b64_tr_b16 a[68:69], v224 offset:15744\n"
    "v_cndmask_b32 v253, v229, v231, s[74:75]\n"
    "v_mfma_f32_16x16x32_bf16 a[4:7], v[76:79], a[76:79], a[4:7]\n"
    "s_waitcnt lgkmcnt(7)\n"
    "ds_read_b64_tr_b16 a[70:71], v224 offset:19584\n"
    "v_mfma_f32_16x16x32_bf16 a[8:11], v[64:67], a[80:83], a[8:11]\n"
    "ds_read_b64_tr_b16 a[72:73], v224 offset:416\n"
    "v_mfma_f32_16x16x32_bf16 a[8:11], v[72:75], a[84:87], a[8:11]\n"
    "ds_read_b64_tr_b16 a[74:75], v224 offset:4256\n"
    "v_mfma_f32_16x16x32_bf16 a[12:15], v[68:71], a[80:83], a[12:15]\n"
    "s_add_u32 m0, s60, 71680\n"
    "ds_read_b64_tr_b16 a[76:77], v224 offset:15776\n"
    "ds_read_b64_tr_b16 a[78:79], v224 offset:19616\n"
    "v_mfma_f32_16x16x32_bf16 a[12:15], v[76:79], a[84:87], a[12:15]\n"
    "s_waitcnt vmcnt(6) lgkmcnt(8)\n"
    "s_barrier\n"
    "buffer_load_dwordx4 v222, s[40:43], s65 offen lds\n"
    "v_mfma_f32_16x16x32_bf16 a[16:19], v[64:67], a[88:91], a[16:19]\n"
    "ds_read_b64_tr_b16 a[80:81], v226 offset:0\n"
    "v_mfma_f32_16x16x32_bf16 a[16:19], v[72:75], a[92:95], a[16:19]\n"
    "ds_read_b64_tr_b16 a[82:83], v226 offset:3840\n"
    "v_mfma_f32_16x16x32_bf16 a[20:23], v[68:71], a[88:91], a[20:23]\n"
    "ds_read_b64_tr_b16 a[84:85], v226 offset:15360\n"
    "v_mfma_f32_16x16x32_bf16 a[20:23], v[76:79], a[92:95], a[20:23]\n"
    "s_waitcnt lgkmcnt(7)\n"
    "ds_read_b64_tr_b16 a[86:87], v226 offset:19200\n"
    "v_mfma_f32_16x16x32_bf16 a[24:27], v[64:67], a[64:67], a[24:27]\n"
    "ds_read_b64_tr_b16 a[88:89], v226 offset:40\n"
    "v_mfma_f32_16x16x32_bf16 a[24:27], v[72:75], a[68:71], a[24:27]\n"
    "ds_read_b64_tr_b16 a[90:91], v226 offset:3880\n"
    "v_mfma_f32_16x16x32_bf16 a[28:31], v[68:71], a[64:67], a[28:31]\n"
    "s_add_u32 m0, s60, 72704\n"
    "ds_read_b64_tr_b16 a[92:93], v226 offset:15400\n"
    "ds_read_b64_tr_b16 a[94:95], v226 offset:19240\n"
    "v_mfma_f32_16x16x32_bf16 a[28:31], v[76:79], a[68:71], a[28:31]\n"
    "s_waitcnt lgkmcnt(8)\n"
    "buffer_load_dwordx4 v223, s[40:43], s65 offen lds\n"
    "v_mfma_f32_16x16x32_bf16 a[32:35], v[64:67], a[72:75], a[32:35]\n"
    "ds_read_b64_tr_b16 a[64:65], v226 offset:72\n"
    "v_mfma_f32_16x16x32_bf16 a[32:35], v[72:75], a[76:79], a[32:35]\n"
    "ds_read_b64_tr_b16 a[66:67], v226 offset:3912\n"
    "v_mfma_f32_16x16x32_bf16 a[36:39], v[68:71], a[72:75], a[36:39]\n"
    "ds_read_b64_tr_b16 a[68:69], v226 offset:15432\n"
    "v_mfma_f32_16x16x32_bf16 a[36:39], v[76:79], a[76:79], a[36:39]\n"
    "s_waitcnt lgkmcnt(7)\n"
    "ds_read_b64_tr_b16 a[70:71], v226 offset:19272\n"
    "v_mfma_f32_16x16x32_bf16 v[144:147], a[80:83], v[80:83], v[144:147]\n"
    "s_mov_b32 s65, s76\n"
    "ds_read_b64_tr_b16 a[72:73], v226 offset:112\n"
    "v_mfma_f32_16x16x32_bf16 v[144:147], a[84:87], v[88:91], v[144:147]\n"
    "ds_read_b64_tr_b16 a[74:75], v226 offset:3952\n"
    "v_mfma_f32_16x16x32_bf16 v[148:151], a[80:83], v[84:87], v[148:151]\n"
    "s_add_u32 m0, s60, 98304\n"
    "ds_read_b64_tr_b16 a[76:77], v226 offset:15472\n"
    "ds_read_b64_tr_b16 a[78:79], v226 offset:19312\n"
    "v_mfma_f32_16x16x32_bf16 v[148:151], a[84:87], v[92:95], v[148:151]\n"
    "s_waitcnt lgkmcnt(8)\n"
    "buffer_load_dwordx4 v216, s[40:43], s65 offen lds\n"
    "v_mfma_f32_16x16x32_bf16 v[152:155], a[88:91], v[80:83], v[152:155]\n"
    "ds_read_b64_tr_b16 a[80:81], v226 offset:144\n"
    "v_add_u32 v224, 0x8000, v226\n"
    "v_mfma_f32_16x16x32_bf16 v[152:155], a[92:95], v[88:91], v[152:155]\n"
    "ds_read_b64_tr_b16 a[82:83], v226 offset:3984\n"
    "v_add_u32 v225, 0x8000, v227\n"
    "v_mfma_f32_16x16x32_bf16 v[156:159], a[88:91], v[84:87], v[156:159]\n"
    "ds_read_b64_tr_b16 a[84:85], v226 offset:15504\n"
    "v_mfma_f32_16x16x32_bf16 v[156:159], a[92:95], v[92:95], v[156:159]\n"
    "s_waitcnt lgkmcnt(7)\n"
    "ds_read_b64_tr_b16 a[86:87], v226 offset:19344\n"
    "v_mfma_f32_16x16x32_bf16 v[160:163], a[64:67], v[80:83], v[160:163]\n"
    "s_add_u32 m0, s60, 99328\n"
    "ds_read_b64_tr_b16 a[88:89], v226 offset:184\n"
    "ds_read_b64_tr_b16 a[90:91], v226 offset:4024\n"
    "v_mfma_f32_16x16x32_bf16 v[160:163], a[68:71], v[88:91], v[160:163]\n"
    "buffer_load_dwordx4 v217, s[40:43], s65 offen lds\n"
    "v_mfma_f32_16x16x32_bf16 v[164:167], a[64:67], v[84:87], v[164:167]\n"
    "ds_read_b64_tr_b16 a[92:93], v226 offset:15544\n"
    "v_mfma_f32_16x16x32_bf16 v[164:167], a[68:71], v[92:95], v[164:167]\n"
    "s_waitcnt lgkmcnt(7)\n"
    "ds_read_b64_tr_b16 a[94:95], v226 offset:19384\n"
    "v_mfma_f32_16x16x32_bf16 v[168:171], a[72:75], v[80:83], v[168:171]\n"
    "ds_read_b64_tr_b16 a[64:65], v226 offset:216\n"
    "v_mfma_f32_16x16x32_bf16 v[168:171], a[76:79], v[88:91], v[168:171]\n"
    "ds_read_b64_tr_b16 a[66:67], v226 offset:4056\n"
    "v_mfma_f32_16x16x32_bf16 v[172:175], a[72:75], v[84:87], v[172:175]\n"
    "ds_read_b64_tr_b16 a[68:69], v226 offset:15576\n"
    "v_mfma_f32_16x16x32_bf16 v[172:175], a[76:79], v[92:95], v[172:175]\n"
    "s_waitcnt lgkmcnt(7)\n"
    "ds_read_b64_tr_b16 a[70:71], v226 offset:19416\n"
    "v_mfma_f32_16x16x32_bf16 v[176:179], a[80:83], v[80:83], v[176:179]\n"
    "s_add_u32 m0, s60, 100352\n"
    "ds_read_b64_tr_b16 a[72:73], v226 offset:256\n"
    "ds_read_b64_tr_b16 a[74:75], v226 offset:4096\n"
    "v_mfma_f32_16x16x32_bf16 v[176:179], a[84:87], v[88:91], v[176:179]\n"
    "buffer_load_dwordx4 v218, s[40:43], s65 offen lds\n"
    "v_mfma_f32_16x16x32_bf16 v[180:183], a[80:83], v[84:87], v[180:183]\n"
    "ds_read_b64_tr_b16 a[76:77], v226 offset:15616\n"
    "v_mfma_f32_16x16x32_bf16 v[180:183], a[84:87], v[92:95], v[180:183]\n"
    "s_waitcnt lgkmcnt(7)\n"
    "ds_read_b64_tr_b16 a[78:79], v226 offset:19456\n"
    "v_mfma_f32_16x16x32_bf16 v[184:187], a[88:91], v[80:83], v[184:187]\n"
    "ds_read_b64_tr_b16 a[80:81], v227 offset:0\n"
    "v_mfma_f32_16x16x32_bf16 v[184:187], a[92:95], v[88:91], v[184:187]\n"
    "ds_read_b64_tr_b16 a[82:83], v227 offset:3840\n"
    "v_mfma_f32_16x16x32_bf16 v[188:191], a[88:91], v[84:87], v[188:191]\n"
    "s_add_u32 m0, s60, 101376\n"
    "ds_read_b64_tr_b16 a[84:85], v227 offset:15360\n"
    "ds_read_b64_tr_b16 a[86:87], v227 offset:19200\n"
    "v_mfma_f32_16x16x32_bf16 v[188:191], a[92:95], v[92:95], v[188:191]\n"
    "s_waitcnt lgkmcnt(8)\n"
    "buffer_load_dwordx4 v219, s[40:43], s65 offen lds\n"
    "v_mfma_f32_16x16x32_bf16 v[192:195], a[64:67], v[80:83], v[192:195]\n"
    "ds_read_b64_tr_b16 a[88:89], v226 offset:288\n"
    "v_mfma_f32_16x16x32_bf16 v[192:195], a[68:71], v[88:91], v[192:195]\n"
    "ds_read_b64_tr_b16 a[90:91], v226 offset:4128\n"
    "v_mfma_f32_16x16x32_bf16 v[196:199], a[64:67], v[84:87], v[196:199]\n"
    "ds_read_b64_tr_b16 a[92:93], v226 offset:15648\n"
    "v_mfma_f32_16x16x32_bf16 v[196:199], a[68:71], v[92:95], v[196:199]\n"
    "s_waitcnt lgkmcnt(7)\n"
    "ds_read_b64_tr_b16 a[94:95], v226 offset:19488\n"
    "v_mfma_f32_16x16x32_bf16 v[200:203], a[72:75], v[80:83], v[200:203]\n"
    "ds_read_b64_tr_b16 a[64:65], v226 offset:320\n"
    "v_mfma_f32_16x16x32_bf16 v[200:203], a[76:79], v[88:91], v[200:203]\n"
    "ds_read_b64_tr_b16 a[66:67], v226 offset:4160\n"
    "v_mfma_f32_16x16x32_bf16 v[204:207], a[72:75], v[84:87], v[204:207]\n"
    "s_add_u32 m0, s60, 102400\n"
    "ds_read_b64_tr_b16 a[68:69], v226 offset:15680\n"
    "ds_read_b64_tr_b16 a[70:71], v226 offset:19520\n"
    "v_mfma_f32_16x16x32_bf16 v[204:207], a[76:79], v[92:95], v[204:207]\n"
    "s_waitcnt lgkmcnt(8)\n"
    "buffer_load_dwordx4 v220, s[40:43], s65 offen lds\n"
    "v_mfma_f32_16x16x32_bf16 v[208:211], a[80:83], v[80:83], v[208:211]\n"
    "ds_read_b64_tr_b16 a[72:73], v226 offset:352\n"
    "v_mfma_f32_16x16x32_bf16 v[208:211], a[84:87], v[88:91], v[208:211]\n"
    "ds_read_b64_tr_b16 a[74:75], v226 offset:4192\n"
    "v_mfma_f32_16x16x32_bf16 v[212:215], a[80:83], v[84:87], v[212:215]\n"
    "ds_read_b64_tr_b16 a[76:77], v226 offset:15712\n"
    "v_mfma_f32_16x16x32_bf16 v[212:215], a[84:87], v[92:95], v[212:215]\n"
    "s_waitcnt lgkmcnt(7)\n"
    "ds_read_b64_tr_b16 a[78:79], v226 offset:19552\n"
    "v_mfma_f32_16x16x32_bf16 a[0:3], v[80:83], a[88:91], a[0:3]\n"
    "s_add_u32 m0, s60, 103424\n"
    "ds_read_b64_tr_b16 a[80:81], v226 offset:384\n"
    "ds_read_b64_tr_b16 a[82:83], v226 offset:4224\n"
    "v_mfma_f32_16x16x32_bf16 a[0:3], v[88:91], a[92:95], a[0:3]\n"
    "buffer_load_dwordx4 v221, s[40:43], s65 offen lds\n"
    "v_mfma_f32_16x16x32_bf16 a[4:7], v[84:87], a[88:91], a[4:7]\n"
    "ds_read_b64_tr_b16 a[84:85], v226 offset:15744\n"
    "v_mfma_f32_16x16x32_bf16 a[4:7], v[92:95], a[92:95], a[4:7]\n"
    "s_waitcnt lgkmcnt(7)\n"
    "ds_read_b64_tr_b16 a[86:87], v226 offset:19584\n"
    "v_mfma_f32_16x16x32_bf16 a[8:11], v[80:83], a[64:67], a[8:11]\n"
    "ds_read_b64_tr_b16 a[88:89], v226 offset:416\n"
    "v_mfma_f32_16x16x32_bf16 a[8:11], v[88:91], a[68:71], a[8:11]\n"
    "ds_read_b64_tr_b16 a[90:91], v226 offset:4256\n"
    "v_mfma_f32_16x16x32_bf16 a[12:15], v[84:87], a[64:67], a[12:15]\n"
    "s_add_u32 m0, s60, 104448\n"
    "ds_read_b64_tr_b16 a[92:93], v226 offset:15776\n"
    "ds_read_b64_tr_b16 a[94:95], v226 offset:19616\n"
    "v_mfma_f32_16x16x32_bf16 a[12:15], v[92:95], a[68:71], a[12:15]\n"
    "s_waitcnt vmcnt(6) lgkmcnt(8)\n"
    "s_barrier\n"
    "buffer_load_dwordx4 v222, s[40:43], s65 offen lds\n"
    "v_mfma_f32_16x16x32_bf16 a[16:19], v[80:83], a[72:75], a[16:19]\n"
    "ds_read_b64_tr_b16 a[64:65], v224 offset:0\n"
    "v_mfma_f32_16x16x32_bf16 a[16:19], v[88:91], a[76:79], a[16:19]\n"
    "ds_read_b64_tr_b16 a[66:67], v224 offset:3840\n"
    "v_mfma_f32_16x16x32_bf16 a[20:23], v[84:87], a[72:75], a[20:23]\n"
    "ds_read_b64_tr_b16 a[68:69], v224 offset:15360\n"
    "v_mfma_f32_16x16x32_bf16 a[20:23], v[92:95], a[76:79], a[20:23]\n"
    "s_waitcnt lgkmcnt(7)\n"
    "ds_read_b64_tr_b16 a[70:71], v224 offset:19200\n"
    "v_mfma_f32_16x16x32_bf16 a[24:27], v[80:83], a[80:83], a[24:27]\n"
    "ds_read_b64_tr_b16 a[72:73], v224 offset:40\n"
    "v_mfma_f32_16x16x32_bf16 a[24:27], v[88:91], a[84:87], a[24:27]\n"
    "ds_read_b64_tr_b16 a[74:75], v224 offset:3880\n"
    "v_mfma_f32_16x16x32_bf16 a[28:31], v[84:87], a[80:83], a[28:31]\n"
    "s_add_u32 m0, s60, 105472\n"
    "ds_read_b64_tr_b16 a[76:77], v224 offset:15400\n"
    "ds_read_b64_tr_b16 a[78:79], v224 offset:19240\n"
    "v_mfma_f32_16x16x32_bf16 a[28:31], v[92:95], a[84:87], a[28:31]\n"
    "s_waitcnt lgkmcnt(8)\n"
    "buffer_load_dwordx4 v223, s[40:43], s65 offen lds\n"
    "v_mfma_f32_16x16x32_bf16 a[32:35], v[80:83], a[88:91], a[32:35]\n"
    "ds_read_b64_tr_b16 a[80:81], v224 offset:72\n"
    "v_mfma_f32_16x16x32_bf16 a[32:35], v[88:91], a[92:95], a[32:35]\n"
    "ds_read_b64_tr_b16 a[82:83], v224 offset:3912\n"
    "v_mfma_f32_16x16x32_bf16 a[36:39], v[84:87], a[88:91], a[36:39]\n"
    "ds_read_b64_tr_b16 a[84:85], v224 offset:15432\n"
    "v_mfma_f32_16x16x32_bf16 a[36:39], v[92:95], a[92:95], a[36:39]\n"
    "s_waitcnt lgkmcnt(7)\n"
    "ds_read_b64_tr_b16 a[86:87], v224 offset:19272\n"
    "v_mfma_f32_16x16x32_bf16 v[144:147], a[64:67], v[96:99], v[144:147]\n"
    "s_mov_b32 s65, s77\n"
    "ds_read_b64_tr_b16 a[88:89], v224 offset:112\n"
    "v_mfma_f32_16x16x32_bf16 v[144:147], a[68:71], v[104:107], v[144:147]\n"
    "ds_read_b64_tr_b16 a[90:91], v224 offset:3952\n"
    "v_mfma_f32_16x16x32_bf16 v[148:151], a[64:67], v[100:103], v[148:151]\n"
    "s_add_u32 m0, s60, 0\n"
    "ds_read_b64_tr_b16 a[92:93], v224 offset:15472\n"
    "ds_read_b64_tr_b16 a[94:95], v224 offset:19312\n"
    "v_mfma_f32_16x16x32_bf16 v[148:151], a[68:71], v[108:111], v[148:151]\n"
    "s_waitcnt lgkmcnt(8)\n"
    "buffer_load_dwordx4 v216, s[40:43], s65 offen lds\n"
    "v_mfma_f32_16x16x32_bf16 v[152:155], a[72:75], v[96:99], v[152:155]\n"
    "ds_read_b64_tr_b16 a[64:65], v224 offset:144\n"
    "v_add_u32 v226, 0x8000, v224\n"
    "v_mfma_f32_16x16x32_bf16 v[152:155], a[76:79], v[104:107], v[152:155]\n"
    "ds_read_b64_tr_b16 a[66:67], v224 offset:3984\n"
    "v_add_u32 v227, 0x8000, v225\n"
    "v_mfma_f32_16x16x32_bf16 v[156:159], a[72:75], v[100:103], v[156:159]\n"
    "ds_read_b64_tr_b16 a[68:69], v224 offset:15504\n"
    "v_mfma_f32_16x16x32_bf16 v[156:159], a[76:79], v[108:111], v[156:159]\n"
    "s_waitcnt lgkmcnt(7)\n"
    "ds_read_b64_tr_b16 a[70:71], v224 offset:19344\n"
    "v_mfma_f32_16x16x32_bf16 v[160:163], a[80:83], v[96:99], v[160:163]\n"
    "s_add_u32 m0, s60, 1024\n"
    "ds_read_b64_tr_b16 a[72:73], v224 offset:184\n"
    "ds_read_b64_tr_b16 a[74:75], v224 offset:4024\n"
    "v_mfma_f32_16x16x32_bf16 v[160:163], a[84:87], v[104:107], v[160:163]\n"
    "buffer_load_dwordx4 v217, s[40:43], s65 offen lds\n"
    "v_mfma_f32_16x16x32_bf16 v[164:167], a[80:83], v[100:103], v[164:167]\n"
    "ds_read_b64_tr_b16 a[76:77], v224 offset:15544\n"
    "v_mfma_f32_16x16x32_bf16 v[164:167], a[84:87], v[108:111], v[164:167]\n"
    "s_waitcnt lgkmcnt(7)\n"
    "ds_read_b64_tr_b16 a[78:79], v224 offset:19384\n"
    "v_mfma_f32_16x16x32_bf16 v[168:171], a[88:91], v[96:99], v[168:171]\n"
    "ds_read_b64_tr_b16 a[80:81], v224 offset:216\n"
    "v_mfma_f32_16x16x32_bf16 v[168:171], a[92:95], v[104:107], v[168:171]\n"
    "ds_read_b64_tr_b16 a[82:83], v224 offset:4056\n"
    "v_mfma_f32_16x16x32_bf16 v[172:175], a[88:91], v[100:103], v[172:175]\n"
    "ds_read_b64_tr_b16 a[84:85], v224 offset:15576\n"
    "v_mfma_f32_16x16x32_bf16 v[172:175], a[92:95], v[108:111], v[172:175]\n"
    "s_waitcnt lgkmcnt(7)\n"
    "ds_read_b64_tr_b16 a[86:87], v224 offset:19416\n"
    "v_mfma_f32_16x16x32_bf16 v[176:179], a[64:67], v[96:99], v[176:179]\n"
    "s_add_u32 m0, s60, 2048\n"
    "ds_read_b64_tr_b16 a[88:89], v224 offset:256\n"
    "ds_read_b64_tr_b16 a[90:91], v224 offset:4096\n"
    "v_mfma_f32_16x16x32_bf16 v[176:179], a[68:71], v[104:107], v[176:179]\n"
    "buffer_load_dwordx4 v218, s[40:43], s65 offen lds\n"
    "v_mfma_f32_16x16x32_bf16 v[180:183], a[64:67], v[100:103], v[180:183]\n"
    "ds_read_b64_tr_b16 a[92:93], v224 offset:15616\n"
    "v_mfma_f32_16x16x32_bf16 v[180:183], a[68:71], v[108:111], v[180:183]\n"
    "s_waitcnt lgkmcnt(7)\n"
    "ds_read_b64_tr_b16 a[94:95], v224 offset:19456\n"
    "v_mfma_f32_16x16x32_bf16 v[184:187], a[72:75], v[96:99], v[184:187]\n"
    "ds_read_b64_tr_b16 a[64:65], v225 offset:0\n"
    "v_mfma_f32_16x16x32_bf16 v[184:187], a[76:79], v[104:107], v[184:187]\n"
    "ds_read_b64_tr_b16 a[66:67], v225 offset:3840\n"
    "v_mfma_f32_16x16x32_bf16 v[188:191], a[72:75], v[100:103], v[188:191]\n"
    "s_add_u32 m0, s60, 3072\n"
    "ds_read_b64_tr_b16 a[68:69], v225 offset:15360\n"
    "ds_read_b64_tr_b16 a[70:71], v225 offset:19200\n"
    "v_mfma_f32_16x16x32_bf16 v[188:191], a[76:79], v[108:111], v[188:191]\n"
    "s_waitcnt lgkmcnt(8)\n"
    "buffer_load_dwordx4 v219, s[40:43], s65 offen lds\n"
    "v_mfma_f32_16x16x32_bf16 v[192:195], a[80:83], v[96:99], v[192:195]\n"
    "ds_read_b64_tr_b16 a[72:73], v224 offset:288\n"
    "v_mfma_f32_16x16x32_bf16 v[192:195], a[84:87], v[104:107], v[192:195]\n"
    "ds_read_b64_tr_b16 a[74:75], v224 offset:4128\n"
    "v_mfma_f32_16x16x32_bf16 v[196:199], a[80:83], v[100:103], v[196:199]\n"
    "ds_read_b64_tr_b16 a[76:77], v224 offset:15648\n"
    "v_mfma_f32_16x16x32_bf16 v[196:199], a[84:87], v[108:111], v[196:199]\n"
    "s_waitcnt lgkmcnt(7)\n"
    "ds_read_b64_tr_b16 a[78:79], v224 offset:19488\n"
    "v_mfma_f32_16x16x32_bf16 v[200:203], a[88:91], v[96:99], v[200:203]\n"
    "ds_read_b64_tr_b16 a[80:81], v224 offset:320\n"
    "v_mfma_f32_16x16x32_bf16 v[200:203], a[92:95], v[104:107], v[200:203]\n"
    "ds_read_b64_tr_b16 a[82:83], v224 offset:4160\n"
    "v_mfma_f32_16x16x32_bf16 v[204:207], a[88:91], v[100:103], v[204:207]\n"
    "s_add_u32 m0, s60, 4096\n"
    "ds_read_b64_tr_b16 a[84:85], v224 offset:15680\n"
    "ds_read_b64_tr_b16 a[86:87], v224 offset:19520\n"
    "v_mfma_f32_16x16x32_bf16 v[204:207], a[92:95], v[108:111], v[204:207]\n"
    "s_waitcnt lgkmcnt(8)\n"
    "buffer_load_dwordx4 v220, s[40:43], s65 offen lds\n"
    "v_mfma_f32_16x16x32_bf16 v[208:211], a[64:67], v[96:99], v[208:211]\n"
    "ds_read_b64_tr_b16 a[88:89], v224 offset:352\n"
    "v_mfma_f32_16x16x32_bf16 v[208:211], a[68:71], v[104:107], v[208:211]\n"
    "ds_read_b64_tr_b16 a[90:91], v224 offset:4192\n"
    "v_mfma_f32_16x16x32_bf16 v[212:215], a[64:67], v[100:103], v[212:215]\n"
    "ds_read_b64_tr_b16 a[92:93], v224 offset:15712\n"
    "v_mfma_f32_16x16x32_bf16 v[212:215], a[68:71], v[108:111], v[212:215]\n"
    "s_waitcnt lgkmcnt(7)\n"
    "ds_read_b64_tr_b16 a[94:95], v224 offset:19552\n"
    "v_mfma_f32_16x16x32_bf16 a[0:3], v[96:99], a[72:75], a[0:3]\n"
    "s_add_u32 m0, s60, 5120\n"
    "ds_read_b64_tr_b16 a[64:65], v224 offset:384\n"
    "ds_read_b64_tr_b16 a[66:67], v224 offset:4224\n"
    "v_mfma_f32_16x16x32_bf16 a[0:3], v[104:107], a[76:79], a[0:3]\n"
    "buffer_load_dwordx4 v221, s[40:43], s65 offen lds\n"
    "v_mfma_f32_16x16x32_bf16 a[4:7], v[100:103], a[72:75], a[4:7]\n"
    "ds_read_b64_tr_b16 a[68:69], v224 offset:15744\n"
    "v_mfma_f32_16x16x32_bf16 a[4:7], v[108:111], a[76:79], a[4:7]\n"
    "s_waitcnt lgkmcnt(7)\n"
    "ds_read_b64_tr_b16 a[70:71], v224 offset:19584\n"
    "v_mfma_f32_16x16x32_bf16 a[8:11], v[96:99], a[80:83], a[8:11]\n"
    "ds_read_b64_tr_b16 a[72:73], v224 offset:416\n"
    "v_mfma_f32_16x16x32_bf16 a[8:11], v[104:107], a[84:87], a[8:11]\n"
    "ds_read_b64_tr_b16 a[74:75], v224 offset:4256\n"
    "v_mfma_f32_16x16x32_bf16 a[12:15], v[100:103], a[80:83], a[12:15]\n"
    "s_add_u32 m0, s60, 6144\n"
    "ds_read_b64_tr_b16 a[76:77], v224 offset:15776\n"
    "ds_read_b64_tr_b16 a[78:79], v224 offset:19616\n"
    "v_mfma_f32_16x16x32_bf16 a[12:15], v[108:111], a[84:87], a[12:15]\n"
    "s_waitcnt vmcnt(6) lgkmcnt(8)\n"
    "s_barrier\n"
    "buffer_load_dwordx4 v222, s[40:43], s65 offen lds\n"
    "v_mfma_f32_16x16x32_bf16 a[16:19], v[96:99], a[88:91], a[16:19]\n"
    "ds_read_b64_tr_b16 a[80:81], v226 offset:0\n"
    "v_mfma_f32_16x16x32_bf16 a[16:19], v[104:107], a[92:95], a[16:19]\n"
    "ds_read_b64_tr_b16 a[82:83], v226 offset:3840\n"
    "v_mfma_f32_16x16x32_bf16 a[20:23], v[100:103], a[88:91], a[20:23]\n"
    "ds_read_b64_tr_b16 a[84:85], v226 offset:15360\n"
    "v_mfma_f32_16x16x32_bf16 a[20:23], v[108:111], a[92:95], a[20:23]\n"
    "s_waitcnt lgkmcnt(7)\n"
    "ds_read_b64_tr_b16 a[86:87], v226 offset:19200\n"
    "v_mfma_f32_16x16x32_bf16 a[24:27], v[96:99], a[64:67], a[24:27]\n"
    "ds_read_b64_tr_b16 a[88:89], v226 offset:40\n"
    "v_mfma_f32_16x16x32_bf16 a[24:27], v[104:107], a[68:71], a[24:27]\n"
    "ds_read_b64_tr_b16 a[90:91], v226 offset:3880\n"
    "v_mfma_f32_16x16x32_bf16 a[28:31], v[100:103], a[64:67], a[28:31]\n"
    "s_add_u32 m0, s60, 7168\n"
    "ds_read_b64_tr_b16 a[92:93], v226 offset:15400\n"
    "ds_read_b64_tr_b16 a[94:95], v226 offset:19240\n"
    "v_mfma_f32_16x16x32_bf16 a[28:31], v[108:111], a[68:71], a[28:31]\n"
    "s_waitcnt lgkmcnt(8)\n"
    "buffer_load_dwordx4 v223, s[40:43], s65 offen lds\n"
    "v_mfma_f32_16x16x32_bf16 a[32:35], v[96:99], a[72:75], a[32:35]\n"
    "ds_read_b64_tr_b16 a[64:65], v226 offset:72\n"
    "v_mfma_f32_16x16x32_bf16 a[32:35], v[104:107], a[76:79], a[32:35]\n"
    "ds_read_b64_tr_b16 a[66:67], v226 offset:3912\n"
    "v_mfma_f32_16x16x32_bf16 a[36:39], v[100:103], a[72:75], a[36:39]\n"
    "ds_read_b64_tr_b16 a[68:69], v226 offset:15432\n"
    "v_mfma_f32_16x16x32_bf16 a[36:39], v[108:111], a[76:79], a[36:39]\n"
    "s_waitcnt lgkmcnt(7)\n"
    "ds_read_b64_tr_b16 a[70:71], v226 offset:19272\n"
    "v_mfma_f32_16x16x32_bf16 v[144:147], a[80:83], v[112:115], v[144:147]\n"
    "ds_read_b64_tr_b16 a[72:73], v226 offset:112\n"
    "v_add_u32 v254, 0, v255\n"
    "v_mfma_f32_16x16x32_bf16 v[144:147], a[84:87], v[120:123], v[144:147]\n"
    "ds_read_b64_tr_b16 a[74:75], v226 offset:3952\n"
    "v_mfma_f32_16x16x32_bf16 v[148:151], a[80:83], v[116:119], v[148:151]\n"
    "ds_read_b64_tr_b16 a[76:77], v226 offset:15472\n"
    "v_mul_u32_u24 v241, 0xcd, v254\n"
    "v_mfma_f32_16x16x32_bf16 v[148:151], a[84:87], v[124:127], v[148:151]\n"
    "s_waitcnt lgkmcnt(7)\n"
    "ds_read_b64_tr_b16 a[78:79], v226 offset:19312\n"
    "v_mfma_f32_16x16x32_bf16 v[152:155], a[88:91], v[112:115], v[152:155]\n"
    "ds_read_b64_tr_b16 a[80:81], v226 offset:144\n"
    "v_add_u32 v224, 0xfffe8000, v226\n"
    "v_mfma_f32_16x16x32_bf16 v[152:155], a[92:95], v[120:123], v[152:155]\n"
    "ds_read_b64_tr_b16 a[82:83], v226 offset:3984\n"
    "v_add_u32 v225, 0xfffe8000, v227\n"
    "v_mfma_f32_16x16x32_bf16 v[156:159], a[88:91], v[116:119], v[156:159]\n"
    "ds_read_b64_tr_b16 a[84:85], v226 offset:15504\n"
    "v_lshrrev_b32 v241, 14, v241\n"
    "v_mfma_f32_16x16x32_bf16 v[156:159], a[92:95], v[124:127], v[156:159]\n"
    "s_waitcnt lgkmcnt(7)\n"
    "ds_read_b64_tr_b16 a[86:87], v226 offset:19344\n"
    "v_mfma_f32_16x16x32_bf16 v[160:163], a[64:67], v[112:115], v[160:163]\n"
    "ds_read_b64_tr_b16 a[88:89], v226 offset:184\n"
    "v_mul_u32_u24 v241, 0x25b00, v241\n"
    "v_mfma_f32_16x16x32_bf16 v[160:163], a[68:71], v[120:123], v[160:163]\n"
    "ds_read_b64_tr_b16 a[90:91], v226 offset:4024\n"
    "v_mfma_f32_16x16x32_bf16 v[164:167], a[64:67], v[116:119], v[164:167]\n"
    "ds_read_b64_tr_b16 a[92:93], v226 offset:15544\n"
    "v_lshl_add_u32 v241, v254, 4, v241\n"
    "v_mfma_f32_16x16x32_bf16 v[164:167], a[68:71], v[124:127], v[164:167]\n"
    "s_waitcnt lgkmcnt(7)\n"
    "ds_read_b64_tr_b16 a[94:95], v226 offset:19384\n"
    "v_mfma_f32_16x16x32_bf16 v[168:171], a[72:75], v[112:115], v[168:171]\n"
    "ds_read_b64_tr_b16 a[64:65], v226 offset:216\n"
    "v_add_u32 v216, 0x400, v241\n"
    "v_mfma_f32_16x16x32_bf16 v[168:171], a[76:79], v[120:123], v[168:171]\n"
    "ds_read_b64_tr_b16 a[66:67], v226 offset:4056\n"
    "v_mfma_f32_16x16x32_bf16 v[172:175], a[72:75], v[116:119], v[172:175]\n"
    "ds_read_b64_tr_b16 a[68:69], v226 offset:15576\n"
    "v_add_u32 v254, 64, v255\n"
    "v_mfma_f32_16x16x32_bf16 v[172:175], a[76:79], v[124:127], v[172:175]\n"
    "s_waitcnt lgkmcnt(7)\n"
    "ds_read_b64_tr_b16 a[70:71], v226 offset:19416\n"
    "v_mfma_f32_16x16x32_bf16 v[176:179], a[80:83], v[112:115], v[176:179]\n"
    "ds_read_b64_tr_b16 a[72:73], v226 offset:256\n"
    "v_mul_u32_u24 v241, 0xcd, v254\n"
    "v_mfma_f32_16x16x32_bf16 v[176:179], a[84:87], v[120:123], v[176:179]\n"
    "ds_read_b64_tr_b16 a[74:75], v226 offset:4096\n"
    "v_mfma_f32_16x16x32_bf16 v[180:183], a[80:83], v[116:119], v[180:183]\n"
    "ds_read_b64_tr_b16 a[76:77], v226 offset:15616\n"
    "v_lshrrev_b32 v241, 14, v241\n"
    "v_mfma_f32_16x16x32_bf16 v[180:183], a[84:87], v[124:127], v[180:183]\n"
    "s_waitcnt lgkmcnt(7)\n"
    "ds_read_b64_tr_b16 a[78:79], v226 offset:19456\n"
    "v_mfma_f32_16x16x32_bf16 v[184:187], a[88:91], v[112:115], v[184:187]\n"
    "ds_read_b64_tr_b16 a[80:81], v227 offset:0\n"
    "v_mul_u32_u24 v241, 0x25b00, v241\n"
    "v_mfma_f32_16x16x32_bf16 v[184:187], a[92:95], v[120:123], v[184:187]\n"
    "ds_read_b64_tr_b16 a[82:83], v227 offset:3840\n"
    "v_mfma_f32_16x16x32_bf16 v[188:191], a[88:91], v[116:119], v[188:191]\n"
    "ds_read_b64_tr_b16 a[84:85], v227 offset:15360\n"
    "v_lshl_add_u32 v241, v254, 4, v241\n"
    "v_mfma_f32_16x16x32_bf16 v[188:191], a[92:95], v[124:127], v[188:191]\n"
    "s_waitcnt lgkmcnt(7)\n"
    "ds_read_b64_tr_b16 a[86:87], v227 offset:19200\n"
    "v_mfma_f32_16x16x32_bf16 v[192:195], a[64:67], v[112:115], v[192:195]\n"
    "ds_read_b64_tr_b16 a[88:89], v226 offset:288\n"
    "v_add_u32 v217, 0x400, v241\n"
    "v_mfma_f32_16x16x32_bf16 v[192:195], a[68:71], v[120:123], v[192:195]\n"
    "ds_read_b64_tr_b16 a[90:91], v226 offset:4128\n"
    "v_mfma_f32_16x16x32_bf16 v[196:199], a[64:67], v[116:119], v[196:199]\n"
    "ds_read_b64_tr_b16 a[92:93], v226 offset:15648\n"
    "v_add_u32 v254, 128, v255\n"
    "v_mfma_f32_16x16x32_bf16 v[196:199], a[68:71], v[124:127], v[196:199]\n"
    "s_waitcnt lgkmcnt(7)\n"
    "ds_read_b64_tr_b16 a[94:95], v226 offset:19488\n"
    "v_mfma_f32_16x16x32_bf16 v[200:203], a[72:75], v[112:115], v[200:203]\n"
    "ds_read_b64_tr_b16 a[64:65], v226 offset:320\n"
    "v_mul_u32_u24 v241, 0xcd, v254\n"
    "v_mfma_f32_16x16x32_bf16 v[200:203], a[76:79], v[120:123], v[200:203]\n"
    "ds_read_b64_tr_b16 a[66:67], v226 offset:4160\n"
    "v_mfma_f32_16x16x32_bf16 v[204:207], a[72:75], v[116:119], v[204:207]\n"
    "ds_read_b64_tr_b16 a[68:69], v226 offset:15680\n"
    "v_lshrrev_b32 v241, 14, v241\n"
    "v_mfma_f32_16x16x32_bf16 v[204:207], a[76:79], v[124:127], v[204:207]\n"
    "s_waitcnt lgkmcnt(7)\n"
    "ds_read_b64_tr_b16 a[70:71], v226 offset:19520\n"
    "v_mfma_f32_16x16x32_bf16 v[208:211], a[80:83], v[112:115], v[208:211]\n"
    "ds_read_b64_tr_b16 a[72:73], v226 offset:352\n"
    "v_mul_u32_u24 v241, 0x25b00, v241\n"
    "v_mfma_f32_16x16x32_bf16 v[208:211], a[84:87], v[120:123], v[208:211]\n"
    "ds_read_b64_tr_b16 a[74:75], v226 offset:4192\n"
    "v_mfma_f32_16x16x32_bf16 v[212:215], a[80:83], v[116:119], v[212:215]\n"
    "ds_read_b64_tr_b16 a[76:77], v226 offset:15712\n"
    "v_lshl_add_u32 v241, v254, 4, v241\n"
    "v_mfma_f32_16x16x32_bf16 v[212:215], a[84:87], v[124:127], v[212:215]\n"
    "s_waitcnt lgkmcnt(7)\n"
    "ds_read_b64_tr_b16 a[78:79], v226 offset:19552\n"
    "v_mfma_f32_16x16x32_bf16 a[0:3], v[112:115], a[88:91], a[0:3]\n"
    "ds_read_b64_tr_b16 a[80:81], v226 offset:384\n"
    "v_add_u32 v218, 0x400, v241\n"
    "v_mfma_f32_16x16x32_bf16 a[0:3], v[120:123], a[92:95], a[0:3]\n"
    "ds_read_b64_tr_b16 a[82:83], v226 offset:4224\n"
    "v_mfma_f32_16x16x32_bf16 a[4:7], v[116:119], a[88:91], a[4:7]\n"
    "ds_read_b64_tr_b16 a[84:85], v226 offset:15744\n"
    "v_add_u32 v254, 192, v255\n"
    "v_mfma_f32_16x16x32_bf16 a[4:7], v[124:127], a[92:95], a[4:7]\n"
    "s_waitcnt lgkmcnt(7)\n"
    "ds_read_b64_tr_b16 a[86:87], v226 offset:19584\n"
    "v_mfma_f32_16x16x32_bf16 a[8:11], v[112:115], a[64:67], a[8:11]\n"
    "ds_read_b64_tr_b16 a[88:89], v226 offset:416\n"
    "v_mul_u32_u24 v241, 0xcd, v254\n"
    "v_mfma_f32_16x16x32_bf16 a[8:11], v[120:123], a[68:71], a[8:11]\n"
    "ds_read_b64_tr_b16 a[90:91], v226 offset:4256\n"
    "v_mfma_f32_16x16x32_bf16 a[12:15], v[116:119], a[64:67], a[12:15]\n"
    "ds_read_b64_tr_b16 a[92:93], v226 offset:15776\n"
    "v_lshrrev_b32 v241, 14, v241\n"
    "v_mfma_f32_16x16x32_bf16 a[12:15], v[124:127], a[68:71], a[12:15]\n"
    "s_waitcnt vmcnt(0) lgkmcnt(7)\n"
    "s_barrier\n"
    "ds_read_b64_tr_b16 a[94:95], v226 offset:19616\n"
    "v_mfma_f32_16x16x32_bf16 a[16:19], v[112:115], a[72:75], a[16:19]\n"
    "ds_read_b64_tr_b16 a[64:65], v224 offset:0\n"
    "v_mul_u32_u24 v241, 0x25b00, v241\n"
    "v_mfma_f32_16x16x32_bf16 a[16:19], v[120:123], a[76:79], a[16:19]\n"
    "ds_read_b64_tr_b16 a[66:67], v224 offset:3840\n"
    "v_mfma_f32_16x16x32_bf16 a[20:23], v[116:119], a[72:75], a[20:23]\n"
    "ds_read_b64_tr_b16 a[68:69], v224 offset:15360\n"
    "v_lshl_add_u32 v241, v254, 4, v241\n"
    "v_mfma_f32_16x16x32_bf16 a[20:23], v[124:127], a[76:79], a[20:23]\n"
    "s_waitcnt lgkmcnt(7)\n"
    "ds_read_b64_tr_b16 a[70:71], v224 offset:19200\n"
    "v_mfma_f32_16x16x32_bf16 a[24:27], v[112:115], a[80:83], a[24:27]\n"
    "ds_read_b64_tr_b16 a[72:73], v224 offset:40\n"
    "v_add_u32 v219, 0x400, v241\n"
    "v_mfma_f32_16x16x32_bf16 a[24:27], v[120:123], a[84:87], a[24:27]\n"
    "ds_read_b64_tr_b16 a[74:75], v224 offset:3880\n"
    "v_mfma_f32_16x16x32_bf16 a[28:31], v[116:119], a[80:83], a[28:31]\n"
    "ds_read_b64_tr_b16 a[76:77], v224 offset:15400\n"
    "v_add_u32 v254, 256, v255\n"
    "v_mfma_f32_16x16x32_bf16 a[28:31], v[124:127], a[84:87], a[28:31]\n"
    "s_waitcnt lgkmcnt(7)\n"
    "ds_read_b64_tr_b16 a[78:79], v224 offset:19240\n"
    "v_mfma_f32_16x16x32_bf16 a[32:35], v[112:115], a[88:91], a[32:35]\n"
    "ds_read_b64_tr_b16 a[80:81], v224 offset:72\n"
    "v_mul_u32_u24 v241, 0xcd, v254\n"
    "v_mfma_f32_16x16x32_bf16 a[32:35], v[120:123], a[92:95], a[32:35]\n"
    "ds_read_b64_tr_b16 a[82:83], v224 offset:3912\n"
    "v_mfma_f32_16x16x32_bf16 a[36:39], v[116:119], a[88:91], a[36:39]\n"
    "ds_read_b64_tr_b16 a[84:85], v224 offset:15432\n"
    "v_lshrrev_b32 v241, 14, v241\n"
    "v_mfma_f32_16x16x32_bf16 a[36:39], v[124:127], a[92:95], a[36:39]\n"
    "s_waitcnt lgkmcnt(7)\n"
    "ds_read_b64_tr_b16 a[86:87], v224 offset:19272\n"
    "v_mfma_f32_16x16x32_bf16 v[144:147], a[64:67], v[128:131], v[144:147]\n"
    "ds_read_b64_tr_b16 a[88:89], v224 offset:112\n"
    "v_mul_u32_u24 v241, 0x25b00, v241\n"
    "v_mfma_f32_16x16x32_bf16 v[144:147], a[68:71], v[136:139], v[144:147]\n"
    "ds_read_b64_tr_b16 a[90:91], v224 offset:3952\n"
    "v_mfma_f32_16x16x32_bf16 v[148:151], a[64:67], v[132:135], v[148:151]\n"
    "ds_read_b64_tr_b16 a[92:93], v224 offset:15472\n"
    "v_lshl_add_u32 v241, v254, 4, v241\n"
    "v_mfma_f32_16x16x32_bf16 v[148:151], a[68:71], v[140:143], v[148:151]\n"
    "s_waitcnt lgkmcnt(7)\n"
    "ds_read_b64_tr_b16 a[94:95], v224 offset:19312\n"
    "v_mfma_f32_16x16x32_bf16 v[152:155], a[72:75], v[128:131], v[152:155]\n"
    "ds_read_b64_tr_b16 a[64:65], v224 offset:144\n"
    "v_add_u32 v220, 0x400, v241\n"
    "v_mfma_f32_16x16x32_bf16 v[152:155], a[76:79], v[136:139], v[152:155]\n"
    "ds_read_b64_tr_b16 a[66:67], v224 offset:3984\n"
    "v_mfma_f32_16x16x32_bf16 v[156:159], a[72:75], v[132:135], v[156:159]\n"
    "ds_read_b64_tr_b16 a[68:69], v224 offset:15504\n"
    "v_add_u32 v221, s53, v239\n"
    "v_mfma_f32_16x16x32_bf16 v[156:159], a[76:79], v[140:143], v[156:159]\n"
    "s_waitcnt lgkmcnt(7)\n"
    "ds_read_b64_tr_b16 a[70:71], v224 offset:19344\n"
    "v_mfma_f32_16x16x32_bf16 v[160:163], a[80:83], v[128:131], v[160:163]\n"
    "ds_read_b64_tr_b16 a[72:73], v224 offset:184\n"
    "v_add_u32 v241, s61, v239\n"
    "v_mfma_f32_16x16x32_bf16 v[160:163], a[84:87], v[136:139], v[160:163]\n"
    "ds_read_b64_tr_b16 a[74:75], v224 offset:4024\n"
    "v_mfma_f32_16x16x32_bf16 v[164:167], a[80:83], v[132:135], v[164:167]\n"
    "ds_read_b64_tr_b16 a[76:77], v224 offset:15544\n"
    "v_lshlrev_b32 v242, 2, v255\n"
    "v_mfma_f32_16x16x32_bf16 v[164:167], a[84:87], v[140:143], v[164:167]\n"
    "s_waitcnt lgkmcnt(7)\n"
    "ds_read_b64_tr_b16 a[78:79], v224 offset:19384\n"
    "v_mfma_f32_16x16x32_bf16 v[168:171], a[88:91], v[128:131], v[168:171]\n"
    "ds_read_b64_tr_b16 a[80:81], v224 offset:216\n"
    "v_add_u32 v242, s61, v242\n"
    "v_mfma_f32_16x16x32_bf16 v[168:171], a[92:95], v[136:139], v[168:171]\n"
    "ds_read_b64_tr_b16 a[82:83], v224 offset:4056\n"
    "v_mfma_f32_16x16x32_bf16 v[172:175], a[88:91], v[132:135], v[172:175]\n"
    "ds_read_b64_tr_b16 a[84:85], v224 offset:15576\n"
    "v_mfma_f32_16x16x32_bf16 v[172:175], a[92:95], v[140:143], v[172:175]\n"
    "s_waitcnt lgkmcnt(7)\n"
    "ds_read_b64_tr_b16 a[86:87], v224 offset:19416\n"
    "v_mfma_f32_16x16x32_bf16 v[176:179], a[64:67], v[128:131], v[176:179]\n"
    "ds_read_b64_tr_b16 a[88:89], v224 offset:256\n"
    "v_mfma_f32_16x16x32_bf16 v[176:179], a[68:71], v[136:139], v[176:179]\n"
    "ds_read_b64_tr_b16 a[90:91], v224 offset:4096\n"
    "v_mfma_f32_16x16x32_bf16 v[180:183], a[64:67], v[132:135], v[180:183]\n"
    "ds_read_b64_tr_b16 a[92:93], v224 offset:15616\n"
    "v_mfma_f32_16x16x32_bf16 v[180:183], a[68:71], v[140:143], v[180:183]\n"
    "s_waitcnt lgkmcnt(7)\n"
    "ds_read_b64_tr_b16 a[94:95], v224 offset:19456\n"
    "v_mfma_f32_16x16x32_bf16 v[184:187], a[72:75], v[128:131], v[184:187]\n"
    "ds_read_b64_tr_b16 a[64:65], v225 offset:0\n"
    "v_mfma_f32_16x16x32_bf16 v[184:187], a[76:79], v[136:139], v[184:187]\n"
    "ds_read_b64_tr_b16 a[66:67], v225 offset:3840\n"
    "v_mfma_f32_16x16x32_bf16 v[188:191], a[72:75], v[132:135], v[188:191]\n"
    "ds_read_b64_tr_b16 a[68:69], v225 offset:15360\n"
    "v_mfma_f32_16x16x32_bf16 v[188:191], a[76:79], v[140:143], v[188:191]\n"
    "s_waitcnt lgkmcnt(7)\n"
    "ds_read_b64_tr_b16 a[70:71], v225 offset:19200\n"
    "v_mfma_f32_16x16x32_bf16 v[192:195], a[80:83], v[128:131], v[192:195]\n"
    "ds_read_b64_tr_b16 a[72:73], v224 offset:288\n"
    "v_mfma_f32_16x16x32_bf16 v[192:195], a[84:87], v[136:139], v[192:195]\n"
    "ds_read_b64_tr_b16 a[74:75], v224 offset:4128\n"
    "v_mfma_f32_16x16x32_bf16 v[196:199], a[80:83], v[132:135], v[196:199]\n"
    "ds_read_b64_tr_b16 a[76:77], v224 offset:15648\n"
    "v_mfma_f32_16x16x32_bf16 v[196:199], a[84:87], v[140:143], v[196:199]\n"
    "s_waitcnt lgkmcnt(7)\n"
    "ds_read_b64_tr_b16 a[78:79], v224 offset:19488\n"
    "v_mfma_f32_16x16x32_bf16 v[200:203], a[88:91], v[128:131], v[200:203]\n"
    "ds_read_b64_tr_b16 a[80:81], v224 offset:320\n"
    "v_mfma_f32_16x16x32_bf16 v[200:203], a[92:95], v[136:139], v[200:203]\n"
    "ds_read_b64_tr_b16 a[82:83], v224 offset:4160\n"
    "v_mfma_f32_16x16x32_bf16 v[204:207], a[88:91], v[132:135], v[204:207]\n"
    "ds_read_b64_tr_b16 a[84:85], v224 offset:15680\n"
    "v_mfma_f32_16x16x32_bf16 v[204:207], a[92:95], v[140:143], v[204:207]\n"
    "s_waitcnt lgkmcnt(7)\n"
    "ds_read_b64_tr_b16 a[86:87], v224 offset:19520\n"
    "v_mfma_f32_16x16x32_bf16 v[208:211], a[64:67], v[128:131], v[208:211]\n"
    "ds_read_b64_tr_b16 a[88:89], v224 offset:352\n"
    "v_mfma_f32_16x16x32_bf16 v[208:211], a[68:71], v[136:139], v[208:211]\n"
    "ds_read_b64_tr_b16 a[90:91], v224 offset:4192\n"
    "v_mfma_f32_16x16x32_bf16 v[212:215], a[64:67], v[132:135], v[212:215]\n"
    "ds_read_b64_tr_b16 a[92:93], v224 offset:15712\n"
    "v_mfma_f32_16x16x32_bf16 v[212:215], a[68:71], v[140:143], v[212:215]\n"
    "s_waitcnt lgkmcnt(7)\n"
    "ds_read_b64_tr_b16 a[94:95], v224 offset:19552\n"
    "v_mfma_f32_16x16x32_bf16 a[0:3], v[128:131], a[72:75], a[0:3]\n"
    "ds_read_b64_tr_b16 a[64:65], v224 offset:384\n"
    "v_mfma_f32_16x16x32_bf16 a[0:3], v[136:139], a[76:79], a[0:3]\n"
    "ds_read_b64_tr_b16 a[66:67], v224 offset:4224\n"
    "v_mfma_f32_16x16x32_bf16 a[4:7], v[132:135], a[72:75], a[4:7]\n"
    "ds_read_b64_tr_b16 a[68:69], v224 offset:15744\n"
    "v_mfma_f32_16x16x32_bf16 a[4:7], v[140:143], a[76:79], a[4:7]\n"
    "s_waitcnt lgkmcnt(7)\n"
    "ds_read_b64_tr_b16 a[70:71], v224 offset:19584\n"
    "v_mfma_f32_16x16x32_bf16 a[8:11], v[128:131], a[80:83], a[8:11]\n"
    "ds_read_b64_tr_b16 a[72:73], v224 offset:416\n"
    "v_mfma_f32_16x16x32_bf16 a[8:11], v[136:139], a[84:87], a[8:11]\n"
    "ds_read_b64_tr_b16 a[74:75], v224 offset:4256\n"
    "v_mfma_f32_16x16x32_bf16 a[12:15], v[132:135], a[80:83], a[12:15]\n"
    "ds_read_b64_tr_b16 a[76:77], v224 offset:15776\n"
    "v_mfma_f32_16x16x32_bf16 a[12:15], v[140:143], a[84:87], a[12:15]\n"
    "s_waitcnt lgkmcnt(7)\n"
    "ds_read_b64_tr_b16 a[78:79], v224 offset:19616\n"
    "v_mfma_f32_16x16x32_bf16 a[16:19], v[128:131], a[88:91], a[16:19]\n"
    "v_mfma_f32_16x16x32_bf16 a[16:19], v[136:139], a[92:95], a[16:19]\n"
    "v_mfma_f32_16x16x32_bf16 a[20:23], v[132:135], a[88:91], a[20:23]\n"
    "v_mfma_f32_16x16x32_bf16 a[20:23], v[140:143], a[92:95], a[20:23]\n"
    "s_waitcnt lgkmcnt(4)\n"
    "v_mfma_f32_16x16x32_bf16 a[24:27], v[128:131], a[64:67], a[24:27]\n"
    "v_mfma_f32_16x16x32_bf16 a[24:27], v[136:139], a[68:71], a[24:27]\n"
    "v_mfma_f32_16x16x32_bf16 a[28:31], v[132:135], a[64:67], a[28:31]\n"
    "v_mfma_f32_16x16x32_bf16 a[28:31], v[140:143], a[68:71], a[28:31]\n"
    "s_waitcnt lgkmcnt(0)\n"
    "v_mfma_f32_16x16x32_bf16 a[32:35], v[128:131], a[72:75], a[32:35]\n"
    "v_mfma_f32_16x16x32_bf16 a[32:35], v[136:139], a[76:79], a[32:35]\n"
    "v_mfma_f32_16x16x32_bf16 a[36:39], v[132:135], a[72:75], a[36:39]\n"
    "v_mfma_f32_16x16x32_bf16 a[36:39], v[140:143], a[76:79], a[36:39]\n"
    "s_mov_b32 exec_hi, 0\n"
    "ds_write_b128 v251, v[232:235]\n"
    "s_mov_b32 exec_hi, -1\n"
    "v_accvgpr_read_b32 v48, a0\n"
    "v_accvgpr_read_b32 v49, a1\n"
    "v_accvgpr_read_b32 v50, a2\n"
    "v_accvgpr_read_b32 v51, a3\n"
    "v_accvgpr_read_b32 v52, a4\n"
    "v_accvgpr_read_b32 v53, a5\n"
    "v_accvgpr_read_b32 v54, a6\n"
    "v_accvgpr_read_b32 v55, a7\n"
    "v_cvt_pk_bf16_f32 v64, v48, v49\n"
    "v_cvt_pk_bf16_f32 v65, v50, v51\n"
    "v_cvt_pk_bf16_f32 v66, v52, v53\n"
    "v_cvt_pk_bf16_f32 v67, v54, v55\n"
    "v_accvgpr_read_b32 v56, a8\n"
    "v_accvgpr_read_b32 v57, a9\n"
    "v_accvgpr_read_b32 v58, a10\n"
    "v_accvgpr_read_b32 v59, a11\n"
    "v_accvgpr_read_b32 v60, a12\n"
    "v_accvgpr_read_b32 v61, a13\n"
    "v_accvgpr_read_b32 v62, a14\n"
    "v_accvgpr_read_b32 v63, a15\n"
    "v_cvt_pk_bf16_f32 v68, v56, v57\n"
    "v_cvt_pk_bf16_f32 v69, v58, v59\n"
    "v_cvt_pk_bf16_f32 v70, v60, v61\n"
    "v_cvt_pk_bf16_f32 v71, v62, v63\n"
    "v_permlane32_swap_b32 v64, v66\n"
    "v_permlane32_swap_b32 v65, v67\n"
    "ds_write_b128 v246, v[64:67]\n"
    "v_accvgpr_read_b32 v48, a16\n"
    "v_accvgpr_read_b32 v49, a17\n"
    "v_accvgpr_read_b32 v50, a18\n"
    "v_accvgpr_read_b32 v51, a19\n"
    "v_accvgpr_read_b32 v52, a20\n"
    "v_accvgpr_read_b32 v53, a21\n"
    "v_accvgpr_read_b32 v54, a22\n"
    "v_accvgpr_read_b32 v55, a23\n"
    "v_cvt_pk_bf16_f32 v72, v48, v49\n"
    "v_cvt_pk_bf16_f32 v73, v50, v51\n"
    "v_cvt_pk_bf16_f32 v74, v52, v53\n"
    "v_cvt_pk_bf16_f32 v75, v54, v55\n"
    "v_permlane32_swap_b32 v68, v70\n"
    "v_permlane32_swap_b32 v69, v71\n"
    "ds_write_b128 v247, v[68:71]\n"
    "v_accvgpr_read_b32 v56, a24\n"
    "v_accvgpr_read_b32 v57, a25\n"
    "v_accvgpr_read_b32 v58, a26\n"
    "v_accvgpr_read_b32 v59, a27\n"
    "v_accvgpr_read_b32 v60, a28\n"
    "v_accvgpr_read_b32 v61, a29\n"
    "v_accvgpr_read_b32 v62, a30\n"
    "v_accvgpr_read_b32 v63, a31\n"
    "v_cvt_pk_bf16_f32 v76, v56, v57\n"
    "v_cvt_pk_bf16_f32 v77, v58, v59\n"
    "v_cvt_pk_bf16_f32 v78, v60, v61\n"
    "v_cvt_pk_bf16_f32 v79, v62, v63\n"
    "v_permlane32_swap_b32 v72, v74\n"
    "v_permlane32_swap_b32 v73, v75\n"
    "ds_write_b128 v248, v[72:75]\n"
    "v_accvgpr_read_b32 v48, a32\n"
    "v_accvgpr_read_b32 v49, a33\n"
    "v_accvgpr_read_b32 v50, a34\n"
    "v_accvgpr_read_b32 v51, a35\n"
    "v_accvgpr_read_b32 v52, a36\n"
    "v_accvgpr_read_b32 v53, a37\n"
    "v_accvgpr_read_b32 v54, a38\n"
    "v_accvgpr_read_b32 v55, a39\n"
    "v_cvt_pk_bf16_f32 v80, v48, v49\n"
    "v_cvt_pk_bf16_f32 v81, v50, v51\n"
    "v_cvt_pk_bf16_f32 v82, v52, v53\n"
    "v_cvt_pk_bf16_f32 v83, v54, v55\n"
    "v_permlane32_swap_b32 v76, v78\n"
    "v_permlane32_swap_b32 v77, v79\n"
    "ds_write_b128 v249, v[76:79]\n"
    "v_mul_f32 v208, v252, v208\n"
    "v_mul_f32 v209, v252, v209\n"
    "v_mul_f32 v210, v252, v210\n"
    "v_mul_f32 v211, v252, v211\n"
    "v_mul_f32 v212, v253, v212\n"
    "v_mul_f32 v213, v253, v213\n"
    "v_mul_f32 v214, v253, v214\n"
    "v_mul_f32 v215, v253, v215\n"
    "v_cvt_pk_bf16_f32 v32, v208, v209\n"
    "v_cvt_pk_bf16_f32 v33, v210, v211\n"
    "v_cvt_pk_bf16_f32 v34, v212, v213\n"
    "v_cvt_pk_bf16_f32 v35, v214, v215\n"
    "v_permlane32_swap_b32 v80, v82\n"
    "v_permlane32_swap_b32 v81, v83\n"
    "s_mov_b32 exec_lo, 0x00ff00ff\n"
    "s_mov_b32 exec_hi, 0x00ff00ff\n"
    "ds_write_b128 v250, v[80:83]\n"
    "s_mov_b64 exec, -1\n"
    "s_waitcnt lgkmcnt(0)\n"
    "ds_read_b128 v[64:67], v221 offset:0\n"
    "ds_read_b128 v[68:71], v221 offset:1024\n"
    "ds_read_b128 v[72:75], v221 offset:2048\n"
    "ds_read_b128 v[76:79], v221 offset:3072\n"
    "ds_read_b128 v[80:83], v221 offset:4096\n"
    "v_mul_f32 v176, v228, v176\n"
    "v_mul_f32 v177, v228, v177\n"
    "v_mul_f32 v178, v228, v178\n"
    "v_mul_f32 v179, v228, v179\n"
    "v_mul_f32 v180, v229, v180\n"
    "v_mul_f32 v181, v229, v181\n"
    "v_mul_f32 v182, v229, v182\n"
    "v_mul_f32 v183, v229, v183\n"
    "v_cvt_pk_bf16_f32 v16, v176, v177\n"
    "v_cvt_pk_bf16_f32 v17, v178, v179\n"
    "v_cvt_pk_bf16_f32 v18, v180, v181\n"
    "v_cvt_pk_bf16_f32 v19, v182, v183\n"
    "v_mul_f32 v184, v228, v184\n"
    "v_mul_f32 v185, v228, v185\n"
    "v_mul_f32 v186, v228, v186\n"
    "v_mul_f32 v187, v228, v187\n"
    "v_mul_f32 v188, v229, v188\n"
    "v_mul_f32 v189, v229, v189\n"
    "v_mul_f32 v190, v229, v190\n"
    "v_mul_f32 v191, v229, v191\n"
    "v_cvt_pk_bf16_f32 v20, v184, v185\n"
    "v_cvt_pk_bf16_f32 v21, v186, v187\n"
    "v_cvt_pk_bf16_f32 v22, v188, v189\n"
    "v_cvt_pk_bf16_f32 v23, v190, v191\n"
    "v_mul_f32 v192, v228, v192\n"
    "v_mul_f32 v193, v228, v193\n"
    "v_mul_f32 v194, v228, v194\n"
    "v_mul_f32 v195, v228, v195\n"
    "v_mul_f32 v196, v229, v196\n"
    "v_mul_f32 v197, v229, v197\n"
    "v_mul_f32 v198, v229, v198\n"
    "v_mul_f32 v199, v229, v199\n"
    "v_cvt_pk_bf16_f32 v24, v192, v193\n"
    "v_cvt_pk_bf16_f32 v25, v194, v195\n"
    "v_cvt_pk_bf16_f32 v26, v196, v197\n"
    "v_cvt_pk_bf16_f32 v27, v198, v199\n"
    "v_mul_f32 v200, v228, v200\n"
    "v_mul_f32 v201, v228, v201\n"
    "v_mul_f32 v202, v228, v202\n"
    "v_mul_f32 v203, v228, v203\n"
    "v_mul_f32 v204, v229, v204\n"
    "v_mul_f32 v205, v229, v205\n"
    "v_mul_f32 v206, v229, v206\n"
    "v_mul_f32 v207, v229, v207\n"
    "v_cvt_pk_bf16_f32 v28, v200, v201\n"
    "v_cvt_pk_bf16_f32 v29, v202, v203\n"
    "v_cvt_pk_bf16_f32 v30, v204, v205\n"
    "v_cvt_pk_bf16_f32 v31, v206, v207\n"
    "s_nop 0\n"
    "v_permlane16_swap_b32 v16, v18\n"
    "v_permlane16_swap_b32 v17, v19\n"
    "s_mov_b32 s68, 0\n"
    "buffer_store_dwordx4 v[16:19], v239, s[44:47], s68 offen sc1\n"
    "v_permlane16_swap_b32 v20, v22\n"
    "v_permlane16_swap_b32 v21, v23\n"
    "s_mov_b32 s68, 155648\n"
    "buffer_store_dwordx4 v[20:23], v239, s[44:47], s68 offen sc1\n"
    "v_permlane16_swap_b32 v24, v26\n"
    "v_permlane16_swap_b32 v25, v27\n"
    "s_mov_b32 s68, 311296\n"
    "buffer_store_dwordx4 v[24:27], v239, s[44:47], s68 offen sc1\n"
    "v_permlane16_swap_b32 v28, v30\n"
    "v_permlane16_swap_b32 v29, v31\n"
    "s_mov_b32 s68, 466944\n"
    "buffer_store_dwordx4 v[28:31], v239, s[44:47], s68 offen sc1\n"
    "s_waitcnt lgkmcnt(0)\n"
    "buffer_store_dwordx4 v[64:67], v216, s[44:47], 0 offen sc1\n"
    "buffer_store_dwordx4 v[68:71], v217, s[44:47], 0 offen sc1\n"
    "buffer_store_dwordx4 v[72:75], v218, s[44:47], 0 offen sc1\n"
    "buffer_store_dwordx4 v[76:79], v219, s[44:47], 0 offen sc1\n"
    "buffer_store_dwordx4 v[80:83], v220, s[44:47], 0 offen sc1\n"
    "v_permlane16_swap_b32 v32, v34\n"
    "v_permlane16_swap_b32 v33, v35\n"
    "s_mov_b32 exec_lo, 0\n"
    "s_mov_b32 s68, 2304\n"
    "buffer_store_dword v32, v240, s[44:47], s68 offen sc1\n"
    "s_mov_b32 s68, 157952\n"
    "buffer_store_dword v33, v240, s[44:47], s68 offen sc1\n"
    "s_mov_b32 s68, 313600\n"
    "buffer_store_dword v34, v240, s[44:47], s68 offen sc1\n"
    "s_mov_b32 s68, 469248\n"
    "buffer_store_dword v35, v240, s[44:47], s68 offen sc1\n"
    "s_mov_b32 exec_lo, -1\n"
    "ds_write_b32 v242, v32 offset:1024\n"
    "ds_write_b32 v242, v33 offset:6144\n"
    "ds_write_b32 v242, v34 offset:11264\n"
    "ds_write_b32 v242, v35 offset:16384\n"
    "v_mul_f32 v144, v230, v144\n"
    "v_mul_f32 v145, v230, v145\n"
    "v_mul_f32 v146, v230, v146\n"
    "v_mul_f32 v147, v230, v147\n"
    "v_mul_f32 v148, v231, v148\n"
    "v_mul_f32 v149, v231, v149\n"
    "v_mul_f32 v150, v231, v150\n"
    "v_mul_f32 v151, v231, v151\n"
    "v_cvt_pk_bf16_f32 v0, v144, v145\n"
    "v_cvt_pk_bf16_f32 v1, v146, v147\n"
    "v_cvt_pk_bf16_f32 v2, v148, v149\n"
    "v_cvt_pk_bf16_f32 v3, v150, v151\n"
    "v_mul_f32 v152, v230, v152\n"
    "v_mul_f32 v153, v230, v153\n"
    "v_mul_f32 v154, v230, v154\n"
    "v_mul_f32 v155, v230, v155\n"
    "v_mul_f32 v156, v231, v156\n"
    "v_mul_f32 v157, v231, v157\n"
    "v_mul_f32 v158, v231, v158\n"
    "v_mul_f32 v159, v231, v159\n"
    "v_cvt_pk_bf16_f32 v4, v152, v153\n"
    "v_cvt_pk_bf16_f32 v5, v154, v155\n"
    "v_cvt_pk_bf16_f32 v6, v156, v157\n"
    "v_cvt_pk_bf16_f32 v7, v158, v159\n"
    "v_mul_f32 v160, v230, v160\n"
    "v_mul_f32 v161, v230, v161\n"
    "v_mul_f32 v162, v230, v162\n"
    "v_mul_f32 v163, v230, v163\n"
    "v_mul_f32 v164, v231, v164\n"
    "v_mul_f32 v165, v231, v165\n"
    "v_mul_f32 v166, v231, v166\n"
    "v_mul_f32 v167, v231, v167\n"
    "v_cvt_pk_bf16_f32 v8, v160, v161\n"
    "v_cvt_pk_bf16_f32 v9, v162, v163\n"
    "v_cvt_pk_bf16_f32 v10, v164, v165\n"
    "v_cvt_pk_bf16_f32 v11, v166, v167\n"
    "v_mul_f32 v168, v230, v168\n"
    "v_mul_f32 v169, v230, v169\n"
    "v_mul_f32 v170, v230, v170\n"
    "v_mul_f32 v171, v230, v171\n"
    "v_mul_f32 v172, v231, v172\n"
    "v_mul_f32 v173, v231, v173\n"
    "v_mul_f32 v174, v231, v174\n"
    "v_mul_f32 v175, v231, v175\n"
    "v_cvt_pk_bf16_f32 v12, v168, v169\n"
    "v_cvt_pk_bf16_f32 v13, v170, v171\n"
    "v_cvt_pk_bf16_f32 v14, v172, v173\n"
    "v_cvt_pk_bf16_f32 v15, v174, v175\n"
    "s_nop 0\n"
    "v_permlane16_swap_b32 v0, v2\n"
    "v_permlane16_swap_b32 v1, v3\n"
    "ds_write_b128 v241, v[0:3] offset:0\n"
    "v_permlane16_swap_b32 v4, v6\n"
    "v_permlane16_swap_b32 v5, v7\n"
    "ds_write_b128 v241, v[4:7] offset:5120\n"
    "v_permlane16_swap_b32 v8, v10\n"
    "v_permlane16_swap_b32 v9, v11\n"
    "ds_write_b128 v241, v[8:11] offset:10240\n"
    "v_permlane16_swap_b32 v12, v14\n"
    "v_permlane16_swap_b32 v13, v15\n"
    "ds_write_b128 v241, v[12:15] offset:15360\n"
    "s_cmp_lg_u32 %7, 0\n"
    "s_cbranch_scc1 20f\n"
    "v_mov_b32 v36, v32\n"
    "v_mov_b32 v37, v32\n"
    "v_accvgpr_write_b32 a98, v16\n"
    "v_accvgpr_write_b32 a99, v17\n"
    "v_accvgpr_write_b32 a100, v18\n"
    "v_accvgpr_write_b32 a101, v19\n"
    "20:\n"
    "s_cmp_lg_u32 %7, 1\n"
    "s_cbranch_scc1 21f\n"
    "v_mov_b32 v36, v33\n"
    "v_mov_b32 v37, v33\n"
    "v_accvgpr_write_b32 a98, v20\n"
    "v_accvgpr_write_b32 a99, v21\n"
    "v_accvgpr_write_b32 a100, v22\n"
    "v_accvgpr_write_b32 a101, v23\n"
    "21:\n"
    "s_cmp_lg_u32 %7, 2\n"
    "s_cbranch_scc1 22f\n"
    "v_mov_b32 v36, v34\n"
    "v_mov_b32 v37, v34\n"
    "v_accvgpr_write_b32 a98, v24\n"
    "v_accvgpr_write_b32 a99, v25\n"
    "v_accvgpr_write_b32 a100, v26\n"
    "v_accvgpr_write_b32 a101, v27\n"
    "22:\n"
    "s_cmp_lg_u32 %7, 3\n"
    "s_cbranch_scc1 23f\n"
    "v_mov_b32 v36, v35\n"
    "v_mov_b32 v37, v35\n"
    "v_accvgpr_write_b32 a98, v28\n"
    "v_accvgpr_write_b32 a99, v29\n"
    "v_accvgpr_write_b32 a100, v30\n"
    "v_accvgpr_write_b32 a101, v31\n"
    "23:\n"
    "s_nop 1\n"
    "v_permlane32_swap_b32 v36, v37\n"
    "s_nop 1\n"
    "v_accvgpr_write_b32 a102, v37\n"
    "v_accvgpr_write_b32 a5, 0\n"
    "v_accvgpr_write_b32 a19, 0\n"
    "v_accvgpr_write_b32 a33, 0\n"
    "v_accvgpr_write_b32 a47, 0\n"
    "v_accvgpr_write_b32 a61, 0\n"
    "v_accvgpr_write_b32 a75, 0\n"
    "v_accvgpr_write_b32 a89, 0\n"
    "v_accvgpr_write_b32 a103, 0\n"
    "v_mov_b32 v88, 0\n"
    "v_mov_b32 v89, 0\n"
    "v_mov_b32 v90, 0\n"
    "v_mov_b32 v91, 0\n"
    "v_mov_b32 v92, 0\n"
    "v_mov_b32 v93, 0\n"
    "v_mov_b32 v94, 0\n"
    "v_mov_b32 v95, 0\n"
    "v_mov_b32 v96, 0\n"
    "v_mov_b32 v97, 0\n"
    "v_mov_b32 v98, 0\n"
    "v_mov_b32 v99, 0\n"
    "v_mov_b32 v100, 0\n"
    "v_mov_b32 v101, 0\n"
    "v_mov_b32 v102, 0\n"
    "v_mov_b32 v103, 0\n"
    "v_mov_b32 v104, 0\n"
    "v_mov_b32 v105, 0\n"
    "v_mov_b32 v106, 0\n"
    "v_mov_b32 v107, 0\n"
    "v_mov_b32 v108, 0\n"
    "v_mov_b32 v109, 0\n"
    "v_mov_b32 v110, 0\n"
    "v_mov_b32 v111, 0\n"
    "v_mov_b32 v112, 0\n"
    "v_mov_b32 v113, 0\n"
    "v_mov_b32 v114, 0\n"
    "v_mov_b32 v115, 0\n"
    "v_mov_b32 v116, 0\n"
    "v_mov_b32 v117, 0\n"
    "v_mov_b32 v118, 0\n"
    "v_mov_b32 v119, 0\n"
    "v_mov_b32 v120, 0\n"
    "v_mov_b32 v121, 0\n"
    "v_mov_b32 v122, 0\n"
    "v_mov_b32 v123, 0\n"
    "v_mov_b32 v124, 0\n"
    "v_mov_b32 v125, 0\n"
    "v_mov_b32 v126, 0\n"
    "v_mov_b32 v127, 0\n"
    "v_mov_b32 v128, 0\n"
    "v_mov_b32 v129, 0\n"
    "v_mov_b32 v130, 0\n"
    "v_mov_b32 v131, 0\n"
    "v_mov_b32 v132, 0\n"
    "v_mov_b32 v133, 0\n"
    "v_mov_b32 v134, 0\n"
    "v_mov_b32 v135, 0\n"
    "v_mov_b32 v136, 0\n"
    "v_mov_b32 v137, 0\n"
    "v_mov_b32 v138, 0\n"
    "v_mov_b32 v139, 0\n"
    "v_mov_b32 v140, 0\n"
    "v_mov_b32 v141, 0\n"
    "v_mov_b32 v142, 0\n"
    "v_mov_b32 v143, 0\n"
    "v_mov_b32 v144, 0\n"
    "v_mov_b32 v145, 0\n"
    "v_mov_b32 v146, 0\n"
    "v_mov_b32 v147, 0\n"
    "v_mov_b32 v148, 0\n"
    "v_mov_b32 v149, 0\n"
    "v_mov_b32 v150, 0\n"
    "v_mov_b32 v151, 0\n"
    "v_mov_b32 v192, 0\n"
    "v_accvgpr_write_b32 a104, 0\n"
    "v_mov_b32 v193, 0\n"
    "v_accvgpr_write_b32 a105, 0\n"
    "v_mov_b32 v194, 0\n"
    "v_accvgpr_write_b32 a106, 0\n"
    "v_mov_b32 v195, 0\n"
    "v_accvgpr_write_b32 a107, 0\n"
    "v_mov_b32 v196, 0\n"
    "v_accvgpr_write_b32 a108, 0\n"
    "v_mov_b32 v197, 0\n"
    "v_accvgpr_write_b32 a109, 0\n"
    "v_mov_b32 v198, 0\n"
    "v_accvgpr_write_b32 a110, 0\n"
    "v_mov_b32 v199, 0\n"
    "v_accvgpr_write_b32 a111, 0\n"
    "s_mul_i32 s67, %7, 165888\n"
    "s_lshl_b32 s58, s54, 6\n"
    "s_add_u32 s58, s58, s67\n"
    "s_lshl_b32 s59, s55, 6\n"
    "s_add_u32 s59, s59, s67\n"
    "s_lshl_b32 s60, s56, 6\n"
    "s_add_u32 s60, s60, s67\n"
    "s_mul_i32 s67, %7, 5120\n"
    "s_add_u32 s67, s67, 32768\n"
    "s_add_u32 s67, s67, %8\n"
    "v_add_u32 v241, s67, v239\n"
    "v_add_u32 v242, s67, v240\n"
    "s_mul_i32 s61, %7, 3072\n"
    "s_add_u32 s61, s61, %8\n"
    "s_lshl_b32 s67, %6, 2\n"
    "s_add_u32 s67, s67, %7\n"
    "s_mul_i32 s67, s67, 155648\n"
    "s_add_u32 s67, s67, 25165824\n"
    "s_mov_b64 s[44:45], %2\n"
    "s_add_u32 s44, s44, s67\n"
    "s_addc_u32 s45, s45, 0\n"
    "s_and_b32 s45, s45, 0xffff\n"
    "s_mov_b32 s46, 155648\n"
    "s_mov_b32 s47, 0x20000\n"
    "s_waitcnt vmcnt(0) lgkmcnt(0)\n"
    "s_barrier\n"
    "s_cmp_lg_u32 %7, 0\n"
    "s_cbranch_scc1 30f\n"
    "s_lshl_b32 s67, %5, 2\n"
    "v_mov_b32 v47, s79\n"
    "v_mov_b32 v46, s67\n"
    "s_mov_b64 exec, 1\n"
    "global_store_dword v46, v47, s[34:35] offset:128 sc1\n"
    "s_mov_b64 exec, -1\n"
    "30:\n"
    "ds_read_b128 v[0:3], v241 offset:0\n"
    "v_mov_b32 v16, 0\n"
    "v_mov_b32 v17, 0\n"
    "ds_read_b128 v[4:7], v241 offset:1280\n"
    "v_mov_b32 v18, 0\n"
    "v_mov_b32 v19, 0\n"
    "ds_read_b128 v[8:11], v241 offset:2560\n"
    "v_mov_b32 v20, 0\n"
    "v_mov_b32 v21, 0\n"
    "ds_read_b128 v[12:15], v241 offset:3840\n"
    "v_mov_b32 v22, 0\n"
    "v_mov_b32 v23, 0\n"
    "s_mov_b32 exec_hi, 0\n"
    "ds_read_b32 v16, v242 offset:1024\n"
    "ds_read_b32 v18, v242 offset:2304\n"
    "ds_read_b32 v20, v242 offset:3584\n"
    "ds_read_b32 v22, v242 offset:4864\n"
    "s_mov_b32 exec_hi, -1\n"
    "s_lshl_b32 s67, %6, 2\n"
    "s_add_u32 s67, s67, %7\n"
    "v_and_b32 v213, 15, v255\n"
    "v_lshlrev_b32 v213, 4, v213\n"
    "v_bfe_u32 v212, v255, 4, 1\n"
    "v_lshl_add_u32 v213, v212, 10, v213\n"
    "v_lshrrev_b32 v212, 5, v255\n"
    "v_lshl_add_u32 v212, v212, 8, v213\n"
    "s_lshr_b32 s68, s67, 1\n"
    "s_lshl_b32 s68, s68, 11\n"
    "s_and_b32 s72, s67, 1\n"
    "s_lshl_b32 s72, s72, 9\n"
    "s_add_u32 s68, s68, s72\n"
    "v_add_u32 v212, s68, v212\n"
    "s_lshl_b32 s72, %7, 2\n"
    "s_add_u32 s72, s72, 24576\n"
    "s_add_u32 s72, s72, %8\n"
    "v_add_u32 v214, s72, v239\n"
    "s_lshl_b32 s72, %7, 9\n"
    "s_add_u32 s72, s72, 24576\n"
    "s_add_u32 s72, s72, %8\n"
    "v_add_u32 v215, s72, v239\n"
    "s_lshr_b32 s68, %6, 2\n"
    "s_add_u32 s68, s68, 16\n"
    "s_lshl_b32 s68, s68, 11\n"
    "s_and_b32 s72, %6, 3\n"
    "s_lshl_b32 s72, s72, 8\n"
    "s_add_u32 s68, s68, s72\n"
    "s_mul_i32 s72, %7, 36864\n"
    "s_add_u32 s68, s68, s72\n"
    "v_add_u32 v213, s68, v213\n"
    "s_mul_i32 s68, %5, 147456\n"
    "s_add_u32 s68, s68, 33554432\n"
    "s_mov_b64 s[52:53], %2\n"
    "s_add_u32 s52, s52, s68\n"
    "s_addc_u32 s53, s53, 0\n"
    "s_waitcnt lgkmcnt(0)\n"
    "v_mfma_f32_32x32x16_bf16 v[152:167], a[98:101], v[0:3], 0\n"
    "v_mfma_f32_32x32x8bf16_1k v[152:167], a[102:103], v[16:17], v[152:167]\n"
    "v_mfma_f32_32x32x16_bf16 v[168:183], a[98:101], v[4:7], 0\n"
    "v_mfma_f32_32x32x8bf16_1k v[168:183], a[102:103], v[18:19], v[168:183]\n"
    "s_nop 7\n"
    "s_nop 7\n"
    "v_max3_f32 v204, v152, v153, v154\n"
    "v_max_f32 v204, v204, v155\n"
    "v_max3_f32 v204, v204, v156, v157\n"
    "v_max3_f32 v204, v204, v158, v159\n"
    "v_max3_f32 v204, v204, v160, v161\n"
    "v_max3_f32 v204, v204, v162, v163\n"
    "v_max3_f32 v204, v204, v164, v165\n"
    "v_max3_f32 v204, v204, v166, v167\n"
    "v_max3_f32 v205, v168, v169, v170\n"
    "v_max_f32 v205, v205, v171\n"
    "v_max3_f32 v205, v205, v172, v173\n"
    "v_max3_f32 v205, v205, v174, v175\n"
    "v_max3_f32 v205, v205, v176, v177\n"
    "v_max3_f32 v205, v205, v178, v179\n"
    "v_max3_f32 v205, v205, v180, v181\n"
    "v_max3_f32 v205, v205, v182, v183\n"
    "v_mfma_f32_32x32x16_bf16 v[152:167], a[98:101], v[8:11], 0\n"
    "v_mfma_f32_32x32x8bf16_1k v[152:167], a[102:103], v[20:21], v[152:167]\n"
    "v_mfma_f32_32x32x16_bf16 v[168:183], a[98:101], v[12:15], 0\n"
    "v_mfma_f32_32x32x8bf16_1k v[168:183], a[102:103], v[22:23], v[168:183]\n"
    "s_nop 7\n"
    "s_nop 7\n"
    "v_max3_f32 v206, v152, v153, v154\n"
    "v_max_f32 v206, v206, v155\n"
    "v_max3_f32 v206, v206, v156, v157\n"
    "v_max3_f32 v206, v206, v158, v159\n"
    "v_max3_f32 v206, v206, v160, v161\n"
    "v_max3_f32 v206, v206, v162, v163\n"
    "v_max3_f32 v206, v206, v164, v165\n"
    "v_max3_f32 v206, v206, v166, v167\n"
    "v_max3_f32 v207, v168, v169, v170\n"
    "v_max_f32 v207, v207, v171\n"
    "v_max3_f32 v207, v207, v172, v173\n"
    "v_max3_f32 v207, v207, v174, v175\n"
    "v_max3_f32 v207, v207, v176, v177\n"
    "v_max3_f32 v207, v207, v178, v179\n"
    "v_max3_f32 v207, v207, v180, v181\n"
    "v_max3_f32 v207, v207, v182, v183\n"
    "v_mov_b32 v208, v204\n"
    "v_mov_b32 v209, v205\n"
    "v_mov_b32 v210, v206\n"
    "v_mov_b32 v211, v207\n"
    "s_nop 1\n"
    "v_permlane32_swap_b32 v204, v208\n"
    "v_permlane32_swap_b32 v205, v209\n"
    "v_permlane32_swap_b32 v206, v210\n"
    "v_permlane32_swap_b32 v207, v211\n"
    "s_nop 1\n"
    "v_max_f32 v204, v204, v208\n"
    "v_max_f32 v205, v205, v209\n"
    "v_max_f32 v206, v206, v210\n"
    "v_max_f32 v207, v207, v211\n"
    "v_sub_f32 v204, 0xc2c00000, v204\n"
    "v_sub_f32 v205, 0xc2c00000, v205\n"
    "v_sub_f32 v206, 0xc2c00000, v206\n"
    "v_sub_f32 v207, 0xc2c00000, v207\n"
    "v_mov_b32 v24, v204\n"
    "v_mov_b32 v25, v204\n"
    "v_mov_b32 v26, v204\n"
    "v_mov_b32 v27, v204\n"
    "v_mov_b32 v28, v204\n"
    "v_mov_b32 v29, v204\n"
    "v_mov_b32 v30, v204\n"
    "v_mov_b32 v31, v204\n"
    "v_mov_b32 v32, v204\n"
    "v_mov_b32 v33, v204\n"
    "v_mov_b32 v34, v204\n"
    "v_mov_b32 v35, v204\n"
    "v_mov_b32 v36, v204\n"
    "v_mov_b32 v37, v204\n"
    "v_mov_b32 v38, v204\n"
    "v_mov_b32 v39, v204\n"
    "v_mov_b32 v40, v205\n"
    "v_mov_b32 v41, v205\n"
    "v_mov_b32 v42, v205\n"
    "v_mov_b32 v43, v205\n"
    "v_mov_b32 v44, v205\n"
    "v_mov_b32 v45, v205\n"
    "v_mov_b32 v46, v205\n"
    "v_mov_b32 v47, v205\n"
    "v_mov_b32 v48, v205\n"
    "v_mov_b32 v49, v205\n"
    "v_mov_b32 v50, v205\n"
    "v_mov_b32 v51, v205\n"
    "v_mov_b32 v52, v205\n"
    "v_mov_b32 v53, v205\n"
    "v_mov_b32 v54, v205\n"
    "v_mov_b32 v55, v205\n"
    "v_mov_b32 v56, v206\n"
    "v_mov_b32 v57, v206\n"
    "v_mov_b32 v58, v206\n"
    "v_mov_b32 v59, v206\n"
    "v_mov_b32 v60, v206\n"
    "v_mov_b32 v61, v206\n"
    "v_mov_b32 v62, v206\n"
    "v_mov_b32 v63, v206\n"
    "v_mov_b32 v64, v206\n"
    "v_mov_b32 v65, v206\n"
    "v_mov_b32 v66, v206\n"
    "v_mov_b32 v67, v206\n"
    "v_mov_b32 v68, v206\n"
    "v_mov_b32 v69, v206\n"
    "v_mov_b32 v70, v206\n"
    "v_mov_b32 v71, v206\n"
    "v_mov_b32 v72, v207\n"
    "v_mov_b32 v73, v207\n"
    "v_mov_b32 v74, v207\n"
    "v_mov_b32 v75, v207\n"
    "v_mov_b32 v76, v207\n"
    "v_mov_b32 v77, v207\n"
    "v_mov_b32 v78, v207\n"
    "v_mov_b32 v79, v207\n"
    "v_mov_b32 v80, v207\n"
    "v_mov_b32 v81, v207\n"
    "v_mov_b32 v82, v207\n"
    "v_mov_b32 v83, v207\n"
    "v_mov_b32 v84, v207\n"
    "v_mov_b32 v85, v207\n"
    "v_mov_b32 v86, v207\n"
    "v_mov_b32 v87, v207\n"
    "s_cmp_lg_u32 %7, 0\n"
    "s_cbranch_scc1 33f\n"
    "v_and_b32 v204, 15, v255\n"
    "v_lshlrev_b32 v204, 2, v204\n"
    "s_mov_b32 s66, 0x80000\n"
    "31:\n"
    "global_load_dword v205, v204, s[34:35] offset:128 sc1\n"
    "s_waitcnt vmcnt(0)\n"
    "v_cmp_ne_u32 vcc, s79, v205\n"
    "s_nop 1\n"
    "s_cmp_eq_u64 vcc, 0\n"
    "s_cbranch_scc1 34f\n"
    "s_sleep 1\n"
    "s_sub_u32 s66, s66, 1\n"
    "s_cmp_lg_u32 s66, 0\n"
    "s_cbranch_scc1 31b\n"
    "34:\n"
    "s_cmp_lg_u32 %5, 0\n"
    "s_cbranch_scc1 33f\n"
    "v_mov_b32 v204, 0\n"
    "v_mov_b32 v205, s79\n"
    "s_mov_b64 exec, 1\n"
    "global_store_dword v204, v205, s[34:35] sc1\n"
    "s_mov_b64 exec, -1\n"
    "33:\n"
    "s_barrier\n"
    "s_mov_b32 s69, 0\n"
    "s_mov_b32 s68, 0\n"
    "buffer_load_dwordx4 a[0:3], v239, s[44:47], s68 offen sc1\n"
    "buffer_load_dword a4, v243, s[44:47], s68 offen sc1\n"
    "buffer_load_dwordx4 a[6:9], v244, s[44:47], s68 offen sc1\n"
    "buffer_load_dwordx4 a[10:13], v244, s[44:47], s68 offen offset:640 sc1\n"
    "s_mov_b32 s68, 2432\n"
    "buffer_load_dwordx4 a[14:17], v239, s[44:47], s68 offen sc1\n"
    "buffer_load_dword a18, v243, s[44:47], s68 offen sc1\n"
    "buffer_load_dwordx4 a[20:23], v244, s[44:47], s68 offen sc1\n"
    "buffer_load_dwordx4 a[24:27], v244, s[44:47], s68 offen offset:640 sc1\n"
    "s_waitcnt vmcnt(6)\n"
    "v_mfma_f32_32x32x16_bf16 v[152:167], a[0:3], v[0:3], v[24:39]\n"
    "v_mfma_f32_32x32x8bf16_1k v[152:167], a[4:5], v[16:17], v[152:167]\n"
    "s_nop 7\n"
    "s_nop 7\n"
    "v_mfma_f32_32x32x16_bf16 v[168:183], a[0:3], v[4:7], v[40:55]\n"
    "s_add_u32 s68, s68, 2432\n"
    "buffer_load_dwordx4 a[28:31], v239, s[44:47], s68 offen sc1\n"
    "buffer_load_dword a32, v243, s[44:47], s68 offen sc1\n"
    "v_exp_f32 v152, v152\n"
    "v_exp_f32 v153, v153\n"
    "v_exp_f32 v154, v154\n"
    "v_mfma_f32_32x32x8bf16_1k v[168:183], a[4:5], v[18:19], v[168:183]\n"
    "buffer_load_dwordx4 a[34:37], v244, s[44:47], s68 offen sc1\n"
    "buffer_load_dwordx4 a[38:41], v244, s[44:47], s68 offen offset:640 sc1\n"
    "v_exp_f32 v155, v155\n"
    "v_cvt_pk_bf16_f32 v184, v152, v153\n"
    "v_exp_f32 v156, v156\n"
    "v_cvt_pk_bf16_f32 v185, v154, v155\n"
    "v_exp_f32 v157, v157\n"
    "v_exp_f32 v158, v158\n"
    "v_cvt_pk_bf16_f32 v186, v156, v157\n"
    "v_exp_f32 v159, v159\n"
    "v_mfma_f32_32x32x16_bf16 v[136:151], a[104:107], v[192:195], v[136:151]\n"
    "s_add_u32 s68, s68, 2432\n"
    "buffer_load_dwordx4 a[42:45], v239, s[44:47], s68 offen sc1\n"
    "buffer_load_dword a46, v243, s[44:47], s68 offen sc1\n"
    "v_exp_f32 v160, v160\n"
    "v_exp_f32 v161, v161\n"
    "v_exp_f32 v162, v162\n"
    "v_mfma_f32_32x32x16_bf16 v[136:151], a[108:111], v[196:199], v[136:151]\n"
    "buffer_load_dwordx4 a[48:51], v244, s[44:47], s68 offen sc1\n"
    "buffer_load_dwordx4 a[52:55], v244, s[44:47], s68 offen offset:640 sc1\n"
    "v_cvt_pk_bf16_f32 v187, v158, v159\n"
    "v_exp_f32 v163, v163\n"
    "v_cvt_pk_bf16_f32 v188, v160, v161\n"
    "v_exp_f32 v164, v164\n"
    "v_cvt_pk_bf16_f32 v189, v162, v163\n"
    "v_exp_f32 v165, v165\n"
    "v_exp_f32 v166, v166\n"
    "v_exp_f32 v167, v167\n"
    "v_cvt_pk_bf16_f32 v190, v164, v165\n"
    "v_cvt_pk_bf16_f32 v191, v166, v167\n"
    "v_mfma_f32_32x32x16_bf16 v[152:167], a[0:3], v[8:11], v[56:71]\n"
    "s_add_u32 s68, s68, 2432\n"
    "buffer_load_dwordx4 a[56:59], v239, s[44:47], s68 offen sc1\n"
    "buffer_load_dword a60, v243, s[44:47], s68 offen sc1\n"
    "v_exp_f32 v168, v168\n"
    "v_exp_f32 v169, v169\n"
    "v_exp_f32 v170, v170\n"
    "v_mfma_f32_32x32x8bf16_1k v[152:167], a[4:5], v[20:21], v[152:167]\n"
    "s_waitcnt vmcnt(14)\n"
    "buffer_load_dwordx4 a[62:65], v244, s[44:47], s68 offen sc1\n"
    "buffer_load_dwordx4 a[66:69], v244, s[44:47], s68 offen offset:640 sc1\n"
    "v_exp_f32 v171, v171\n"
    "v_cvt_pk_bf16_f32 v192, v168, v169\n"
    "v_exp_f32 v172, v172\n"
    "v_cvt_pk_bf16_f32 v193, v170, v171\n"
    "v_exp_f32 v173, v173\n"
    "v_exp_f32 v174, v174\n"
    "v_cvt_pk_bf16_f32 v194, v172, v173\n"
    "v_exp_f32 v175, v175\n"
    "v_mfma_f32_32x32x16_bf16 v[88:103], a[6:9], v[184:187], v[88:103]\n"
    "s_add_u32 s68, s68, 2432\n"
    "buffer_load_dwordx4 a[70:73], v239, s[44:47], s68 offen sc1\n"
    "buffer_load_dword a74, v243, s[44:47], s68 offen sc1\n"
    "v_exp_f32 v176, v176\n"
    "v_exp_f32 v177, v177\n"
    "v_exp_f32 v178, v178\n"
    "v_mfma_f32_32x32x16_bf16 v[88:103], a[10:13], v[188:191], v[88:103]\n"
    "buffer_load_dwordx4 a[76:79], v244, s[44:47], s68 offen sc1\n"
    "buffer_load_dwordx4 a[80:83], v244, s[44:47], s68 offen offset:640 sc1\n"
    "v_cvt_pk_bf16_f32 v195, v174, v175\n"
    "v_exp_f32 v179, v179\n"
    "v_cvt_pk_bf16_f32 v196, v176, v177\n"
    "v_exp_f32 v180, v180\n"
    "v_cvt_pk_bf16_f32 v197, v178, v179\n"
    "v_exp_f32 v181, v181\n"
    "v_exp_f32 v182, v182\n"
    "v_exp_f32 v183, v183\n"
    "v_cvt_pk_bf16_f32 v198, v180, v181\n"
    "v_cvt_pk_bf16_f32 v199, v182, v183\n"
    "v_mfma_f32_32x32x16_bf16 v[168:183], a[0:3], v[12:15], v[72:87]\n"
    "s_add_u32 s68, s68, 2432\n"
    "buffer_load_dwordx4 a[84:87], v239, s[44:47], s68 offen sc1\n"
    "buffer_load_dword a88, v243, s[44:47], s68 offen sc1\n"
    "v_exp_f32 v152, v152\n"
    "v_exp_f32 v153, v153\n"
    "v_exp_f32 v154, v154\n"
    "v_mfma_f32_32x32x8bf16_1k v[168:183], a[4:5], v[22:23], v[168:183]\n"
    "buffer_load_dwordx4 a[90:93], v244, s[44:47], s68 offen sc1\n"
    "buffer_load_dwordx4 a[94:97], v244, s[44:47], s68 offen offset:640 sc1\n"
    "v_exp_f32 v155, v155\n"
    "v_cvt_pk_bf16_f32 v184, v152, v153\n"
    "v_exp_f32 v156, v156\n"
    "v_cvt_pk_bf16_f32 v185, v154, v155\n"
    "v_exp_f32 v157, v157\n"
    "v_exp_f32 v158, v158\n"
    "v_cvt_pk_bf16_f32 v186, v156, v157\n"
    "v_exp_f32 v159, v159\n"
    "v_mfma_f32_32x32x16_bf16 v[104:119], a[6:9], v[192:195], v[104:119]\n"
    "s_add_u32 s68, s68, 2432\n"
    "buffer_load_dwordx4 a[98:101], v239, s[44:47], s68 offen sc1\n"
    "buffer_load_dword a102, v243, s[44:47], s68 offen sc1\n"
    "v_exp_f32 v160, v160\n"
    "v_exp_f32 v161, v161\n"
    "v_exp_f32 v162, v162\n"
    "v_mfma_f32_32x32x16_bf16 v[104:119], a[10:13], v[196:199], v[104:119]\n"
    "s_waitcnt vmcnt(24)\n"
    "buffer_load_dwordx4 a[104:107], v244, s[44:47], s68 offen sc1\n"
    "buffer_load_dwordx4 a[108:111], v244, s[44:47], s68 offen offset:640 sc1\n"
    "v_cvt_pk_bf16_f32 v187, v158, v159\n"
    "v_exp_f32 v163, v163\n"
    "v_cvt_pk_bf16_f32 v188, v160, v161\n"
    "v_exp_f32 v164, v164\n"
    "v_cvt_pk_bf16_f32 v189, v162, v163\n"
    "v_exp_f32 v165, v165\n"
    "v_exp_f32 v166, v166\n"
    "v_exp_f32 v167, v167\n"
    "v_cvt_pk_bf16_f32 v190, v164, v165\n"
    "v_cvt_pk_bf16_f32 v191, v166, v167\n"
    "v_mfma_f32_32x32x16_bf16 v[152:167], a[14:17], v[0:3], v[24:39]\n"
    "v_exp_f32 v168, v168\n"
    "v_exp_f32 v169, v169\n"
    "v_exp_f32 v170, v170\n"
    "v_mfma_f32_32x32x8bf16_1k v[152:167], a[18:19], v[16:17], v[152:167]\n"
    "v_exp_f32 v171, v171\n"
    "v_cvt_pk_bf16_f32 v192, v168, v169\n"
    "v_exp_f32 v172, v172\n"
    "v_cvt_pk_bf16_f32 v193, v170, v171\n"
    "v_exp_f32 v173, v173\n"
    "v_exp_f32 v174, v174\n"
    "v_cvt_pk_bf16_f32 v194, v172, v173\n"
    "v_exp_f32 v175, v175\n"
    "v_mfma_f32_32x32x16_bf16 v[120:135], a[6:9], v[184:187], v[120:135]\n"
    "v_exp_f32 v176, v176\n"
    "v_exp_f32 v177, v177\n"
    "v_exp_f32 v178, v178\n"
    "v_mfma_f32_32x32x16_bf16 v[120:135], a[10:13], v[188:191], v[120:135]\n"
    "v_cvt_pk_bf16_f32 v195, v174, v175\n"
    "v_exp_f32 v179, v179\n"
    "v_cvt_pk_bf16_f32 v196, v176, v177\n"
    "v_exp_f32 v180, v180\n"
    "v_cvt_pk_bf16_f32 v197, v178, v179\n"
    "v_exp_f32 v181, v181\n"
    "v_exp_f32 v182, v182\n"
    "v_exp_f32 v183, v183\n"
    "v_cvt_pk_bf16_f32 v198, v180, v181\n"
    "v_cvt_pk_bf16_f32 v199, v182, v183\n"
    "v_mfma_f32_32x32x16_bf16 v[168:183], a[14:17], v[4:7], v[40:55]\n"
    "v_exp_f32 v152, v152\n"
    "v_exp_f32 v153, v153\n"
    "v_exp_f32 v154, v154\n"
    "v_mfma_f32_32x32x8bf16_1k v[168:183], a[18:19], v[18:19], v[168:183]\n"
    "v_exp_f32 v155, v155\n"
    "v_cvt_pk_bf16_f32 v184, v152, v153\n"
    "v_exp_f32 v156, v156\n"
    "v_cvt_pk_bf16_f32 v185, v154, v155\n"
    "v_exp_f32 v157, v157\n"
    "v_exp_f32 v158, v158\n"
    "v_cvt_pk_bf16_f32 v186, v156, v157\n"
    "v_exp_f32 v159, v159\n"
    "v_mfma_f32_32x32x16_bf16 v[136:151], a[6:9], v[192:195], v[136:151]\n"
    "v_exp_f32 v160, v160\n"
    "v_exp_f32 v161, v161\n"
    "v_exp_f32 v162, v162\n"
    "v_mfma_f32_32x32x16_bf16 v[136:151], a[10:13], v[196:199], v[136:151]\n"
    "v_cvt_pk_bf16_f32 v187, v158, v159\n"
    "v_exp_f32 v163, v163\n"
    "v_cvt_pk_bf16_f32 v188, v160, v161\n"
    "v_exp_f32 v164, v164\n"
    "v_cvt_pk_bf16_f32 v189, v162, v163\n"
    "v_exp_f32 v165, v165\n"
    "v_exp_f32 v166, v166\n"
    "v_exp_f32 v167, v167\n"
    "v_cvt_pk_bf16_f32 v190, v164, v165\n"
    "v_cvt_pk_bf16_f32 v191, v166, v167\n"
    "v_mfma_f32_32x32x16_bf16 v[152:167], a[14:17], v[8:11], v[56:71]\n"
    "v_exp_f32 v168, v168\n"
    "v_exp_f32 v169, v169\n"
    "v_exp_f32 v170, v170\n"
    "v_mfma_f32_32x32x8bf16_1k v[152:167], a[18:19], v[20:21], v[152:167]\n"
    "s_waitcnt vmcnt(24)\n"
    "v_exp_f32 v171, v171\n"
    "v_cvt_pk_bf16_f32 v192, v168, v169\n"
    "v_exp_f32 v172, v172\n"
    "v_cvt_pk_bf16_f32 v193, v170, v171\n"
    "v_exp_f32 v173, v173\n"
    "v_exp_f32 v174, v174\n"
    "v_cvt_pk_bf16_f32 v194, v172, v173\n"
    "v_exp_f32 v175, v175\n"
    "v_mfma_f32_32x32x16_bf16 v[88:103], a[20:23], v[184:187], v[88:103]\n"
    "s_add_u32 s68, s68, 2432\n"
    "v_exp_f32 v176, v176\n"
    "v_exp_f32 v177, v177\n"
    "v_exp_f32 v178, v178\n"
    "v_mfma_f32_32x32x16_bf16 v[88:103], a[24:27], v[188:191], v[88:103]\n"
    "buffer_load_dwordx4 a[0:3], v239, s[44:47], s68 offen sc1\n"
    "v_cvt_pk_bf16_f32 v195, v174, v175\n"
    "v_exp_f32 v179, v179\n"
    "v_cvt_pk_bf16_f32 v196, v176, v177\n"
    "v_exp_f32 v180, v180\n"
    "v_cvt_pk_bf16_f32 v197, v178, v179\n"
    "v_exp_f32 v181, v181\n"
    "v_exp_f32 v182, v182\n"
    "v_exp_f32 v183, v183\n"
    "v_cvt_pk_bf16_f32 v198, v180, v181\n"
    "v_cvt_pk_bf16_f32 v199, v182, v183\n"
    "v_mfma_f32_32x32x16_bf16 v[168:183], a[14:17], v[12:15], v[72:87]\n"
    "v_exp_f32 v152, v152\n"
    "v_exp_f32 v153, v153\n"
    "v_exp_f32 v154, v154\n"
    "v_mfma_f32_32x32x8bf16_1k v[168:183], a[18:19], v[22:23], v[168:183]\n"
    "buffer_load_dword a4, v243, s[44:47], s68 offen sc1\n"
    "v_exp_f32 v155, v155\n"
    "v_cvt_pk_bf16_f32 v184, v152, v153\n"
    "v_exp_f32 v156, v156\n"
    "v_cvt_pk_bf16_f32 v185, v154, v155\n"
    "v_exp_f32 v157, v157\n"
    "v_exp_f32 v158, v158\n"
    "v_cvt_pk_bf16_f32 v186, v156, v157\n"
    "v_exp_f32 v159, v159\n"
    "v_mfma_f32_32x32x16_bf16 v[104:119], a[20:23], v[192:195], v[104:119]\n"
    "v_exp_f32 v160, v160\n"
    "v_exp_f32 v161, v161\n"
    "v_exp_f32 v162, v162\n"
    "v_mfma_f32_32x32x16_bf16 v[104:119], a[24:27], v[196:199], v[104:119]\n"
    "s_waitcnt vmcnt(24)\n"
    "buffer_load_dwordx4 a[6:9], v244, s[44:47], s68 offen sc1\n"
    "v_cvt_pk_bf16_f32 v187, v158, v159\n"
    "v_exp_f32 v163, v163\n"
    "v_cvt_pk_bf16_f32 v188, v160, v161\n"
    "v_exp_f32 v164, v164\n"
    "v_cvt_pk_bf16_f32 v189, v162, v163\n"
    "v_exp_f32 v165, v165\n"
    "v_exp_f32 v166, v166\n"
    "v_exp_f32 v167, v167\n"
    "v_cvt_pk_bf16_f32 v190, v164, v165\n"
    "v_cvt_pk_bf16_f32 v191, v166, v167\n"
    "v_mfma_f32_32x32x16_bf16 v[152:167], a[28:31], v[0:3], v[24:39]\n"
    "v_exp_f32 v168, v168\n"
    "v_exp_f32 v169, v169\n"
    "v_exp_f32 v170, v170\n"
    "v_mfma_f32_32x32x8bf16_1k v[152:167], a[32:33], v[16:17], v[152:167]\n"
    "buffer_load_dwordx4 a[10:13], v244, s[44:47], s68 offen offset:640 sc1\n"
    "v_exp_f32 v171, v171\n"
    "v_cvt_pk_bf16_f32 v192, v168, v169\n"
    "v_exp_f32 v172, v172\n"
    "v_cvt_pk_bf16_f32 v193, v170, v171\n"
    "v_exp_f32 v173, v173\n"
    "v_exp_f32 v174, v174\n"
    "v_cvt_pk_bf16_f32 v194, v172, v173\n"
    "v_exp_f32 v175, v175\n"
    "v_mfma_f32_32x32x16_bf16 v[120:135], a[20:23], v[184:187], v[120:135]\n"
    "v_exp_f32 v176, v176\n"
    "v_exp_f32 v177, v177\n"
    "v_exp_f32 v178, v178\n"
    "v_mfma_f32_32x32x16_bf16 v[120:135], a[24:27], v[188:191], v[120:135]\n"
    "v_cvt_pk_bf16_f32 v195, v174, v175\n"
    "v_exp_f32 v179, v179\n"
    "v_cvt_pk_bf16_f32 v196, v176, v177\n"
    "v_exp_f32 v180, v180\n"
    "v_cvt_pk_bf16_f32 v197, v178, v179\n"
    "v_exp_f32 v181, v181\n"
    "v_exp_f32 v182, v182\n"
    "v_exp_f32 v183, v183\n"
    "v_cvt_pk_bf16_f32 v198, v180, v181\n"
    "v_cvt_pk_bf16_f32 v199, v182, v183\n"
    "v_mfma_f32_32x32x16_bf16 v[168:183], a[28:31], v[4:7], v[40:55]\n"
    "s_lshl_b32 s67, %12, 2\n"
    "s_add_u32 s67, s67, %7\n"
    "v_exp_f32 v152, v152\n"
    "v_exp_f32 v153, v153\n"
    "v_exp_f32 v154, v154\n"
    "v_mfma_f32_32x32x8bf16_1k v[168:183], a[32:33], v[18:19], v[168:183]\n"
    "s_mul_i32 s67, s67, 36864\n"
    "s_lshl_b32 s73, s54, 6\n"
    "v_exp_f32 v155, v155\n"
    "v_cvt_pk_bf16_f32 v184, v152, v153\n"
    "v_exp_f32 v156, v156\n"
    "v_cvt_pk_bf16_f32 v185, v154, v155\n"
    "v_exp_f32 v157, v157\n"
    "v_exp_f32 v158, v158\n"
    "v_cvt_pk_bf16_f32 v186, v156, v157\n"
    "v_exp_f32 v159, v159\n"
    "v_mfma_f32_32x32x16_bf16 v[136:151], a[20:23], v[192:195], v[136:151]\n"
    "s_add_u32 s73, s73, s67\n"
    "s_add_u32 m0, s61, 0\n"
    "v_exp_f32 v160, v160\n"
    "v_exp_f32 v161, v161\n"
    "v_exp_f32 v162, v162\n"
    "v_mfma_f32_32x32x16_bf16 v[136:151], a[24:27], v[196:199], v[136:151]\n"
    "s_add_u32 s73, s73, 0\n"
    "buffer_load_dwordx4 v245, s[36:39], s73 offen lds\n"
    "v_cvt_pk_bf16_f32 v187, v158, v159\n"
    "v_exp_f32 v163, v163\n"
    "v_cvt_pk_bf16_f32 v188, v160, v161\n"
    "v_exp_f32 v164, v164\n"
    "v_cvt_pk_bf16_f32 v189, v162, v163\n"
    "v_exp_f32 v165, v165\n"
    "v_exp_f32 v166, v166\n"
    "v_exp_f32 v167, v167\n"
    "v_cvt_pk_bf16_f32 v190, v164, v165\n"
    "v_cvt_pk_bf16_f32 v191, v166, v167\n"
    "v_mfma_f32_32x32x16_bf16 v[152:167], a[28:31], v[8:11], v[56:71]\n"
    "v_exp_f32 v168, v168\n"
    "v_exp_f32 v169, v169\n"
    "v_exp_f32 v170, v170\n"
    "v_mfma_f32_32x32x8bf16_1k v[152:167], a[32:33], v[20:21], v[152:167]\n"
    "s_waitcnt vmcnt(25)\n"
    "v_exp_f32 v171, v171\n"
    "v_cvt_pk_bf16_f32 v192, v168, v169\n"
    "v_exp_f32 v172, v172\n"
    "v_cvt_pk_bf16_f32 v193, v170, v171\n"
    "v_exp_f32 v173, v173\n"
    "v_exp_f32 v174, v174\n"
    "v_cvt_pk_bf16_f32 v194, v172, v173\n"
    "v_exp_f32 v175, v175\n"
    "v_mfma_f32_32x32x16_bf16 v[88:103], a[34:37], v[184:187], v[88:103]\n"
    "s_add_u32 s68, s68, 2432\n"
    "v_exp_f32 v176, v176\n"
    "v_exp_f32 v177, v177\n"
    "v_exp_f32 v178, v178\n"
    "v_mfma_f32_32x32x16_bf16 v[88:103], a[38:41], v[188:191], v[88:103]\n"
    "buffer_load_dwordx4 a[14:17], v239, s[44:47], s68 offen sc1\n"
    "v_cvt_pk_bf16_f32 v195, v174, v175\n"
    "v_exp_f32 v179, v179\n"
    "v_cvt_pk_bf16_f32 v196, v176, v177\n"
    "v_exp_f32 v180, v180\n"
    "v_cvt_pk_bf16_f32 v197, v178, v179\n"
    "v_exp_f32 v181, v181\n"
    "v_exp_f32 v182, v182\n"
    "v_exp_f32 v183, v183\n"
    "v_cvt_pk_bf16_f32 v198, v180, v181\n"
    "v_cvt_pk_bf16_f32 v199, v182, v183\n"
    "v_mfma_f32_32x32x16_bf16 v[168:183], a[28:31], v[12:15], v[72:87]\n"
    "v_exp_f32 v152, v152\n"
    "v_exp_f32 v153, v153\n"
    "v_exp_f32 v154, v154\n"
    "v_mfma_f32_32x32x8bf16_1k v[168:183], a[32:33], v[22:23], v[168:183]\n"
    "buffer_load_dword a18, v243, s[44:47], s68 offen sc1\n"
    "v_exp_f32 v155, v155\n"
    "v_cvt_pk_bf16_f32 v184, v152, v153\n"
    "v_exp_f32 v156, v156\n"
    "v_cvt_pk_bf16_f32 v185, v154, v155\n"
    "v_exp_f32 v157, v157\n"
    "v_exp_f32 v158, v158\n"
    "v_cvt_pk_bf16_f32 v186, v156, v157\n"
    "v_exp_f32 v159, v159\n"
    "v_mfma_f32_32x32x16_bf16 v[104:119], a[34:37], v[192:195], v[104:119]\n"
    "v_exp_f32 v160, v160\n"
    "v_exp_f32 v161, v161\n"
    "v_exp_f32 v162, v162\n"
    "v_mfma_f32_32x32x16_bf16 v[104:119], a[38:41], v[196:199], v[104:119]\n"
    "s_waitcnt vmcnt(25)\n"
    "buffer_load_dwordx4 a[20:23], v244, s[44:47], s68 offen sc1\n"
    "v_cvt_pk_bf16_f32 v187, v158, v159\n"
    "v_exp_f32 v163, v163\n"
    "v_cvt_pk_bf16_f32 v188, v160, v161\n"
    "v_exp_f32 v164, v164\n"
    "v_cvt_pk_bf16_f32 v189, v162, v163\n"
    "v_exp_f32 v165, v165\n"
    "v_exp_f32 v166, v166\n"
    "v_exp_f32 v167, v167\n"
    "v_cvt_pk_bf16_f32 v190, v164, v165\n"
    "v_cvt_pk_bf16_f32 v191, v166, v167\n"
    "v_mfma_f32_32x32x16_bf16 v[152:167], a[42:45], v[0:3], v[24:39]\n"
    "v_exp_f32 v168, v168\n"
    "v_exp_f32 v169, v169\n"
    "v_exp_f32 v170, v170\n"
    "v_mfma_f32_32x32x8bf16_1k v[152:167], a[46:47], v[16:17], v[152:167]\n"
    "buffer_load_dwordx4 a[24:27], v244, s[44:47], s68 offen offset:640 sc1\n"
    "v_exp_f32 v171, v171\n"
    "v_cvt_pk_bf16_f32 v192, v168, v169\n"
    "v_exp_f32 v172, v172\n"
    "v_cvt_pk_bf16_f32 v193, v170, v171\n"
    "v_exp_f32 v173, v173\n"
    "v_exp_f32 v174, v174\n"
    "v_cvt_pk_bf16_f32 v194, v172, v173\n"
    "v_exp_f32 v175, v175\n"
    "v_mfma_f32_32x32x16_bf16 v[120:135], a[34:37], v[184:187], v[120:135]\n"
    "v_exp_f32 v176, v176\n"
    "v_exp_f32 v177, v177\n"
    "v_exp_f32 v178, v178\n"
    "v_mfma_f32_32x32x16_bf16 v[120:135], a[38:41], v[188:191], v[120:135]\n"
    "v_cvt_pk_bf16_f32 v195, v174, v175\n"
    "v_exp_f32 v179, v179\n"
    "v_cvt_pk_bf16_f32 v196, v176, v177\n"
    "v_exp_f32 v180, v180\n"
    "v_cvt_pk_bf16_f32 v197, v178, v179\n"
    "v_exp_f32 v181, v181\n"
    "v_exp_f32 v182, v182\n"
    "v_exp_f32 v183, v183\n"
    "v_cvt_pk_bf16_f32 v198, v180, v181\n"
    "v_cvt_pk_bf16_f32 v199, v182, v183\n"
    "v_mfma_f32_32x32x16_bf16 v[168:183], a[42:45], v[4:7], v[40:55]\n"
    "s_add_u32 m0, %8, 147456\n"
    "v_exp_f32 v152, v152\n"
    "v_exp_f32 v153, v153\n"
    "v_exp_f32 v154, v154\n"
    "v_mfma_f32_32x32x8bf16_1k v[168:183], a[46:47], v[18:19], v[168:183]\n"
    "global_load_lds_dword v[236:237], off\n"
    "v_exp_f32 v155, v155\n"
    "v_cvt_pk_bf16_f32 v184, v152, v153\n"
    "v_exp_f32 v156, v156\n"
    "v_cvt_pk_bf16_f32 v185, v154, v155\n"
    "v_exp_f32 v157, v157\n"
    "v_exp_f32 v158, v158\n"
    "v_cvt_pk_bf16_f32 v186, v156, v157\n"
    "v_exp_f32 v159, v159\n"
    "v_mfma_f32_32x32x16_bf16 v[136:151], a[34:37], v[192:195], v[136:151]\n"
    "v_exp_f32 v160, v160\n"
    "v_exp_f32 v161, v161\n"
    "v_exp_f32 v162, v162\n"
    "v_mfma_f32_32x32x16_bf16 v[136:151], a[38:41], v[196:199], v[136:151]\n"
    "v_add_co_u32 v236, vcc, v238, v236\n"
    "v_cvt_pk_bf16_f32 v187, v158, v159\n"
    "v_exp_f32 v163, v163\n"
    "v_cvt_pk_bf16_f32 v188, v160, v161\n"
    "v_exp_f32 v164, v164\n"
    "v_cvt_pk_bf16_f32 v189, v162, v163\n"
    "v_exp_f32 v165, v165\n"
    "v_exp_f32 v166, v166\n"
    "v_exp_f32 v167, v167\n"
    "v_cvt_pk_bf16_f32 v190, v164, v165\n"
    "v_cvt_pk_bf16_f32 v191, v166, v167\n"
    "v_mfma_f32_32x32x16_bf16 v[152:167], a[42:45], v[8:11], v[56:71]\n"
    "v_exp_f32 v168, v168\n"
    "v_exp_f32 v169, v169\n"
    "v_exp_f32 v170, v170\n"
    "v_mfma_f32_32x32x8bf16_1k v[152:167], a[46:47], v[20:21], v[152:167]\n"
    "s_waitcnt vmcnt(26)\n"
    "v_addc_co_u32 v237, vcc, 0, v237, vcc\n"
    "v_exp_f32 v171, v171\n"
    "v_cvt_pk_bf16_f32 v192, v168, v169\n"
    "v_exp_f32 v172, v172\n"
    "v_cvt_pk_bf16_f32 v193, v170, v171\n"
    "v_exp_f32 v173, v173\n"
    "v_exp_f32 v174, v174\n"
    "v_cvt_pk_bf16_f32 v194, v172, v173\n"
    "v_exp_f32 v175, v175\n"
    "v_mfma_f32_32x32x16_bf16 v[88:103], a[48:51], v[184:187], v[88:103]\n"
    "s_add_u32 s68, s68, 2432\n"
    "v_exp_f32 v176, v176\n"
    "v_exp_f32 v177, v177\n"
    "v_exp_f32 v178, v178\n"
    "v_mfma_f32_32x32x16_bf16 v[88:103], a[52:55], v[188:191], v[88:103]\n"
    "buffer_load_dwordx4 a[28:31], v239, s[44:47], s68 offen sc1\n"
    "v_cvt_pk_bf16_f32 v195, v174, v175\n"
    "v_exp_f32 v179, v179\n"
    "v_cvt_pk_bf16_f32 v196, v176, v177\n"
    "v_exp_f32 v180, v180\n"
    "v_cvt_pk_bf16_f32 v197, v178, v179\n"
    "v_exp_f32 v181, v181\n"
    "v_exp_f32 v182, v182\n"
    "v_exp_f32 v183, v183\n"
    "v_cvt_pk_bf16_f32 v198, v180, v181\n"
    "v_cvt_pk_bf16_f32 v199, v182, v183\n"
    "v_mfma_f32_32x32x16_bf16 v[168:183], a[42:45], v[12:15], v[72:87]\n"
    "v_exp_f32 v152, v152\n"
    "v_exp_f32 v153, v153\n"
    "v_exp_f32 v154, v154\n"
    "v_mfma_f32_32x32x8bf16_1k v[168:183], a[46:47], v[22:23], v[168:183]\n"
    "buffer_load_dword a32, v243, s[44:47], s68 offen sc1\n"
    "v_exp_f32 v155, v155\n"
    "v_cvt_pk_bf16_f32 v184, v152, v153\n"
    "v_exp_f32 v156, v156\n"
    "v_cvt_pk_bf16_f32 v185, v154, v155\n"
    "v_exp_f32 v157, v157\n"
    "v_exp_f32 v158, v158\n"
    "v_cvt_pk_bf16_f32 v186, v156, v157\n"
    "v_exp_f32 v159, v159\n"
    "v_mfma_f32_32x32x16_bf16 v[104:119], a[48:51], v[192:195], v[104:119]\n"
    "v_exp_f32 v160, v160\n"
    "v_exp_f32 v161, v161\n"
    "v_exp_f32 v162, v162\n"
    "v_mfma_f32_32x32x16_bf16 v[104:119], a[52:55], v[196:199], v[104:119]\n"
    "s_waitcnt vmcnt(26)\n"
    "buffer_load_dwordx4 a[34:37], v244, s[44:47], s68 offen sc1\n"
    "v_cvt_pk_bf16_f32 v187, v158, v159\n"
    "v_exp_f32 v163, v163\n"
    "v_cvt_pk_bf16_f32 v188, v160, v161\n"
    "v_exp_f32 v164, v164\n"
    "v_cvt_pk_bf16_f32 v189, v162, v163\n"
    "v_exp_f32 v165, v165\n"
    "v_exp_f32 v166, v166\n"
    "v_exp_f32 v167, v167\n"
    "v_cvt_pk_bf16_f32 v190, v164, v165\n"
    "v_cvt_pk_bf16_f32 v191, v166, v167\n"
    "v_mfma_f32_32x32x16_bf16 v[152:167], a[56:59], v[0:3], v[24:39]\n"
    "v_exp_f32 v168, v168\n"
    "v_exp_f32 v169, v169\n"
    "v_exp_f32 v170, v170\n"
    "v_mfma_f32_32x32x8bf16_1k v[152:167], a[60:61], v[16:17], v[152:167]\n"
    "buffer_load_dwordx4 a[38:41], v244, s[44:47], s68 offen offset:640 sc1\n"
    "v_exp_f32 v171, v171\n"
    "v_cvt_pk_bf16_f32 v192, v168, v169\n"
    "v_exp_f32 v172, v172\n"
    "v_cvt_pk_bf16_f32 v193, v170, v171\n"
    "v_exp_f32 v173, v173\n"
    "v_exp_f32 v174, v174\n"
    "v_cvt_pk_bf16_f32 v194, v172, v173\n"
    "v_exp_f32 v175, v175\n"
    "v_mfma_f32_32x32x16_bf16 v[120:135], a[48:51], v[184:187], v[120:135]\n"
    "v_exp_f32 v176, v176\n"
    "v_exp_f32 v177, v177\n"
    "v_exp_f32 v178, v178\n"
    "v_mfma_f32_32x32x16_bf16 v[120:135], a[52:55], v[188:191], v[120:135]\n"
    "v_cvt_pk_bf16_f32 v195, v174, v175\n"
    "v_exp_f32 v179, v179\n"
    "v_cvt_pk_bf16_f32 v196, v176, v177\n"
    "v_exp_f32 v180, v180\n"
    "v_cvt_pk_bf16_f32 v197, v178, v179\n"
    "v_exp_f32 v181, v181\n"
    "v_exp_f32 v182, v182\n"
    "v_exp_f32 v183, v183\n"
    "v_cvt_pk_bf16_f32 v198, v180, v181\n"
    "v_cvt_pk_bf16_f32 v199, v182, v183\n"
    "v_mfma_f32_32x32x16_bf16 v[168:183], a[56:59], v[4:7], v[40:55]\n"
    "s_lshl_b32 s67, %12, 2\n"
    "s_add_u32 s67, s67, %7\n"
    "v_exp_f32 v152, v152\n"
    "v_exp_f32 v153, v153\n"
    "v_exp_f32 v154, v154\n"
    "v_mfma_f32_32x32x8bf16_1k v[168:183], a[60:61], v[18:19], v[168:183]\n"
    "s_mul_i32 s67, s67, 36864\n"
    "s_lshl_b32 s73, s55, 6\n"
    "v_exp_f32 v155, v155\n"
    "v_cvt_pk_bf16_f32 v184, v152, v153\n"
    "v_exp_f32 v156, v156\n"
    "v_cvt_pk_bf16_f32 v185, v154, v155\n"
    "v_exp_f32 v157, v157\n"
    "v_exp_f32 v158, v158\n"
    "v_cvt_pk_bf16_f32 v186, v156, v157\n"
    "v_exp_f32 v159, v159\n"
    "v_mfma_f32_32x32x16_bf16 v[136:151], a[48:51], v[192:195], v[136:151]\n"
    "s_add_u32 s73, s73, s67\n"
    "s_add_u32 m0, s61, 1024\n"
    "v_exp_f32 v160, v160\n"
    "v_exp_f32 v161, v161\n"
    "v_exp_f32 v162, v162\n"
    "v_mfma_f32_32x32x16_bf16 v[136:151], a[52:55], v[196:199], v[136:151]\n"
    "s_add_u32 s73, s73, 0\n"
    "buffer_load_dwordx4 v245, s[36:39], s73 offen lds\n"
    "v_cvt_pk_bf16_f32 v187, v158, v159\n"
    "v_exp_f32 v163, v163\n"
    "v_cvt_pk_bf16_f32 v188, v160, v161\n"
    "v_exp_f32 v164, v164\n"
    "v_cvt_pk_bf16_f32 v189, v162, v163\n"
    "v_exp_f32 v165, v165\n"
    "v_exp_f32 v166, v166\n"
    "v_exp_f32 v167, v167\n"
    "v_cvt_pk_bf16_f32 v190, v164, v165\n"
    "v_cvt_pk_bf16_f32 v191, v166, v167\n"
    "v_mfma_f32_32x32x16_bf16 v[152:167], a[56:59], v[8:11], v[56:71]\n"
    "v_exp_f32 v168, v168\n"
    "v_exp_f32 v169, v169\n"
    "v_exp_f32 v170, v170\n"
    "v_mfma_f32_32x32x8bf16_1k v[152:167], a[60:61], v[20:21], v[152:167]\n"
    "s_waitcnt vmcnt(27)\n"
    "v_exp_f32 v171, v171\n"
    "v_cvt_pk_bf16_f32 v192, v168, v169\n"
    "v_exp_f32 v172, v172\n"
    "v_cvt_pk_bf16_f32 v193, v170, v171\n"
    "v_exp_f32 v173, v173\n"
    "v_exp_f32 v174, v174\n"
    "v_cvt_pk_bf16_f32 v194, v172, v173\n"
    "v_exp_f32 v175, v175\n"
    "v_mfma_f32_32x32x16_bf16 v[88:103], a[62:65], v[184:187], v[88:103]\n"
    "s_add_u32 s68, s68, 2432\n"
    "v_exp_f32 v176, v176\n"
    "v_exp_f32 v177, v177\n"
    "v_exp_f32 v178, v178\n"
    "v_mfma_f32_32x32x16_bf16 v[88:103], a[66:69], v[188:191], v[88:103]\n"
    "buffer_load_dwordx4 a[42:45], v239, s[44:47], s68 offen sc1\n"
    "v_cvt_pk_bf16_f32 v195, v174, v175\n"
    "v_exp_f32 v179, v179\n"
    "v_cvt_pk_bf16_f32 v196, v176, v177\n"
    "v_exp_f32 v180, v180\n"
    "v_cvt_pk_bf16_f32 v197, v178, v179\n"
    "v_exp_f32 v181, v181\n"
    "v_exp_f32 v182, v182\n"
    "v_exp_f32 v183, v183\n"
    "v_cvt_pk_bf16_f32 v198, v180, v181\n"
    "v_cvt_pk_bf16_f32 v199, v182, v183\n"
    "v_mfma_f32_32x32x16_bf16 v[168:183], a[56:59], v[12:15], v[72:87]\n"
    "v_exp_f32 v152, v152\n"
    "v_exp_f32 v153, v153\n"
    "v_exp_f32 v154, v154\n"
    "v_mfma_f32_32x32x8bf16_1k v[168:183], a[60:61], v[22:23], v[168:183]\n"
    "buffer_load_dword a46, v243, s[44:47], s68 offen sc1\n"
    "v_exp_f32 v155, v155\n"
    "v_cvt_pk_bf16_f32 v184, v152, v153\n"
    "v_exp_f32 v156, v156\n"
    "v_cvt_pk_bf16_f32 v185, v154, v155\n"
    "v_exp_f32 v157, v157\n"
    "v_exp_f32 v158, v158\n"
    "v_cvt_pk_bf16_f32 v186, v156, v157\n"
    "v_exp_f32 v159, v159\n"
    "v_mfma_f32_32x32x16_bf16 v[104:119], a[62:65], v[192:195], v[104:119]\n"
    "v_exp_f32 v160, v160\n"
    "v_exp_f32 v161, v161\n"
    "v_exp_f32 v162, v162\n"
    "v_mfma_f32_32x32x16_bf16 v[104:119], a[66:69], v[196:199], v[104:119]\n"
    "s_waitcnt vmcnt(27)\n"
    "buffer_load_dwordx4 a[48:51], v244, s[44:47], s68 offen sc1\n"
    "v_cvt_pk_bf16_f32 v187, v158, v159\n"
    "v_exp_f32 v163, v163\n"
    "v_cvt_pk_bf16_f32 v188, v160, v161\n"
    "v_exp_f32 v164, v164\n"
    "v_cvt_pk_bf16_f32 v189, v162, v163\n"
    "v_exp_f32 v165, v165\n"
    "v_exp_f32 v166, v166\n"
    "v_exp_f32 v167, v167\n"
    "v_cvt_pk_bf16_f32 v190, v164, v165\n"
    "v_cvt_pk_bf16_f32 v191, v166, v167\n"
    "v_mfma_f32_32x32x16_bf16 v[152:167], a[70:73], v[0:3], v[24:39]\n"
    "v_exp_f32 v168, v168\n"
    "v_exp_f32 v169, v169\n"
    "v_exp_f32 v170, v170\n"
    "v_mfma_f32_32x32x8bf16_1k v[152:167], a[74:75], v[16:17], v[152:167]\n"
    "buffer_load_dwordx4 a[52:55], v244, s[44:47], s68 offen offset:640 sc1\n"
    "v_exp_f32 v171, v171\n"
    "v_cvt_pk_bf16_f32 v192, v168, v169\n"
    "v_exp_f32 v172, v172\n"
    "v_cvt_pk_bf16_f32 v193, v170, v171\n"
    "v_exp_f32 v173, v173\n"
    "v_exp_f32 v174, v174\n"
    "v_cvt_pk_bf16_f32 v194, v172, v173\n"
    "v_exp_f32 v175, v175\n"
    "v_mfma_f32_32x32x16_bf16 v[120:135], a[62:65], v[184:187], v[120:135]\n"
    "s_add_u32 m0, s57, 0\n"
    "v_exp_f32 v176, v176\n"
    "v_exp_f32 v177, v177\n"
    "v_exp_f32 v178, v178\n"
    "v_mfma_f32_32x32x16_bf16 v[120:135], a[66:69], v[188:191], v[120:135]\n"
    "buffer_load_dwordx4 v245, s[48:51], s58 offen lds\n"
    "v_cvt_pk_bf16_f32 v195, v174, v175\n"
    "v_exp_f32 v179, v179\n"
    "v_cvt_pk_bf16_f32 v196, v176, v177\n"
    "v_exp_f32 v180, v180\n"
    "v_cvt_pk_bf16_f32 v197, v178, v179\n"
    "v_exp_f32 v181, v181\n"
    "v_exp_f32 v182, v182\n"
    "v_exp_f32 v183, v183\n"
    "v_cvt_pk_bf16_f32 v198, v180, v181\n"
    "v_cvt_pk_bf16_f32 v199, v182, v183\n"
    "v_mfma_f32_32x32x16_bf16 v[168:183], a[70:73], v[4:7], v[40:55]\n"
    "s_add_u32 m0, s57, 36864\n"
    "v_exp_f32 v152, v152\n"
    "v_exp_f32 v153, v153\n"
    "v_exp_f32 v154, v154\n"
    "v_mfma_f32_32x32x8bf16_1k v[168:183], a[74:75], v[18:19], v[168:183]\n"
    "buffer_load_dwordx4 v245, s[48:51], s59 offen lds\n"
    "v_exp_f32 v155, v155\n"
    "v_cvt_pk_bf16_f32 v184, v152, v153\n"
    "v_exp_f32 v156, v156\n"
    "v_cvt_pk_bf16_f32 v185, v154, v155\n"
    "v_exp_f32 v157, v157\n"
    "v_exp_f32 v158, v158\n"
    "v_cvt_pk_bf16_f32 v186, v156, v157\n"
    "v_exp_f32 v159, v159\n"
    "v_mfma_f32_32x32x16_bf16 v[136:151], a[62:65], v[192:195], v[136:151]\n"
    "s_add_u32 m0, s57, 73728\n"
    "v_exp_f32 v160, v160\n"
    "v_exp_f32 v161, v161\n"
    "v_exp_f32 v162, v162\n"
    "v_mfma_f32_32x32x16_bf16 v[136:151], a[66:69], v[196:199], v[136:151]\n"
    "buffer_load_dwordx4 v245, s[48:51], s60 offen lds\n"
    "v_cvt_pk_bf16_f32 v187, v158, v159\n"
    "v_exp_f32 v163, v163\n"
    "v_cvt_pk_bf16_f32 v188, v160, v161\n"
    "v_exp_f32 v164, v164\n"
    "v_cvt_pk_bf16_f32 v189, v162, v163\n"
    "v_exp_f32 v165, v165\n"
    "v_exp_f32 v166, v166\n"
    "v_exp_f32 v167, v167\n"
    "v_cvt_pk_bf16_f32 v190, v164, v165\n"
    "v_cvt_pk_bf16_f32 v191, v166, v167\n"
    "v_mfma_f32_32x32x16_bf16 v[152:167], a[70:73], v[8:11], v[56:71]\n"
    "s_add_u32 s57, s57, 0x400\n"
    "v_exp_f32 v168, v168\n"
    "v_exp_f32 v169, v169\n"
    "v_exp_f32 v170, v170\n"
    "v_mfma_f32_32x32x8bf16_1k v[152:167], a[74:75], v[20:21], v[152:167]\n"
    "s_waitcnt vmcnt(30)\n"
    "v_exp_f32 v171, v171\n"
    "v_cvt_pk_bf16_f32 v192, v168, v169\n"
    "v_exp_f32 v172, v172\n"
    "v_cvt_pk_bf16_f32 v193, v170, v171\n"
    "v_exp_f32 v173, v173\n"
    "v_exp_f32 v174, v174\n"
    "v_cvt_pk_bf16_f32 v194, v172, v173\n"
    "v_exp_f32 v175, v175\n"
    "v_mfma_f32_32x32x16_bf16 v[88:103], a[76:79], v[184:187], v[88:103]\n"
    "s_add_u32 s68, s68, 2432\n"
    "v_exp_f32 v176, v176\n"
    "v_exp_f32 v177, v177\n"
    "v_exp_f32 v178, v178\n"
    "v_mfma_f32_32x32x16_bf16 v[88:103], a[80:83], v[188:191], v[88:103]\n"
    "buffer_load_dwordx4 a[56:59], v239, s[44:47], s68 offen sc1\n"
    "v_cvt_pk_bf16_f32 v195, v174, v175\n"
    "v_exp_f32 v179, v179\n"
    "v_cvt_pk_bf16_f32 v196, v176, v177\n"
    "v_exp_f32 v180, v180\n"
    "v_cvt_pk_bf16_f32 v197, v178, v179\n"
    "v_exp_f32 v181, v181\n"
    "v_exp_f32 v182, v182\n"
    "v_exp_f32 v183, v183\n"
    "v_cvt_pk_bf16_f32 v198, v180, v181\n"
    "v_cvt_pk_bf16_f32 v199, v182, v183\n"
    "v_mfma_f32_32x32x16_bf16 v[168:183], a[70:73], v[12:15], v[72:87]\n"
    "s_add_u32 s58, s58, 18432\n"
    "v_exp_f32 v152, v152\n"
    "v_exp_f32 v153, v153\n"
    "v_exp_f32 v154, v154\n"
    "v_mfma_f32_32x32x8bf16_1k v[168:183], a[74:75], v[22:23], v[168:183]\n"
    "buffer_load_dword a60, v243, s[44:47], s68 offen sc1\n"
    "v_exp_f32 v155, v155\n"
    "v_cvt_pk_bf16_f32 v184, v152, v153\n"
    "v_exp_f32 v156, v156\n"
    "v_cvt_pk_bf16_f32 v185, v154, v155\n"
    "v_exp_f32 v157, v157\n"
    "v_exp_f32 v158, v158\n"
    "v_cvt_pk_bf16_f32 v186, v156, v157\n"
    "v_exp_f32 v159, v159\n"
    "v_mfma_f32_32x32x16_bf16 v[104:119], a[76:79], v[192:195], v[104:119]\n"
    "s_add_u32 s59, s59, 18432\n"
    "v_exp_f32 v160, v160\n"
    "v_exp_f32 v161, v161\n"
    "v_exp_f32 v162, v162\n"
    "v_mfma_f32_32x32x16_bf16 v[104:119], a[80:83], v[196:199], v[104:119]\n"
    "s_waitcnt vmcnt(30)\n"
    "buffer_load_dwordx4 a[62:65], v244, s[44:47], s68 offen sc1\n"
    "v_cvt_pk_bf16_f32 v187, v158, v159\n"
    "v_exp_f32 v163, v163\n"
    "v_cvt_pk_bf16_f32 v188, v160, v161\n"
    "v_exp_f32 v164, v164\n"
    "v_cvt_pk_bf16_f32 v189, v162, v163\n"
    "v_exp_f32 v165, v165\n"
    "v_exp_f32 v166, v166\n"
    "v_exp_f32 v167, v167\n"
    "v_cvt_pk_bf16_f32 v190, v164, v165\n"
    "v_cvt_pk_bf16_f32 v191, v166, v167\n"
    "v_mfma_f32_32x32x16_bf16 v[152:167], a[84:87], v[0:3], v[24:39]\n"
    "s_add_u32 s60, s60, 18432\n"
    "v_exp_f32 v168, v168\n"
    "v_exp_f32 v169, v169\n"
    "v_exp_f32 v170, v170\n"
    "v_mfma_f32_32x32x8bf16_1k v[152:167], a[88:89], v[16:17], v[152:167]\n"
    "buffer_load_dwordx4 a[66:69], v244, s[44:47], s68 offen offset:640 sc1\n"
    "v_exp_f32 v171, v171\n"
    "v_cvt_pk_bf16_f32 v192, v168, v169\n"
    "v_exp_f32 v172, v172\n"
    "v_cvt_pk_bf16_f32 v193, v170, v171\n"
    "v_exp_f32 v173, v173\n"
    "v_exp_f32 v174, v174\n"
    "v_cvt_pk_bf16_f32 v194, v172, v173\n"
    "v_exp_f32 v175, v175\n"
    "v_mfma_f32_32x32x16_bf16 v[120:135], a[76:79], v[184:187], v[120:135]\n"
    "v_exp_f32 v176, v176\n"
    "v_exp_f32 v177, v177\n"
    "v_exp_f32 v178, v178\n"
    "v_mfma_f32_32x32x16_bf16 v[120:135], a[80:83], v[188:191], v[120:135]\n"
    "v_cvt_pk_bf16_f32 v195, v174, v175\n"
    "v_exp_f32 v179, v179\n"
    "v_cvt_pk_bf16_f32 v196, v176, v177\n"
    "v_exp_f32 v180, v180\n"
    "v_cvt_pk_bf16_f32 v197, v178, v179\n"
    "v_exp_f32 v181, v181\n"
    "v_exp_f32 v182, v182\n"
    "v_exp_f32 v183, v183\n"
    "v_cvt_pk_bf16_f32 v198, v180, v181\n"
    "v_cvt_pk_bf16_f32 v199, v182, v183\n"
    "v_mfma_f32_32x32x16_bf16 v[168:183], a[84:87], v[4:7], v[40:55]\n"
    "s_lshl_b32 s67, %12, 2\n"
    "s_add_u32 s67, s67, %7\n"
    "v_exp_f32 v152, v152\n"
    "v_exp_f32 v153, v153\n"
    "v_exp_f32 v154, v154\n"
    "v_mfma_f32_32x32x8bf16_1k v[168:183], a[88:89], v[18:19], v[168:183]\n"
    "s_mul_i32 s67, s67, 36864\n"
    "s_lshl_b32 s73, s56, 6\n"
    "v_exp_f32 v155, v155\n"
    "v_cvt_pk_bf16_f32 v184, v152, v153\n"
    "v_exp_f32 v156, v156\n"
    "v_cvt_pk_bf16_f32 v185, v154, v155\n"
    "v_exp_f32 v157, v157\n"
    "v_exp_f32 v158, v158\n"
    "v_cvt_pk_bf16_f32 v186, v156, v157\n"
    "v_exp_f32 v159, v159\n"
    "v_mfma_f32_32x32x16_bf16 v[136:151], a[76:79], v[192:195], v[136:151]\n"
    "s_add_u32 s73, s73, s67\n"
    "s_add_u32 m0, s61, 2048\n"
    "v_exp_f32 v160, v160\n"
    "v_exp_f32 v161, v161\n"
    "v_exp_f32 v162, v162\n"
    "v_mfma_f32_32x32x16_bf16 v[136:151], a[80:83], v[196:199], v[136:151]\n"
    "s_add_u32 s73, s73, 0\n"
    "buffer_load_dwordx4 v245, s[36:39], s73 offen lds\n"
    "v_cvt_pk_bf16_f32 v187, v158, v159\n"
    "v_exp_f32 v163, v163\n"
    "v_cvt_pk_bf16_f32 v188, v160, v161\n"
    "v_exp_f32 v164, v164\n"
    "v_cvt_pk_bf16_f32 v189, v162, v163\n"
    "v_exp_f32 v165, v165\n"
    "v_exp_f32 v166, v166\n"
    "v_exp_f32 v167, v167\n"
    "v_cvt_pk_bf16_f32 v190, v164, v165\n"
    "v_cvt_pk_bf16_f32 v191, v166, v167\n"
    "v_mfma_f32_32x32x16_bf16 v[152:167], a[84:87], v[8:11], v[56:71]\n"
    "v_exp_f32 v168, v168\n"
    "v_exp_f32 v169, v169\n"
    "v_exp_f32 v170, v170\n"
    "v_mfma_f32_32x32x8bf16_1k v[152:167], a[88:89], v[20:21], v[152:167]\n"
    "s_waitcnt vmcnt(31)\n"
    "v_exp_f32 v171, v171\n"
    "v_cvt_pk_bf16_f32 v192, v168, v169\n"
    "v_exp_f32 v172, v172\n"
    "v_cvt_pk_bf16_f32 v193, v170, v171\n"
    "v_exp_f32 v173, v173\n"
    "v_exp_f32 v174, v174\n"
    "v_cvt_pk_bf16_f32 v194, v172, v173\n"
    "v_exp_f32 v175, v175\n"
    "v_mfma_f32_32x32x16_bf16 v[88:103], a[90:93], v[184:187], v[88:103]\n"
    "s_add_u32 s68, s68, 2432\n"
    "v_exp_f32 v176, v176\n"
    "v_exp_f32 v177, v177\n"
    "v_exp_f32 v178, v178\n"
    "v_mfma_f32_32x32x16_bf16 v[88:103], a[94:97], v[188:191], v[88:103]\n"
    "buffer_load_dwordx4 a[70:73], v239, s[44:47], s68 offen sc1\n"
    "v_cvt_pk_bf16_f32 v195, v174, v175\n"
    "v_exp_f32 v179, v179\n"
    "v_cvt_pk_bf16_f32 v196, v176, v177\n"
    "v_exp_f32 v180, v180\n"
    "v_cvt_pk_bf16_f32 v197, v178, v179\n"
    "v_exp_f32 v181, v181\n"
    "v_exp_f32 v182, v182\n"
    "v_exp_f32 v183, v183\n"
    "v_cvt_pk_bf16_f32 v198, v180, v181\n"
    "v_cvt_pk_bf16_f32 v199, v182, v183\n"
    "v_mfma_f32_32x32x16_bf16 v[168:183], a[84:87], v[12:15], v[72:87]\n"
    "v_exp_f32 v152, v152\n"
    "v_exp_f32 v153, v153\n"
    "v_exp_f32 v154, v154\n"
    "v_mfma_f32_32x32x8bf16_1k v[168:183], a[88:89], v[22:23], v[168:183]\n"
    "buffer_load_dword a74, v243, s[44:47], s68 offen sc1\n"
    "v_exp_f32 v155, v155\n"
    "v_cvt_pk_bf16_f32 v184, v152, v153\n"
    "v_exp_f32 v156, v156\n"
    "v_cvt_pk_bf16_f32 v185, v154, v155\n"
    "v_exp_f32 v157, v157\n"
    "v_exp_f32 v158, v158\n"
    "v_cvt_pk_bf16_f32 v186, v156, v157\n"
    "v_exp_f32 v159, v159\n"
    "v_mfma_f32_32x32x16_bf16 v[104:119], a[90:93], v[192:195], v[104:119]\n"
    "v_exp_f32 v160, v160\n"
    "v_exp_f32 v161, v161\n"
    "v_exp_f32 v162, v162\n"
    "v_mfma_f32_32x32x16_bf16 v[104:119], a[94:97], v[196:199], v[104:119]\n"
    "s_waitcnt vmcnt(31)\n"
    "buffer_load_dwordx4 a[76:79], v244, s[44:47], s68 offen sc1\n"
    "v_cvt_pk_bf16_f32 v187, v158, v159\n"
    "v_exp_f32 v163, v163\n"
    "v_cvt_pk_bf16_f32 v188, v160, v161\n"
    "v_exp_f32 v164, v164\n"
    "v_cvt_pk_bf16_f32 v189, v162, v163\n"
    "v_exp_f32 v165, v165\n"
    "v_exp_f32 v166, v166\n"
    "v_exp_f32 v167, v167\n"
    "v_cvt_pk_bf16_f32 v190, v164, v165\n"
    "v_cvt_pk_bf16_f32 v191, v166, v167\n"
    "v_mfma_f32_32x32x16_bf16 v[152:167], a[98:101], v[0:3], v[24:39]\n"
    "v_exp_f32 v168, v168\n"
    "v_exp_f32 v169, v169\n"
    "v_exp_f32 v170, v170\n"
    "v_mfma_f32_32x32x8bf16_1k v[152:167], a[102:103], v[16:17], v[152:167]\n"
    "buffer_load_dwordx4 a[80:83], v244, s[44:47], s68 offen offset:640 sc1\n"
    "v_exp_f32 v171, v171\n"
    "v_cvt_pk_bf16_f32 v192, v168, v169\n"
    "v_exp_f32 v172, v172\n"
    "v_cvt_pk_bf16_f32 v193, v170, v171\n"
    "v_exp_f32 v173, v173\n"
    "v_exp_f32 v174, v174\n"
    "v_cvt_pk_bf16_f32 v194, v172, v173\n"
    "v_exp_f32 v175, v175\n"
    "v_mfma_f32_32x32x16_bf16 v[120:135], a[90:93], v[184:187], v[120:135]\n"
    "v_exp_f32 v176, v176\n"
    "v_exp_f32 v177, v177\n"
    "v_exp_f32 v178, v178\n"
    "v_mfma_f32_32x32x16_bf16 v[120:135], a[94:97], v[188:191], v[120:135]\n"
    "v_cvt_pk_bf16_f32 v195, v174, v175\n"
    "v_exp_f32 v179, v179\n"
    "v_cvt_pk_bf16_f32 v196, v176, v177\n"
    "v_exp_f32 v180, v180\n"
    "v_cvt_pk_bf16_f32 v197, v178, v179\n"
    "v_exp_f32 v181, v181\n"
    "v_exp_f32 v182, v182\n"
    "v_exp_f32 v183, v183\n"
    "v_cvt_pk_bf16_f32 v198, v180, v181\n"
    "v_cvt_pk_bf16_f32 v199, v182, v183\n"
    "v_mfma_f32_32x32x16_bf16 v[168:183], a[98:101], v[4:7], v[40:55]\n"
    "s_add_u32 m0, %8, 147456\n"
    "v_exp_f32 v152, v152\n"
    "v_exp_f32 v153, v153\n"
    "v_exp_f32 v154, v154\n"
    "v_mfma_f32_32x32x8bf16_1k v[168:183], a[102:103], v[18:19], v[168:183]\n"
    "global_load_lds_dword v[236:237], off\n"
    "v_exp_f32 v155, v155\n"
    "v_cvt_pk_bf16_f32 v184, v152, v153\n"
    "v_exp_f32 v156, v156\n"
    "v_cvt_pk_bf16_f32 v185, v154, v155\n"
    "v_exp_f32 v157, v157\n"
    "v_exp_f32 v158, v158\n"
    "v_cvt_pk_bf16_f32 v186, v156, v157\n"
    "v_exp_f32 v159, v159\n"
    "v_mfma_f32_32x32x16_bf16 v[136:151], a[90:93], v[192:195], v[136:151]\n"
    "v_exp_f32 v160, v160\n"
    "v_exp_f32 v161, v161\n"
    "v_exp_f32 v162, v162\n"
    "v_mfma_f32_32x32x16_bf16 v[136:151], a[94:97], v[196:199], v[136:151]\n"
    "v_add_co_u32 v236, vcc, v238, v236\n"
    "v_cvt_pk_bf16_f32 v187, v158, v159\n"
    "v_exp_f32 v163, v163\n"
    "v_cvt_pk_bf16_f32 v188, v160, v161\n"
    "v_exp_f32 v164, v164\n"
    "v_cvt_pk_bf16_f32 v189, v162, v163\n"
    "v_exp_f32 v165, v165\n"
    "v_exp_f32 v166, v166\n"
    "v_exp_f32 v167, v167\n"
    "v_cvt_pk_bf16_f32 v190, v164, v165\n"
    "v_cvt_pk_bf16_f32 v191, v166, v167\n"
    "v_mfma_f32_32x32x16_bf16 v[152:167], a[98:101], v[8:11], v[56:71]\n"
    "v_exp_f32 v168, v168\n"
    "v_exp_f32 v169, v169\n"
    "v_exp_f32 v170, v170\n"
    "v_mfma_f32_32x32x8bf16_1k v[152:167], a[102:103], v[20:21], v[152:167]\n"
    "s_waitcnt vmcnt(32)\n"
    "v_addc_co_u32 v237, vcc, 0, v237, vcc\n"
    "v_exp_f32 v171, v171\n"
    "v_cvt_pk_bf16_f32 v192, v168, v169\n"
    "v_exp_f32 v172, v172\n"
    "v_cvt_pk_bf16_f32 v193, v170, v171\n"
    "v_exp_f32 v173, v173\n"
    "v_exp_f32 v174, v174\n"
    "v_cvt_pk_bf16_f32 v194, v172, v173\n"
    "v_exp_f32 v175, v175\n"
    "v_mfma_f32_32x32x16_bf16 v[88:103], a[104:107], v[184:187], v[88:103]\n"
    "s_add_u32 s68, s68, 2432\n"
    "v_exp_f32 v176, v176\n"
    "v_exp_f32 v177, v177\n"
    "v_exp_f32 v178, v178\n"
    "v_mfma_f32_32x32x16_bf16 v[88:103], a[108:111], v[188:191], v[88:103]\n"
    "buffer_load_dwordx4 a[84:87], v239, s[44:47], s68 offen sc1\n"
    "v_cvt_pk_bf16_f32 v195, v174, v175\n"
    "v_exp_f32 v179, v179\n"
    "v_cvt_pk_bf16_f32 v196, v176, v177\n"
    "v_exp_f32 v180, v180\n"
    "v_cvt_pk_bf16_f32 v197, v178, v179\n"
    "v_exp_f32 v181, v181\n"
    "v_exp_f32 v182, v182\n"
    "v_exp_f32 v183, v183\n"
    "v_cvt_pk_bf16_f32 v198, v180, v181\n"
    "v_cvt_pk_bf16_f32 v199, v182, v183\n"
    "v_mfma_f32_32x32x16_bf16 v[168:183], a[98:101], v[12:15], v[72:87]\n"
    "v_exp_f32 v152, v152\n"
    "v_exp_f32 v153, v153\n"
    "v_exp_f32 v154, v154\n"
    "v_mfma_f32_32x32x8bf16_1k v[168:183], a[102:103], v[22:23], v[168:183]\n"
    "buffer_load_dword a88, v243, s[44:47], s68 offen sc1\n"
    "v_exp_f32 v155, v155\n"
    "v_cvt_pk_bf16_f32 v184, v152, v153\n"
    "v_exp_f32 v156, v156\n"
    "v_cvt_pk_bf16_f32 v185, v154, v155\n"
    "v_exp_f32 v157, v157\n"
    "v_exp_f32 v158, v158\n"
    "v_cvt_pk_bf16_f32 v186, v156, v157\n"
    "v_exp_f32 v159, v159\n"
    "v_mfma_f32_32x32x16_bf16 v[104:119], a[104:107], v[192:195], v[104:119]\n"
    "v_exp_f32 v160, v160\n"
    "v_exp_f32 v161, v161\n"
    "v_exp_f32 v162, v162\n"
    "v_mfma_f32_32x32x16_bf16 v[104:119], a[108:111], v[196:199], v[104:119]\n"
    "s_waitcnt vmcnt(32)\n"
    "buffer_load_dwordx4 a[90:93], v244, s[44:47], s68 offen sc1\n"
    "v_cvt_pk_bf16_f32 v187, v158, v159\n"
    "v_exp_f32 v163, v163\n"
    "v_cvt_pk_bf16_f32 v188, v160, v161\n"
    "v_exp_f32 v164, v164\n"
    "v_cvt_pk_bf16_f32 v189, v162, v163\n"
    "v_exp_f32 v165, v165\n"
    "v_exp_f32 v166, v166\n"
    "v_exp_f32 v167, v167\n"
    "v_cvt_pk_bf16_f32 v190, v164, v165\n"
    "v_cvt_pk_bf16_f32 v191, v166, v167\n"
    "v_mfma_f32_32x32x16_bf16 v[152:167], a[0:3], v[0:3], v[24:39]\n"
    "v_exp_f32 v168, v168\n"
    "v_exp_f32 v169, v169\n"
    "v_exp_f32 v170, v170\n"
    "v_mfma_f32_32x32x8bf16_1k v[152:167], a[4:5], v[16:17], v[152:167]\n"
    "buffer_load_dwordx4 a[94:97], v244, s[44:47], s68 offen offset:640 sc1\n"
    "v_exp_f32 v171, v171\n"
    "v_cvt_pk_bf16_f32 v192, v168, v169\n"
    "v_exp_f32 v172, v172\n"
    "v_cvt_pk_bf16_f32 v193, v170, v171\n"
    "v_exp_f32 v173, v173\n"
    "v_exp_f32 v174, v174\n"
    "v_cvt_pk_bf16_f32 v194, v172, v173\n"
    "v_exp_f32 v175, v175\n"
    "v_mfma_f32_32x32x16_bf16 v[120:135], a[104:107], v[184:187], v[120:135]\n"
    "v_exp_f32 v176, v176\n"
    "v_exp_f32 v177, v177\n"
    "v_exp_f32 v178, v178\n"
    "v_mfma_f32_32x32x16_bf16 v[120:135], a[108:111], v[188:191], v[120:135]\n"
    "v_cvt_pk_bf16_f32 v195, v174, v175\n"
    "v_exp_f32 v179, v179\n"
    "v_cvt_pk_bf16_f32 v196, v176, v177\n"
    "v_exp_f32 v180, v180\n"
    "v_cvt_pk_bf16_f32 v197, v178, v179\n"
    "v_exp_f32 v181, v181\n"
    "v_exp_f32 v182, v182\n"
    "v_exp_f32 v183, v183\n"
    "v_cvt_pk_bf16_f32 v198, v180, v181\n"
    "v_cvt_pk_bf16_f32 v199, v182, v183\n"
    "s_mov_b32 s66, 4\n"
    "2:\n"
    "v_mfma_f32_32x32x16_bf16 v[168:183], a[0:3], v[4:7], v[40:55]\n"
    "v_exp_f32 v152, v152\n"
    "v_exp_f32 v153, v153\n"
    "v_exp_f32 v154, v154\n"
    "v_mfma_f32_32x32x8bf16_1k v[168:183], a[4:5], v[18:19], v[168:183]\n"
    "v_exp_f32 v155, v155\n"
    "v_cvt_pk_bf16_f32 v184, v152, v153\n"
    "v_exp_f32 v156, v156\n"
    "v_cvt_pk_bf16_f32 v185, v154, v155\n"
    "v_exp_f32 v157, v157\n"
    "v_exp_f32 v158, v158\n"
    "v_cvt_pk_bf16_f32 v186, v156, v157\n"
    "v_exp_f32 v159, v159\n"
    "v_mfma_f32_32x32x16_bf16 v[136:151], a[104:107], v[192:195], v[136:151]\n"
    "v_exp_f32 v160, v160\n"
    "v_exp_f32 v161, v161\n"
    "v_exp_f32 v162, v162\n"
    "v_mfma_f32_32x32x16_bf16 v[136:151], a[108:111], v[196:199], v[136:151]\n"
    "v_cvt_pk_bf16_f32 v187, v158, v159\n"
    "v_exp_f32 v163, v163\n"
    "v_cvt_pk_bf16_f32 v188, v160, v161\n"
    "v_exp_f32 v164, v164\n"
    "v_cvt_pk_bf16_f32 v189, v162, v163\n"
    "v_exp_f32 v165, v165\n"
    "v_exp_f32 v166, v166\n"
    "v_exp_f32 v167, v167\n"
    "v_cvt_pk_bf16_f32 v190, v164, v165\n"
    "v_cvt_pk_bf16_f32 v191, v166, v167\n"
    "v_mfma_f32_32x32x16_bf16 v[152:167], a[0:3], v[8:11], v[56:71]\n"
    "v_exp_f32 v168, v168\n"
    "v_exp_f32 v169, v169\n"
    "v_exp_f32 v170, v170\n"
    "v_mfma_f32_32x32x8bf16_1k v[152:167], a[4:5], v[20:21], v[152:167]\n"
    "s_waitcnt vmcnt(28)\n"
    "v_exp_f32 v171, v171\n"
    "v_cvt_pk_bf16_f32 v192, v168, v169\n"
    "v_exp_f32 v172, v172\n"
    "v_cvt_pk_bf16_f32 v193, v170, v171\n"
    "v_exp_f32 v173, v173\n"
    "v_exp_f32 v174, v174\n"
    "v_cvt_pk_bf16_f32 v194, v172, v173\n"
    "v_exp_f32 v175, v175\n"
    "v_mfma_f32_32x32x16_bf16 v[88:103], a[6:9], v[184:187], v[88:103]\n"
    "s_add_u32 s68, s68, 2432\n"
    "v_exp_f32 v176, v176\n"
    "v_exp_f32 v177, v177\n"
    "v_exp_f32 v178, v178\n"
    "v_mfma_f32_32x32x16_bf16 v[88:103], a[10:13], v[188:191], v[88:103]\n"
    "buffer_load_dwordx4 a[98:101], v239, s[44:47], s68 offen sc1\n"
    "v_cvt_pk_bf16_f32 v195, v174, v175\n"
    "v_exp_f32 v179, v179\n"
    "v_cvt_pk_bf16_f32 v196, v176, v177\n"
    "v_exp_f32 v180, v180\n"
    "v_cvt_pk_bf16_f32 v197, v178, v179\n"
    "v_exp_f32 v181, v181\n"
    "v_exp_f32 v182, v182\n"
    "v_exp_f32 v183, v183\n"
    "v_cvt_pk_bf16_f32 v198, v180, v181\n"
    "v_cvt_pk_bf16_f32 v199, v182, v183\n"
    "v_mfma_f32_32x32x16_bf16 v[168:183], a[0:3], v[12:15], v[72:87]\n"
    "v_exp_f32 v152, v152\n"
    "v_exp_f32 v153, v153\n"
    "v_exp_f32 v154, v154\n"
    "v_mfma_f32_32x32x8bf16_1k v[168:183], a[4:5], v[22:23], v[168:183]\n"
    "buffer_load_dword a102, v243, s[44:47], s68 offen sc1\n"
    "v_exp_f32 v155, v155\n"
    "v_cvt_pk_bf16_f32 v184, v152, v153\n"
    "v_exp_f32 v156, v156\n"
    "v_cvt_pk_bf16_f32 v185, v154, v155\n"
    "v_exp_f32 v157, v157\n"
    "v_exp_f32 v158, v158\n"
    "v_cvt_pk_bf16_f32 v186, v156, v157\n"
    "v_exp_f32 v159, v159\n"
    "v_mfma_f32_32x32x16_bf16 v[104:119], a[6:9], v[192:195], v[104:119]\n"
    "v_exp_f32 v160, v160\n"
    "v_exp_f32 v161, v161\n"
    "v_exp_f32 v162, v162\n"
    "v_mfma_f32_32x32x16_bf16 v[104:119], a[10:13], v[196:199], v[104:119]\n"
    "s_waitcnt vmcnt(28)\n"
    "buffer_load_dwordx4 a[104:107], v244, s[44:47], s68 offen sc1\n"
    "v_cvt_pk_bf16_f32 v187, v158, v159\n"
    "v_exp_f32 v163, v163\n"
    "v_cvt_pk_bf16_f32 v188, v160, v161\n"
    "v_exp_f32 v164, v164\n"
    "v_cvt_pk_bf16_f32 v189, v162, v163\n"
    "v_exp_f32 v165, v165\n"
    "v_exp_f32 v166, v166\n"
    "v_exp_f32 v167, v167\n"
    "v_cvt_pk_bf16_f32 v190, v164, v165\n"
    "v_cvt_pk_bf16_f32 v191, v166, v167\n"
    "v_mfma_f32_32x32x16_bf16 v[152:167], a[14:17], v[0:3], v[24:39]\n"
    "v_exp_f32 v168, v168\n"
    "v_exp_f32 v169, v169\n"
    "v_exp_f32 v170, v170\n"
    "v_mfma_f32_32x32x8bf16_1k v[152:167], a[18:19], v[16:17], v[152:167]\n"
    "buffer_load_dwordx4 a[108:111], v244, s[44:47], s68 offen offset:640 sc1\n"
    "v_exp_f32 v171, v171\n"
    "v_cvt_pk_bf16_f32 v192, v168, v169\n"
    "v_exp_f32 v172, v172\n"
    "v_cvt_pk_bf16_f32 v193, v170, v171\n"
    "v_exp_f32 v173, v173\n"
    "v_exp_f32 v174, v174\n"
    "v_cvt_pk_bf16_f32 v194, v172, v173\n"
    "v_exp_f32 v175, v175\n"
    "v_mfma_f32_32x32x16_bf16 v[120:135], a[6:9], v[184:187], v[120:135]\n"
    "s_add_u32 m0, s57, 0\n"
    "v_exp_f32 v176, v176\n"
    "v_exp_f32 v177, v177\n"
    "v_exp_f32 v178, v178\n"
    "v_mfma_f32_32x32x16_bf16 v[120:135], a[10:13], v[188:191], v[120:135]\n"
    "buffer_load_dwordx4 v245, s[48:51], s58 offen lds\n"
    "v_cvt_pk_bf16_f32 v195, v174, v175\n"
    "v_exp_f32 v179, v179\n"
    "v_cvt_pk_bf16_f32 v196, v176, v177\n"
    "v_exp_f32 v180, v180\n"
    "v_cvt_pk_bf16_f32 v197, v178, v179\n"
    "v_exp_f32 v181, v181\n"
    "v_exp_f32 v182, v182\n"
    "v_exp_f32 v183, v183\n"
    "v_cvt_pk_bf16_f32 v198, v180, v181\n"
    "v_cvt_pk_bf16_f32 v199, v182, v183\n"
    "v_mfma_f32_32x32x16_bf16 v[168:183], a[14:17], v[4:7], v[40:55]\n"
    "s_add_u32 m0, s57, 36864\n"
    "v_exp_f32 v152, v152\n"
    "v_exp_f32 v153, v153\n"
    "v_exp_f32 v154, v154\n"
    "v_mfma_f32_32x32x8bf16_1k v[168:183], a[18:19], v[18:19], v[168:183]\n"
    "buffer_load_dwordx4 v245, s[48:51], s59 offen lds\n"
    "v_exp_f32 v155, v155\n"
    "v_cvt_pk_bf16_f32 v184, v152, v153\n"
    "v_exp_f32 v156, v156\n"
    "v_cvt_pk_bf16_f32 v185, v154, v155\n"
    "v_exp_f32 v157, v157\n"
    "v_exp_f32 v158, v158\n"
    "v_cvt_pk_bf16_f32 v186, v156, v157\n"
    "v_exp_f32 v159, v159\n"
    "v_mfma_f32_32x32x16_bf16 v[136:151], a[6:9], v[192:195], v[136:151]\n"
    "s_add_u32 m0, s57, 73728\n"
    "v_exp_f32 v160, v160\n"
    "v_exp_f32 v161, v161\n"
    "v_exp_f32 v162, v162\n"
    "v_mfma_f32_32x32x16_bf16 v[136:151], a[10:13], v[196:199], v[136:151]\n"
    "buffer_load_dwordx4 v245, s[48:51], s60 offen lds\n"
    "v_cvt_pk_bf16_f32 v187, v158, v159\n"
    "v_exp_f32 v163, v163\n"
    "v_cvt_pk_bf16_f32 v188, v160, v161\n"
    "v_exp_f32 v164, v164\n"
    "v_cvt_pk_bf16_f32 v189, v162, v163\n"
    "v_exp_f32 v165, v165\n"
    "v_exp_f32 v166, v166\n"
    "v_exp_f32 v167, v167\n"
    "v_cvt_pk_bf16_f32 v190, v164, v165\n"
    "v_cvt_pk_bf16_f32 v191, v166, v167\n"
    "v_mfma_f32_32x32x16_bf16 v[152:167], a[14:17], v[8:11], v[56:71]\n"
    "s_add_u32 s57, s57, 0x400\n"
    "v_exp_f32 v168, v168\n"
    "v_exp_f32 v169, v169\n"
    "v_exp_f32 v170, v170\n"
    "v_mfma_f32_32x32x8bf16_1k v[152:167], a[18:19], v[20:21], v[152:167]\n"
    "s_waitcnt vmcnt(31)\n"
    "v_exp_f32 v171, v171\n"
    "v_cvt_pk_bf16_f32 v192, v168, v169\n"
    "v_exp_f32 v172, v172\n"
    "v_cvt_pk_bf16_f32 v193, v170, v171\n"
    "v_exp_f32 v173, v173\n"
    "v_exp_f32 v174, v174\n"
    "v_cvt_pk_bf16_f32 v194, v172, v173\n"
    "v_exp_f32 v175, v175\n"
    "v_mfma_f32_32x32x16_bf16 v[88:103], a[20:23], v[184:187], v[88:103]\n"
    "s_add_u32 s68, s68, 2432\n"
    "v_exp_f32 v176, v176\n"
    "v_exp_f32 v177, v177\n"
    "v_exp_f32 v178, v178\n"
    "v_mfma_f32_32x32x16_bf16 v[88:103], a[24:27], v[188:191], v[88:103]\n"
    "buffer_load_dwordx4 a[0:3], v239, s[44:47], s68 offen sc1\n"
    "v_cvt_pk_bf16_f32 v195, v174, v175\n"
    "v_exp_f32 v179, v179\n"
    "v_cvt_pk_bf16_f32 v196, v176, v177\n"
    "v_exp_f32 v180, v180\n"
    "v_cvt_pk_bf16_f32 v197, v178, v179\n"
    "v_exp_f32 v181, v181\n"
    "v_exp_f32 v182, v182\n"
    "v_exp_f32 v183, v183\n"
    "v_cvt_pk_bf16_f32 v198, v180, v181\n"
    "v_cvt_pk_bf16_f32 v199, v182, v183\n"
    "v_mfma_f32_32x32x16_bf16 v[168:183], a[14:17], v[12:15], v[72:87]\n"
    "s_add_u32 s58, s58, 18432\n"
    "v_exp_f32 v152, v152\n"
    "v_exp_f32 v153, v153\n"
    "v_exp_f32 v154, v154\n"
    "v_mfma_f32_32x32x8bf16_1k v[168:183], a[18:19], v[22:23], v[168:183]\n"
    "buffer_load_dword a4, v243, s[44:47], s68 offen sc1\n"
    "v_exp_f32 v155, v155\n"
    "v_cvt_pk_bf16_f32 v184, v152, v153\n"
    "v_exp_f32 v156, v156\n"
    "v_cvt_pk_bf16_f32 v185, v154, v155\n"
    "v_exp_f32 v157, v157\n"
    "v_exp_f32 v158, v158\n"
    "v_cvt_pk_bf16_f32 v186, v156, v157\n"
    "v_exp_f32 v159, v159\n"
    "v_mfma_f32_32x32x16_bf16 v[104:119], a[20:23], v[192:195], v[104:119]\n"
    "s_add_u32 s59, s59, 18432\n"
    "v_exp_f32 v160, v160\n"
    "v_exp_f32 v161, v161\n"
    "v_exp_f32 v162, v162\n"
    "v_mfma_f32_32x32x16_bf16 v[104:119], a[24:27], v[196:199], v[104:119]\n"
    "s_waitcnt vmcnt(30)\n"
    "buffer_load_dwordx4 a[6:9], v244, s[44:47], s68 offen sc1\n"
    "v_cvt_pk_bf16_f32 v187, v158, v159\n"
    "v_exp_f32 v163, v163\n"
    "v_cvt_pk_bf16_f32 v188, v160, v161\n"
    "v_exp_f32 v164, v164\n"
    "v_cvt_pk_bf16_f32 v189, v162, v163\n"
    "v_exp_f32 v165, v165\n"
    "v_exp_f32 v166, v166\n"
    "v_exp_f32 v167, v167\n"
    "v_cvt_pk_bf16_f32 v190, v164, v165\n"
    "v_cvt_pk_bf16_f32 v191, v166, v167\n"
    "v_mfma_f32_32x32x16_bf16 v[152:167], a[28:31], v[0:3], v[24:39]\n"
    "s_add_u32 s60, s60, 18432\n"
    "v_exp_f32 v168, v168\n"
    "v_exp_f32 v169, v169\n"
    "v_exp_f32 v170, v170\n"
    "v_mfma_f32_32x32x8bf16_1k v[152:167], a[32:33], v[16:17], v[152:167]\n"
    "buffer_load_dwordx4 a[10:13], v244, s[44:47], s68 offen offset:640 sc1\n"
    "v_exp_f32 v171, v171\n"
    "v_cvt_pk_bf16_f32 v192, v168, v169\n"
    "v_exp_f32 v172, v172\n"
    "v_cvt_pk_bf16_f32 v193, v170, v171\n"
    "v_exp_f32 v173, v173\n"
    "v_exp_f32 v174, v174\n"
    "v_cvt_pk_bf16_f32 v194, v172, v173\n"
    "v_exp_f32 v175, v175\n"
    "v_mfma_f32_32x32x16_bf16 v[120:135], a[20:23], v[184:187], v[120:135]\n"
    "v_exp_f32 v176, v176\n"
    "v_exp_f32 v177, v177\n"
    "v_exp_f32 v178, v178\n"
    "v_mfma_f32_32x32x16_bf16 v[120:135], a[24:27], v[188:191], v[120:135]\n"
    "v_cvt_pk_bf16_f32 v195, v174, v175\n"
    "v_exp_f32 v179, v179\n"
    "v_cvt_pk_bf16_f32 v196, v176, v177\n"
    "v_exp_f32 v180, v180\n"
    "v_cvt_pk_bf16_f32 v197, v178, v179\n"
    "v_exp_f32 v181, v181\n"
    "v_exp_f32 v182, v182\n"
    "v_exp_f32 v183, v183\n"
    "v_cvt_pk_bf16_f32 v198, v180, v181\n"
    "v_cvt_pk_bf16_f32 v199, v182, v183\n"
    "v_mfma_f32_32x32x16_bf16 v[168:183], a[28:31], v[4:7], v[40:55]\n"
    "v_exp_f32 v152, v152\n"
    "v_exp_f32 v153, v153\n"
    "v_exp_f32 v154, v154\n"
    "v_mfma_f32_32x32x8bf16_1k v[168:183], a[32:33], v[18:19], v[168:183]\n"
    "v_exp_f32 v155, v155\n"
    "v_cvt_pk_bf16_f32 v184, v152, v153\n"
    "v_exp_f32 v156, v156\n"
    "v_cvt_pk_bf16_f32 v185, v154, v155\n"
    "v_exp_f32 v157, v157\n"
    "v_exp_f32 v158, v158\n"
    "v_cvt_pk_bf16_f32 v186, v156, v157\n"
    "v_exp_f32 v159, v159\n"
    "v_mfma_f32_32x32x16_bf16 v[136:151], a[20:23], v[192:195], v[136:151]\n"
    "v_exp_f32 v160, v160\n"
    "v_exp_f32 v161, v161\n"
    "v_exp_f32 v162, v162\n"
    "v_mfma_f32_32x32x16_bf16 v[136:151], a[24:27], v[196:199], v[136:151]\n"
    "v_cvt_pk_bf16_f32 v187, v158, v159\n"
    "v_exp_f32 v163, v163\n"
    "v_cvt_pk_bf16_f32 v188, v160, v161\n"
    "v_exp_f32 v164, v164\n"
    "v_cvt_pk_bf16_f32 v189, v162, v163\n"
    "v_exp_f32 v165, v165\n"
    "v_exp_f32 v166, v166\n"
    "v_exp_f32 v167, v167\n"
    "v_cvt_pk_bf16_f32 v190, v164, v165\n"
    "v_cvt_pk_bf16_f32 v191, v166, v167\n"
    "v_mfma_f32_32x32x16_bf16 v[152:167], a[28:31], v[8:11], v[56:71]\n"
    "v_exp_f32 v168, v168\n"
    "v_exp_f32 v169, v169\n"
    "v_exp_f32 v170, v170\n"
    "v_mfma_f32_32x32x8bf16_1k v[152:167], a[32:33], v[20:21], v[152:167]\n"
    "s_waitcnt vmcnt(30)\n"
    "v_exp_f32 v171, v171\n"
    "v_cvt_pk_bf16_f32 v192, v168, v169\n"
    "v_exp_f32 v172, v172\n"
    "v_cvt_pk_bf16_f32 v193, v170, v171\n"
    "v_exp_f32 v173, v173\n"
    "v_exp_f32 v174, v174\n"
    "v_cvt_pk_bf16_f32 v194, v172, v173\n"
    "v_exp_f32 v175, v175\n"
    "v_mfma_f32_32x32x16_bf16 v[88:103], a[34:37], v[184:187], v[88:103]\n"
    "s_add_u32 s68, s68, 2432\n"
    "v_exp_f32 v176, v176\n"
    "v_exp_f32 v177, v177\n"
    "v_exp_f32 v178, v178\n"
    "v_mfma_f32_32x32x16_bf16 v[88:103], a[38:41], v[188:191], v[88:103]\n"
    "buffer_load_dwordx4 a[14:17], v239, s[44:47], s68 offen sc1\n"
    "v_cvt_pk_bf16_f32 v195, v174, v175\n"
    "v_exp_f32 v179, v179\n"
    "v_cvt_pk_bf16_f32 v196, v176, v177\n"
    "v_exp_f32 v180, v180\n"
    "v_cvt_pk_bf16_f32 v197, v178, v179\n"
    "v_exp_f32 v181, v181\n"
    "v_exp_f32 v182, v182\n"
    "v_exp_f32 v183, v183\n"
    "v_cvt_pk_bf16_f32 v198, v180, v181\n"
    "v_cvt_pk_bf16_f32 v199, v182, v183\n"
    "v_mfma_f32_32x32x16_bf16 v[168:183], a[28:31], v[12:15], v[72:87]\n"
    "v_exp_f32 v152, v152\n"
    "v_exp_f32 v153, v153\n"
    "v_exp_f32 v154, v154\n"
    "v_mfma_f32_32x32x8bf16_1k v[168:183], a[32:33], v[22:23], v[168:183]\n"
    "buffer_load_dword a18, v243, s[44:47], s68 offen sc1\n"
    "v_exp_f32 v155, v155\n"
    "v_cvt_pk_bf16_f32 v184, v152, v153\n"
    "v_exp_f32 v156, v156\n"
    "v_cvt_pk_bf16_f32 v185, v154, v155\n"
    "v_exp_f32 v157, v157\n"
    "v_exp_f32 v158, v158\n"
    "v_cvt_pk_bf16_f32 v186, v156, v157\n"
    "v_exp_f32 v159, v159\n"
    "v_mfma_f32_32x32x16_bf16 v[104:119], a[34:37], v[192:195], v[104:119]\n"
    "v_exp_f32 v160, v160\n"
    "v_exp_f32 v161, v161\n"
    "v_exp_f32 v162, v162\n"
    "v_mfma_f32_32x32x16_bf16 v[104:119], a[38:41], v[196:199], v[104:119]\n"
    "s_waitcnt vmcnt(30)\n"
    "buffer_load_dwordx4 a[20:23], v244, s[44:47], s68 offen sc1\n"
    "v_cvt_pk_bf16_f32 v187, v158, v159\n"
    "v_exp_f32 v163, v163\n"
    "v_cvt_pk_bf16_f32 v188, v160, v161\n"
    "v_exp_f32 v164, v164\n"
    "v_cvt_pk_bf16_f32 v189, v162, v163\n"
    "v_exp_f32 v165, v165\n"
    "v_exp_f32 v166, v166\n"
    "v_exp_f32 v167, v167\n"
    "v_cvt_pk_bf16_f32 v190, v164, v165\n"
    "v_cvt_pk_bf16_f32 v191, v166, v167\n"
    "v_mfma_f32_32x32x16_bf16 v[152:167], a[42:45], v[0:3], v[24:39]\n"
    "v_exp_f32 v168, v168\n"
    "v_exp_f32 v169, v169\n"
    "v_exp_f32 v170, v170\n"
    "v_mfma_f32_32x32x8bf16_1k v[152:167], a[46:47], v[16:17], v[152:167]\n"
    "buffer_load_dwordx4 a[24:27], v244, s[44:47], s68 offen offset:640 sc1\n"
    "v_exp_f32 v171, v171\n"
    "v_cvt_pk_bf16_f32 v192, v168, v169\n"
    "v_exp_f32 v172, v172\n"
    "v_cvt_pk_bf16_f32 v193, v170, v171\n"
    "v_exp_f32 v173, v173\n"
    "v_exp_f32 v174, v174\n"
    "v_cvt_pk_bf16_f32 v194, v172, v173\n"
    "v_exp_f32 v175, v175\n"
    "v_mfma_f32_32x32x16_bf16 v[120:135], a[34:37], v[184:187], v[120:135]\n"
    "v_exp_f32 v176, v176\n"
    "v_exp_f32 v177, v177\n"
    "v_exp_f32 v178, v178\n"
    "v_mfma_f32_32x32x16_bf16 v[120:135], a[38:41], v[188:191], v[120:135]\n"
    "v_cvt_pk_bf16_f32 v195, v174, v175\n"
    "v_exp_f32 v179, v179\n"
    "v_cvt_pk_bf16_f32 v196, v176, v177\n"
    "v_exp_f32 v180, v180\n"
    "v_cvt_pk_bf16_f32 v197, v178, v179\n"
    "v_exp_f32 v181, v181\n"
    "v_exp_f32 v182, v182\n"
    "v_exp_f32 v183, v183\n"
    "v_cvt_pk_bf16_f32 v198, v180, v181\n"
    "v_cvt_pk_bf16_f32 v199, v182, v183\n"
    "v_mfma_f32_32x32x16_bf16 v[168:183], a[42:45], v[4:7], v[40:55]\n"
    "s_add_u32 m0, %8, 147456\n"
    "v_exp_f32 v152, v152\n"
    "v_exp_f32 v153, v153\n"
    "v_exp_f32 v154, v154\n"
    "v_mfma_f32_32x32x8bf16_1k v[168:183], a[46:47], v[18:19], v[168:183]\n"
    "global_load_lds_dword v[236:237], off\n"
    "v_exp_f32 v155, v155\n"
    "v_cvt_pk_bf16_f32 v184, v152, v153\n"
    "v_exp_f32 v156, v156\n"
    "v_cvt_pk_bf16_f32 v185, v154, v155\n"
    "v_exp_f32 v157, v157\n"
    "v_exp_f32 v158, v158\n"
    "v_cvt_pk_bf16_f32 v186, v156, v157\n"
    "v_exp_f32 v159, v159\n"
    "v_mfma_f32_32x32x16_bf16 v[136:151], a[34:37], v[192:195], v[136:151]\n"
    "v_exp_f32 v160, v160\n"
    "v_exp_f32 v161, v161\n"
    "v_exp_f32 v162, v162\n"
    "v_mfma_f32_32x32x16_bf16 v[136:151], a[38:41], v[196:199], v[136:151]\n"
    "v_add_co_u32 v236, vcc, v238, v236\n"
    "v_cvt_pk_bf16_f32 v187, v158, v159\n"
    "v_exp_f32 v163, v163\n"
    "v_cvt_pk_bf16_f32 v188, v160, v161\n"
    "v_exp_f32 v164, v164\n"
    "v_cvt_pk_bf16_f32 v189, v162, v163\n"
    "v_exp_f32 v165, v165\n"
    "v_exp_f32 v166, v166\n"
    "v_exp_f32 v167, v167\n"
    "v_cvt_pk_bf16_f32 v190, v164, v165\n"
    "v_cvt_pk_bf16_f32 v191, v166, v167\n"
    "v_mfma_f32_32x32x16_bf16 v[152:167], a[42:45], v[8:11], v[56:71]\n"
    "v_exp_f32 v168, v168\n"
    "v_exp_f32 v169, v169\n"
    "v_exp_f32 v170, v170\n"
    "v_mfma_f32_32x32x8bf16_1k v[152:167], a[46:47], v[20:21], v[152:167]\n"
    "s_waitcnt vmcnt(31)\n"
    "v_addc_co_u32 v237, vcc, 0, v237, vcc\n"
    "v_exp_f32 v171, v171\n"
    "v_cvt_pk_bf16_f32 v192, v168, v169\n"
    "v_exp_f32 v172, v172\n"
    "v_cvt_pk_bf16_f32 v193, v170, v171\n"
    "v_exp_f32 v173, v173\n"
    "v_exp_f32 v174, v174\n"
    "v_cvt_pk_bf16_f32 v194, v172, v173\n"
    "v_exp_f32 v175, v175\n"
    "v_mfma_f32_32x32x16_bf16 v[88:103], a[48:51], v[184:187], v[88:103]\n"
    "s_add_u32 s68, s68, 2432\n"
    "v_exp_f32 v176, v176\n"
    "v_exp_f32 v177, v177\n"
    "v_exp_f32 v178, v178\n"
    "v_mfma_f32_32x32x16_bf16 v[88:103], a[52:55], v[188:191], v[88:103]\n"
    "buffer_load_dwordx4 a[28:31], v239, s[44:47], s68 offen sc1\n"
    "v_cvt_pk_bf16_f32 v195, v174, v175\n"
    "v_exp_f32 v179, v179\n"
    "v_cvt_pk_bf16_f32 v196, v176, v177\n"
    "v_exp_f32 v180, v180\n"
    "v_cvt_pk_bf16_f32 v197, v178, v179\n"
    "v_exp_f32 v181, v181\n"
    "v_exp_f32 v182, v182\n"
    "v_exp_f32 v183, v183\n"
    "v_cvt_pk_bf16_f32 v198, v180, v181\n"
    "v_cvt_pk_bf16_f32 v199, v182, v183\n"
    "v_mfma_f32_32x32x16_bf16 v[168:183], a[42:45], v[12:15], v[72:87]\n"
    "v_exp_f32 v152, v152\n"
    "v_exp_f32 v153, v153\n"
    "v_exp_f32 v154, v154\n"
    "v_mfma_f32_32x32x8bf16_1k v[168:183], a[46:47], v[22:23], v[168:183]\n"
    "buffer_load_dword a32, v243, s[44:47], s68 offen sc1\n"
    "v_exp_f32 v155, v155\n"
    "v_cvt_pk_bf16_f32 v184, v152, v153\n"
    "v_exp_f32 v156, v156\n"
    "v_cvt_pk_bf16_f32 v185, v154, v155\n"
    "v_exp_f32 v157, v157\n"
    "v_exp_f32 v158, v158\n"
    "v_cvt_pk_bf16_f32 v186, v156, v157\n"
    "v_exp_f32 v159, v159\n"
    "v_mfma_f32_32x32x16_bf16 v[104:119], a[48:51], v[192:195], v[104:119]\n"
    "v_exp_f32 v160, v160\n"
    "v_exp_f32 v161, v161\n"
    "v_exp_f32 v162, v162\n"
    "v_mfma_f32_32x32x16_bf16 v[104:119], a[52:55], v[196:199], v[104:119]\n"
    "s_waitcnt vmcnt(28)\n"
    "buffer_load_dwordx4 a[34:37], v244, s[44:47], s68 offen sc1\n"
    "v_cvt_pk_bf16_f32 v187, v158, v159\n"
    "v_exp_f32 v163, v163\n"
    "v_cvt_pk_bf16_f32 v188, v160, v161\n"
    "v_exp_f32 v164, v164\n"
    "v_cvt_pk_bf16_f32 v189, v162, v163\n"
    "v_exp_f32 v165, v165\n"
    "v_exp_f32 v166, v166\n"
    "v_exp_f32 v167, v167\n"
    "v_cvt_pk_bf16_f32 v190, v164, v165\n"
    "v_cvt_pk_bf16_f32 v191, v166, v167\n"
    "v_mfma_f32_32x32x16_bf16 v[152:167], a[56:59], v[0:3], v[24:39]\n"
    "v_exp_f32 v168, v168\n"
    "v_exp_f32 v169, v169\n"
    "v_exp_f32 v170, v170\n"
    "v_mfma_f32_32x32x8bf16_1k v[152:167], a[60:61], v[16:17], v[152:167]\n"
    "buffer_load_dwordx4 a[38:41], v244, s[44:47], s68 offen offset:640 sc1\n"
    "v_exp_f32 v171, v171\n"
    "v_cvt_pk_bf16_f32 v192, v168, v169\n"
    "v_exp_f32 v172, v172\n"
    "v_cvt_pk_bf16_f32 v193, v170, v171\n"
    "v_exp_f32 v173, v173\n"
    "v_exp_f32 v174, v174\n"
    "v_cvt_pk_bf16_f32 v194, v172, v173\n"
    "v_exp_f32 v175, v175\n"
    "v_mfma_f32_32x32x16_bf16 v[120:135], a[48:51], v[184:187], v[120:135]\n"
    "v_exp_f32 v176, v176\n"
    "v_exp_f32 v177, v177\n"
    "v_exp_f32 v178, v178\n"
    "v_mfma_f32_32x32x16_bf16 v[120:135], a[52:55], v[188:191], v[120:135]\n"
    "v_cvt_pk_bf16_f32 v195, v174, v175\n"
    "v_exp_f32 v179, v179\n"
    "v_cvt_pk_bf16_f32 v196, v176, v177\n"
    "v_exp_f32 v180, v180\n"
    "v_cvt_pk_bf16_f32 v197, v178, v179\n"
    "v_exp_f32 v181, v181\n"
    "v_exp_f32 v182, v182\n"
    "v_exp_f32 v183, v183\n"
    "v_cvt_pk_bf16_f32 v198, v180, v181\n"
    "v_cvt_pk_bf16_f32 v199, v182, v183\n"
    "v_mfma_f32_32x32x16_bf16 v[168:183], a[56:59], v[4:7], v[40:55]\n"
    "v_exp_f32 v152, v152\n"
    "v_exp_f32 v153, v153\n"
    "v_exp_f32 v154, v154\n"
    "v_mfma_f32_32x32x8bf16_1k v[168:183], a[60:61], v[18:19], v[168:183]\n"
    "v_exp_f32 v155, v155\n"
    "v_cvt_pk_bf16_f32 v184, v152, v153\n"
    "v_exp_f32 v156, v156\n"
    "v_cvt_pk_bf16_f32 v185, v154, v155\n"
    "v_exp_f32 v157, v157\n"
    "v_exp_f32 v158, v158\n"
    "v_cvt_pk_bf16_f32 v186, v156, v157\n"
    "v_exp_f32 v159, v159\n"
    "v_mfma_f32_32x32x16_bf16 v[136:151], a[48:51], v[192:195], v[136:151]\n"
    "v_exp_f32 v160, v160\n"
    "v_exp_f32 v161, v161\n"
    "v_exp_f32 v162, v162\n"
    "v_mfma_f32_32x32x16_bf16 v[136:151], a[52:55], v[196:199], v[136:151]\n"
    "v_cvt_pk_bf16_f32 v187, v158, v159\n"
    "v_exp_f32 v163, v163\n"
    "v_cvt_pk_bf16_f32 v188, v160, v161\n"
    "v_exp_f32 v164, v164\n"
    "v_cvt_pk_bf16_f32 v189, v162, v163\n"
    "v_exp_f32 v165, v165\n"
    "v_exp_f32 v166, v166\n"
    "v_exp_f32 v167, v167\n"
    "v_cvt_pk_bf16_f32 v190, v164, v165\n"
    "v_cvt_pk_bf16_f32 v191, v166, v167\n"
    "v_mfma_f32_32x32x16_bf16 v[152:167], a[56:59], v[8:11], v[56:71]\n"
    "v_exp_f32 v168, v168\n"
    "v_exp_f32 v169, v169\n"
    "v_exp_f32 v170, v170\n"
    "v_mfma_f32_32x32x8bf16_1k v[152:167], a[60:61], v[20:21], v[152:167]\n"
    "s_waitcnt vmcnt(28)\n"
    "v_exp_f32 v171, v171\n"
    "v_cvt_pk_bf16_f32 v192, v168, v169\n"
    "v_exp_f32 v172, v172\n"
    "v_cvt_pk_bf16_f32 v193, v170, v171\n"
    "v_exp_f32 v173, v173\n"
    "v_exp_f32 v174, v174\n"
    "v_cvt_pk_bf16_f32 v194, v172, v173\n"
    "v_exp_f32 v175, v175\n"
    "v_mfma_f32_32x32x16_bf16 v[88:103], a[62:65], v[184:187], v[88:103]\n"
    "s_add_u32 s68, s68, 2432\n"
    "v_exp_f32 v176, v176\n"
    "v_exp_f32 v177, v177\n"
    "v_exp_f32 v178, v178\n"
    "v_mfma_f32_32x32x16_bf16 v[88:103], a[66:69], v[188:191], v[88:103]\n"
    "buffer_load_dwordx4 a[42:45], v239, s[44:47], s68 offen sc1\n"
    "v_cvt_pk_bf16_f32 v195, v174, v175\n"
    "v_exp_f32 v179, v179\n"
    "v_cvt_pk_bf16_f32 v196, v176, v177\n"
    "v_exp_f32 v180, v180\n"
    "v_cvt_pk_bf16_f32 v197, v178, v179\n"
    "v_exp_f32 v181, v181\n"
    "v_exp_f32 v182, v182\n"
    "v_exp_f32 v183, v183\n"
    "v_cvt_pk_bf16_f32 v198, v180, v181\n"
    "v_cvt_pk_bf16_f32 v199, v182, v183\n"
    "v_mfma_f32_32x32x16_bf16 v[168:183], a[56:59], v[12:15], v[72:87]\n"
    "v_exp_f32 v152, v152\n"
    "v_exp_f32 v153, v153\n"
    "v_exp_f32 v154, v154\n"
    "v_mfma_f32_32x32x8bf16_1k v[168:183], a[60:61], v[22:23], v[168:183]\n"
    "buffer_load_dword a46, v243, s[44:47], s68 offen sc1\n"
    "v_exp_f32 v155, v155\n"
    "v_cvt_pk_bf16_f32 v184, v152, v153\n"
    "v_exp_f32 v156, v156\n"
    "v_cvt_pk_bf16_f32 v185, v154, v155\n"
    "v_exp_f32 v157, v157\n"
    "v_exp_f32 v158, v158\n"
    "v_cvt_pk_bf16_f32 v186, v156, v157\n"
    "v_exp_f32 v159, v159\n"
    "v_mfma_f32_32x32x16_bf16 v[104:119], a[62:65], v[192:195], v[104:119]\n"
    "v_exp_f32 v160, v160\n"
    "v_exp_f32 v161, v161\n"
    "v_exp_f32 v162, v162\n"
    "v_mfma_f32_32x32x16_bf16 v[104:119], a[66:69], v[196:199], v[104:119]\n"
    "s_waitcnt vmcnt(28)\n"
    "buffer_load_dwordx4 a[48:51], v244, s[44:47], s68 offen sc1\n"
    "v_cvt_pk_bf16_f32 v187, v158, v159\n"
    "v_exp_f32 v163, v163\n"
    "v_cvt_pk_bf16_f32 v188, v160, v161\n"
    "v_exp_f32 v164, v164\n"
    "v_cvt_pk_bf16_f32 v189, v162, v163\n"
    "v_exp_f32 v165, v165\n"
    "v_exp_f32 v166, v166\n"
    "v_exp_f32 v167, v167\n"
    "v_cvt_pk_bf16_f32 v190, v164, v165\n"
    "v_cvt_pk_bf16_f32 v191, v166, v167\n"
    "v_mfma_f32_32x32x16_bf16 v[152:167], a[70:73], v[0:3], v[24:39]\n"
    "v_exp_f32 v168, v168\n"
    "v_exp_f32 v169, v169\n"
    "v_exp_f32 v170, v170\n"
    "v_mfma_f32_32x32x8bf16_1k v[152:167], a[74:75], v[16:17], v[152:167]\n"
    "buffer_load_dwordx4 a[52:55], v244, s[44:47], s68 offen offset:640 sc1\n"
    "v_exp_f32 v171, v171\n"
    "v_cvt_pk_bf16_f32 v192, v168, v169\n"
    "v_exp_f32 v172, v172\n"
    "v_cvt_pk_bf16_f32 v193, v170, v171\n"
    "v_exp_f32 v173, v173\n"
    "v_exp_f32 v174, v174\n"
    "v_cvt_pk_bf16_f32 v194, v172, v173\n"
    "v_exp_f32 v175, v175\n"
    "v_mfma_f32_32x32x16_bf16 v[120:135], a[62:65], v[184:187], v[120:135]\n"
    "s_add_u32 m0, s57, 0\n"
    "v_exp_f32 v176, v176\n"
    "v_exp_f32 v177, v177\n"
    "v_exp_f32 v178, v178\n"
    "v_mfma_f32_32x32x16_bf16 v[120:135], a[66:69], v[188:191], v[120:135]\n"
    "buffer_load_dwordx4 v245, s[48:51], s58 offen lds\n"
    "v_cvt_pk_bf16_f32 v195, v174, v175\n"
    "v_exp_f32 v179, v179\n"
    "v_cvt_pk_bf16_f32 v196, v176, v177\n"
    "v_exp_f32 v180, v180\n"
    "v_cvt_pk_bf16_f32 v197, v178, v179\n"
    "v_exp_f32 v181, v181\n"
    "v_exp_f32 v182, v182\n"
    "v_exp_f32 v183, v183\n"
    "v_cvt_pk_bf16_f32 v198, v180, v181\n"
    "v_cvt_pk_bf16_f32 v199, v182, v183\n"
    "v_mfma_f32_32x32x16_bf16 v[168:183], a[70:73], v[4:7], v[40:55]\n"
    "s_add_u32 m0, s57, 36864\n"
    "v_exp_f32 v152, v152\n"
    "v_exp_f32 v153, v153\n"
    "v_exp_f32 v154, v154\n"
    "v_mfma_f32_32x32x8bf16_1k v[168:183], a[74:75], v[18:19], v[168:183]\n"
    "buffer_load_dwordx4 v245, s[48:51], s59 offen lds\n"
    "v_exp_f32 v155, v155\n"
    "v_cvt_pk_bf16_f32 v184, v152, v153\n"
    "v_exp_f32 v156, v156\n"
    "v_cvt_pk_bf16_f32 v185, v154, v155\n"
    "v_exp_f32 v157, v157\n"
    "v_exp_f32 v158, v158\n"
    "v_cvt_pk_bf16_f32 v186, v156, v157\n"
    "v_exp_f32 v159, v159\n"
    "v_mfma_f32_32x32x16_bf16 v[136:151], a[62:65], v[192:195], v[136:151]\n"
    "s_add_u32 m0, s57, 73728\n"
    "v_exp_f32 v160, v160\n"
    "v_exp_f32 v161, v161\n"
    "v_exp_f32 v162, v162\n"
    "v_mfma_f32_32x32x16_bf16 v[136:151], a[66:69], v[196:199], v[136:151]\n"
    "buffer_load_dwordx4 v245, s[48:51], s60 offen lds\n"
    "v_cvt_pk_bf16_f32 v187, v158, v159\n"
    "v_exp_f32 v163, v163\n"
    "v_cvt_pk_bf16_f32 v188, v160, v161\n"
    "v_exp_f32 v164, v164\n"
    "v_cvt_pk_bf16_f32 v189, v162, v163\n"
    "v_exp_f32 v165, v165\n"
    "v_exp_f32 v166, v166\n"
    "v_exp_f32 v167, v167\n"
    "v_cvt_pk_bf16_f32 v190, v164, v165\n"
    "v_cvt_pk_bf16_f32 v191, v166, v167\n"
    "v_mfma_f32_32x32x16_bf16 v[152:167], a[70:73], v[8:11], v[56:71]\n"
    "s_add_u32 s57, s57, 0x400\n"
    "v_exp_f32 v168, v168\n"
    "v_exp_f32 v169, v169\n"
    "v_exp_f32 v170, v170\n"
    "v_mfma_f32_32x32x8bf16_1k v[152:167], a[74:75], v[20:21], v[152:167]\n"
    "s_waitcnt vmcnt(31)\n"
    "v_exp_f32 v171, v171\n"
    "v_cvt_pk_bf16_f32 v192, v168, v169\n"
    "v_exp_f32 v172, v172\n"
    "v_cvt_pk_bf16_f32 v193, v170, v171\n"
    "v_exp_f32 v173, v173\n"
    "v_exp_f32 v174, v174\n"
    "v_cvt_pk_bf16_f32 v194, v172, v173\n"
    "v_exp_f32 v175, v175\n"
    "v_mfma_f32_32x32x16_bf16 v[88:103], a[76:79], v[184:187], v[88:103]\n"
    "s_add_u32 s68, s68, 2432\n"
    "v_exp_f32 v176, v176\n"
    "v_exp_f32 v177, v177\n"
    "v_exp_f32 v178, v178\n"
    "v_mfma_f32_32x32x16_bf16 v[88:103], a[80:83], v[188:191], v[88:103]\n"
    "buffer_load_dwordx4 a[56:59], v239, s[44:47], s68 offen sc1\n"
    "v_cvt_pk_bf16_f32 v195, v174, v175\n"
    "v_exp_f32 v179, v179\n"
    "v_cvt_pk_bf16_f32 v196, v176, v177\n"
    "v_exp_f32 v180, v180\n"
    "v_cvt_pk_bf16_f32 v197, v178, v179\n"
    "v_exp_f32 v181, v181\n"
    "v_exp_f32 v182, v182\n"
    "v_exp_f32 v183, v183\n"
    "v_cvt_pk_bf16_f32 v198, v180, v181\n"
    "v_cvt_pk_bf16_f32 v199, v182, v183\n"
    "v_mfma_f32_32x32x16_bf16 v[168:183], a[70:73], v[12:15], v[72:87]\n"
    "s_add_u32 s58, s58, 18432\n"
    "v_exp_f32 v152, v152\n"
    "v_exp_f32 v153, v153\n"
    "v_exp_f32 v154, v154\n"
    "v_mfma_f32_32x32x8bf16_1k v[168:183], a[74:75], v[22:23], v[168:183]\n"
    "buffer_load_dword a60, v243, s[44:47], s68 offen sc1\n"
    "v_exp_f32 v155, v155\n"
    "v_cvt_pk_bf16_f32 v184, v152, v153\n"
    "v_exp_f32 v156, v156\n"
    "v_cvt_pk_bf16_f32 v185, v154, v155\n"
    "v_exp_f32 v157, v157\n"
    "v_exp_f32 v158, v158\n"
    "v_cvt_pk_bf16_f32 v186, v156, v157\n"
    "v_exp_f32 v159, v159\n"
    "v_mfma_f32_32x32x16_bf16 v[104:119], a[76:79], v[192:195], v[104:119]\n"
    "s_add_u32 s59, s59, 18432\n"
    "v_exp_f32 v160, v160\n"
    "v_exp_f32 v161, v161\n"
    "v_exp_f32 v162, v162\n"
    "v_mfma_f32_32x32x16_bf16 v[104:119], a[80:83], v[196:199], v[104:119]\n"
    "s_waitcnt vmcnt(31)\n"
    "buffer_load_dwordx4 a[62:65], v244, s[44:47], s68 offen sc1\n"
    "v_cvt_pk_bf16_f32 v187, v158, v159\n"
    "v_exp_f32 v163, v163\n"
    "v_cvt_pk_bf16_f32 v188, v160, v161\n"
    "v_exp_f32 v164, v164\n"
    "v_cvt_pk_bf16_f32 v189, v162, v163\n"
    "v_exp_f32 v165, v165\n"
    "v_exp_f32 v166, v166\n"
    "v_exp_f32 v167, v167\n"
    "v_cvt_pk_bf16_f32 v190, v164, v165\n"
    "v_cvt_pk_bf16_f32 v191, v166, v167\n"
    "v_mfma_f32_32x32x16_bf16 v[152:167], a[84:87], v[0:3], v[24:39]\n"
    "s_add_u32 s60, s60, 18432\n"
    "v_exp_f32 v168, v168\n"
    "v_exp_f32 v169, v169\n"
    "v_exp_f32 v170, v170\n"
    "v_mfma_f32_32x32x8bf16_1k v[152:167], a[88:89], v[16:17], v[152:167]\n"
    "buffer_load_dwordx4 a[66:69], v244, s[44:47], s68 offen offset:640 sc1\n"
    "v_exp_f32 v171, v171\n"
    "v_cvt_pk_bf16_f32 v192, v168, v169\n"
    "v_exp_f32 v172, v172\n"
    "v_cvt_pk_bf16_f32 v193, v170, v171\n"
    "v_exp_f32 v173, v173\n"
    "v_exp_f32 v174, v174\n"
    "v_cvt_pk_bf16_f32 v194, v172, v173\n"
    "v_exp_f32 v175, v175\n"
    "v_mfma_f32_32x32x16_bf16 v[120:135], a[76:79], v[184:187], v[120:135]\n"
    "v_exp_f32 v176, v176\n"
    "v_exp_f32 v177, v177\n"
    "v_exp_f32 v178, v178\n"
    "v_mfma_f32_32x32x16_bf16 v[120:135], a[80:83], v[188:191], v[120:135]\n"
    "v_cvt_pk_bf16_f32 v195, v174, v175\n"
    "v_exp_f32 v179, v179\n"
    "v_cvt_pk_bf16_f32 v196, v176, v177\n"
    "v_exp_f32 v180, v180\n"
    "v_cvt_pk_bf16_f32 v197, v178, v179\n"
    "v_exp_f32 v181, v181\n"
    "v_exp_f32 v182, v182\n"
    "v_exp_f32 v183, v183\n"
    "v_cvt_pk_bf16_f32 v198, v180, v181\n"
    "v_cvt_pk_bf16_f32 v199, v182, v183\n"
    "v_mfma_f32_32x32x16_bf16 v[168:183], a[84:87], v[4:7], v[40:55]\n"
    "v_exp_f32 v152, v152\n"
    "v_exp_f32 v153, v153\n"
    "v_exp_f32 v154, v154\n"
    "v_mfma_f32_32x32x8bf16_1k v[168:183], a[88:89], v[18:19], v[168:183]\n"
    "v_exp_f32 v155, v155\n"
    "v_cvt_pk_bf16_f32 v184, v152, v153\n"
    "v_exp_f32 v156, v156\n"
    "v_cvt_pk_bf16_f32 v185, v154, v155\n"
    "v_exp_f32 v157, v157\n"
    "v_exp_f32 v158, v158\n"
    "v_cvt_pk_bf16_f32 v186, v156, v157\n"
    "v_exp_f32 v159, v159\n"
    "v_mfma_f32_32x32x16_bf16 v[136:151], a[76:79], v[192:195], v[136:151]\n"
    "v_exp_f32 v160, v160\n"
    "v_exp_f32 v161, v161\n"
    "v_exp_f32 v162, v162\n"
    "v_mfma_f32_32x32x16_bf16 v[136:151], a[80:83], v[196:199], v[136:151]\n"
    "v_cvt_pk_bf16_f32 v187, v158, v159\n"
    "v_exp_f32 v163, v163\n"
    "v_cvt_pk_bf16_f32 v188, v160, v161\n"
    "v_exp_f32 v164, v164\n"
    "v_cvt_pk_bf16_f32 v189, v162, v163\n"
    "v_exp_f32 v165, v165\n"
    "v_exp_f32 v166, v166\n"
    "v_exp_f32 v167, v167\n"
    "v_cvt_pk_bf16_f32 v190, v164, v165\n"
    "v_cvt_pk_bf16_f32 v191, v166, v167\n"
    "v_mfma_f32_32x32x16_bf16 v[152:167], a[84:87], v[8:11], v[56:71]\n"
    "v_exp_f32 v168, v168\n"
    "v_exp_f32 v169, v169\n"
    "v_exp_f32 v170, v170\n"
    "v_mfma_f32_32x32x8bf16_1k v[152:167], a[88:89], v[20:21], v[152:167]\n"
    "s_waitcnt vmcnt(31)\n"
    "v_exp_f32 v171, v171\n"
    "v_cvt_pk_bf16_f32 v192, v168, v169\n"
    "v_exp_f32 v172, v172\n"
    "v_cvt_pk_bf16_f32 v193, v170, v171\n"
    "v_exp_f32 v173, v173\n"
    "v_exp_f32 v174, v174\n"
    "v_cvt_pk_bf16_f32 v194, v172, v173\n"
    "v_exp_f32 v175, v175\n"
    "v_mfma_f32_32x32x16_bf16 v[88:103], a[90:93], v[184:187], v[88:103]\n"
    "s_add_u32 s68, s68, 2432\n"
    "v_exp_f32 v176, v176\n"
    "v_exp_f32 v177, v177\n"
    "v_exp_f32 v178, v178\n"
    "v_mfma_f32_32x32x16_bf16 v[88:103], a[94:97], v[188:191], v[88:103]\n"
    "buffer_load_dwordx4 a[70:73], v239, s[44:47], s68 offen sc1\n"
    "v_cvt_pk_bf16_f32 v195, v174, v175\n"
    "v_exp_f32 v179, v179\n"
    "v_cvt_pk_bf16_f32 v196, v176, v177\n"
    "v_exp_f32 v180, v180\n"
    "v_cvt_pk_bf16_f32 v197, v178, v179\n"
    "v_exp_f32 v181, v181\n"
    "v_exp_f32 v182, v182\n"
    "v_exp_f32 v183, v183\n"
    "v_cvt_pk_bf16_f32 v198, v180, v181\n"
    "v_cvt_pk_bf16_f32 v199, v182, v183\n"
    "v_mfma_f32_32x32x16_bf16 v[168:183], a[84:87], v[12:15], v[72:87]\n"
    "v_exp_f32 v152, v152\n"
    "v_exp_f32 v153, v153\n"
    "v_exp_f32 v154, v154\n"
    "v_mfma_f32_32x32x8bf16_1k v[168:183], a[88:89], v[22:23], v[168:183]\n"
    "buffer_load_dword a74, v243, s[44:47], s68 offen sc1\n"
    "v_exp_f32 v155, v155\n"
    "v_cvt_pk_bf16_f32 v184, v152, v153\n"
    "v_exp_f32 v156, v156\n"
    "v_cvt_pk_bf16_f32 v185, v154, v155\n"
    "v_exp_f32 v157, v157\n"
    "v_exp_f32 v158, v158\n"
    "v_cvt_pk_bf16_f32 v186, v156, v157\n"
    "v_exp_f32 v159, v159\n"
    "v_mfma_f32_32x32x16_bf16 v[104:119], a[90:93], v[192:195], v[104:119]\n"
    "v_exp_f32 v160, v160\n"
    "v_exp_f32 v161, v161\n"
    "v_exp_f32 v162, v162\n"
    "v_mfma_f32_32x32x16_bf16 v[104:119], a[94:97], v[196:199], v[104:119]\n"
    "s_waitcnt vmcnt(31)\n"
    "buffer_load_dwordx4 a[76:79], v244, s[44:47], s68 offen sc1\n"
    "v_cvt_pk_bf16_f32 v187, v158, v159\n"
    "v_exp_f32 v163, v163\n"
    "v_cvt_pk_bf16_f32 v188, v160, v161\n"
    "v_exp_f32 v164, v164\n"
    "v_cvt_pk_bf16_f32 v189, v162, v163\n"
    "v_exp_f32 v165, v165\n"
    "v_exp_f32 v166, v166\n"
    "v_exp_f32 v167, v167\n"
    "v_cvt_pk_bf16_f32 v190, v164, v165\n"
    "v_cvt_pk_bf16_f32 v191, v166, v167\n"
    "v_mfma_f32_32x32x16_bf16 v[152:167], a[98:101], v[0:3], v[24:39]\n"
    "v_exp_f32 v168, v168\n"
    "v_exp_f32 v169, v169\n"
    "v_exp_f32 v170, v170\n"
    "v_mfma_f32_32x32x8bf16_1k v[152:167], a[102:103], v[16:17], v[152:167]\n"
    "buffer_load_dwordx4 a[80:83], v244, s[44:47], s68 offen offset:640 sc1\n"
    "v_exp_f32 v171, v171\n"
    "v_cvt_pk_bf16_f32 v192, v168, v169\n"
    "v_exp_f32 v172, v172\n"
    "v_cvt_pk_bf16_f32 v193, v170, v171\n"
    "v_exp_f32 v173, v173\n"
    "v_exp_f32 v174, v174\n"
    "v_cvt_pk_bf16_f32 v194, v172, v173\n"
    "v_exp_f32 v175, v175\n"
    "v_mfma_f32_32x32x16_bf16 v[120:135], a[90:93], v[184:187], v[120:135]\n"
    "v_exp_f32 v176, v176\n"
    "v_exp_f32 v177, v177\n"
    "v_exp_f32 v178, v178\n"
    "v_mfma_f32_32x32x16_bf16 v[120:135], a[94:97], v[188:191], v[120:135]\n"
    "v_cvt_pk_bf16_f32 v195, v174, v175\n"
    "v_exp_f32 v179, v179\n"
    "v_cvt_pk_bf16_f32 v196, v176, v177\n"
    "v_exp_f32 v180, v180\n"
    "v_cvt_pk_bf16_f32 v197, v178, v179\n"
    "v_exp_f32 v181, v181\n"
    "v_exp_f32 v182, v182\n"
    "v_exp_f32 v183, v183\n"
    "v_cvt_pk_bf16_f32 v198, v180, v181\n"
    "v_cvt_pk_bf16_f32 v199, v182, v183\n"
    "v_mfma_f32_32x32x16_bf16 v[168:183], a[98:101], v[4:7], v[40:55]\n"
    "v_exp_f32 v152, v152\n"
    "v_exp_f32 v153, v153\n"
    "v_exp_f32 v154, v154\n"
    "v_mfma_f32_32x32x8bf16_1k v[168:183], a[102:103], v[18:19], v[168:183]\n"
    "v_exp_f32 v155, v155\n"
    "v_cvt_pk_bf16_f32 v184, v152, v153\n"
    "v_exp_f32 v156, v156\n"
    "v_cvt_pk_bf16_f32 v185, v154, v155\n"
    "v_exp_f32 v157, v157\n"
    "v_exp_f32 v158, v158\n"
    "v_cvt_pk_bf16_f32 v186, v156, v157\n"
    "v_exp_f32 v159, v159\n"
    "v_mfma_f32_32x32x16_bf16 v[136:151], a[90:93], v[192:195], v[136:151]\n"
    "v_exp_f32 v160, v160\n"
    "v_exp_f32 v161, v161\n"
    "v_exp_f32 v162, v162\n"
    "v_mfma_f32_32x32x16_bf16 v[136:151], a[94:97], v[196:199], v[136:151]\n"
    "v_cvt_pk_bf16_f32 v187, v158, v159\n"
    "v_exp_f32 v163, v163\n"
    "v_cvt_pk_bf16_f32 v188, v160, v161\n"
    "v_exp_f32 v164, v164\n"
    "v_cvt_pk_bf16_f32 v189, v162, v163\n"
    "v_exp_f32 v165, v165\n"
    "v_exp_f32 v166, v166\n"
    "v_exp_f32 v167, v167\n"
    "v_cvt_pk_bf16_f32 v190, v164, v165\n"
    "v_cvt_pk_bf16_f32 v191, v166, v167\n"
    "v_mfma_f32_32x32x16_bf16 v[152:167], a[98:101], v[8:11], v[56:71]\n"
    "v_exp_f32 v168, v168\n"
    "v_exp_f32 v169, v169\n"
    "v_exp_f32 v170, v170\n"
    "v_mfma_f32_32x32x8bf16_1k v[152:167], a[102:103], v[20:21], v[152:167]\n"
    "s_waitcnt vmcnt(31)\n"
    "v_exp_f32 v171, v171\n"
    "v_cvt_pk_bf16_f32 v192, v168, v169\n"
    "v_exp_f32 v172, v172\n"
    "v_cvt_pk_bf16_f32 v193, v170, v171\n"
    "v_exp_f32 v173, v173\n"
    "v_exp_f32 v174, v174\n"
    "v_cvt_pk_bf16_f32 v194, v172, v173\n"
    "v_exp_f32 v175, v175\n"
    "v_mfma_f32_32x32x16_bf16 v[88:103], a[104:107], v[184:187], v[88:103]\n"
    "s_add_u32 s68, s68, 2432\n"
    "v_exp_f32 v176, v176\n"
    "v_exp_f32 v177, v177\n"
    "v_exp_f32 v178, v178\n"
    "v_mfma_f32_32x32x16_bf16 v[88:103], a[108:111], v[188:191], v[88:103]\n"
    "buffer_load_dwordx4 a[84:87], v239, s[44:47], s68 offen sc1\n"
    "v_cvt_pk_bf16_f32 v195, v174, v175\n"
    "v_exp_f32 v179, v179\n"
    "v_cvt_pk_bf16_f32 v196, v176, v177\n"
    "v_exp_f32 v180, v180\n"
    "v_cvt_pk_bf16_f32 v197, v178, v179\n"
    "v_exp_f32 v181, v181\n"
    "v_exp_f32 v182, v182\n"
    "v_exp_f32 v183, v183\n"
    "v_cvt_pk_bf16_f32 v198, v180, v181\n"
    "v_cvt_pk_bf16_f32 v199, v182, v183\n"
    "v_mfma_f32_32x32x16_bf16 v[168:183], a[98:101], v[12:15], v[72:87]\n"
    "v_exp_f32 v152, v152\n"
    "v_exp_f32 v153, v153\n"
    "v_exp_f32 v154, v154\n"
    "v_mfma_f32_32x32x8bf16_1k v[168:183], a[102:103], v[22:23], v[168:183]\n"
    "buffer_load_dword a88, v243, s[44:47], s68 offen sc1\n"
    "v_exp_f32 v155, v155\n"
    "v_cvt_pk_bf16_f32 v184, v152, v153\n"
    "v_exp_f32 v156, v156\n"
    "v_cvt_pk_bf16_f32 v185, v154, v155\n"
    "v_exp_f32 v157, v157\n"
    "v_exp_f32 v158, v158\n"
    "v_cvt_pk_bf16_f32 v186, v156, v157\n"
    "v_exp_f32 v159, v159\n"
    "v_mfma_f32_32x32x16_bf16 v[104:119], a[104:107], v[192:195], v[104:119]\n"
    "v_exp_f32 v160, v160\n"
    "v_exp_f32 v161, v161\n"
    "v_exp_f32 v162, v162\n"
    "v_mfma_f32_32x32x16_bf16 v[104:119], a[108:111], v[196:199], v[104:119]\n"
    "s_waitcnt vmcnt(28)\n"
    "buffer_load_dwordx4 a[90:93], v244, s[44:47], s68 offen sc1\n"
    "v_cvt_pk_bf16_f32 v187, v158, v159\n"
    "v_exp_f32 v163, v163\n"
    "v_cvt_pk_bf16_f32 v188, v160, v161\n"
    "v_exp_f32 v164, v164\n"
    "v_cvt_pk_bf16_f32 v189, v162, v163\n"
    "v_exp_f32 v165, v165\n"
    "v_exp_f32 v166, v166\n"
    "v_exp_f32 v167, v167\n"
    "v_cvt_pk_bf16_f32 v190, v164, v165\n"
    "v_cvt_pk_bf16_f32 v191, v166, v167\n"
    "v_mfma_f32_32x32x16_bf16 v[152:167], a[0:3], v[0:3], v[24:39]\n"
    "v_exp_f32 v168, v168\n"
    "v_exp_f32 v169, v169\n"
    "v_exp_f32 v170, v170\n"
    "v_mfma_f32_32x32x8bf16_1k v[152:167], a[4:5], v[16:17], v[152:167]\n"
    "buffer_load_dwordx4 a[94:97], v244, s[44:47], s68 offen offset:640 sc1\n"
    "v_exp_f32 v171, v171\n"
    "v_cvt_pk_bf16_f32 v192, v168, v169\n"
    "v_exp_f32 v172, v172\n"
    "v_cvt_pk_bf16_f32 v193, v170, v171\n"
    "v_exp_f32 v173, v173\n"
    "v_exp_f32 v174, v174\n"
    "v_cvt_pk_bf16_f32 v194, v172, v173\n"
    "v_exp_f32 v175, v175\n"
    "v_mfma_f32_32x32x16_bf16 v[120:135], a[104:107], v[184:187], v[120:135]\n"
    "s_sub_u32 s66, s66, 1\n"
    "v_exp_f32 v176, v176\n"
    "v_exp_f32 v177, v177\n"
    "v_exp_f32 v178, v178\n"
    "v_mfma_f32_32x32x16_bf16 v[120:135], a[108:111], v[188:191], v[120:135]\n"
    "s_cmp_lg_u32 s66, 0\n"
    "v_cvt_pk_bf16_f32 v195, v174, v175\n"
    "v_exp_f32 v179, v179\n"
    "v_cvt_pk_bf16_f32 v196, v176, v177\n"
    "v_exp_f32 v180, v180\n"
    "v_cvt_pk_bf16_f32 v197, v178, v179\n"
    "v_exp_f32 v181, v181\n"
    "v_exp_f32 v182, v182\n"
    "v_exp_f32 v183, v183\n"
    "v_cvt_pk_bf16_f32 v198, v180, v181\n"
    "v_cvt_pk_bf16_f32 v199, v182, v183\n"
    "s_cbranch_scc1 2b\n"
    "s_mov_b32 s66, 3\n"
    "1:\n"
    "v_mfma_f32_32x32x16_bf16 v[168:183], a[0:3], v[4:7], v[40:55]\n"
    "v_exp_f32 v152, v152\n"
    "v_exp_f32 v153, v153\n"
    "v_exp_f32 v154, v154\n"
    "v_mfma_f32_32x32x8bf16_1k v[168:183], a[4:5], v[18:19], v[168:183]\n"
    "v_exp_f32 v155, v155\n"
    "v_cvt_pk_bf16_f32 v184, v152, v153\n"
    "v_exp_f32 v156, v156\n"
    "v_cvt_pk_bf16_f32 v185, v154, v155\n"
    "v_exp_f32 v157, v157\n"
    "v_exp_f32 v158, v158\n"
    "v_cvt_pk_bf16_f32 v186, v156, v157\n"
    "v_exp_f32 v159, v159\n"
    "v_mfma_f32_32x32x16_bf16 v[136:151], a[104:107], v[192:195], v[136:151]\n"
    "v_exp_f32 v160, v160\n"
    "v_exp_f32 v161, v161\n"
    "v_exp_f32 v162, v162\n"
    "v_mfma_f32_32x32x16_bf16 v[136:151], a[108:111], v[196:199], v[136:151]\n"
    "v_cvt_pk_bf16_f32 v187, v158, v159\n"
    "v_exp_f32 v163, v163\n"
    "v_cvt_pk_bf16_f32 v188, v160, v161\n"
    "v_exp_f32 v164, v164\n"
    "v_cvt_pk_bf16_f32 v189, v162, v163\n"
    "v_exp_f32 v165, v165\n"
    "v_exp_f32 v166, v166\n"
    "v_exp_f32 v167, v167\n"
    "v_cvt_pk_bf16_f32 v190, v164, v165\n"
    "v_cvt_pk_bf16_f32 v191, v166, v167\n"
    "v_mfma_f32_32x32x16_bf16 v[152:167], a[0:3], v[8:11], v[56:71]\n"
    "v_exp_f32 v168, v168\n"
    "v_exp_f32 v169, v169\n"
    "v_exp_f32 v170, v170\n"
    "v_mfma_f32_32x32x8bf16_1k v[152:167], a[4:5], v[20:21], v[152:167]\n"
    "s_waitcnt vmcnt(24)\n"
    "v_exp_f32 v171, v171\n"
    "v_cvt_pk_bf16_f32 v192, v168, v169\n"
    "v_exp_f32 v172, v172\n"
    "v_cvt_pk_bf16_f32 v193, v170, v171\n"
    "v_exp_f32 v173, v173\n"
    "v_exp_f32 v174, v174\n"
    "v_cvt_pk_bf16_f32 v194, v172, v173\n"
    "v_exp_f32 v175, v175\n"
    "v_mfma_f32_32x32x16_bf16 v[88:103], a[6:9], v[184:187], v[88:103]\n"
    "s_add_u32 s68, s68, 2432\n"
    "v_exp_f32 v176, v176\n"
    "v_exp_f32 v177, v177\n"
    "v_exp_f32 v178, v178\n"
    "v_mfma_f32_32x32x16_bf16 v[88:103], a[10:13], v[188:191], v[88:103]\n"
    "buffer_load_dwordx4 a[98:101], v239, s[44:47], s68 offen sc1\n"
    "v_cvt_pk_bf16_f32 v195, v174, v175\n"
    "v_exp_f32 v179, v179\n"
    "v_cvt_pk_bf16_f32 v196, v176, v177\n"
    "v_exp_f32 v180, v180\n"
    "v_cvt_pk_bf16_f32 v197, v178, v179\n"
    "v_exp_f32 v181, v181\n"
    "v_exp_f32 v182, v182\n"
    "v_exp_f32 v183, v183\n"
    "v_cvt_pk_bf16_f32 v198, v180, v181\n"
    "v_cvt_pk_bf16_f32 v199, v182, v183\n"
    "v_mfma_f32_32x32x16_bf16 v[168:183], a[0:3], v[12:15], v[72:87]\n"
    "v_exp_f32 v152, v152\n"
    "v_exp_f32 v153, v153\n"
    "v_exp_f32 v154, v154\n"
    "v_mfma_f32_32x32x8bf16_1k v[168:183], a[4:5], v[22:23], v[168:183]\n"
    "buffer_load_dword a102, v243, s[44:47], s68 offen sc1\n"
    "v_exp_f32 v155, v155\n"
    "v_cvt_pk_bf16_f32 v184, v152, v153\n"
    "v_exp_f32 v156, v156\n"
    "v_cvt_pk_bf16_f32 v185, v154, v155\n"
    "v_exp_f32 v157, v157\n"
    "v_exp_f32 v158, v158\n"
    "v_cvt_pk_bf16_f32 v186, v156, v157\n"
    "v_exp_f32 v159, v159\n"
    "v_mfma_f32_32x32x16_bf16 v[104:119], a[6:9], v[192:195], v[104:119]\n"
    "v_exp_f32 v160, v160\n"
    "v_exp_f32 v161, v161\n"
    "v_exp_f32 v162, v162\n"
    "v_mfma_f32_32x32x16_bf16 v[104:119], a[10:13], v[196:199], v[104:119]\n"
    "s_waitcnt vmcnt(24)\n"
    "buffer_load_dwordx4 a[104:107], v244, s[44:47], s68 offen sc1\n"
    "v_cvt_pk_bf16_f32 v187, v158, v159\n"
    "v_exp_f32 v163, v163\n"
    "v_cvt_pk_bf16_f32 v188, v160, v161\n"
    "v_exp_f32 v164, v164\n"
    "v_cvt_pk_bf16_f32 v189, v162, v163\n"
    "v_exp_f32 v165, v165\n"
    "v_exp_f32 v166, v166\n"
    "v_exp_f32 v167, v167\n"
    "v_cvt_pk_bf16_f32 v190, v164, v165\n"
    "v_cvt_pk_bf16_f32 v191, v166, v167\n"
    "v_mfma_f32_32x32x16_bf16 v[152:167], a[14:17], v[0:3], v[24:39]\n"
    "v_exp_f32 v168, v168\n"
    "v_exp_f32 v169, v169\n"
    "v_exp_f32 v170, v170\n"
    "v_mfma_f32_32x32x8bf16_1k v[152:167], a[18:19], v[16:17], v[152:167]\n"
    "buffer_load_dwordx4 a[108:111], v244, s[44:47], s68 offen offset:640 sc1\n"
    "v_exp_f32 v171, v171\n"
    "v_cvt_pk_bf16_f32 v192, v168, v169\n"
    "v_exp_f32 v172, v172\n"
    "v_cvt_pk_bf16_f32 v193, v170, v171\n"
    "v_exp_f32 v173, v173\n"
    "v_exp_f32 v174, v174\n"
    "v_cvt_pk_bf16_f32 v194, v172, v173\n"
    "v_exp_f32 v175, v175\n"
    "v_mfma_f32_32x32x16_bf16 v[120:135], a[6:9], v[184:187], v[120:135]\n"
    "v_exp_f32 v176, v176\n"
    "v_exp_f32 v177, v177\n"
    "v_exp_f32 v178, v178\n"
    "v_mfma_f32_32x32x16_bf16 v[120:135], a[10:13], v[188:191], v[120:135]\n"
    "v_cvt_pk_bf16_f32 v195, v174, v175\n"
    "v_exp_f32 v179, v179\n"
    "v_cvt_pk_bf16_f32 v196, v176, v177\n"
    "v_exp_f32 v180, v180\n"
    "v_cvt_pk_bf16_f32 v197, v178, v179\n"
    "v_exp_f32 v181, v181\n"
    "v_exp_f32 v182, v182\n"
    "v_exp_f32 v183, v183\n"
    "v_cvt_pk_bf16_f32 v198, v180, v181\n"
    "v_cvt_pk_bf16_f32 v199, v182, v183\n"
    "v_mfma_f32_32x32x16_bf16 v[168:183], a[14:17], v[4:7], v[40:55]\n"
    "v_exp_f32 v152, v152\n"
    "v_exp_f32 v153, v153\n"
    "v_exp_f32 v154, v154\n"
    "v_mfma_f32_32x32x8bf16_1k v[168:183], a[18:19], v[18:19], v[168:183]\n"
    "v_exp_f32 v155, v155\n"
    "v_cvt_pk_bf16_f32 v184, v152, v153\n"
    "v_exp_f32 v156, v156\n"
    "v_cvt_pk_bf16_f32 v185, v154, v155\n"
    "v_exp_f32 v157, v157\n"
    "v_exp_f32 v158, v158\n"
    "v_cvt_pk_bf16_f32 v186, v156, v157\n"
    "v_exp_f32 v159, v159\n"
    "v_mfma_f32_32x32x16_bf16 v[136:151], a[6:9], v[192:195], v[136:151]\n"
    "v_exp_f32 v160, v160\n"
    "v_exp_f32 v161, v161\n"
    "v_exp_f32 v162, v162\n"
    "v_mfma_f32_32x32x16_bf16 v[136:151], a[10:13], v[196:199], v[136:151]\n"
    "v_cvt_pk_bf16_f32 v187, v158, v159\n"
    "v_exp_f32 v163, v163\n"
    "v_cvt_pk_bf16_f32 v188, v160, v161\n"
    "v_exp_f32 v164, v164\n"
    "v_cvt_pk_bf16_f32 v189, v162, v163\n"
    "v_exp_f32 v165, v165\n"
    "v_exp_f32 v166, v166\n"
    "v_exp_f32 v167, v167\n"
    "v_cvt_pk_bf16_f32 v190, v164, v165\n"
    "v_cvt_pk_bf16_f32 v191, v166, v167\n"
    "v_mfma_f32_32x32x16_bf16 v[152:167], a[14:17], v[8:11], v[56:71]\n"
    "v_exp_f32 v168, v168\n"
    "v_exp_f32 v169, v169\n"
    "v_exp_f32 v170, v170\n"
    "v_mfma_f32_32x32x8bf16_1k v[152:167], a[18:19], v[20:21], v[152:167]\n"
    "s_waitcnt vmcnt(24)\n"
    "v_exp_f32 v171, v171\n"
    "v_cvt_pk_bf16_f32 v192, v168, v169\n"
    "v_exp_f32 v172, v172\n"
    "v_cvt_pk_bf16_f32 v193, v170, v171\n"
    "v_exp_f32 v173, v173\n"
    "v_exp_f32 v174, v174\n"
    "v_cvt_pk_bf16_f32 v194, v172, v173\n"
    "v_exp_f32 v175, v175\n"
    "v_mfma_f32_32x32x16_bf16 v[88:103], a[20:23], v[184:187], v[88:103]\n"
    "s_add_u32 s68, s68, 2432\n"
    "v_exp_f32 v176, v176\n"
    "v_exp_f32 v177, v177\n"
    "v_exp_f32 v178, v178\n"
    "v_mfma_f32_32x32x16_bf16 v[88:103], a[24:27], v[188:191], v[88:103]\n"
    "buffer_load_dwordx4 a[0:3], v239, s[44:47], s68 offen sc1\n"
    "v_cvt_pk_bf16_f32 v195, v174, v175\n"
    "v_exp_f32 v179, v179\n"
    "v_cvt_pk_bf16_f32 v196, v176, v177\n"
    "v_exp_f32 v180, v180\n"
    "v_cvt_pk_bf16_f32 v197, v178, v179\n"
    "v_exp_f32 v181, v181\n"
    "v_exp_f32 v182, v182\n"
    "v_exp_f32 v183, v183\n"
    "v_cvt_pk_bf16_f32 v198, v180, v181\n"
    "v_cvt_pk_bf16_f32 v199, v182, v183\n"
    "v_mfma_f32_32x32x16_bf16 v[168:183], a[14:17], v[12:15], v[72:87]\n"
    "v_exp_f32 v152, v152\n"
    "v_exp_f32 v153, v153\n"
    "v_exp_f32 v154, v154\n"
    "v_mfma_f32_32x32x8bf16_1k v[168:183], a[18:19], v[22:23], v[168:183]\n"
    "buffer_load_dword a4, v243, s[44:47], s68 offen sc1\n"
    "v_exp_f32 v155, v155\n"
    "v_cvt_pk_bf16_f32 v184, v152, v153\n"
    "v_exp_f32 v156, v156\n"
    "v_cvt_pk_bf16_f32 v185, v154, v155\n"
    "v_exp_f32 v157, v157\n"
    "v_exp_f32 v158, v158\n"
    "v_cvt_pk_bf16_f32 v186, v156, v157\n"
    "v_exp_f32 v159, v159\n"
    "v_mfma_f32_32x32x16_bf16 v[104:119], a[20:23], v[192:195], v[104:119]\n"
    "v_exp_f32 v160, v160\n"
    "v_exp_f32 v161, v161\n"
    "v_exp_f32 v162, v162\n"
    "v_mfma_f32_32x32x16_bf16 v[104:119], a[24:27], v[196:199], v[104:119]\n"
    "s_waitcnt vmcnt(24)\n"
    "buffer_load_dwordx4 a[6:9], v244, s[44:47], s68 offen sc1\n"
    "v_cvt_pk_bf16_f32 v187, v158, v159\n"
    "v_exp_f32 v163, v163\n"
    "v_cvt_pk_bf16_f32 v188, v160, v161\n"
    "v_exp_f32 v164, v164\n"
    "v_cvt_pk_bf16_f32 v189, v162, v163\n"
    "v_exp_f32 v165, v165\n"
    "v_exp_f32 v166, v166\n"
    "v_exp_f32 v167, v167\n"
    "v_cvt_pk_bf16_f32 v190, v164, v165\n"
    "v_cvt_pk_bf16_f32 v191, v166, v167\n"
    "v_mfma_f32_32x32x16_bf16 v[152:167], a[28:31], v[0:3], v[24:39]\n"
    "v_exp_f32 v168, v168\n"
    "v_exp_f32 v169, v169\n"
    "v_exp_f32 v170, v170\n"
    "v_mfma_f32_32x32x8bf16_1k v[152:167], a[32:33], v[16:17], v[152:167]\n"
    "buffer_load_dwordx4 a[10:13], v244, s[44:47], s68 offen offset:640 sc1\n"
    "v_exp_f32 v171, v171\n"
    "v_cvt_pk_bf16_f32 v192, v168, v169\n"
    "v_exp_f32 v172, v172\n"
    "v_cvt_pk_bf16_f32 v193, v170, v171\n"
    "v_exp_f32 v173, v173\n"
    "v_exp_f32 v174, v174\n"
    "v_cvt_pk_bf16_f32 v194, v172, v173\n"
    "v_exp_f32 v175, v175\n"
    "v_mfma_f32_32x32x16_bf16 v[120:135], a[20:23], v[184:187], v[120:135]\n"
    "v_exp_f32 v176, v176\n"
    "v_exp_f32 v177, v177\n"
    "v_exp_f32 v178, v178\n"
    "v_mfma_f32_32x32x16_bf16 v[120:135], a[24:27], v[188:191], v[120:135]\n"
    "v_cvt_pk_bf16_f32 v195, v174, v175\n"
    "v_exp_f32 v179, v179\n"
    "v_cvt_pk_bf16_f32 v196, v176, v177\n"
    "v_exp_f32 v180, v180\n"
    "v_cvt_pk_bf16_f32 v197, v178, v179\n"
    "v_exp_f32 v181, v181\n"
    "v_exp_f32 v182, v182\n"
    "v_exp_f32 v183, v183\n"
    "v_cvt_pk_bf16_f32 v198, v180, v181\n"
    "v_cvt_pk_bf16_f32 v199, v182, v183\n"
    "v_mfma_f32_32x32x16_bf16 v[168:183], a[28:31], v[4:7], v[40:55]\n"
    "v_exp_f32 v152, v152\n"
    "v_exp_f32 v153, v153\n"
    "v_exp_f32 v154, v154\n"
    "v_mfma_f32_32x32x8bf16_1k v[168:183], a[32:33], v[18:19], v[168:183]\n"
    "v_exp_f32 v155, v155\n"
    "v_cvt_pk_bf16_f32 v184, v152, v153\n"
    "v_exp_f32 v156, v156\n"
    "v_cvt_pk_bf16_f32 v185, v154, v155\n"
    "v_exp_f32 v157, v157\n"
    "v_exp_f32 v158, v158\n"
    "v_cvt_pk_bf16_f32 v186, v156, v157\n"
    "v_exp_f32 v159, v159\n"
    "v_mfma_f32_32x32x16_bf16 v[136:151], a[20:23], v[192:195], v[136:151]\n"
    "v_exp_f32 v160, v160\n"
    "v_exp_f32 v161, v161\n"
    "v_exp_f32 v162, v162\n"
    "v_mfma_f32_32x32x16_bf16 v[136:151], a[24:27], v[196:199], v[136:151]\n"
    "v_cvt_pk_bf16_f32 v187, v158, v159\n"
    "v_exp_f32 v163, v163\n"
    "v_cvt_pk_bf16_f32 v188, v160, v161\n"
    "v_exp_f32 v164, v164\n"
    "v_cvt_pk_bf16_f32 v189, v162, v163\n"
    "v_exp_f32 v165, v165\n"
    "v_exp_f32 v166, v166\n"
    "v_exp_f32 v167, v167\n"
    "v_cvt_pk_bf16_f32 v190, v164, v165\n"
    "v_cvt_pk_bf16_f32 v191, v166, v167\n"
    "v_mfma_f32_32x32x16_bf16 v[152:167], a[28:31], v[8:11], v[56:71]\n"
    "v_exp_f32 v168, v168\n"
    "v_exp_f32 v169, v169\n"
    "v_exp_f32 v170, v170\n"
    "v_mfma_f32_32x32x8bf16_1k v[152:167], a[32:33], v[20:21], v[152:167]\n"
    "s_waitcnt vmcnt(24)\n"
    "v_exp_f32 v171, v171\n"
    "v_cvt_pk_bf16_f32 v192, v168, v169\n"
    "v_exp_f32 v172, v172\n"
    "v_cvt_pk_bf16_f32 v193, v170, v171\n"
    "v_exp_f32 v173, v173\n"
    "v_exp_f32 v174, v174\n"
    "v_cvt_pk_bf16_f32 v194, v172, v173\n"
    "v_exp_f32 v175, v175\n"
    "v_mfma_f32_32x32x16_bf16 v[88:103], a[34:37], v[184:187], v[88:103]\n"
    "s_add_u32 s68, s68, 2432\n"
    "v_exp_f32 v176, v176\n"
    "v_exp_f32 v177, v177\n"
    "v_exp_f32 v178, v178\n"
    "v_mfma_f32_32x32x16_bf16 v[88:103], a[38:41], v[188:191], v[88:103]\n"
    "buffer_load_dwordx4 a[14:17], v239, s[44:47], s68 offen sc1\n"
    "v_cvt_pk_bf16_f32 v195, v174, v175\n"
    "v_exp_f32 v179, v179\n"
    "v_cvt_pk_bf16_f32 v196, v176, v177\n"
    "v_exp_f32 v180, v180\n"
    "v_cvt_pk_bf16_f32 v197, v178, v179\n"
    "v_exp_f32 v181, v181\n"
    "v_exp_f32 v182, v182\n"
    "v_exp_f32 v183, v183\n"
    "v_cvt_pk_bf16_f32 v198, v180, v181\n"
    "v_cvt_pk_bf16_f32 v199, v182, v183\n"
    "v_mfma_f32_32x32x16_bf16 v[168:183], a[28:31], v[12:15], v[72:87]\n"
    "v_exp_f32 v152, v152\n"
    "v_exp_f32 v153, v153\n"
    "v_exp_f32 v154, v154\n"
    "v_mfma_f32_32x32x8bf16_1k v[168:183], a[32:33], v[22:23], v[168:183]\n"
    "buffer_load_dword a18, v243, s[44:47], s68 offen sc1\n"
    "v_exp_f32 v155, v155\n"
    "v_cvt_pk_bf16_f32 v184, v152, v153\n"
    "v_exp_f32 v156, v156\n"
    "v_cvt_pk_bf16_f32 v185, v154, v155\n"
    "v_exp_f32 v157, v157\n"
    "v_exp_f32 v158, v158\n"
    "v_cvt_pk_bf16_f32 v186, v156, v157\n"
    "v_exp_f32 v159, v159\n"
    "v_mfma_f32_32x32x16_bf16 v[104:119], a[34:37], v[192:195], v[104:119]\n"
    "v_exp_f32 v160, v160\n"
    "v_exp_f32 v161, v161\n"
    "v_exp_f32 v162, v162\n"
    "v_mfma_f32_32x32x16_bf16 v[104:119], a[38:41], v[196:199], v[104:119]\n"
    "s_waitcnt vmcnt(24)\n"
    "buffer_load_dwordx4 a[20:23], v244, s[44:47], s68 offen sc1\n"
    "v_cvt_pk_bf16_f32 v187, v158, v159\n"
    "v_exp_f32 v163, v163\n"
    "v_cvt_pk_bf16_f32 v188, v160, v161\n"
    "v_exp_f32 v164, v164\n"
    "v_cvt_pk_bf16_f32 v189, v162, v163\n"
    "v_exp_f32 v165, v165\n"
    "v_exp_f32 v166, v166\n"
    "v_exp_f32 v167, v167\n"
    "v_cvt_pk_bf16_f32 v190, v164, v165\n"
    "v_cvt_pk_bf16_f32 v191, v166, v167\n"
    "v_mfma_f32_32x32x16_bf16 v[152:167], a[42:45], v[0:3], v[24:39]\n"
    "v_exp_f32 v168, v168\n"
    "v_exp_f32 v169, v169\n"
    "v_exp_f32 v170, v170\n"
    "v_mfma_f32_32x32x8bf16_1k v[152:167], a[46:47], v[16:17], v[152:167]\n"
    "buffer_load_dwordx4 a[24:27], v244, s[44:47], s68 offen offset:640 sc1\n"
    "v_exp_f32 v171, v171\n"
    "v_cvt_pk_bf16_f32 v192, v168, v169\n"
    "v_exp_f32 v172, v172\n"
    "v_cvt_pk_bf16_f32 v193, v170, v171\n"
    "v_exp_f32 v173, v173\n"
    "v_exp_f32 v174, v174\n"
    "v_cvt_pk_bf16_f32 v194, v172, v173\n"
    "v_exp_f32 v175, v175\n"
    "v_mfma_f32_32x32x16_bf16 v[120:135], a[34:37], v[184:187], v[120:135]\n"
    "v_exp_f32 v176, v176\n"
    "v_exp_f32 v177, v177\n"
    "v_exp_f32 v178, v178\n"
    "v_mfma_f32_32x32x16_bf16 v[120:135], a[38:41], v[188:191], v[120:135]\n"
    "v_cvt_pk_bf16_f32 v195, v174, v175\n"
    "v_exp_f32 v179, v179\n"
    "v_cvt_pk_bf16_f32 v196, v176, v177\n"
    "v_exp_f32 v180, v180\n"
    "v_cvt_pk_bf16_f32 v197, v178, v179\n"
    "v_exp_f32 v181, v181\n"
    "v_exp_f32 v182, v182\n"
    "v_exp_f32 v183, v183\n"
    "v_cvt_pk_bf16_f32 v198, v180, v181\n"
    "v_cvt_pk_bf16_f32 v199, v182, v183\n"
    "v_mfma_f32_32x32x16_bf16 v[168:183], a[42:45], v[4:7], v[40:55]\n"
    "v_exp_f32 v152, v152\n"
    "v_exp_f32 v153, v153\n"
    "v_exp_f32 v154, v154\n"
    "v_mfma_f32_32x32x8bf16_1k v[168:183], a[46:47], v[18:19], v[168:183]\n"
    "v_exp_f32 v155, v155\n"
    "v_cvt_pk_bf16_f32 v184, v152, v153\n"
    "v_exp_f32 v156, v156\n"
    "v_cvt_pk_bf16_f32 v185, v154, v155\n"
    "v_exp_f32 v157, v157\n"
    "v_exp_f32 v158, v158\n"
    "v_cvt_pk_bf16_f32 v186, v156, v157\n"
    "v_exp_f32 v159, v159\n"
    "v_mfma_f32_32x32x16_bf16 v[136:151], a[34:37], v[192:195], v[136:151]\n"
    "v_exp_f32 v160, v160\n"
    "v_exp_f32 v161, v161\n"
    "v_exp_f32 v162, v162\n"
    "v_mfma_f32_32x32x16_bf16 v[136:151], a[38:41], v[196:199], v[136:151]\n"
    "v_cvt_pk_bf16_f32 v187, v158, v159\n"
    "v_exp_f32 v163, v163\n"
    "v_cvt_pk_bf16_f32 v188, v160, v161\n"
    "v_exp_f32 v164, v164\n"
    "v_cvt_pk_bf16_f32 v189, v162, v163\n"
    "v_exp_f32 v165, v165\n"
    "v_exp_f32 v166, v166\n"
    "v_exp_f32 v167, v167\n"
    "v_cvt_pk_bf16_f32 v190, v164, v165\n"
    "v_cvt_pk_bf16_f32 v191, v166, v167\n"
    "v_mfma_f32_32x32x16_bf16 v[152:167], a[42:45], v[8:11], v[56:71]\n"
    "v_exp_f32 v168, v168\n"
    "v_exp_f32 v169, v169\n"
    "v_exp_f32 v170, v170\n"
    "v_mfma_f32_32x32x8bf16_1k v[152:167], a[46:47], v[20:21], v[152:167]\n"
    "s_waitcnt vmcnt(24)\n"
    "v_exp_f32 v171, v171\n"
    "v_cvt_pk_bf16_f32 v192, v168, v169\n"
    "v_exp_f32 v172, v172\n"
    "v_cvt_pk_bf16_f32 v193, v170, v171\n"
    "v_exp_f32 v173, v173\n"
    "v_exp_f32 v174, v174\n"
    "v_cvt_pk_bf16_f32 v194, v172, v173\n"
    "v_exp_f32 v175, v175\n"
    "v_mfma_f32_32x32x16_bf16 v[88:103], a[48:51], v[184:187], v[88:103]\n"
    "s_add_u32 s68, s68, 2432\n"
    "v_exp_f32 v176, v176\n"
    "v_exp_f32 v177, v177\n"
    "v_exp_f32 v178, v178\n"
    "v_mfma_f32_32x32x16_bf16 v[88:103], a[52:55], v[188:191], v[88:103]\n"
    "buffer_load_dwordx4 a[28:31], v239, s[44:47], s68 offen sc1\n"
    "v_cvt_pk_bf16_f32 v195, v174, v175\n"
    "v_exp_f32 v179, v179\n"
    "v_cvt_pk_bf16_f32 v196, v176, v177\n"
    "v_exp_f32 v180, v180\n"
    "v_cvt_pk_bf16_f32 v197, v178, v179\n"
    "v_exp_f32 v181, v181\n"
    "v_exp_f32 v182, v182\n"
    "v_exp_f32 v183, v183\n"
    "v_cvt_pk_bf16_f32 v198, v180, v181\n"
    "v_cvt_pk_bf16_f32 v199, v182, v183\n"
    "v_mfma_f32_32x32x16_bf16 v[168:183], a[42:45], v[12:15], v[72:87]\n"
    "v_exp_f32 v152, v152\n"
    "v_exp_f32 v153, v153\n"
    "v_exp_f32 v154, v154\n"
    "v_mfma_f32_32x32x8bf16_1k v[168:183], a[46:47], v[22:23], v[168:183]\n"
    "buffer_load_dword a32, v243, s[44:47], s68 offen sc1\n"
    "v_exp_f32 v155, v155\n"
    "v_cvt_pk_bf16_f32 v184, v152, v153\n"
    "v_exp_f32 v156, v156\n"
    "v_cvt_pk_bf16_f32 v185, v154, v155\n"
    "v_exp_f32 v157, v157\n"
    "v_exp_f32 v158, v158\n"
    "v_cvt_pk_bf16_f32 v186, v156, v157\n"
    "v_exp_f32 v159, v159\n"
    "v_mfma_f32_32x32x16_bf16 v[104:119], a[48:51], v[192:195], v[104:119]\n"
    "v_exp_f32 v160, v160\n"
    "v_exp_f32 v161, v161\n"
    "v_exp_f32 v162, v162\n"
    "v_mfma_f32_32x32x16_bf16 v[104:119], a[52:55], v[196:199], v[104:119]\n"
    "s_waitcnt vmcnt(24)\n"
    "buffer_load_dwordx4 a[34:37], v244, s[44:47], s68 offen sc1\n"
    "v_cvt_pk_bf16_f32 v187, v158, v159\n"
    "v_exp_f32 v163, v163\n"
    "v_cvt_pk_bf16_f32 v188, v160, v161\n"
    "v_exp_f32 v164, v164\n"
    "v_cvt_pk_bf16_f32 v189, v162, v163\n"
    "v_exp_f32 v165, v165\n"
    "v_exp_f32 v166, v166\n"
    "v_exp_f32 v167, v167\n"
    "v_cvt_pk_bf16_f32 v190, v164, v165\n"
    "v_cvt_pk_bf16_f32 v191, v166, v167\n"
    "v_mfma_f32_32x32x16_bf16 v[152:167], a[56:59], v[0:3], v[24:39]\n"
    "v_exp_f32 v168, v168\n"
    "v_exp_f32 v169, v169\n"
    "v_exp_f32 v170, v170\n"
    "v_mfma_f32_32x32x8bf16_1k v[152:167], a[60:61], v[16:17], v[152:167]\n"
    "buffer_load_dwordx4 a[38:41], v244, s[44:47], s68 offen offset:640 sc1\n"
    "v_exp_f32 v171, v171\n"
    "v_cvt_pk_bf16_f32 v192, v168, v169\n"
    "v_exp_f32 v172, v172\n"
    "v_cvt_pk_bf16_f32 v193, v170, v171\n"
    "v_exp_f32 v173, v173\n"
    "v_exp_f32 v174, v174\n"
    "v_cvt_pk_bf16_f32 v194, v172, v173\n"
    "v_exp_f32 v175, v175\n"
    "v_mfma_f32_32x32x16_bf16 v[120:135], a[48:51], v[184:187], v[120:135]\n"
    "v_exp_f32 v176, v176\n"
    "v_exp_f32 v177, v177\n"
    "v_exp_f32 v178, v178\n"
    "v_mfma_f32_32x32x16_bf16 v[120:135], a[52:55], v[188:191], v[120:135]\n"
    "v_cvt_pk_bf16_f32 v195, v174, v175\n"
    "v_exp_f32 v179, v179\n"
    "v_cvt_pk_bf16_f32 v196, v176, v177\n"
    "v_exp_f32 v180, v180\n"
    "v_cvt_pk_bf16_f32 v197, v178, v179\n"
    "v_exp_f32 v181, v181\n"
    "v_exp_f32 v182, v182\n"
    "v_exp_f32 v183, v183\n"
    "v_cvt_pk_bf16_f32 v198, v180, v181\n"
    "v_cvt_pk_bf16_f32 v199, v182, v183\n"
    "v_mfma_f32_32x32x16_bf16 v[168:183], a[56:59], v[4:7], v[40:55]\n"
    "v_exp_f32 v152, v152\n"
    "v_exp_f32 v153, v153\n"
    "v_exp_f32 v154, v154\n"
    "v_mfma_f32_32x32x8bf16_1k v[168:183], a[60:61], v[18:19], v[168:183]\n"
    "v_exp_f32 v155, v155\n"
    "v_cvt_pk_bf16_f32 v184, v152, v153\n"
    "v_exp_f32 v156, v156\n"
    "v_cvt_pk_bf16_f32 v185, v154, v155\n"
    "v_exp_f32 v157, v157\n"
    "v_exp_f32 v158, v158\n"
    "v_cvt_pk_bf16_f32 v186, v156, v157\n"
    "v_exp_f32 v159, v159\n"
    "v_mfma_f32_32x32x16_bf16 v[136:151], a[48:51], v[192:195], v[136:151]\n"
    "v_exp_f32 v160, v160\n"
    "v_exp_f32 v161, v161\n"
    "v_exp_f32 v162, v162\n"
    "v_mfma_f32_32x32x16_bf16 v[136:151], a[52:55], v[196:199], v[136:151]\n"
    "v_cvt_pk_bf16_f32 v187, v158, v159\n"
    "v_exp_f32 v163, v163\n"
    "v_cvt_pk_bf16_f32 v188, v160, v161\n"
    "v_exp_f32 v164, v164\n"
    "v_cvt_pk_bf16_f32 v189, v162, v163\n"
    "v_exp_f32 v165, v165\n"
    "v_exp_f32 v166, v166\n"
    "v_exp_f32 v167, v167\n"
    "v_cvt_pk_bf16_f32 v190, v164, v165\n"
    "v_cvt_pk_bf16_f32 v191, v166, v167\n"
    "v_mfma_f32_32x32x16_bf16 v[152:167], a[56:59], v[8:11], v[56:71]\n"
    "v_exp_f32 v168, v168\n"
    "v_exp_f32 v169, v169\n"
    "v_exp_f32 v170, v170\n"
    "v_mfma_f32_32x32x8bf16_1k v[152:167], a[60:61], v[20:21], v[152:167]\n"
    "s_waitcnt vmcnt(24)\n"
    "v_exp_f32 v171, v171\n"
    "v_cvt_pk_bf16_f32 v192, v168, v169\n"
    "v_exp_f32 v172, v172\n"
    "v_cvt_pk_bf16_f32 v193, v170, v171\n"
    "v_exp_f32 v173, v173\n"
    "v_exp_f32 v174, v174\n"
    "v_cvt_pk_bf16_f32 v194, v172, v173\n"
    "v_exp_f32 v175, v175\n"
    "v_mfma_f32_32x32x16_bf16 v[88:103], a[62:65], v[184:187], v[88:103]\n"
    "s_add_u32 s68, s68, 2432\n"
    "v_exp_f32 v176, v176\n"
    "v_exp_f32 v177, v177\n"
    "v_exp_f32 v178, v178\n"
    "v_mfma_f32_32x32x16_bf16 v[88:103], a[66:69], v[188:191], v[88:103]\n"
    "buffer_load_dwordx4 a[42:45], v239, s[44:47], s68 offen sc1\n"
    "v_cvt_pk_bf16_f32 v195, v174, v175\n"
    "v_exp_f32 v179, v179\n"
    "v_cvt_pk_bf16_f32 v196, v176, v177\n"
    "v_exp_f32 v180, v180\n"
    "v_cvt_pk_bf16_f32 v197, v178, v179\n"
    "v_exp_f32 v181, v181\n"
    "v_exp_f32 v182, v182\n"
    "v_exp_f32 v183, v183\n"
    "v_cvt_pk_bf16_f32 v198, v180, v181\n"
    "v_cvt_pk_bf16_f32 v199, v182, v183\n"
    "v_mfma_f32_32x32x16_bf16 v[168:183], a[56:59], v[12:15], v[72:87]\n"
    "v_exp_f32 v152, v152\n"
    "v_exp_f32 v153, v153\n"
    "v_exp_f32 v154, v154\n"
    "v_mfma_f32_32x32x8bf16_1k v[168:183], a[60:61], v[22:23], v[168:183]\n"
    "buffer_load_dword a46, v243, s[44:47], s68 offen sc1\n"
    "v_exp_f32 v155, v155\n"
    "v_cvt_pk_bf16_f32 v184, v152, v153\n"
    "v_exp_f32 v156, v156\n"
    "v_cvt_pk_bf16_f32 v185, v154, v155\n"
    "v_exp_f32 v157, v157\n"
    "v_exp_f32 v158, v158\n"
    "v_cvt_pk_bf16_f32 v186, v156, v157\n"
    "v_exp_f32 v159, v159\n"
    "v_mfma_f32_32x32x16_bf16 v[104:119], a[62:65], v[192:195], v[104:119]\n"
    "v_exp_f32 v160, v160\n"
    "v_exp_f32 v161, v161\n"
    "v_exp_f32 v162, v162\n"
    "v_mfma_f32_32x32x16_bf16 v[104:119], a[66:69], v[196:199], v[104:119]\n"
    "s_waitcnt vmcnt(24)\n"
    "buffer_load_dwordx4 a[48:51], v244, s[44:47], s68 offen sc1\n"
    "v_cvt_pk_bf16_f32 v187, v158, v159\n"
    "v_exp_f32 v163, v163\n"
    "v_cvt_pk_bf16_f32 v188, v160, v161\n"
    "v_exp_f32 v164, v164\n"
    "v_cvt_pk_bf16_f32 v189, v162, v163\n"
    "v_exp_f32 v165, v165\n"
    "v_exp_f32 v166, v166\n"
    "v_exp_f32 v167, v167\n"
    "v_cvt_pk_bf16_f32 v190, v164, v165\n"
    "v_cvt_pk_bf16_f32 v191, v166, v167\n"
    "v_mfma_f32_32x32x16_bf16 v[152:167], a[70:73], v[0:3], v[24:39]\n"
    "v_exp_f32 v168, v168\n"
    "v_exp_f32 v169, v169\n"
    "v_exp_f32 v170, v170\n"
    "v_mfma_f32_32x32x8bf16_1k v[152:167], a[74:75], v[16:17], v[152:167]\n"
    "buffer_load_dwordx4 a[52:55], v244, s[44:47], s68 offen offset:640 sc1\n"
    "v_exp_f32 v171, v171\n"
    "v_cvt_pk_bf16_f32 v192, v168, v169\n"
    "v_exp_f32 v172, v172\n"
    "v_cvt_pk_bf16_f32 v193, v170, v171\n"
    "v_exp_f32 v173, v173\n"
    "v_exp_f32 v174, v174\n"
    "v_cvt_pk_bf16_f32 v194, v172, v173\n"
    "v_exp_f32 v175, v175\n"
    "v_mfma_f32_32x32x16_bf16 v[120:135], a[62:65], v[184:187], v[120:135]\n"
    "v_exp_f32 v176, v176\n"
    "v_exp_f32 v177, v177\n"
    "v_exp_f32 v178, v178\n"
    "v_mfma_f32_32x32x16_bf16 v[120:135], a[66:69], v[188:191], v[120:135]\n"
    "v_cvt_pk_bf16_f32 v195, v174, v175\n"
    "v_exp_f32 v179, v179\n"
    "v_cvt_pk_bf16_f32 v196, v176, v177\n"
    "v_exp_f32 v180, v180\n"
    "v_cvt_pk_bf16_f32 v197, v178, v179\n"
    "v_exp_f32 v181, v181\n"
    "v_exp_f32 v182, v182\n"
    "v_exp_f32 v183, v183\n"
    "v_cvt_pk_bf16_f32 v198, v180, v181\n"
    "v_cvt_pk_bf16_f32 v199, v182, v183\n"
    "v_mfma_f32_32x32x16_bf16 v[168:183], a[70:73], v[4:7], v[40:55]\n"
    "v_exp_f32 v152, v152\n"
    "v_exp_f32 v153, v153\n"
    "v_exp_f32 v154, v154\n"
    "v_mfma_f32_32x32x8bf16_1k v[168:183], a[74:75], v[18:19], v[168:183]\n"
    "v_exp_f32 v155, v155\n"
    "v_cvt_pk_bf16_f32 v184, v152, v153\n"
    "v_exp_f32 v156, v156\n"
    "v_cvt_pk_bf16_f32 v185, v154, v155\n"
    "v_exp_f32 v157, v157\n"
    "v_exp_f32 v158, v158\n"
    "v_cvt_pk_bf16_f32 v186, v156, v157\n"
    "v_exp_f32 v159, v159\n"
    "v_mfma_f32_32x32x16_bf16 v[136:151], a[62:65], v[192:195], v[136:151]\n"
    "v_exp_f32 v160, v160\n"
    "v_exp_f32 v161, v161\n"
    "v_exp_f32 v162, v162\n"
    "v_mfma_f32_32x32x16_bf16 v[136:151], a[66:69], v[196:199], v[136:151]\n"
    "v_cvt_pk_bf16_f32 v187, v158, v159\n"
    "v_exp_f32 v163, v163\n"
    "v_cvt_pk_bf16_f32 v188, v160, v161\n"
    "v_exp_f32 v164, v164\n"
    "v_cvt_pk_bf16_f32 v189, v162, v163\n"
    "v_exp_f32 v165, v165\n"
    "v_exp_f32 v166, v166\n"
    "v_exp_f32 v167, v167\n"
    "v_cvt_pk_bf16_f32 v190, v164, v165\n"
    "v_cvt_pk_bf16_f32 v191, v166, v167\n"
    "v_mfma_f32_32x32x16_bf16 v[152:167], a[70:73], v[8:11], v[56:71]\n"
    "v_exp_f32 v168, v168\n"
    "v_exp_f32 v169, v169\n"
    "v_exp_f32 v170, v170\n"
    "v_mfma_f32_32x32x8bf16_1k v[152:167], a[74:75], v[20:21], v[152:167]\n"
    "s_waitcnt vmcnt(24)\n"
    "v_exp_f32 v171, v171\n"
    "v_cvt_pk_bf16_f32 v192, v168, v169\n"
    "v_exp_f32 v172, v172\n"
    "v_cvt_pk_bf16_f32 v193, v170, v171\n"
    "v_exp_f32 v173, v173\n"
    "v_exp_f32 v174, v174\n"
    "v_cvt_pk_bf16_f32 v194, v172, v173\n"
    "v_exp_f32 v175, v175\n"
    "v_mfma_f32_32x32x16_bf16 v[88:103], a[76:79], v[184:187], v[88:103]\n"
    "s_add_u32 s68, s68, 2432\n"
    "v_exp_f32 v176, v176\n"
    "v_exp_f32 v177, v177\n"
    "v_exp_f32 v178, v178\n"
    "v_mfma_f32_32x32x16_bf16 v[88:103], a[80:83], v[188:191], v[88:103]\n"
    "buffer_load_dwordx4 a[56:59], v239, s[44:47], s68 offen sc1\n"
    "v_cvt_pk_bf16_f32 v195, v174, v175\n"
    "v_exp_f32 v179, v179\n"
    "v_cvt_pk_bf16_f32 v196, v176, v177\n"
    "v_exp_f32 v180, v180\n"
    "v_cvt_pk_bf16_f32 v197, v178, v179\n"
    "v_exp_f32 v181, v181\n"
    "v_exp_f32 v182, v182\n"
    "v_exp_f32 v183, v183\n"
    "v_cvt_pk_bf16_f32 v198, v180, v181\n"
    "v_cvt_pk_bf16_f32 v199, v182, v183\n"
    "v_mfma_f32_32x32x16_bf16 v[168:183], a[70:73], v[12:15], v[72:87]\n"
    "v_exp_f32 v152, v152\n"
    "v_exp_f32 v153, v153\n"
    "v_exp_f32 v154, v154\n"
    "v_mfma_f32_32x32x8bf16_1k v[168:183], a[74:75], v[22:23], v[168:183]\n"
    "buffer_load_dword a60, v243, s[44:47], s68 offen sc1\n"
    "v_exp_f32 v155, v155\n"
    "v_cvt_pk_bf16_f32 v184, v152, v153\n"
    "v_exp_f32 v156, v156\n"
    "v_cvt_pk_bf16_f32 v185, v154, v155\n"
    "v_exp_f32 v157, v157\n"
    "v_exp_f32 v158, v158\n"
    "v_cvt_pk_bf16_f32 v186, v156, v157\n"
    "v_exp_f32 v159, v159\n"
    "v_mfma_f32_32x32x16_bf16 v[104:119], a[76:79], v[192:195], v[104:119]\n"
    "v_exp_f32 v160, v160\n"
    "v_exp_f32 v161, v161\n"
    "v_exp_f32 v162, v162\n"
    "v_mfma_f32_32x32x16_bf16 v[104:119], a[80:83], v[196:199], v[104:119]\n"
    "s_waitcnt vmcnt(24)\n"
    "buffer_load_dwordx4 a[62:65], v244, s[44:47], s68 offen sc1\n"
    "v_cvt_pk_bf16_f32 v187, v158, v159\n"
    "v_exp_f32 v163, v163\n"
    "v_cvt_pk_bf16_f32 v188, v160, v161\n"
    "v_exp_f32 v164, v164\n"
    "v_cvt_pk_bf16_f32 v189, v162, v163\n"
    "v_exp_f32 v165, v165\n"
    "v_exp_f32 v166, v166\n"
    "v_exp_f32 v167, v167\n"
    "v_cvt_pk_bf16_f32 v190, v164, v165\n"
    "v_cvt_pk_bf16_f32 v191, v166, v167\n"
    "v_mfma_f32_32x32x16_bf16 v[152:167], a[84:87], v[0:3], v[24:39]\n"
    "v_exp_f32 v168, v168\n"
    "v_exp_f32 v169, v169\n"
    "v_exp_f32 v170, v170\n"
    "v_mfma_f32_32x32x8bf16_1k v[152:167], a[88:89], v[16:17], v[152:167]\n"
    "buffer_load_dwordx4 a[66:69], v244, s[44:47], s68 offen offset:640 sc1\n"
    "v_exp_f32 v171, v171\n"
    "v_cvt_pk_bf16_f32 v192, v168, v169\n"
    "v_exp_f32 v172, v172\n"
    "v_cvt_pk_bf16_f32 v193, v170, v171\n"
    "v_exp_f32 v173, v173\n"
    "v_exp_f32 v174, v174\n"
    "v_cvt_pk_bf16_f32 v194, v172, v173\n"
    "v_exp_f32 v175, v175\n"
    "v_mfma_f32_32x32x16_bf16 v[120:135], a[76:79], v[184:187], v[120:135]\n"
    "v_exp_f32 v176, v176\n"
    "v_exp_f32 v177, v177\n"
    "v_exp_f32 v178, v178\n"
    "v_mfma_f32_32x32x16_bf16 v[120:135], a[80:83], v[188:191], v[120:135]\n"
    "v_cvt_pk_bf16_f32 v195, v174, v175\n"
    "v_exp_f32 v179, v179\n"
    "v_cvt_pk_bf16_f32 v196, v176, v177\n"
    "v_exp_f32 v180, v180\n"
    "v_cvt_pk_bf16_f32 v197, v178, v179\n"
    "v_exp_f32 v181, v181\n"
    "v_exp_f32 v182, v182\n"
    "v_exp_f32 v183, v183\n"
    "v_cvt_pk_bf16_f32 v198, v180, v181\n"
    "v_cvt_pk_bf16_f32 v199, v182, v183\n"
    "v_mfma_f32_32x32x16_bf16 v[168:183], a[84:87], v[4:7], v[40:55]\n"
    "v_exp_f32 v152, v152\n"
    "v_exp_f32 v153, v153\n"
    "v_exp_f32 v154, v154\n"
    "v_mfma_f32_32x32x8bf16_1k v[168:183], a[88:89], v[18:19], v[168:183]\n"
    "v_exp_f32 v155, v155\n"
    "v_cvt_pk_bf16_f32 v184, v152, v153\n"
    "v_exp_f32 v156, v156\n"
    "v_cvt_pk_bf16_f32 v185, v154, v155\n"
    "v_exp_f32 v157, v157\n"
    "v_exp_f32 v158, v158\n"
    "v_cvt_pk_bf16_f32 v186, v156, v157\n"
    "v_exp_f32 v159, v159\n"
    "v_mfma_f32_32x32x16_bf16 v[136:151], a[76:79], v[192:195], v[136:151]\n"
    "v_exp_f32 v160, v160\n"
    "v_exp_f32 v161, v161\n"
    "v_exp_f32 v162, v162\n"
    "v_mfma_f32_32x32x16_bf16 v[136:151], a[80:83], v[196:199], v[136:151]\n"
    "v_cvt_pk_bf16_f32 v187, v158, v159\n"
    "v_exp_f32 v163, v163\n"
    "v_cvt_pk_bf16_f32 v188, v160, v161\n"
    "v_exp_f32 v164, v164\n"
    "v_cvt_pk_bf16_f32 v189, v162, v163\n"
    "v_exp_f32 v165, v165\n"
    "v_exp_f32 v166, v166\n"
    "v_exp_f32 v167, v167\n"
    "v_cvt_pk_bf16_f32 v190, v164, v165\n"
    "v_cvt_pk_bf16_f32 v191, v166, v167\n"
    "v_mfma_f32_32x32x16_bf16 v[152:167], a[84:87], v[8:11], v[56:71]\n"
    "v_exp_f32 v168, v168\n"
    "v_exp_f32 v169, v169\n"
    "v_exp_f32 v170, v170\n"
    "v_mfma_f32_32x32x8bf16_1k v[152:167], a[88:89], v[20:21], v[152:167]\n"
    "s_waitcnt vmcnt(24)\n"
    "v_exp_f32 v171, v171\n"
    "v_cvt_pk_bf16_f32 v192, v168, v169\n"
    "v_exp_f32 v172, v172\n"
    "v_cvt_pk_bf16_f32 v193, v170, v171\n"
    "v_exp_f32 v173, v173\n"
    "v_exp_f32 v174, v174\n"
    "v_cvt_pk_bf16_f32 v194, v172, v173\n"
    "v_exp_f32 v175, v175\n"
    "v_mfma_f32_32x32x16_bf16 v[88:103], a[90:93], v[184:187], v[88:103]\n"
    "s_add_u32 s68, s68, 2432\n"
    "v_exp_f32 v176, v176\n"
    "v_exp_f32 v177, v177\n"
    "v_exp_f32 v178, v178\n"
    "v_mfma_f32_32x32x16_bf16 v[88:103], a[94:97], v[188:191], v[88:103]\n"
    "buffer_load_dwordx4 a[70:73], v239, s[44:47], s68 offen sc1\n"
    "v_cvt_pk_bf16_f32 v195, v174, v175\n"
    "v_exp_f32 v179, v179\n"
    "v_cvt_pk_bf16_f32 v196, v176, v177\n"
    "v_exp_f32 v180, v180\n"
    "v_cvt_pk_bf16_f32 v197, v178, v179\n"
    "v_exp_f32 v181, v181\n"
    "v_exp_f32 v182, v182\n"
    "v_exp_f32 v183, v183\n"
    "v_cvt_pk_bf16_f32 v198, v180, v181\n"
    "v_cvt_pk_bf16_f32 v199, v182, v183\n"
    "v_mfma_f32_32x32x16_bf16 v[168:183], a[84:87], v[12:15], v[72:87]\n"
    "v_exp_f32 v152, v152\n"
    "v_exp_f32 v153, v153\n"
    "v_exp_f32 v154, v154\n"
    "v_mfma_f32_32x32x8bf16_1k v[168:183], a[88:89], v[22:23], v[168:183]\n"
    "buffer_load_dword a74, v243, s[44:47], s68 offen sc1\n"
    "v_exp_f32 v155, v155\n"
    "v_cvt_pk_bf16_f32 v184, v152, v153\n"
    "v_exp_f32 v156, v156\n"
    "v_cvt_pk_bf16_f32 v185, v154, v155\n"
    "v_exp_f32 v157, v157\n"
    "v_exp_f32 v158, v158\n"
    "v_cvt_pk_bf16_f32 v186, v156, v157\n"
    "v_exp_f32 v159, v159\n"
    "v_mfma_f32_32x32x16_bf16 v[104:119], a[90:93], v[192:195], v[104:119]\n"
    "v_exp_f32 v160, v160\n"
    "v_exp_f32 v161, v161\n"
    "v_exp_f32 v162, v162\n"
    "v_mfma_f32_32x32x16_bf16 v[104:119], a[94:97], v[196:199], v[104:119]\n"
    "s_waitcnt vmcnt(24)\n"
    "buffer_load_dwordx4 a[76:79], v244, s[44:47], s68 offen sc1\n"
    "v_cvt_pk_bf16_f32 v187, v158, v159\n"
    "v_exp_f32 v163, v163\n"
    "v_cvt_pk_bf16_f32 v188, v160, v161\n"
    "v_exp_f32 v164, v164\n"
    "v_cvt_pk_bf16_f32 v189, v162, v163\n"
    "v_exp_f32 v165, v165\n"
    "v_exp_f32 v166, v166\n"
    "v_exp_f32 v167, v167\n"
    "v_cvt_pk_bf16_f32 v190, v164, v165\n"
    "v_cvt_pk_bf16_f32 v191, v166, v167\n"
    "v_mfma_f32_32x32x16_bf16 v[152:167], a[98:101], v[0:3], v[24:39]\n"
    "v_exp_f32 v168, v168\n"
    "v_exp_f32 v169, v169\n"
    "v_exp_f32 v170, v170\n"
    "v_mfma_f32_32x32x8bf16_1k v[152:167], a[102:103], v[16:17], v[152:167]\n"
    "buffer_load_dwordx4 a[80:83], v244, s[44:47], s68 offen offset:640 sc1\n"
    "v_exp_f32 v171, v171\n"
    "v_cvt_pk_bf16_f32 v192, v168, v169\n"
    "v_exp_f32 v172, v172\n"
    "v_cvt_pk_bf16_f32 v193, v170, v171\n"
    "v_exp_f32 v173, v173\n"
    "v_exp_f32 v174, v174\n"
    "v_cvt_pk_bf16_f32 v194, v172, v173\n"
    "v_exp_f32 v175, v175\n"
    "v_mfma_f32_32x32x16_bf16 v[120:135], a[90:93], v[184:187], v[120:135]\n"
    "v_exp_f32 v176, v176\n"
    "v_exp_f32 v177, v177\n"
    "v_exp_f32 v178, v178\n"
    "v_mfma_f32_32x32x16_bf16 v[120:135], a[94:97], v[188:191], v[120:135]\n"
    "v_cvt_pk_bf16_f32 v195, v174, v175\n"
    "v_exp_f32 v179, v179\n"
    "v_cvt_pk_bf16_f32 v196, v176, v177\n"
    "v_exp_f32 v180, v180\n"
    "v_cvt_pk_bf16_f32 v197, v178, v179\n"
    "v_exp_f32 v181, v181\n"
    "v_exp_f32 v182, v182\n"
    "v_exp_f32 v183, v183\n"
    "v_cvt_pk_bf16_f32 v198, v180, v181\n"
    "v_cvt_pk_bf16_f32 v199, v182, v183\n"
    "v_mfma_f32_32x32x16_bf16 v[168:183], a[98:101], v[4:7], v[40:55]\n"
    "v_exp_f32 v152, v152\n"
    "v_exp_f32 v153, v153\n"
    "v_exp_f32 v154, v154\n"
    "v_mfma_f32_32x32x8bf16_1k v[168:183], a[102:103], v[18:19], v[168:183]\n"
    "v_exp_f32 v155, v155\n"
    "v_cvt_pk_bf16_f32 v184, v152, v153\n"
    "v_exp_f32 v156, v156\n"
    "v_cvt_pk_bf16_f32 v185, v154, v155\n"
    "v_exp_f32 v157, v157\n"
    "v_exp_f32 v158, v158\n"
    "v_cvt_pk_bf16_f32 v186, v156, v157\n"
    "v_exp_f32 v159, v159\n"
    "v_mfma_f32_32x32x16_bf16 v[136:151], a[90:93], v[192:195], v[136:151]\n"
    "v_exp_f32 v160, v160\n"
    "v_exp_f32 v161, v161\n"
    "v_exp_f32 v162, v162\n"
    "v_mfma_f32_32x32x16_bf16 v[136:151], a[94:97], v[196:199], v[136:151]\n"
    "v_cvt_pk_bf16_f32 v187, v158, v159\n"
    "v_exp_f32 v163, v163\n"
    "v_cvt_pk_bf16_f32 v188, v160, v161\n"
    "v_exp_f32 v164, v164\n"
    "v_cvt_pk_bf16_f32 v189, v162, v163\n"
    "v_exp_f32 v165, v165\n"
    "v_exp_f32 v166, v166\n"
    "v_exp_f32 v167, v167\n"
    "v_cvt_pk_bf16_f32 v190, v164, v165\n"
    "v_cvt_pk_bf16_f32 v191, v166, v167\n"
    "v_mfma_f32_32x32x16_bf16 v[152:167], a[98:101], v[8:11], v[56:71]\n"
    "v_exp_f32 v168, v168\n"
    "v_exp_f32 v169, v169\n"
    "v_exp_f32 v170, v170\n"
    "v_mfma_f32_32x32x8bf16_1k v[152:167], a[102:103], v[20:21], v[152:167]\n"
    "s_waitcnt vmcnt(24)\n"
    "v_exp_f32 v171, v171\n"
    "v_cvt_pk_bf16_f32 v192, v168, v169\n"
    "v_exp_f32 v172, v172\n"
    "v_cvt_pk_bf16_f32 v193, v170, v171\n"
    "v_exp_f32 v173, v173\n"
    "v_exp_f32 v174, v174\n"
    "v_cvt_pk_bf16_f32 v194, v172, v173\n"
    "v_exp_f32 v175, v175\n"
    "v_mfma_f32_32x32x16_bf16 v[88:103], a[104:107], v[184:187], v[88:103]\n"
    "s_add_u32 s68, s68, 2432\n"
    "v_exp_f32 v176, v176\n"
    "v_exp_f32 v177, v177\n"
    "v_exp_f32 v178, v178\n"
    "v_mfma_f32_32x32x16_bf16 v[88:103], a[108:111], v[188:191], v[88:103]\n"
    "buffer_load_dwordx4 a[84:87], v239, s[44:47], s68 offen sc1\n"
    "v_cvt_pk_bf16_f32 v195, v174, v175\n"
    "v_exp_f32 v179, v179\n"
    "v_cvt_pk_bf16_f32 v196, v176, v177\n"
    "v_exp_f32 v180, v180\n"
    "v_cvt_pk_bf16_f32 v197, v178, v179\n"
    "v_exp_f32 v181, v181\n"
    "v_exp_f32 v182, v182\n"
    "v_exp_f32 v183, v183\n"
    "v_cvt_pk_bf16_f32 v198, v180, v181\n"
    "v_cvt_pk_bf16_f32 v199, v182, v183\n"
    "v_mfma_f32_32x32x16_bf16 v[168:183], a[98:101], v[12:15], v[72:87]\n"
    "v_exp_f32 v152, v152\n"
    "v_exp_f32 v153, v153\n"
    "v_exp_f32 v154, v154\n"
    "v_mfma_f32_32x32x8bf16_1k v[168:183], a[102:103], v[22:23], v[168:183]\n"
    "buffer_load_dword a88, v243, s[44:47], s68 offen sc1\n"
    "v_exp_f32 v155, v155\n"
    "v_cvt_pk_bf16_f32 v184, v152, v153\n"
    "v_exp_f32 v156, v156\n"
    "v_cvt_pk_bf16_f32 v185, v154, v155\n"
    "v_exp_f32 v157, v157\n"
    "v_exp_f32 v158, v158\n"
    "v_cvt_pk_bf16_f32 v186, v156, v157\n"
    "v_exp_f32 v159, v159\n"
    "v_mfma_f32_32x32x16_bf16 v[104:119], a[104:107], v[192:195], v[104:119]\n"
    "v_exp_f32 v160, v160\n"
    "v_exp_f32 v161, v161\n"
    "v_exp_f32 v162, v162\n"
    "v_mfma_f32_32x32x16_bf16 v[104:119], a[108:111], v[196:199], v[104:119]\n"
    "s_waitcnt vmcnt(24)\n"
    "buffer_load_dwordx4 a[90:93], v244, s[44:47], s68 offen sc1\n"
    "v_cvt_pk_bf16_f32 v187, v158, v159\n"
    "v_exp_f32 v163, v163\n"
    "v_cvt_pk_bf16_f32 v188, v160, v161\n"
    "v_exp_f32 v164, v164\n"
    "v_cvt_pk_bf16_f32 v189, v162, v163\n"
    "v_exp_f32 v165, v165\n"
    "v_exp_f32 v166, v166\n"
    "v_exp_f32 v167, v167\n"
    "v_cvt_pk_bf16_f32 v190, v164, v165\n"
    "v_cvt_pk_bf16_f32 v191, v166, v167\n"
    "v_mfma_f32_32x32x16_bf16 v[152:167], a[0:3], v[0:3], v[24:39]\n"
    "v_exp_f32 v168, v168\n"
    "v_exp_f32 v169, v169\n"
    "v_exp_f32 v170, v170\n"
    "v_mfma_f32_32x32x8bf16_1k v[152:167], a[4:5], v[16:17], v[152:167]\n"
    "buffer_load_dwordx4 a[94:97], v244, s[44:47], s68 offen offset:640 sc1\n"
    "v_exp_f32 v171, v171\n"
    "v_cvt_pk_bf16_f32 v192, v168, v169\n"
    "v_exp_f32 v172, v172\n"
    "v_cvt_pk_bf16_f32 v193, v170, v171\n"
    "v_exp_f32 v173, v173\n"
    "v_exp_f32 v174, v174\n"
    "v_cvt_pk_bf16_f32 v194, v172, v173\n"
    "v_exp_f32 v175, v175\n"
    "v_mfma_f32_32x32x16_bf16 v[120:135], a[104:107], v[184:187], v[120:135]\n"
    "s_sub_u32 s66, s66, 1\n"
    "v_exp_f32 v176, v176\n"
    "v_exp_f32 v177, v177\n"
    "v_exp_f32 v178, v178\n"
    "v_mfma_f32_32x32x16_bf16 v[120:135], a[108:111], v[188:191], v[120:135]\n"
    "s_cmp_lg_u32 s66, 0\n"
    "v_cvt_pk_bf16_f32 v195, v174, v175\n"
    "v_exp_f32 v179, v179\n"
    "v_cvt_pk_bf16_f32 v196, v176, v177\n"
    "v_exp_f32 v180, v180\n"
    "v_cvt_pk_bf16_f32 v197, v178, v179\n"
    "v_exp_f32 v181, v181\n"
    "v_exp_f32 v182, v182\n"
    "v_exp_f32 v183, v183\n"
    "v_cvt_pk_bf16_f32 v198, v180, v181\n"
    "v_cvt_pk_bf16_f32 v199, v182, v183\n"
    "s_cbranch_scc1 1b\n"
    "95:\n"
    "s_nop 1\n"
    "v_mfma_f32_32x32x16_bf16 v[136:151], a[104:107], v[192:195], v[136:151]\n"
    "v_mfma_f32_32x32x16_bf16 v[136:151], a[108:111], v[196:199], v[136:151]\n"
    "s_mov_b64 s[70:71], 0\n"
    "s_mov_b32 s72, 0x7b800000\n"
    "s_movk_i32 s73, 0x207\n"
    "s_nop 3\n"
    "v_mov_b32 v208, v98\n"
    "s_nop 1\n"
    "v_permlane32_swap_b32 v98, v208\n"
    "s_nop 1\n"
    "v_cmp_ngt_f32 vcc, s72, v98\n"
    "v_rcp_f32 v204, v98\n"
    "s_or_b64 s[70:71], s[70:71], vcc\n"
    "v_mul_f32 v88, v204, v88\n"
    "v_mul_f32 v89, v204, v89\n"
    "v_mul_f32 v90, v204, v90\n"
    "v_mul_f32 v91, v204, v91\n"
    "v_mul_f32 v92, v204, v92\n"
    "v_mul_f32 v93, v204, v93\n"
    "v_mul_f32 v94, v204, v94\n"
    "v_mul_f32 v95, v204, v95\n"
    "v_mul_f32 v96, v204, v96\n"
    "v_mul_f32 v97, v204, v97\n"
    "v_add_f32_e64 v208, |v88|, |v89|\n"
    "v_add_f32_e64 v208, v208, |v90|\n"
    "v_add_f32_e64 v208, v208, |v91|\n"
    "v_add_f32_e64 v208, v208, |v92|\n"
    "v_add_f32_e64 v208, v208, |v93|\n"
    "v_add_f32_e64 v208, v208, |v94|\n"
    "v_add_f32_e64 v208, v208, |v95|\n"
    "v_add_f32_e64 v208, v208, |v96|\n"
    "v_add_f32_e64 v208, v208, |v97|\n"
    "v_cmp_class_f32_e64 vcc, v208, s73\n"
    "s_nop 0\n"
    "s_or_b64 s[70:71], s[70:71], vcc\n"
    "v_mov_b32 v209, v114\n"
    "s_nop 1\n"
    "v_permlane32_swap_b32 v114, v209\n"
    "s_nop 1\n"
    "v_cmp_ngt_f32 vcc, s72, v114\n"
    "v_rcp_f32 v205, v114\n"
    "s_or_b64 s[70:71], s[70:71], vcc\n"
    "v_mul_f32 v104, v205, v104\n"
    "v_mul_f32 v105, v205, v105\n"
    "v_mul_f32 v106, v205, v106\n"
    "v_mul_f32 v107, v205, v107\n"
    "v_mul_f32 v108, v205, v108\n"
    "v_mul_f32 v109, v205, v109\n"
    "v_mul_f32 v110, v205, v110\n"
    "v_mul_f32 v111, v205, v111\n"
    "v_mul_f32 v112, v205, v112\n"
    "v_mul_f32 v113, v205, v113\n"
    "v_add_f32_e64 v209, |v104|, |v105|\n"
    "v_add_f32_e64 v209, v209, |v106|\n"
    "v_add_f32_e64 v209, v209, |v107|\n"
    "v_add_f32_e64 v209, v209, |v108|\n"
    "v_add_f32_e64 v209, v209, |v109|\n"
    "v_add_f32_e64 v209, v209, |v110|\n"
    "v_add_f32_e64 v209, v209, |v111|\n"
    "v_add_f32_e64 v209, v209, |v112|\n"
    "v_add_f32_e64 v209, v209, |v113|\n"
    "v_cmp_class_f32_e64 vcc, v209, s73\n"
    "s_nop 0\n"
    "s_or_b64 s[70:71], s[70:71], vcc\n"
    "v_mov_b32 v210, v130\n"
    "s_nop 1\n"
    "v_permlane32_swap_b32 v130, v210\n"
    "s_nop 1\n"
    "v_cmp_ngt_f32 vcc, s72, v130\n"
    "v_rcp_f32 v206, v130\n"
    "s_or_b64 s[70:71], s[70:71], vcc\n"
    "v_mul_f32 v120, v206, v120\n"
    "v_mul_f32 v121, v206, v121\n"
    "v_mul_f32 v122, v206, v122\n"
    "v_mul_f32 v123, v206, v123\n"
    "v_mul_f32 v124, v206, v124\n"
    "v_mul_f32 v125, v206, v125\n"
    "v_mul_f32 v126, v206, v126\n"
    "v_mul_f32 v127, v206, v127\n"
    "v_mul_f32 v128, v206, v128\n"
    "v_mul_f32 v129, v206, v129\n"
    "v_add_f32_e64 v210, |v120|, |v121|\n"
    "v_add_f32_e64 v210, v210, |v122|\n"
    "v_add_f32_e64 v210, v210, |v123|\n"
    "v_add_f32_e64 v210, v210, |v124|\n"
    "v_add_f32_e64 v210, v210, |v125|\n"
    "v_add_f32_e64 v210, v210, |v126|\n"
    "v_add_f32_e64 v210, v210, |v127|\n"
    "v_add_f32_e64 v210, v210, |v128|\n"
    "v_add_f32_e64 v210, v210, |v129|\n"
    "v_cmp_class_f32_e64 vcc, v210, s73\n"
    "s_nop 0\n"
    "s_or_b64 s[70:71], s[70:71], vcc\n"
    "v_mov_b32 v211, v146\n"
    "s_nop 1\n"
    "v_permlane32_swap_b32 v146, v211\n"
    "s_nop 1\n"
    "v_cmp_ngt_f32 vcc, s72, v146\n"
    "v_rcp_f32 v207, v146\n"
    "s_or_b64 s[70:71], s[70:71], vcc\n"
    "v_mul_f32 v136, v207, v136\n"
    "v_mul_f32 v137, v207, v137\n"
    "v_mul_f32 v138, v207, v138\n"
    "v_mul_f32 v139, v207, v139\n"
    "v_mul_f32 v140, v207, v140\n"
    "v_mul_f32 v141, v207, v141\n"
    "v_mul_f32 v142, v207, v142\n"
    "v_mul_f32 v143, v207, v143\n"
    "v_mul_f32 v144, v207, v144\n"
    "v_mul_f32 v145, v207, v145\n"
    "v_add_f32_e64 v211, |v136|, |v137|\n"
    "v_add_f32_e64 v211, v211, |v138|\n"
    "v_add_f32_e64 v211, v211, |v139|\n"
    "v_add_f32_e64 v211, v211, |v140|\n"
    "v_add_f32_e64 v211, v211, |v141|\n"
    "v_add_f32_e64 v211, v211, |v142|\n"
    "v_add_f32_e64 v211, v211, |v143|\n"
    "v_add_f32_e64 v211, v211, |v144|\n"
    "v_add_f32_e64 v211, v211, |v145|\n"
    "v_cmp_class_f32_e64 vcc, v211, s73\n"
    "s_nop 0\n"
    "s_or_b64 s[70:71], s[70:71], vcc\n"
    "s_cmp_eq_u64 s[70:71], 0\n"
    "s_cbranch_scc1 50f\n"
    "s_cmp_lg_u32 s69, 0\n"
    "s_cbranch_scc1 50f\n"
    "s_mov_b32 s69, 1\n"
    "s_mov_b32 s68, 0\n"
    "s_waitcnt vmcnt(0)\n"
    "v_mov_b32 v204, 0xff800000\n"
    "v_mov_b32 v205, 0xff800000\n"
    "v_mov_b32 v206, 0xff800000\n"
    "v_mov_b32 v207, 0xff800000\n"
    "41:\n"
    "buffer_load_dwordx4 a[0:3], v239, s[44:47], s68 offen sc1\n"
    "buffer_load_dword a4, v243, s[44:47], s68 offen sc1\n"
    "s_waitcnt vmcnt(0)\n"
    "v_mfma_f32_32x32x16_bf16 v[152:167], a[0:3], v[0:3], 0\n"
    "v_mfma_f32_32x32x8bf16_1k v[152:167], a[4:5], v[16:17], v[152:167]\n"
    "v_mfma_f32_32x32x16_bf16 v[168:183], a[0:3], v[4:7], 0\n"
    "v_mfma_f32_32x32x8bf16_1k v[168:183], a[4:5], v[18:19], v[168:183]\n"
    "s_nop 7\n"
    "s_nop 7\n"
    "v_max3_f32 v204, v204, v152, v153\n"
    "v_max3_f32 v204, v204, v154, v155\n"
    "v_max3_f32 v204, v204, v156, v157\n"
    "v_max3_f32 v204, v204, v158, v159\n"
    "v_max3_f32 v204, v204, v160, v161\n"
    "v_max3_f32 v204, v204, v162, v163\n"
    "v_max3_f32 v204, v204, v164, v165\n"
    "v_max3_f32 v204, v204, v166, v167\n"
    "v_max3_f32 v205, v205, v168, v169\n"
    "v_max3_f32 v205, v205, v170, v171\n"
    "v_max3_f32 v205, v205, v172, v173\n"
    "v_max3_f32 v205, v205, v174, v175\n"
    "v_max3_f32 v205, v205, v176, v177\n"
    "v_max3_f32 v205, v205, v178, v179\n"
    "v_max3_f32 v205, v205, v180, v181\n"
    "v_max3_f32 v205, v205, v182, v183\n"
    "v_mfma_f32_32x32x16_bf16 v[152:167], a[0:3], v[8:11], 0\n"
    "v_mfma_f32_32x32x8bf16_1k v[152:167], a[4:5], v[20:21], v[152:167]\n"
    "v_mfma_f32_32x32x16_bf16 v[168:183], a[0:3], v[12:15], 0\n"
    "v_mfma_f32_32x32x8bf16_1k v[168:183], a[4:5], v[22:23], v[168:183]\n"
    "s_nop 7\n"
    "s_nop 7\n"
    "v_max3_f32 v206, v206, v152, v153\n"
    "v_max3_f32 v206, v206, v154, v155\n"
    "v_max3_f32 v206, v206, v156, v157\n"
    "v_max3_f32 v206, v206, v158, v159\n"
    "v_max3_f32 v206, v206, v160, v161\n"
    "v_max3_f32 v206, v206, v162, v163\n"
    "v_max3_f32 v206, v206, v164, v165\n"
    "v_max3_f32 v206, v206, v166, v167\n"
    "v_max3_f32 v207, v207, v168, v169\n"
    "v_max3_f32 v207, v207, v170, v171\n"
    "v_max3_f32 v207, v207, v172, v173\n"
    "v_max3_f32 v207, v207, v174, v175\n"
    "v_max3_f32 v207, v207, v176, v177\n"
    "v_max3_f32 v207, v207, v178, v179\n"
    "v_max3_f32 v207, v207, v180, v181\n"
    "v_max3_f32 v207, v207, v182, v183\n"
    "s_add_u32 s68, s68, 2432\n"
    "s_cmp_lt_u32 s68, 155648\n"
    "s_cbranch_scc1 41b\n"
    "v_mov_b32 v208, v204\n"
    "v_mov_b32 v209, v205\n"
    "v_mov_b32 v210, v206\n"
    "v_mov_b32 v211, v207\n"
    "s_nop 1\n"
    "v_permlane32_swap_b32 v204, v208\n"
    "v_permlane32_swap_b32 v205, v209\n"
    "v_permlane32_swap_b32 v206, v210\n"
    "v_permlane32_swap_b32 v207, v211\n"
    "s_nop 1\n"
    "v_max_f32 v204, v204, v208\n"
    "v_max_f32 v205, v205, v209\n"
    "v_max_f32 v206, v206, v210\n"
    "v_max_f32 v207, v207, v211\n"
    "v_sub_f32 v204, 0xc2c00000, v204\n"
    "v_sub_f32 v205, 0xc2c00000, v205\n"
    "v_sub_f32 v206, 0xc2c00000, v206\n"
    "v_sub_f32 v207, 0xc2c00000, v207\n"
    "v_mov_b32 v24, v204\n"
    "v_mov_b32 v25, v204\n"
    "v_mov_b32 v26, v204\n"
    "v_mov_b32 v27, v204\n"
    "v_mov_b32 v28, v204\n"
    "v_mov_b32 v29, v204\n"
    "v_mov_b32 v30, v204\n"
    "v_mov_b32 v31, v204\n"
    "v_mov_b32 v32, v204\n"
    "v_mov_b32 v33, v204\n"
    "v_mov_b32 v34, v204\n"
    "v_mov_b32 v35, v204\n"
    "v_mov_b32 v36, v204\n"
    "v_mov_b32 v37, v204\n"
    "v_mov_b32 v38, v204\n"
    "v_mov_b32 v39, v204\n"
    "v_mov_b32 v40, v205\n"
    "v_mov_b32 v41, v205\n"
    "v_mov_b32 v42, v205\n"
    "v_mov_b32 v43, v205\n"
    "v_mov_b32 v44, v205\n"
    "v_mov_b32 v45, v205\n"
    "v_mov_b32 v46, v205\n"
    "v_mov_b32 v47, v205\n"
    "v_mov_b32 v48, v205\n"
    "v_mov_b32 v49, v205\n"
    "v_mov_b32 v50, v205\n"
    "v_mov_b32 v51, v205\n"
    "v_mov_b32 v52, v205\n"
    "v_mov_b32 v53, v205\n"
    "v_mov_b32 v54, v205\n"
    "v_mov_b32 v55, v205\n"
    "v_mov_b32 v56, v206\n"
    "v_mov_b32 v57, v206\n"
    "v_mov_b32 v58, v206\n"
    "v_mov_b32 v59, v206\n"
    "v_mov_b32 v60, v206\n"
    "v_mov_b32 v61, v206\n"
    "v_mov_b32 v62, v206\n"
    "v_mov_b32 v63, v206\n"
    "v_mov_b32 v64, v206\n"
    "v_mov_b32 v65, v206\n"
    "v_mov_b32 v66, v206\n"
    "v_mov_b32 v67, v206\n"
    "v_mov_b32 v68, v206\n"
    "v_mov_b32 v69, v206\n"
    "v_mov_b32 v70, v206\n"
    "v_mov_b32 v71, v206\n"
    "v_mov_b32 v72, v207\n"
    "v_mov_b32 v73, v207\n"
    "v_mov_b32 v74, v207\n"
    "v_mov_b32 v75, v207\n"
    "v_mov_b32 v76, v207\n"
    "v_mov_b32 v77, v207\n"
    "v_mov_b32 v78, v207\n"
    "v_mov_b32 v79, v207\n"
    "v_mov_b32 v80, v207\n"
    "v_mov_b32 v81, v207\n"
    "v_mov_b32 v82, v207\n"
    "v_mov_b32 v83, v207\n"
    "v_mov_b32 v84, v207\n"
    "v_mov_b32 v85, v207\n"
    "v_mov_b32 v86, v207\n"
    "v_mov_b32 v87, v207\n"
    "v_mov_b32 v88, 0\n"
    "v_mov_b32 v89, 0\n"
    "v_mov_b32 v90, 0\n"
    "v_mov_b32 v91, 0\n"
    "v_mov_b32 v92, 0\n"
    "v_mov_b32 v93, 0\n"
    "v_mov_b32 v94, 0\n"
    "v_mov_b32 v95, 0\n"
    "v_mov_b32 v96, 0\n"
    "v_mov_b32 v97, 0\n"
    "v_mov_b32 v98, 0\n"
    "v_mov_b32 v99, 0\n"
    "v_mov_b32 v100, 0\n"
    "v_mov_b32 v101, 0\n"
    "v_mov_b32 v102, 0\n"
    "v_mov_b32 v103, 0\n"
    "v_mov_b32 v104, 0\n"
    "v_mov_b32 v105, 0\n"
    "v_mov_b32 v106, 0\n"
    "v_mov_b32 v107, 0\n"
    "v_mov_b32 v108, 0\n"
    "v_mov_b32 v109, 0\n"
    "v_mov_b32 v110, 0\n"
    "v_mov_b32 v111, 0\n"
    "v_mov_b32 v112, 0\n"
    "v_mov_b32 v113, 0\n"
    "v_mov_b32 v114, 0\n"
    "v_mov_b32 v115, 0\n"
    "v_mov_b32 v116, 0\n"
    "v_mov_b32 v117, 0\n"
    "v_mov_b32 v118, 0\n"
    "v_mov_b32 v119, 0\n"
    "v_mov_b32 v120, 0\n"
    "v_mov_b32 v121, 0\n"
    "v_mov_b32 v122, 0\n"
    "v_mov_b32 v123, 0\n"
    "v_mov_b32 v124, 0\n"
    "v_mov_b32 v125, 0\n"
    "v_mov_b32 v126, 0\n"
    "v_mov_b32 v127, 0\n"
    "v_mov_b32 v128, 0\n"
    "v_mov_b32 v129, 0\n"
    "v_mov_b32 v130, 0\n"
    "v_mov_b32 v131, 0\n"
    "v_mov_b32 v132, 0\n"
    "v_mov_b32 v133, 0\n"
    "v_mov_b32 v134, 0\n"
    "v_mov_b32 v135, 0\n"
    "v_mov_b32 v136, 0\n"
    "v_mov_b32 v137, 0\n"
    "v_mov_b32 v138, 0\n"
    "v_mov_b32 v139, 0\n"
    "v_mov_b32 v140, 0\n"
    "v_mov_b32 v141, 0\n"
    "v_mov_b32 v142, 0\n"
    "v_mov_b32 v143, 0\n"
    "v_mov_b32 v144, 0\n"
    "v_mov_b32 v145, 0\n"
    "v_mov_b32 v146, 0\n"
    "v_mov_b32 v147, 0\n"
    "v_mov_b32 v148, 0\n"
    "v_mov_b32 v149, 0\n"
    "v_mov_b32 v150, 0\n"
    "v_mov_b32 v151, 0\n"
    "v_mov_b32 v192, 0\n"
    "v_accvgpr_write_b32 a104, 0\n"
    "v_mov_b32 v193, 0\n"
    "v_accvgpr_write_b32 a105, 0\n"
    "v_mov_b32 v194, 0\n"
    "v_accvgpr_write_b32 a106, 0\n"
    "v_mov_b32 v195, 0\n"
    "v_accvgpr_write_b32 a107, 0\n"
    "v_mov_b32 v196, 0\n"
    "v_accvgpr_write_b32 a108, 0\n"
    "v_mov_b32 v197, 0\n"
    "v_accvgpr_write_b32 a109, 0\n"
    "v_mov_b32 v198, 0\n"
    "v_accvgpr_write_b32 a110, 0\n"
    "v_mov_b32 v199, 0\n"
    "v_accvgpr_write_b32 a111, 0\n"
    "s_mov_b32 s68, 0\n"
    "buffer_load_dwordx4 a[0:3], v239, s[44:47], s68 offen sc1\n"
    "buffer_load_dword a4, v243, s[44:47], s68 offen sc1\n"
    "buffer_load_dwordx4 a[6:9], v244, s[44:47], s68 offen sc1\n"
    "buffer_load_dwordx4 a[10:13], v244, s[44:47], s68 offen offset:640 sc1\n"
    "s_mov_b32 s68, 2432\n"
    "buffer_load_dwordx4 a[14:17], v239, s[44:47], s68 offen sc1\n"
    "buffer_load_dword a18, v243, s[44:47], s68 offen sc1\n"
    "buffer_load_dwordx4 a[20:23], v244, s[44:47], s68 offen sc1\n"
    "buffer_load_dwordx4 a[24:27], v244, s[44:47], s68 offen offset:640 sc1\n"
    "s_mov_b32 s68, 4864\n"
    "buffer_load_dwordx4 a[28:31], v239, s[44:47], s68 offen sc1\n"
    "buffer_load_dword a32, v243, s[44:47], s68 offen sc1\n"
    "buffer_load_dwordx4 a[34:37], v244, s[44:47], s68 offen sc1\n"
    "buffer_load_dwordx4 a[38:41], v244, s[44:47], s68 offen offset:640 sc1\n"
    "s_mov_b32 s68, 7296\n"
    "buffer_load_dwordx4 a[42:45], v239, s[44:47], s68 offen sc1\n"
    "buffer_load_dword a46, v243, s[44:47], s68 offen sc1\n"
    "buffer_load_dwordx4 a[48:51], v244, s[44:47], s68 offen sc1\n"
    "buffer_load_dwordx4 a[52:55], v244, s[44:47], s68 offen offset:640 sc1\n"
    "s_mov_b32 s68, 9728\n"
    "buffer_load_dwordx4 a[56:59], v239, s[44:47], s68 offen sc1\n"
    "buffer_load_dword a60, v243, s[44:47], s68 offen sc1\n"
    "buffer_load_dwordx4 a[62:65], v244, s[44:47], s68 offen sc1\n"
    "buffer_load_dwordx4 a[66:69], v244, s[44:47], s68 offen offset:640 sc1\n"
    "s_mov_b32 s68, 12160\n"
    "buffer_load_dwordx4 a[70:73], v239, s[44:47], s68 offen sc1\n"
    "buffer_load_dword a74, v243, s[44:47], s68 offen sc1\n"
    "buffer_load_dwordx4 a[76:79], v244, s[44:47], s68 offen sc1\n"
    "buffer_load_dwordx4 a[80:83], v244, s[44:47], s68 offen offset:640 sc1\n"
    "s_mov_b32 s68, 14592\n"
    "buffer_load_dwordx4 a[84:87], v239, s[44:47], s68 offen sc1\n"
    "buffer_load_dword a88, v243, s[44:47], s68 offen sc1\n"
    "buffer_load_dwordx4 a[90:93], v244, s[44:47], s68 offen sc1\n"
    "buffer_load_dwordx4 a[94:97], v244, s[44:47], s68 offen offset:640 sc1\n"
    "s_waitcnt vmcnt(26)\n"
    "v_mfma_f32_32x32x16_bf16 v[152:167], a[0:3], v[0:3], v[24:39]\n"
    "v_mfma_f32_32x32x8bf16_1k v[152:167], a[4:5], v[16:17], v[152:167]\n"
    "s_mov_b32 s66, 8\n"
    "s_nop 7\n"
    "s_nop 7\n"
    "s_branch 1b\n"
    "50:\n"
    "v_cvt_pk_bf16_f32 v32, v96, v97\n"
    "v_cvt_pk_bf16_f32 v33, v112, v113\n"
    "v_cvt_pk_bf16_f32 v34, v128, v129\n"
    "v_cvt_pk_bf16_f32 v35, v144, v145\n"
    "s_mov_b32 exec_hi, 0\n"
    "ds_write_b32 v214, v32 offset:0\n"
    "ds_write_b32 v214, v33 offset:512\n"
    "ds_write_b32 v214, v34 offset:1024\n"
    "ds_write_b32 v214, v35 offset:1536\n"
    "s_mov_b32 exec_hi, -1\n"
    "s_mov_b64 s[48:49], s[52:53]\n"
    "v_cvt_pk_bf16_f32 v0, v88, v89\n"
    "v_cvt_pk_bf16_f32 v1, v90, v91\n"
    "v_cvt_pk_bf16_f32 v2, v92, v93\n"
    "v_cvt_pk_bf16_f32 v3, v94, v95\n"
    "global_store_dwordx4 v212, v[0:3], s[48:49] sc1\n"
    "s_add_u32 s48, s48, 36864\n"
    "s_addc_u32 s49, s49, 0\n"
    "v_cvt_pk_bf16_f32 v8, v104, v105\n"
    "v_cvt_pk_bf16_f32 v9, v106, v107\n"
    "v_cvt_pk_bf16_f32 v10, v108, v109\n"
    "v_cvt_pk_bf16_f32 v11, v110, v111\n"
    "s_waitcnt lgkmcnt(0)\n"
    "s_barrier\n"
    "ds_read_b128 v[40:43], v215\n"
    "global_store_dwordx4 v212, v[8:11], s[48:49] sc1\n"
    "s_add_u32 s48, s48, 36864\n"
    "s_addc_u32 s49, s49, 0\n"
    "v_cvt_pk_bf16_f32 v16, v120, v121\n"
    "v_cvt_pk_bf16_f32 v17, v122, v123\n"
    "v_cvt_pk_bf16_f32 v18, v124, v125\n"
    "v_cvt_pk_bf16_f32 v19, v126, v127\n"
    "global_store_dwordx4 v212, v[16:19], s[48:49] sc1\n"
    "s_add_u32 s48, s48, 36864\n"
    "s_addc_u32 s49, s49, 0\n"
    "v_cvt_pk_bf16_f32 v24, v136, v137\n"
    "v_cvt_pk_bf16_f32 v25, v138, v139\n"
    "v_cvt_pk_bf16_f32 v26, v140, v141\n"
    "v_cvt_pk_bf16_f32 v27, v142, v143\n"
    "global_store_dwordx4 v212, v[24:27], s[48:49] sc1\n"
    "s_add_u32 s48, s48, 36864\n"
    "s_addc_u32 s49, s49, 0\n"
    "s_waitcnt lgkmcnt(0)\n"
    "s_mov_b32 exec_hi, 0\n"
    "global_store_dwordx4 v213, v[40:43], s[52:53] sc1\n"
    "s_mov_b32 exec_hi, -1\n"
    "s_add_u32 s58, %8, 32768\n"
    "s_add_u32 s59, %8, 69632\n"
    "s_add_u32 s60, %8, 106496\n"
    "s_lshl_b32 s67, %12, 2\n"
    "s_add_u32 s67, s67, %7\n"
    "s_mul_i32 s68, s67, 36864\n"
    "s_add_u32 s69, s68, 33554432\n"
    "s_mov_b64 s[40:41], %2\n"
    "s_add_u32 s40, s40, s69\n"
    "s_addc_u32 s41, s41, 0\n"
    "s_and_b32 s41, s41, 0xffff\n"
    "s_mov_b32 s42, 36864\n"
    "s_mov_b32 s43, 0x20000\n"
    "s_add_u32 s69, s68, 1048576\n"
    "s_mov_b64 s[44:45], %2\n"
    "s_add_u32 s44, s44, s69\n"
    "s_addc_u32 s45, s45, 0\n"
    "s_and_b32 s45, s45, 0xffff\n"
    "s_mov_b32 s46, 16551936\n"
    "s_mov_b32 s47, 0x20000\n"
    "s_add_u32 s68, s68, 0\n"
    "s_mov_b64 s[50:51], %4\n"
    "s_add_u32 s50, s50, s68\n"
    "s_addc_u32 s51, s51, 0\n"
    "s_and_b32 s57, %13, 3\n"
    "s_cmp_eq_u32 s57, %7\n"
    "s_cselect_b32 s57, 1, 0\n"
    "v_lshlrev_b32 v129, 4, v255\n"
    "v_and_b32 v126, 15, v255\n"
    "v_mul_u32_u24 v130, 0x480, v126\n"
    "v_lshrrev_b32 v127, 4, v255\n"
    "v_and_b32 v131, 1, v127\n"
    "v_lshl_add_u32 v130, v131, 5, v130\n"
    "v_lshrrev_b32 v131, 1, v127\n"
    "v_lshl_add_u32 v130, v131, 4, v130\n"
    "v_lshlrev_b32 v125, 6, v126\n"
    "v_lshl_add_u32 v125, v127, 3, v125\n"
    "v_bfe_u32 v131, v126, 2, 1\n"
    "v_lshl_add_u32 v125, v131, 5, v125\n"
    "v_add_u32 v125, s61, v125\n"
    "v_xor_b32 v135, 32, v125\n"
    "s_lshl_b32 s67, s54, 6\n"
    "v_add_u32 v122, s67, v130\n"
    "s_lshl_b32 s67, s55, 6\n"
    "v_add_u32 v123, s67, v130\n"
    "s_lshl_b32 s67, s56, 6\n"
    "v_add_u32 v124, s67, v130\n"
    "v_lshlrev_b32 v131, 2, v127\n"
    "v_sub_u32 v131, v126, v131\n"
    "v_mov_b32 v132, 0x3f80\n"
    "v_mov_b32 v133, 0x3f800000\n"
    "v_lshlrev_b32 v126, 2, v126\n"
    "s_mul_i32 s67, %7, 0\n"
    "v_add_u32 v126, s67, v126\n"
    "v_cmp_eq_u32 vcc, 0, v131\n"
    "v_cmp_eq_u32 s[74:75], 1, v131\n"
    "v_mov_b32 v108, 0\n"
    "v_cndmask_b32 v110, 0, v132, vcc\n"
    "v_mov_b32 v112, 0\n"
    "v_cndmask_b32 v110, v110, v133, s[74:75]\n"
    "v_cmp_eq_u32 vcc, 2, v131\n"
    "v_cmp_eq_u32 s[74:75], 3, v131\n"
    "v_mov_b32 v109, 0\n"
    "v_cndmask_b32 v111, 0, v132, vcc\n"
    "v_mov_b32 v113, 0\n"
    "v_cndmask_b32 v111, v111, v133, s[74:75]\n"
    "v_bfe_u32 v131, v255, 2, 2\n"
    "v_and_b32 v132, 3, v255\n"
    "v_lshlrev_b32 v132, 3, v132\n"
    "v_lshrrev_b32 v133, 1, v127\n"
    "v_mul_u32_u24 v133, 18, v133\n"
    "v_and_b32 v134, 1, v127\n"
    "v_lshl_add_u32 v133, v134, 2, v133\n"
    "v_add_u32 v133, v133, v131\n"
    "v_bfe_u32 v134, v133, 2, 1\n"
    "v_lshl_add_u32 v133, v133, 6, v132\n"
    "v_lshl_add_u32 v133, v134, 5, v133\n"
    "v_lshrrev_b32 v134, 1, v131\n"
    "v_lshl_add_u32 v134, v127, 2, v134\n"
    "v_mul_u32_u24 v134, 18, v134\n"
    "v_and_b32 v131, 1, v131\n"
    "v_add3_u32 v134, v134, v131, 16\n"
    "v_bfe_u32 v131, v134, 2, 1\n"
    "v_lshl_add_u32 v134, v134, 6, v132\n"
    "v_lshl_add_u32 v134, v131, 5, v134\n"
    "v_add_u32 v114, s58, v133\n"
    "v_add_u32 v117, s58, v134\n"
    "v_xor_b32 v180, 32, v114\n"
    "v_xor_b32 v183, 32, v117\n"
    "v_add_u32 v115, s59, v133\n"
    "v_add_u32 v118, s59, v134\n"
    "v_xor_b32 v181, 32, v115\n"
    "v_xor_b32 v184, 32, v118\n"
    "v_add_u32 v116, s60, v133\n"
    "v_add_u32 v119, s60, v134\n"
    "v_xor_b32 v182, 32, v116\n"
    "v_xor_b32 v185, 32, v119\n"
    "s_lshl_b32 s63, s54, 11\n"
    "s_lshl_b32 s64, s55, 11\n"
    "s_lshl_b32 s65, s56, 11\n"
    "s_waitcnt vmcnt(0)\n"
    "s_barrier\n"
    "s_cmp_lg_u32 %7, 0\n"
    "s_cbranch_scc1 60f\n"
    "s_lshl_b32 s67, %6, 2\n"
    "v_mov_b32 v47, s78\n"
    "v_mov_b32 v46, s67\n"
    "s_mov_b64 exec, 1\n"
    "global_store_dword v46, v47, s[30:31] offset:128 sc1\n"
    "s_mov_b64 exec, -1\n"
    "60:\n"
    "ds_read_b64 v[96:97], v125 offset:0\n"
    "ds_read_b64 v[98:99], v135 offset:0\n"
    "ds_read_b64 v[100:101], v125 offset:1024\n"
    "ds_read_b64 v[102:103], v135 offset:1024\n"
    "ds_read_b64 v[104:105], v125 offset:2048\n"
    "ds_read_b64 v[106:107], v135 offset:2048\n"
    "s_mov_b32 s70, 0\n"
    "s_mov_b32 s66, 0x80000\n"
    "s_mov_b32 s69, 0\n"
    "61:\n"
    "global_load_dword v128, v126, s[32:33] offset:128 sc1\n"
    "s_waitcnt vmcnt(0) lgkmcnt(0)\n"
    "v_cmp_eq_u32 vcc, s62, v128\n"
    "s_nop 1\n"
    "s_and_b32 s72, vcc_lo, 0xffff\n"
    "s_andn2_b32 s72, s72, s69\n"
    "s_or_b32 s67, s72, s69\n"
    "s_and_b32 s67, s67, 0xff\n"
    "s_cmp_eq_u32 s67, 0xff\n"
    "s_cbranch_scc1 59f\n"
    "s_and_b32 s71, s72, 0xff00\n"
    "s_and_b32 s72, s72, 0xff\n"
    "59:\n"
    "s_cmp_lg_u32 s72, 0\n"
    "s_cbranch_scc0 68f\n"
    "s_or_b32 s69, s69, s72\n"
    "s_cmp_eq_u32 s57, 0\n"
    "s_cbranch_scc1 62f\n"
    "s_bitcmp1_b32 s72, 8\n"
    "s_cbranch_scc0 65f\n"
    "s_add_u32 s67, s65, 0\n"
    "buffer_load_dwordx4 a[64:67], v129, s[44:47], s67 offen sc1\n"
    "65:\n"
    "s_bitcmp1_b32 s72, 9\n"
    "s_cbranch_scc0 65f\n"
    "s_add_u32 s67, s65, 2359296\n"
    "buffer_load_dwordx4 a[68:71], v129, s[44:47], s67 offen sc1\n"
    "65:\n"
    "s_bitcmp1_b32 s72, 10\n"
    "s_cbranch_scc0 65f\n"
    "s_add_u32 s67, s65, 4718592\n"
    "buffer_load_dwordx4 a[72:75], v129, s[44:47], s67 offen sc1\n"
    "65:\n"
    "s_bitcmp1_b32 s72, 11\n"
    "s_cbranch_scc0 65f\n"
    "s_add_u32 s67, s65, 7077888\n"
    "buffer_load_dwordx4 a[76:79], v129, s[44:47], s67 offen sc1\n"
    "65:\n"
    "s_bitcmp1_b32 s72, 12\n"
    "s_cbranch_scc0 65f\n"
    "s_add_u32 s67, s65, 9437184\n"
    "buffer_load_dwordx4 a[80:83], v129, s[44:47], s67 offen sc1\n"
    "65:\n"
    "s_bitcmp1_b32 s72, 13\n"
    "s_cbranch_scc0 65f\n"
    "s_add_u32 s67, s65, 11796480\n"
    "buffer_load_dwordx4 a[84:87], v129, s[44:47], s67 offen sc1\n"
    "65:\n"
    "s_bitcmp1_b32 s72, 14\n"
    "s_cbranch_scc0 65f\n"
    "s_add_u32 s67, s65, 14155776\n"
    "buffer_load_dwordx4 a[88:91], v129, s[44:47], s67 offen sc1\n"
    "65:\n"
    "s_bitcmp1_b32 s72, 15\n"
    "s_cbranch_scc0 65f\n"
    "s_add_u32 s67, s65, 16515072\n"
    "buffer_load_dwordx4 a[92:95], v129, s[44:47], s67 offen sc1\n"
    "65:\n"
    "62:\n"
    "s_bitcmp1_b32 s70, 0\n"
    "s_cbranch_scc1 63f\n"
    "s_bitcmp1_b32 s72, 0\n"
    "s_cbranch_scc0 65f\n"
    "s_mov_b32 s67, 0\n"
    "buffer_load_dwordx4 v[0:3], v129, s[40:43], s67 offen sc1\n"
    "buffer_load_dwordx4 v[4:7], v129, s[40:43], s67 offen offset:2048 sc1\n"
    "65:\n"
    "s_bitcmp1_b32 s72, 1\n"
    "s_cbranch_scc0 65f\n"
    "s_mov_b32 s67, 4096\n"
    "buffer_load_dwordx4 v[8:11], v129, s[40:43], s67 offen sc1\n"
    "buffer_load_dwordx4 v[12:15], v129, s[40:43], s67 offen offset:2048 sc1\n"
    "65:\n"
    "s_bitcmp1_b32 s72, 2\n"
    "s_cbranch_scc0 65f\n"
    "s_mov_b32 s67, 8192\n"
    "buffer_load_dwordx4 v[16:19], v129, s[40:43], s67 offen sc1\n"
    "buffer_load_dwordx4 v[20:23], v129, s[40:43], s67 offen offset:2048 sc1\n"
    "65:\n"
    "s_bitcmp1_b32 s72, 3\n"
    "s_cbranch_scc0 65f\n"
    "s_mov_b32 s67, 12288\n"
    "buffer_load_dwordx4 v[24:27], v129, s[40:43], s67 offen sc1\n"
    "buffer_load_dwordx4 v[28:31], v129, s[40:43], s67 offen offset:2048 sc1\n"
    "65:\n"
    "s_bitcmp1_b32 s72, 4\n"
    "s_cbranch_scc0 65f\n"
    "s_mov_b32 s67, 16384\n"
    "buffer_load_dwordx4 v[32:35], v129, s[40:43], s67 offen sc1\n"
    "buffer_load_dwordx4 v[36:39], v129, s[40:43], s67 offen offset:2048 sc1\n"
    "65:\n"
    "s_bitcmp1_b32 s72, 5\n"
    "s_cbranch_scc0 65f\n"
    "s_mov_b32 s67, 20480\n"
    "buffer_load_dwordx4 v[40:43], v129, s[40:43], s67 offen sc1\n"
    "buffer_load_dwordx4 v[44:47], v129, s[40:43], s67 offen offset:2048 sc1\n"
    "65:\n"
    "s_bitcmp1_b32 s72, 6\n"
    "s_cbranch_scc0 65f\n"
    "s_mov_b32 s67, 24576\n"
    "buffer_load_dwordx4 v[48:51], v129, s[40:43], s67 offen sc1\n"
    "buffer_load_dwordx4 v[52:55], v129, s[40:43], s67 offen offset:2048 sc1\n"
    "65:\n"
    "s_bitcmp1_b32 s72, 7\n"
    "s_cbranch_scc0 65f\n"
    "s_mov_b32 s67, 28672\n"
    "buffer_load_dwordx4 v[56:59], v129, s[40:43], s67 offen sc1\n"
    "buffer_load_dwordx4 v[60:63], v129, s[40:43], s67 offen offset:2048 sc1\n"
    "65:\n"
    "s_and_b32 s67, s69, 0xff\n"
    "s_cmp_eq_u32 s67, 0xff\n"
    "s_cbranch_scc0 63f\n"
    "s_bitset1_b32 s70, 0\n"
    "s_mov_b32 s67, 32768\n"
    "buffer_load_dwordx4 v[64:67], v129, s[40:43], s67 offen sc1\n"
    "buffer_load_dwordx4 v[68:71], v129, s[40:43], s67 offen offset:2048 sc1\n"
    "s_and_b32 s67, s72, 0xff\n"
    "s_cmp_eq_u32 s67, 0xff\n"
    "s_cbranch_scc1 64f\n"
    "s_waitcnt vmcnt(2)\n"
    "64:\n"
    "ds_read_b64_tr_b16 a[96:97], v114 offset:0\n"
    "ds_read_b64_tr_b16 a[98:99], v114 offset:512\n"
    "ds_read_b64_tr_b16 a[100:101], v180 offset:0\n"
    "ds_read_b64_tr_b16 a[102:103], v180 offset:512\n"
    "ds_read_b64_tr_b16 a[104:105], v115 offset:0\n"
    "ds_read_b64_tr_b16 a[106:107], v115 offset:512\n"
    "ds_read_b64_tr_b16 a[108:109], v181 offset:0\n"
    "ds_read_b64_tr_b16 a[110:111], v181 offset:512\n"
    "ds_read_b64_tr_b16 a[112:113], v180 offset:2304\n"
    "ds_read_b64_tr_b16 a[114:115], v180 offset:2816\n"
    "ds_read_b64_tr_b16 a[116:117], v114 offset:2304\n"
    "ds_read_b64_tr_b16 a[118:119], v114 offset:2816\n"
    "s_waitcnt lgkmcnt(8) vmcnt(17)\n"
    "v_mfma_f32_16x16x32_bf16 v[148:151], a[96:99], v[0:3], 0\n"
    "ds_read_b64_tr_b16 a[120:121], v181 offset:2304\n"
    "ds_read_b64_tr_b16 a[122:123], v181 offset:2816\n"
    "s_bitcmp1_b32 s72, 8\n"
    "s_cbranch_scc1 94f\n"
    "s_mov_b32 s46, 0\n"
    "buffer_load_dwordx4 a[128:131], v129, s[44:47], 0 offen\n"
    "buffer_load_dwordx4 a[128:131], v129, s[44:47], 0 offen\n"
    "s_mov_b32 s46, 16551936\n"
    "s_branch 95f\n"
    "94:\n"
    "s_add_u32 s67, s63, 0\n"
    "buffer_load_dwordx4 a[0:3], v129, s[44:47], s67 offen sc1\n"
    "s_add_u32 s67, s64, 0\n"
    "buffer_load_dwordx4 a[32:35], v129, s[44:47], s67 offen sc1\n"
    "95:\n"
    "v_mfma_f32_16x16x32_bf16 v[152:155], a[100:103], v[0:3], 0\n"
    "s_waitcnt lgkmcnt(6)\n"
    "ds_read_b64_tr_b16 a[124:125], v115 offset:2304\n"
    "ds_read_b64_tr_b16 a[126:127], v115 offset:2816\n"
    "v_mfma_f32_16x16x32_bf16 v[156:159], a[104:107], v[0:3], 0\n"
    "ds_read_b64_tr_b16 a[96:97], v114 offset:4608\n"
    "ds_read_b64_tr_b16 a[98:99], v114 offset:5120\n"
    "v_mfma_f32_16x16x32_bf16 v[160:163], a[108:111], v[0:3], 0\n"
    "s_waitcnt lgkmcnt(6) vmcnt(18)\n"
    "ds_read_b64_tr_b16 a[100:101], v180 offset:4608\n"
    "ds_read_b64_tr_b16 a[102:103], v180 offset:5120\n"
    "v_mfma_f32_16x16x32_bf16 v[148:151], a[112:115], v[4:7], v[148:151]\n"
    "ds_read_b64_tr_b16 a[104:105], v115 offset:4608\n"
    "ds_read_b64_tr_b16 a[106:107], v115 offset:5120\n"
    "v_mfma_f32_16x16x32_bf16 v[152:155], a[116:119], v[4:7], v[152:155]\n"
    "s_waitcnt lgkmcnt(6)\n"
    "ds_read_b64_tr_b16 a[108:109], v181 offset:4608\n"
    "ds_read_b64_tr_b16 a[110:111], v181 offset:5120\n"
    "v_mfma_f32_16x16x32_bf16 v[156:159], a[120:123], v[4:7], v[156:159]\n"
    "ds_read_b64_tr_b16 a[112:113], v180 offset:6912\n"
    "ds_read_b64_tr_b16 a[114:115], v180 offset:7424\n"
    "v_mfma_f32_16x16x32_bf16 v[160:163], a[124:127], v[4:7], v[160:163]\n"
    "s_waitcnt lgkmcnt(6) vmcnt(17)\n"
    "ds_read_b64_tr_b16 a[116:117], v114 offset:6912\n"
    "ds_read_b64_tr_b16 a[118:119], v114 offset:7424\n"
    "v_mfma_f32_16x16x32_bf16 v[148:151], a[96:99], v[8:11], v[148:151]\n"
    "ds_read_b64_tr_b16 a[120:121], v181 offset:6912\n"
    "ds_read_b64_tr_b16 a[122:123], v181 offset:7424\n"
    "s_bitcmp1_b32 s72, 9\n"
    "s_cbranch_scc1 94f\n"
    "s_mov_b32 s46, 0\n"
    "buffer_load_dwordx4 a[128:131], v129, s[44:47], 0 offen\n"
    "buffer_load_dwordx4 a[128:131], v129, s[44:47], 0 offen\n"
    "s_mov_b32 s46, 16551936\n"
    "s_branch 95f\n"
    "94:\n"
    "s_add_u32 s67, s63, 2359296\n"
    "buffer_load_dwordx4 a[4:7], v129, s[44:47], s67 offen sc1\n"
    "s_add_u32 s67, s64, 2359296\n"
    "buffer_load_dwordx4 a[36:39], v129, s[44:47], s67 offen sc1\n"
    "95:\n"
    "v_mfma_f32_16x16x32_bf16 v[152:155], a[100:103], v[8:11], v[152:155]\n"
    "s_waitcnt lgkmcnt(6)\n"
    "ds_read_b64_tr_b16 a[124:125], v115 offset:6912\n"
    "ds_read_b64_tr_b16 a[126:127], v115 offset:7424\n"
    "v_mfma_f32_16x16x32_bf16 v[156:159], a[104:107], v[8:11], v[156:159]\n"
    "ds_read_b64_tr_b16 a[96:97], v114 offset:9216\n"
    "ds_read_b64_tr_b16 a[98:99], v114 offset:9728\n"
    "v_mfma_f32_16x16x32_bf16 v[160:163], a[108:111], v[8:11], v[160:163]\n"
    "s_waitcnt lgkmcnt(6) vmcnt(18)\n"
    "ds_read_b64_tr_b16 a[100:101], v180 offset:9216\n"
    "ds_read_b64_tr_b16 a[102:103], v180 offset:9728\n"
    "v_mfma_f32_16x16x32_bf16 v[148:151], a[112:115], v[12:15], v[148:151]\n"
    "ds_read_b64_tr_b16 a[104:105], v115 offset:9216\n"
    "ds_read_b64_tr_b16 a[106:107], v115 offset:9728\n"
    "v_mfma_f32_16x16x32_bf16 v[152:155], a[116:119], v[12:15], v[152:155]\n"
    "s_waitcnt lgkmcnt(6)\n"
    "ds_read_b64_tr_b16 a[108:109], v181 offset:9216\n"
    "ds_read_b64_tr_b16 a[110:111], v181 offset:9728\n"
    "v_mfma_f32_16x16x32_bf16 v[156:159], a[120:123], v[12:15], v[156:159]\n"
    "ds_read_b64_tr_b16 a[112:113], v180 offset:11520\n"
    "ds_read_b64_tr_b16 a[114:115], v180 offset:12032\n"
    "v_mfma_f32_16x16x32_bf16 v[160:163], a[124:127], v[12:15], v[160:163]\n"
    "s_waitcnt lgkmcnt(6) vmcnt(17)\n"
    "ds_read_b64_tr_b16 a[116:117], v114 offset:11520\n"
    "ds_read_b64_tr_b16 a[118:119], v114 offset:12032\n"
    "v_mfma_f32_16x16x32_bf16 v[148:151], a[96:99], v[16:19], v[148:151]\n"
    "ds_read_b64_tr_b16 a[120:121], v181 offset:11520\n"
    "ds_read_b64_tr_b16 a[122:123], v181 offset:12032\n"
    "s_bitcmp1_b32 s72, 10\n"
    "s_cbranch_scc1 94f\n"
    "s_mov_b32 s46, 0\n"
    "buffer_load_dwordx4 a[128:131], v129, s[44:47], 0 offen\n"
    "buffer_load_dwordx4 a[128:131], v129, s[44:47], 0 offen\n"
    "s_mov_b32 s46, 16551936\n"
    "s_branch 95f\n"
    "94:\n"
    "s_add_u32 s67, s63, 4718592\n"
    "buffer_load_dwordx4 a[8:11], v129, s[44:47], s67 offen sc1\n"
    "s_add_u32 s67, s64, 4718592\n"
    "buffer_load_dwordx4 a[40:43], v129, s[44:47], s67 offen sc1\n"
    "95:\n"
    "v_mfma_f32_16x16x32_bf16 v[152:155], a[100:103], v[16:19], v[152:155]\n"
    "s_waitcnt lgkmcnt(6)\n"
    "ds_read_b64_tr_b16 a[124:125], v115 offset:11520\n"
    "ds_read_b64_tr_b16 a[126:127], v115 offset:12032\n"
    "v_mfma_f32_16x16x32_bf16 v[156:159], a[104:107], v[16:19], v[156:159]\n"
    "ds_read_b64_tr_b16 a[96:97], v114 offset:13824\n"
    "ds_read_b64_tr_b16 a[98:99], v114 offset:14336\n"
    "v_mfma_f32_16x16x32_bf16 v[160:163], a[108:111], v[16:19], v[160:163]\n"
    "s_waitcnt lgkmcnt(6) vmcnt(18)\n"
    "ds_read_b64_tr_b16 a[100:101], v180 offset:13824\n"
    "ds_read_b64_tr_b16 a[102:103], v180 offset:14336\n"
    "v_mfma_f32_16x16x32_bf16 v[148:151], a[112:115], v[20:23], v[148:151]\n"
    "ds_read_b64_tr_b16 a[104:105], v115 offset:13824\n"
    "ds_read_b64_tr_b16 a[106:107], v115 offset:14336\n"
    "v_mfma_f32_16x16x32_bf16 v[152:155], a[116:119], v[20:23], v[152:155]\n"
    "s_waitcnt lgkmcnt(6)\n"
    "ds_read_b64_tr_b16 a[108:109], v181 offset:13824\n"
    "ds_read_b64_tr_b16 a[110:111], v181 offset:14336\n"
    "v_mfma_f32_16x16x32_bf16 v[156:159], a[120:123], v[20:23], v[156:159]\n"
    "ds_read_b64_tr_b16 a[112:113], v180 offset:16128\n"
    "ds_read_b64_tr_b16 a[114:115], v180 offset:16640\n"
    "v_mfma_f32_16x16x32_bf16 v[160:163], a[124:127], v[20:23], v[160:163]\n"
    "s_waitcnt lgkmcnt(6) vmcnt(17)\n"
    "ds_read_b64_tr_b16 a[116:117], v114 offset:16128\n"
    "ds_read_b64_tr_b16 a[118:119], v114 offset:16640\n"
    "v_mfma_f32_16x16x32_bf16 v[148:151], a[96:99], v[24:27], v[148:151]\n"
    "ds_read_b64_tr_b16 a[120:121], v181 offset:16128\n"
    "ds_read_b64_tr_b16 a[122:123], v181 offset:16640\n"
    "s_bitcmp1_b32 s72, 11\n"
    "s_cbranch_scc1 94f\n"
    "s_mov_b32 s46, 0\n"
    "buffer_load_dwordx4 a[128:131], v129, s[44:47], 0 offen\n"
    "buffer_load_dwordx4 a[128:131], v129, s[44:47], 0 offen\n"
    "s_mov_b32 s46, 16551936\n"
    "s_branch 95f\n"
    "94:\n"
    "s_add_u32 s67, s63, 7077888\n"
    "buffer_load_dwordx4 a[12:15], v129, s[44:47], s67 offen sc1\n"
    "s_add_u32 s67, s64, 7077888\n"
    "buffer_load_dwordx4 a[44:47], v129, s[44:47], s67 offen sc1\n"
    "95:\n"
    "v_mfma_f32_16x16x32_bf16 v[152:155], a[100:103], v[24:27], v[152:155]\n"
    "s_waitcnt lgkmcnt(6)\n"
    "ds_read_b64_tr_b16 a[124:125], v115 offset:16128\n"
    "ds_read_b64_tr_b16 a[126:127], v115 offset:16640\n"
    "v_mfma_f32_16x16x32_bf16 v[156:159], a[104:107], v[24:27], v[156:159]\n"
    "ds_read_b64_tr_b16 a[96:97], v114 offset:18432\n"
    "ds_read_b64_tr_b16 a[98:99], v114 offset:18944\n"
    "v_mfma_f32_16x16x32_bf16 v[160:163], a[108:111], v[24:27], v[160:163]\n"
    "s_waitcnt lgkmcnt(6) vmcnt(18)\n"
    "ds_read_b64_tr_b16 a[100:101], v180 offset:18432\n"
    "ds_read_b64_tr_b16 a[102:103], v180 offset:18944\n"
    "v_mfma_f32_16x16x32_bf16 v[148:151], a[112:115], v[28:31], v[148:151]\n"
    "ds_read_b64_tr_b16 a[104:105], v115 offset:18432\n"
    "ds_read_b64_tr_b16 a[106:107], v115 offset:18944\n"
    "v_mfma_f32_16x16x32_bf16 v[152:155], a[116:119], v[28:31], v[152:155]\n"
    "s_waitcnt lgkmcnt(6)\n"
    "ds_read_b64_tr_b16 a[108:109], v181 offset:18432\n"
    "ds_read_b64_tr_b16 a[110:111], v181 offset:18944\n"
    "v_mfma_f32_16x16x32_bf16 v[156:159], a[120:123], v[28:31], v[156:159]\n"
    "ds_read_b64_tr_b16 a[112:113], v180 offset:20736\n"
    "ds_read_b64_tr_b16 a[114:115], v180 offset:21248\n"
    "v_mfma_f32_16x16x32_bf16 v[160:163], a[124:127], v[28:31], v[160:163]\n"
    "s_waitcnt lgkmcnt(6) vmcnt(17)\n"
    "ds_read_b64_tr_b16 a[116:117], v114 offset:20736\n"
    "ds_read_b64_tr_b16 a[118:119], v114 offset:21248\n"
    "v_mfma_f32_16x16x32_bf16 v[148:151], a[96:99], v[32:35], v[148:151]\n"
    "ds_read_b64_tr_b16 a[120:121], v181 offset:20736\n"
    "ds_read_b64_tr_b16 a[122:123], v181 offset:21248\n"
    "s_bitcmp1_b32 s72, 12\n"
    "s_cbranch_scc1 94f\n"
    "s_mov_b32 s46, 0\n"
    "buffer_load_dwordx4 a[128:131], v129, s[44:47], 0 offen\n"
    "buffer_load_dwordx4 a[128:131], v129, s[44:47], 0 offen\n"
    "s_mov_b32 s46, 16551936\n"
    "s_branch 95f\n"
    "94:\n"
    "s_add_u32 s67, s63, 9437184\n"
    "buffer_load_dwordx4 a[16:19], v129, s[44:47], s67 offen sc1\n"
    "s_add_u32 s67, s64, 9437184\n"
    "buffer_load_dwordx4 a[48:51], v129, s[44:47], s67 offen sc1\n"
    "95:\n"
    "v_mfma_f32_16x16x32_bf16 v[152:155], a[100:103], v[32:35], v[152:155]\n"
    "s_waitcnt lgkmcnt(6)\n"
    "ds_read_b64_tr_b16 a[124:125], v115 offset:20736\n"
    "ds_read_b64_tr_b16 a[126:127], v115 offset:21248\n"
    "v_mfma_f32_16x16x32_bf16 v[156:159], a[104:107], v[32:35], v[156:159]\n"
    "ds_read_b64_tr_b16 a[96:97], v114 offset:23040\n"
    "ds_read_b64_tr_b16 a[98:99], v114 offset:23552\n"
    "v_mfma_f32_16x16x32_bf16 v[160:163], a[108:111], v[32:35], v[160:163]\n"
    "s_waitcnt lgkmcnt(6) vmcnt(18)\n"
    "ds_read_b64_tr_b16 a[100:101], v180 offset:23040\n"
    "ds_read_b64_tr_b16 a[102:103], v180 offset:23552\n"
    "v_mfma_f32_16x16x32_bf16 v[148:151], a[112:115], v[36:39], v[148:151]\n"
    "ds_read_b64_tr_b16 a[104:105], v115 offset:23040\n"
    "ds_read_b64_tr_b16 a[106:107], v115 offset:23552\n"
    "v_mfma_f32_16x16x32_bf16 v[152:155], a[116:119], v[36:39], v[152:155]\n"
    "s_waitcnt lgkmcnt(6)\n"
    "ds_read_b64_tr_b16 a[108:109], v181 offset:23040\n"
    "ds_read_b64_tr_b16 a[110:111], v181 offset:23552\n"
    "v_mfma_f32_16x16x32_bf16 v[156:159], a[120:123], v[36:39], v[156:159]\n"
    "ds_read_b64_tr_b16 a[112:113], v180 offset:25344\n"
    "ds_read_b64_tr_b16 a[114:115], v180 offset:25856\n"
    "v_mfma_f32_16x16x32_bf16 v[160:163], a[124:127], v[36:39], v[160:163]\n"
    "s_waitcnt lgkmcnt(6) vmcnt(17)\n"
    "ds_read_b64_tr_b16 a[116:117], v114 offset:25344\n"
    "ds_read_b64_tr_b16 a[118:119], v114 offset:25856\n"
    "v_mfma_f32_16x16x32_bf16 v[148:151], a[96:99], v[40:43], v[148:151]\n"
    "ds_read_b64_tr_b16 a[120:121], v181 offset:25344\n"
    "ds_read_b64_tr_b16 a[122:123], v181 offset:25856\n"
    "s_bitcmp1_b32 s72, 13\n"
    "s_cbranch_scc1 94f\n"
    "s_mov_b32 s46, 0\n"
    "buffer_load_dwordx4 a[128:131], v129, s[44:47], 0 offen\n"
    "buffer_load_dwordx4 a[128:131], v129, s[44:47], 0 offen\n"
    "s_mov_b32 s46, 16551936\n"
    "s_branch 95f\n"
    "94:\n"
    "s_add_u32 s67, s63, 11796480\n"
    "buffer_load_dwordx4 a[20:23], v129, s[44:47], s67 offen sc1\n"
    "s_add_u32 s67, s64, 11796480\n"
    "buffer_load_dwordx4 a[52:55], v129, s[44:47], s67 offen sc1\n"
    "95:\n"
    "v_mfma_f32_16x16x32_bf16 v[152:155], a[100:103], v[40:43], v[152:155]\n"
    "s_waitcnt lgkmcnt(6)\n"
    "ds_read_b64_tr_b16 a[124:125], v115 offset:25344\n"
    "ds_read_b64_tr_b16 a[126:127], v115 offset:25856\n"
    "v_mfma_f32_16x16x32_bf16 v[156:159], a[104:107], v[40:43], v[156:159]\n"
    "ds_read_b64_tr_b16 a[96:97], v114 offset:27648\n"
    "ds_read_b64_tr_b16 a[98:99], v114 offset:28160\n"
    "v_mfma_f32_16x16x32_bf16 v[160:163], a[108:111], v[40:43], v[160:163]\n"
    "s_waitcnt lgkmcnt(6) vmcnt(18)\n"
    "ds_read_b64_tr_b16 a[100:101], v180 offset:27648\n"
    "ds_read_b64_tr_b16 a[102:103], v180 offset:28160\n"
    "v_mfma_f32_16x16x32_bf16 v[148:151], a[112:115], v[44:47], v[148:151]\n"
    "ds_read_b64_tr_b16 a[104:105], v115 offset:27648\n"
    "ds_read_b64_tr_b16 a[106:107], v115 offset:28160\n"
    "v_mfma_f32_16x16x32_bf16 v[152:155], a[116:119], v[44:47], v[152:155]\n"
    "s_waitcnt lgkmcnt(6)\n"
    "ds_read_b64_tr_b16 a[108:109], v181 offset:27648\n"
    "ds_read_b64_tr_b16 a[110:111], v181 offset:28160\n"
    "v_mfma_f32_16x16x32_bf16 v[156:159], a[120:123], v[44:47], v[156:159]\n"
    "ds_read_b64_tr_b16 a[112:113], v180 offset:29952\n"
    "ds_read_b64_tr_b16 a[114:115], v180 offset:30464\n"
    "v_mfma_f32_16x16x32_bf16 v[160:163], a[124:127], v[44:47], v[160:163]\n"
    "s_waitcnt lgkmcnt(6) vmcnt(17)\n"
    "ds_read_b64_tr_b16 a[116:117], v114 offset:29952\n"
    "ds_read_b64_tr_b16 a[118:119], v114 offset:30464\n"
    "v_mfma_f32_16x16x32_bf16 v[148:151], a[96:99], v[48:51], v[148:151]\n"
    "ds_read_b64_tr_b16 a[120:121], v181 offset:29952\n"
    "ds_read_b64_tr_b16 a[122:123], v181 offset:30464\n"
    "s_bitcmp1_b32 s72, 14\n"
    "s_cbranch_scc1 94f\n"
    "s_mov_b32 s46, 0\n"
    "buffer_load_dwordx4 a[128:131], v129, s[44:47], 0 offen\n"
    "buffer_load_dwordx4 a[128:131], v129, s[44:47], 0 offen\n"
    "s_mov_b32 s46, 16551936\n"
    "s_branch 95f\n"
    "94:\n"
    "s_add_u32 s67, s63, 14155776\n"
    "buffer_load_dwordx4 a[24:27], v129, s[44:47], s67 offen sc1\n"
    "s_add_u32 s67, s64, 14155776\n"
    "buffer_load_dwordx4 a[56:59], v129, s[44:47], s67 offen sc1\n"
    "95:\n"
    "v_mfma_f32_16x16x32_bf16 v[152:155], a[100:103], v[48:51], v[152:155]\n"
    "s_waitcnt lgkmcnt(6)\n"
    "ds_read_b64_tr_b16 a[124:125], v115 offset:29952\n"
    "ds_read_b64_tr_b16 a[126:127], v115 offset:30464\n"
    "v_mfma_f32_16x16x32_bf16 v[156:159], a[104:107], v[48:51], v[156:159]\n"
    "ds_read_b64_tr_b16 a[96:97], v114 offset:32256\n"
    "ds_read_b64_tr_b16 a[98:99], v114 offset:32768\n"
    "v_mfma_f32_16x16x32_bf16 v[160:163], a[108:111], v[48:51], v[160:163]\n"
    "s_waitcnt lgkmcnt(6) vmcnt(18)\n"
    "ds_read_b64_tr_b16 a[100:101], v180 offset:32256\n"
    "ds_read_b64_tr_b16 a[102:103], v180 offset:32768\n"
    "v_mfma_f32_16x16x32_bf16 v[148:151], a[112:115], v[52:55], v[148:151]\n"
    "ds_read_b64_tr_b16 a[104:105], v115 offset:32256\n"
    "ds_read_b64_tr_b16 a[106:107], v115 offset:32768\n"
    "v_mfma_f32_16x16x32_bf16 v[152:155], a[116:119], v[52:55], v[152:155]\n"
    "s_waitcnt lgkmcnt(6)\n"
    "ds_read_b64_tr_b16 a[108:109], v181 offset:32256\n"
    "ds_read_b64_tr_b16 a[110:111], v181 offset:32768\n"
    "v_mfma_f32_16x16x32_bf16 v[156:159], a[120:123], v[52:55], v[156:159]\n"
    "ds_read_b64_tr_b16 a[112:113], v180 offset:34560\n"
    "ds_read_b64_tr_b16 a[114:115], v180 offset:35072\n"
    "v_mfma_f32_16x16x32_bf16 v[160:163], a[124:127], v[52:55], v[160:163]\n"
    "s_waitcnt lgkmcnt(6) vmcnt(17)\n"
    "ds_read_b64_tr_b16 a[116:117], v114 offset:34560\n"
    "ds_read_b64_tr_b16 a[118:119], v114 offset:35072\n"
    "v_mfma_f32_16x16x32_bf16 v[148:151], a[96:99], v[56:59], v[148:151]\n"
    "ds_read_b64_tr_b16 a[120:121], v181 offset:34560\n"
    "ds_read_b64_tr_b16 a[122:123], v181 offset:35072\n"
    "s_bitcmp1_b32 s72, 15\n"
    "s_cbranch_scc1 94f\n"
    "s_mov_b32 s46, 0\n"
    "buffer_load_dwordx4 a[128:131], v129, s[44:47], 0 offen\n"
    "buffer_load_dwordx4 a[128:131], v129, s[44:47], 0 offen\n"
    "s_mov_b32 s46, 16551936\n"
    "s_branch 95f\n"
    "94:\n"
    "s_add_u32 s67, s63, 16515072\n"
    "buffer_load_dwordx4 a[28:31], v129, s[44:47], s67 offen sc1\n"
    "s_add_u32 s67, s64, 16515072\n"
    "buffer_load_dwordx4 a[60:63], v129, s[44:47], s67 offen sc1\n"
    "95:\n"
    "v_mfma_f32_16x16x32_bf16 v[152:155], a[100:103], v[56:59], v[152:155]\n"
    "s_waitcnt lgkmcnt(6)\n"
    "ds_read_b64_tr_b16 a[124:125], v115 offset:34560\n"
    "ds_read_b64_tr_b16 a[126:127], v115 offset:35072\n"
    "v_mfma_f32_16x16x32_bf16 v[156:159], a[104:107], v[56:59], v[156:159]\n"
    "ds_read_b64_tr_b16 a[96:97], v117 offset:0\n"
    "ds_read_b64_tr_b16 a[98:99], v183 offset:2304\n"
    "v_mfma_f32_16x16x32_bf16 v[160:163], a[108:111], v[56:59], v[160:163]\n"
    "s_waitcnt lgkmcnt(6) vmcnt(18)\n"
    "ds_read_b64_tr_b16 a[100:101], v183 offset:0\n"
    "ds_read_b64_tr_b16 a[102:103], v117 offset:2304\n"
    "v_mfma_f32_16x16x32_bf16 v[148:151], a[112:115], v[60:63], v[148:151]\n"
    "ds_read_b64_tr_b16 a[104:105], v118 offset:0\n"
    "ds_read_b64_tr_b16 a[106:107], v184 offset:2304\n"
    "v_mfma_f32_16x16x32_bf16 v[152:155], a[116:119], v[60:63], v[152:155]\n"
    "s_waitcnt lgkmcnt(6)\n"
    "ds_read_b64_tr_b16 a[108:109], v184 offset:0\n"
    "ds_read_b64_tr_b16 a[110:111], v118 offset:2304\n"
    "v_mfma_f32_16x16x32_bf16 v[156:159], a[120:123], v[60:63], v[156:159]\n"
    "ds_read_b64_tr_b16 a[112:113], v117 offset:18432\n"
    "ds_read_b64_tr_b16 a[114:115], v183 offset:20736\n"
    "v_mfma_f32_16x16x32_bf16 v[160:163], a[124:127], v[60:63], v[160:163]\n"
    "s_waitcnt lgkmcnt(6) vmcnt(17)\n"
    "ds_read_b64_tr_b16 a[116:117], v183 offset:18432\n"
    "ds_read_b64_tr_b16 a[118:119], v117 offset:20736\n"
    "v_mfma_f32_16x16x32_bf16 v[148:151], a[96:99], v[64:67], v[148:151]\n"
    "ds_read_b64_tr_b16 a[120:121], v118 offset:18432\n"
    "ds_read_b64_tr_b16 a[122:123], v184 offset:20736\n"
    "v_mfma_f32_16x16x32_bf16 v[152:155], a[100:103], v[64:67], v[152:155]\n"
    "s_waitcnt lgkmcnt(6)\n"
    "ds_read_b64_tr_b16 a[124:125], v184 offset:18432\n"
    "ds_read_b64_tr_b16 a[126:127], v118 offset:20736\n"
    "v_mfma_f32_16x16x32_bf16 v[156:159], a[104:107], v[64:67], v[156:159]\n"
    "v_mfma_f32_16x16x32_bf16 v[160:163], a[108:111], v[64:67], v[160:163]\n"
    "s_waitcnt lgkmcnt(4) vmcnt(16)\n"
    "v_mfma_f32_16x16x32_bf16 v[148:151], a[112:115], v[68:71], v[148:151]\n"
    "v_mfma_f32_16x16x32_bf16 v[152:155], a[116:119], v[68:71], v[152:155]\n"
    "s_waitcnt lgkmcnt(0)\n"
    "v_mfma_f32_16x16x32_bf16 v[156:159], a[120:123], v[68:71], v[156:159]\n"
    "v_mfma_f32_16x16x32_bf16 v[160:163], a[124:127], v[68:71], v[160:163]\n"
    "s_cmp_eq_u32 s57, 0\n"
    "s_cbranch_scc1 66f\n"
    "ds_read_b64_tr_b16 a[96:97], v116 offset:0\n"
    "ds_read_b64_tr_b16 a[98:99], v116 offset:512\n"
    "ds_read_b64_tr_b16 a[100:101], v182 offset:0\n"
    "ds_read_b64_tr_b16 a[102:103], v182 offset:512\n"
    "ds_read_b64_tr_b16 a[104:105], v182 offset:2304\n"
    "ds_read_b64_tr_b16 a[106:107], v182 offset:2816\n"
    "ds_read_b64_tr_b16 a[108:109], v116 offset:2304\n"
    "ds_read_b64_tr_b16 a[110:111], v116 offset:2816\n"
    "ds_read_b64_tr_b16 a[112:113], v116 offset:4608\n"
    "ds_read_b64_tr_b16 a[114:115], v116 offset:5120\n"
    "ds_read_b64_tr_b16 a[116:117], v182 offset:4608\n"
    "ds_read_b64_tr_b16 a[118:119], v182 offset:5120\n"
    "s_waitcnt lgkmcnt(8)\n"
    "v_mfma_f32_16x16x32_bf16 v[164:167], a[96:99], v[0:3], 0\n"
    "ds_read_b64_tr_b16 a[120:121], v182 offset:6912\n"
    "ds_read_b64_tr_b16 a[122:123], v182 offset:7424\n"
    "v_mfma_f32_16x16x32_bf16 v[168:171], a[100:103], v[0:3], 0\n"
    "s_waitcnt lgkmcnt(6)\n"
    "ds_read_b64_tr_b16 a[124:125], v116 offset:6912\n"
    "ds_read_b64_tr_b16 a[126:127], v116 offset:7424\n"
    "v_mfma_f32_16x16x32_bf16 v[164:167], a[104:107], v[4:7], v[164:167]\n"
    "ds_read_b64_tr_b16 a[96:97], v116 offset:9216\n"
    "ds_read_b64_tr_b16 a[98:99], v116 offset:9728\n"
    "v_mfma_f32_16x16x32_bf16 v[168:171], a[108:111], v[4:7], v[168:171]\n"
    "s_waitcnt lgkmcnt(6)\n"
    "ds_read_b64_tr_b16 a[100:101], v182 offset:9216\n"
    "ds_read_b64_tr_b16 a[102:103], v182 offset:9728\n"
    "v_mfma_f32_16x16x32_bf16 v[164:167], a[112:115], v[8:11], v[164:167]\n"
    "ds_read_b64_tr_b16 a[104:105], v182 offset:11520\n"
    "ds_read_b64_tr_b16 a[106:107], v182 offset:12032\n"
    "v_mfma_f32_16x16x32_bf16 v[168:171], a[116:119], v[8:11], v[168:171]\n"
    "s_waitcnt lgkmcnt(6)\n"
    "ds_read_b64_tr_b16 a[108:109], v116 offset:11520\n"
    "ds_read_b64_tr_b16 a[110:111], v116 offset:12032\n"
    "v_mfma_f32_16x16x32_bf16 v[164:167], a[120:123], v[12:15], v[164:167]\n"
    "ds_read_b64_tr_b16 a[112:113], v116 offset:13824\n"
    "ds_read_b64_tr_b16 a[114:115], v116 offset:14336\n"
    "v_mfma_f32_16x16x32_bf16 v[168:171], a[124:127], v[12:15], v[168:171]\n"
    "s_waitcnt lgkmcnt(6)\n"
    "ds_read_b64_tr_b16 a[116:117], v182 offset:13824\n"
    "ds_read_b64_tr_b16 a[118:119], v182 offset:14336\n"
    "v_mfma_f32_16x16x32_bf16 v[164:167], a[96:99], v[16:19], v[164:167]\n"
    "ds_read_b64_tr_b16 a[120:121], v182 offset:16128\n"
    "ds_read_b64_tr_b16 a[122:123], v182 offset:16640\n"
    "v_mfma_f32_16x16x32_bf16 v[168:171], a[100:103], v[16:19], v[168:171]\n"
    "s_waitcnt lgkmcnt(6)\n"
    "ds_read_b64_tr_b16 a[124:125], v116 offset:16128\n"
    "ds_read_b64_tr_b16 a[126:127], v116 offset:16640\n"
    "v_mfma_f32_16x16x32_bf16 v[164:167], a[104:107], v[20:23], v[164:167]\n"
    "ds_read_b64_tr_b16 a[96:97], v116 offset:18432\n"
    "ds_read_b64_tr_b16 a[98:99], v116 offset:18944\n"
    "v_mfma_f32_16x16x32_bf16 v[168:171], a[108:111], v[20:23], v[168:171]\n"
    "s_waitcnt lgkmcnt(6)\n"
    "ds_read_b64_tr_b16 a[100:101], v182 offset:18432\n"
    "ds_read_b64_tr_b16 a[102:103], v182 offset:18944\n"
    "v_mfma_f32_16x16x32_bf16 v[164:167], a[112:115], v[24:27], v[164:167]\n"
    "ds_read_b64_tr_b16 a[104:105], v182 offset:20736\n"
    "ds_read_b64_tr_b16 a[106:107], v182 offset:21248\n"
    "v_mfma_f32_16x16x32_bf16 v[168:171], a[116:119], v[24:27], v[168:171]\n"
    "s_waitcnt lgkmcnt(6)\n"
    "ds_read_b64_tr_b16 a[108:109], v116 offset:20736\n"
    "ds_read_b64_tr_b16 a[110:111], v116 offset:21248\n"
    "v_mfma_f32_16x16x32_bf16 v[164:167], a[120:123], v[28:31], v[164:167]\n"
    "ds_read_b64_tr_b16 a[112:113], v116 offset:23040\n"
    "ds_read_b64_tr_b16 a[114:115], v116 offset:23552\n"
    "v_mfma_f32_16x16x32_bf16 v[168:171], a[124:127], v[28:31], v[168:171]\n"
    "s_waitcnt lgkmcnt(6)\n"
    "ds_read_b64_tr_b16 a[116:117], v182 offset:23040\n"
    "ds_read_b64_tr_b16 a[118:119], v182 offset:23552\n"
    "v_mfma_f32_16x16x32_bf16 v[164:167], a[96:99], v[32:35], v[164:167]\n"
    "ds_read_b64_tr_b16 a[120:121], v182 offset:25344\n"
    "ds_read_b64_tr_b16 a[122:123], v182 offset:25856\n"
    "v_mfma_f32_16x16x32_bf16 v[168:171], a[100:103], v[32:35], v[168:171]\n"
    "s_waitcnt lgkmcnt(6)\n"
    "ds_read_b64_tr_b16 a[124:125], v116 offset:25344\n"
    "ds_read_b64_tr_b16 a[126:127], v116 offset:25856\n"
    "v_mfma_f32_16x16x32_bf16 v[164:167], a[104:107], v[36:39], v[164:167]\n"
    "ds_read_b64_tr_b16 a[96:97], v116 offset:27648\n"
    "ds_read_b64_tr_b16 a[98:99], v116 offset:28160\n"
    "v_mfma_f32_16x16x32_bf16 v[168:171], a[108:111], v[36:39], v[168:171]\n"
    "s_waitcnt lgkmcnt(6)\n"
    "ds_read_b64_tr_b16 a[100:101], v182 offset:27648\n"
    "ds_read_b64_tr_b16 a[102:103], v182 offset:28160\n"
    "v_mfma_f32_16x16x32_bf16 v[164:167], a[112:115], v[40:43], v[164:167]\n"
    "ds_read_b64_tr_b16 a[104:105], v182 offset:29952\n"
    "ds_read_b64_tr_b16 a[106:107], v182 offset:30464\n"
    "v_mfma_f32_16x16x32_bf16 v[168:171], a[116:119], v[40:43], v[168:171]\n"
    "s_waitcnt lgkmcnt(6)\n"
    "ds_read_b64_tr_b16 a[108:109], v116 offset:29952\n"
    "ds_read_b64_tr_b16 a[110:111], v116 offset:30464\n"
    "v_mfma_f32_16x16x32_bf16 v[164:167], a[120:123], v[44:47], v[164:167]\n"
    "ds_read_b64_tr_b16 a[112:113], v116 offset:32256\n"
    "ds_read_b64_tr_b16 a[114:115], v116 offset:32768\n"
    "v_mfma_f32_16x16x32_bf16 v[168:171], a[124:127], v[44:47], v[168:171]\n"
    "s_waitcnt lgkmcnt(6)\n"
    "ds_read_b64_tr_b16 a[116:117], v182 offset:32256\n"
    "ds_read_b64_tr_b16 a[118:119], v182 offset:32768\n"
    "v_mfma_f32_16x16x32_bf16 v[164:167], a[96:99], v[48:51], v[164:167]\n"
    "ds_read_b64_tr_b16 a[120:121], v182 offset:34560\n"
    "ds_read_b64_tr_b16 a[122:123], v182 offset:35072\n"
    "v_mfma_f32_16x16x32_bf16 v[168:171], a[100:103], v[48:51], v[168:171]\n"
    "s_waitcnt lgkmcnt(6)\n"
    "ds_read_b64_tr_b16 a[124:125], v116 offset:34560\n"
    "ds_read_b64_tr_b16 a[126:127], v116 offset:35072\n"
    "v_mfma_f32_16x16x32_bf16 v[164:167], a[104:107], v[52:55], v[164:167]\n"
    "ds_read_b64_tr_b16 a[96:97], v119 offset:0\n"
    "ds_read_b64_tr_b16 a[98:99], v185 offset:2304\n"
    "v_mfma_f32_16x16x32_bf16 v[168:171], a[108:111], v[52:55], v[168:171]\n"
    "s_waitcnt lgkmcnt(6)\n"
    "ds_read_b64_tr_b16 a[100:101], v185 offset:0\n"
    "ds_read_b64_tr_b16 a[102:103], v119 offset:2304\n"
    "v_mfma_f32_16x16x32_bf16 v[164:167], a[112:115], v[56:59], v[164:167]\n"
    "ds_read_b64_tr_b16 a[104:105], v119 offset:18432\n"
    "ds_read_b64_tr_b16 a[106:107], v185 offset:20736\n"
    "v_mfma_f32_16x16x32_bf16 v[168:171], a[116:119], v[56:59], v[168:171]\n"
    "s_waitcnt lgkmcnt(6)\n"
    "ds_read_b64_tr_b16 a[108:109], v185 offset:18432\n"
    "ds_read_b64_tr_b16 a[110:111], v119 offset:20736\n"
    "v_mfma_f32_16x16x32_bf16 v[164:167], a[120:123], v[60:63], v[164:167]\n"
    "v_mfma_f32_16x16x32_bf16 v[168:171], a[124:127], v[60:63], v[168:171]\n"
    "s_waitcnt lgkmcnt(4)\n"
    "v_mfma_f32_16x16x32_bf16 v[164:167], a[96:99], v[64:67], v[164:167]\n"
    "v_mfma_f32_16x16x32_bf16 v[168:171], a[100:103], v[64:67], v[168:171]\n"
    "s_waitcnt lgkmcnt(0)\n"
    "v_mfma_f32_16x16x32_bf16 v[164:167], a[104:107], v[68:71], v[164:167]\n"
    "v_mfma_f32_16x16x32_bf16 v[168:171], a[108:111], v[68:71], v[168:171]\n"
    "s_branch 66f\n"
    "63:\n"
    "s_bitcmp1_b32 s72, 8\n"
    "s_cbranch_scc0 65f\n"
    "s_add_u32 s67, s63, 0\n"
    "buffer_load_dwordx4 a[0:3], v129, s[44:47], s67 offen sc1\n"
    "s_add_u32 s67, s64, 0\n"
    "buffer_load_dwordx4 a[32:35], v129, s[44:47], s67 offen sc1\n"
    "65:\n"
    "s_bitcmp1_b32 s72, 9\n"
    "s_cbranch_scc0 65f\n"
    "s_add_u32 s67, s63, 2359296\n"
    "buffer_load_dwordx4 a[4:7], v129, s[44:47], s67 offen sc1\n"
    "s_add_u32 s67, s64, 2359296\n"
    "buffer_load_dwordx4 a[36:39], v129, s[44:47], s67 offen sc1\n"
    "65:\n"
    "s_bitcmp1_b32 s72, 10\n"
    "s_cbranch_scc0 65f\n"
    "s_add_u32 s67, s63, 4718592\n"
    "buffer_load_dwordx4 a[8:11], v129, s[44:47], s67 offen sc1\n"
    "s_add_u32 s67, s64, 4718592\n"
    "buffer_load_dwordx4 a[40:43], v129, s[44:47], s67 offen sc1\n"
    "65:\n"
    "s_bitcmp1_b32 s72, 11\n"
    "s_cbranch_scc0 65f\n"
    "s_add_u32 s67, s63, 7077888\n"
    "buffer_load_dwordx4 a[12:15], v129, s[44:47], s67 offen sc1\n"
    "s_add_u32 s67, s64, 7077888\n"
    "buffer_load_dwordx4 a[44:47], v129, s[44:47], s67 offen sc1\n"
    "65:\n"
    "s_bitcmp1_b32 s72, 12\n"
    "s_cbranch_scc0 65f\n"
    "s_add_u32 s67, s63, 9437184\n"
    "buffer_load_dwordx4 a[16:19], v129, s[44:47], s67 offen sc1\n"
    "s_add_u32 s67, s64, 9437184\n"
    "buffer_load_dwordx4 a[48:51], v129, s[44:47], s67 offen sc1\n"
    "65:\n"
    "s_bitcmp1_b32 s72, 13\n"
    "s_cbranch_scc0 65f\n"
    "s_add_u32 s67, s63, 11796480\n"
    "buffer_load_dwordx4 a[20:23], v129, s[44:47], s67 offen sc1\n"
    "s_add_u32 s67, s64, 11796480\n"
    "buffer_load_dwordx4 a[52:55], v129, s[44:47], s67 offen sc1\n"
    "65:\n"
    "s_bitcmp1_b32 s72, 14\n"
    "s_cbranch_scc0 65f\n"
    "s_add_u32 s67, s63, 14155776\n"
    "buffer_load_dwordx4 a[24:27], v129, s[44:47], s67 offen sc1\n"
    "s_add_u32 s67, s64, 14155776\n"
    "buffer_load_dwordx4 a[56:59], v129, s[44:47], s67 offen sc1\n"
    "65:\n"
    "s_bitcmp1_b32 s72, 15\n"
    "s_cbranch_scc0 65f\n"
    "s_add_u32 s67, s63, 16515072\n"
    "buffer_load_dwordx4 a[28:31], v129, s[44:47], s67 offen sc1\n"
    "s_add_u32 s67, s64, 16515072\n"
    "buffer_load_dwordx4 a[60:63], v129, s[44:47], s67 offen sc1\n"
    "65:\n"
    "66:\n"
    "s_cmp_eq_u32 s69, 0xffff\n"
    "s_cbranch_scc1 86f\n"
    "s_branch 61b\n"
    "68:\n"
    "s_bitcmp1_b32 s70, 1\n"
    "s_cbranch_scc1 69f\n"
    "s_cmp_lt_u32 s69, 0xff00\n"
    "s_cbranch_scc1 69f\n"
    "s_bitset1_b32 s70, 1\n"
    "s_branch 84f\n"
    "69:\n"
    "s_sleep 1\n"
    "s_sub_u32 s66, s66, 1\n"
    "s_cmp_lg_u32 s66, 0\n"
    "s_cbranch_scc1 61b\n"
    "86:\n"
    "s_mov_b32 s69, 0xffff\n"
    "s_waitcnt vmcnt(0)\n"
    "s_bitcmp1_b32 s70, 1\n"
    "s_cbranch_scc1 88f\n"
    "84:\n"
    "v_mfma_f32_16x16x32_bf16 v[72:75], v[110:113], v[96:99], 0\n"
    "v_mfma_f32_16x16x32_bf16 v[76:79], v[108:111], v[96:99], 0\n"
    "v_mfma_f32_16x16x32_bf16 v[80:83], v[110:113], v[100:103], 0\n"
    "v_mfma_f32_16x16x32_bf16 v[84:87], v[108:111], v[100:103], 0\n"
    "v_mfma_f32_16x16x32_bf16 v[72:75], v[110:113], a[0:3], v[72:75]\n"
    "v_mfma_f32_16x16x32_bf16 v[76:79], v[108:111], a[0:3], v[76:79]\n"
    "v_mfma_f32_16x16x32_bf16 v[80:83], v[110:113], a[32:35], v[80:83]\n"
    "v_mfma_f32_16x16x32_bf16 v[84:87], v[108:111], a[32:35], v[84:87]\n"
    "v_mfma_f32_16x16x32_bf16 v[72:75], v[110:113], a[4:7], v[72:75]\n"
    "v_mfma_f32_16x16x32_bf16 v[76:79], v[108:111], a[4:7], v[76:79]\n"
    "v_mfma_f32_16x16x32_bf16 v[80:83], v[110:113], a[36:39], v[80:83]\n"
    "v_mfma_f32_16x16x32_bf16 v[84:87], v[108:111], a[36:39], v[84:87]\n"
    "v_mfma_f32_16x16x32_bf16 v[72:75], v[110:113], a[8:11], v[72:75]\n"
    "v_mfma_f32_16x16x32_bf16 v[76:79], v[108:111], a[8:11], v[76:79]\n"
    "v_mfma_f32_16x16x32_bf16 v[80:83], v[110:113], a[40:43], v[80:83]\n"
    "v_mfma_f32_16x16x32_bf16 v[84:87], v[108:111], a[40:43], v[84:87]\n"
    "v_mfma_f32_16x16x32_bf16 v[72:75], v[110:113], a[12:15], v[72:75]\n"
    "v_mfma_f32_16x16x32_bf16 v[76:79], v[108:111], a[12:15], v[76:79]\n"
    "v_mfma_f32_16x16x32_bf16 v[80:83], v[110:113], a[44:47], v[80:83]\n"
    "v_mfma_f32_16x16x32_bf16 v[84:87], v[108:111], a[44:47], v[84:87]\n"
    "v_mfma_f32_16x16x32_bf16 v[72:75], v[110:113], a[16:19], v[72:75]\n"
    "v_mfma_f32_16x16x32_bf16 v[76:79], v[108:111], a[16:19], v[76:79]\n"
    "v_mfma_f32_16x16x32_bf16 v[80:83], v[110:113], a[48:51], v[80:83]\n"
    "v_mfma_f32_16x16x32_bf16 v[84:87], v[108:111], a[48:51], v[84:87]\n"
    "v_mfma_f32_16x16x32_bf16 v[72:75], v[110:113], a[20:23], v[72:75]\n"
    "v_mfma_f32_16x16x32_bf16 v[76:79], v[108:111], a[20:23], v[76:79]\n"
    "v_mfma_f32_16x16x32_bf16 v[80:83], v[110:113], a[52:55], v[80:83]\n"
    "v_mfma_f32_16x16x32_bf16 v[84:87], v[108:111], a[52:55], v[84:87]\n"
    "v_mfma_f32_16x16x32_bf16 v[72:75], v[110:113], a[24:27], v[72:75]\n"
    "v_mfma_f32_16x16x32_bf16 v[76:79], v[108:111], a[24:27], v[76:79]\n"
    "v_mfma_f32_16x16x32_bf16 v[80:83], v[110:113], a[56:59], v[80:83]\n"
    "v_mfma_f32_16x16x32_bf16 v[84:87], v[108:111], a[56:59], v[84:87]\n"
    "v_mfma_f32_16x16x32_bf16 v[72:75], v[110:113], a[28:31], v[72:75]\n"
    "v_mfma_f32_16x16x32_bf16 v[76:79], v[108:111], a[28:31], v[76:79]\n"
    "v_mfma_f32_16x16x32_bf16 v[80:83], v[110:113], a[60:63], v[80:83]\n"
    "v_mfma_f32_16x16x32_bf16 v[84:87], v[108:111], a[60:63], v[84:87]\n"
    "s_cmp_eq_u32 s57, 0\n"
    "s_cbranch_scc1 85f\n"
    "v_mfma_f32_16x16x32_bf16 v[88:91], v[110:113], v[104:107], 0\n"
    "v_mfma_f32_16x16x32_bf16 v[92:95], v[108:111], v[104:107], 0\n"
    "v_mfma_f32_16x16x32_bf16 v[88:91], v[110:113], a[64:67], v[88:91]\n"
    "v_mfma_f32_16x16x32_bf16 v[92:95], v[108:111], a[64:67], v[92:95]\n"
    "v_mfma_f32_16x16x32_bf16 v[88:91], v[110:113], a[68:71], v[88:91]\n"
    "v_mfma_f32_16x16x32_bf16 v[92:95], v[108:111], a[68:71], v[92:95]\n"
    "v_mfma_f32_16x16x32_bf16 v[88:91], v[110:113], a[72:75], v[88:91]\n"
    "v_mfma_f32_16x16x32_bf16 v[92:95], v[108:111], a[72:75], v[92:95]\n"
    "v_mfma_f32_16x16x32_bf16 v[88:91], v[110:113], a[76:79], v[88:91]\n"
    "v_mfma_f32_16x16x32_bf16 v[92:95], v[108:111], a[76:79], v[92:95]\n"
    "v_mfma_f32_16x16x32_bf16 v[88:91], v[110:113], a[80:83], v[88:91]\n"
    "v_mfma_f32_16x16x32_bf16 v[92:95], v[108:111], a[80:83], v[92:95]\n"
    "v_mfma_f32_16x16x32_bf16 v[88:91], v[110:113], a[84:87], v[88:91]\n"
    "v_mfma_f32_16x16x32_bf16 v[92:95], v[108:111], a[84:87], v[92:95]\n"
    "v_mfma_f32_16x16x32_bf16 v[88:91], v[110:113], a[88:91], v[88:91]\n"
    "v_mfma_f32_16x16x32_bf16 v[92:95], v[108:111], a[88:91], v[92:95]\n"
    "v_mfma_f32_16x16x32_bf16 v[88:91], v[110:113], a[92:95], v[88:91]\n"
    "v_mfma_f32_16x16x32_bf16 v[92:95], v[108:111], a[92:95], v[92:95]\n"
    "85:\n"
    "s_cmp_lg_u32 s69, 0xffff\n"
    "s_cbranch_scc1 61b\n"
    "88:\n"
    "s_nop 7\n"
    "s_cmp_eq_u32 s57, 0\n"
    "s_cbranch_scc1 87f\n"
    "v_add_f32 v88, v88, v164\n"
    "v_add_f32 v89, v89, v165\n"
    "v_add_f32 v90, v90, v166\n"
    "v_add_f32 v91, v91, v167\n"
    "v_add_f32 v92, v92, v168\n"
    "v_add_f32 v93, v93, v169\n"
    "v_add_f32 v94, v94, v170\n"
    "v_add_f32 v95, v95, v171\n"
    "v_cvt_pk_bf16_f32 v144, v88, v89\n"
    "v_cvt_pk_bf16_f32 v145, v90, v91\n"
    "v_cvt_pk_bf16_f32 v146, v92, v93\n"
    "v_cvt_pk_bf16_f32 v147, v94, v95\n"
    "s_nop 1\n"
    "v_permlane16_swap_b32 v144, v146\n"
    "v_permlane16_swap_b32 v145, v147\n"
    "global_store_dwordx4 v124, v[144:147], s[50:51] sc1\n"
    "87:\n"
    "v_add_f32 v72, v72, v148\n"
    "v_add_f32 v73, v73, v149\n"
    "v_add_f32 v74, v74, v150\n"
    "v_add_f32 v75, v75, v151\n"
    "v_add_f32 v76, v76, v152\n"
    "v_add_f32 v77, v77, v153\n"
    "v_add_f32 v78, v78, v154\n"
    "v_add_f32 v79, v79, v155\n"
    "v_cvt_pk_bf16_f32 v136, v72, v73\n"
    "v_cvt_pk_bf16_f32 v137, v74, v75\n"
    "v_cvt_pk_bf16_f32 v138, v76, v77\n"
    "v_cvt_pk_bf16_f32 v139, v78, v79\n"
    "s_nop 1\n"
    "v_permlane16_swap_b32 v136, v138\n"
    "v_permlane16_swap_b32 v137, v139\n"
    "global_store_dwordx4 v122, v[136:139], s[50:51] sc1\n"
    "v_add_f32 v80, v80, v156\n"
    "v_add_f32 v81, v81, v157\n"
    "v_add_f32 v82, v82, v158\n"
    "v_add_f32 v83, v83, v159\n"
    "v_add_f32 v84, v84, v160\n"
    "v_add_f32 v85, v85, v161\n"
    "v_add_f32 v86, v86, v162\n"
    "v_add_f32 v87, v87, v163\n"
    "v_cvt_pk_bf16_f32 v140, v80, v81\n"
    "v_cvt_pk_bf16_f32 v141, v82, v83\n"
    "v_cvt_pk_bf16_f32 v142, v84, v85\n"
    "v_cvt_pk_bf16_f32 v143, v86, v87\n"
    "s_nop 1\n"
    "v_permlane16_swap_b32 v140, v142\n"
    "v_permlane16_swap_b32 v141, v143\n"
    "global_store_dwordx4 v123, v[140:143], s[50:51] sc1\n"
    "71:\n"
    "s_or_b32 s67, %13, %7\n"
    "s_cmp_lg_u32 s67, 0\n"
    "s_cbranch_scc1 77f\n"
    "v_min_u32 v126, 23, v255\n"
    "v_lshlrev_b32 v126, 2, v126\n"
    "s_mov_b32 s66, 0x80000\n"
    "75:\n"
    "global_load_dword v128, v126, s[32:33] offset:128 sc1\n"
    "s_waitcnt vmcnt(0)\n"
    "v_cmp_ne_u32 vcc, s62, v128\n"
    "s_nop 1\n"
    "s_cmp_eq_u64 vcc, 0\n"
    "s_cbranch_scc1 76f\n"
    "s_sleep 1\n"
    "s_sub_u32 s66, s66, 1\n"
    "s_cmp_lg_u32 s66, 0\n"
    "s_cbranch_scc1 75b\n"
    "76:\n"
    "v_mov_b32 v126, 0\n"
    "v_mov_b32 v128, s62\n"
    "s_mov_b64 exec, 1\n"
    "global_store_dword v126, v128, s[32:33] sc1\n"
    "s_mov_b64 exec, -1\n"
    "77:\n"
    "99:\n"
    :: "s"(x), "s"(wqkv), "s"(ws), "s"(wo), "s"(out), "s"(t), "s"(s), "s"(wid), "s"(ldsb), "s"(wg), "s"(wu), "s"(wd), "s"(tf), "s"(sf), "s"(b), "s"(rot) : "v0","v1","v2","v3","v4","v5","v6","v7","v8","v9","v10","v11","v12","v13","v14","v15","v16","v17","v18","v19","v20","v21","v22","v23","v24","v25","v26","v27","v28","v29","v30","v31","v32","v33","v34","v35","v36","v37","v38","v39","v40","v41","v42","v43","v44","v45","v46","v47","v48","v49","v50","v51","v52","v53","v54","v55","v56","v57","v58","v59","v60","v61","v62","v63","v64","v65","v66","v67","v68","v69","v70","v71","v72","v73","v74","v75","v76","v77","v78","v79","v80","v81","v82","v83","v84","v85","v86","v87","v88","v89","v90","v91","v92","v93","v94","v95","v96","v97","v98","v99","v100","v101","v102","v103","v104","v105","v106","v107","v108","v109","v110","v111","v112","v113","v114","v115","v116","v117","v118","v119","v120","v121","v122","v123","v124","v125","v126","v127","v128","v129","v130","v131","v132","v133","v134","v135","v136","v137","v138","v139","v140","v141","v142","v143","v144","v145","v146","v147","v148","v149","v150","v151","v152","v153","v154","v155","v156","v157","v158","v159","v160","v161","v162","v163","v164","v165","v166","v167","v168","v169","v170","v171","v172","v173","v174","v175","v176","v177","v178","v179","v180","v181","v182","v183","v184","v185","v186","v187","v188","v189","v190","v191","v192","v193","v194","v195","v196","v197","v198","v199","v200","v201","v202","v203","v204","v205","v206","v207","v208","v209","v210","v211","v212","v213","v214","v215","v216","v217","v218","v219","v220","v221","v222","v223","v224","v225","v226","v227","v228","v229","v230","v231","v232","v233","v234","v235","v236","v237","v238","v239","v240","v241","v242","v243","v244","v245","v246","v247","v248","v249","v250","v251","v252","v253","v254","v255","a0","a1","a2","a3","a4","a5","a6","a7","a8","a9","a10","a11","a12","a13","a14","a15","a16","a17","a18","a19","a20","a21","a22","a23","a24","a25","a26","a27","a28","a29","a30","a31","a32","a33","a34","a35","a36","a37","a38","a39","a40","a41","a42","a43","a44","a45","a46","a47","a48","a49","a50","a51","a52","a53","a54","a55","a56","a57","a58","a59","a60","a61","a62","a63","a64","a65","a66","a67","a68","a69","a70","a71","a72","a73","a74","a75","a76","a77","a78","a79","a80","a81","a82","a83","a84","a85","a86","a87","a88","a89","a90","a91","a92","a93","a94","a95","a96","a97","a98","a99","a100","a101","a102","a103","a104","a105","a106","a107","a108","a109","a110","a111","a112","a113","a114","a115","a116","a117","a118","a119","a120","a121","a122","a123","a124","a125","a126","a127","a128","a129","a130","a131","a132","a133","a134","a135","a136","a137","a138","a139","a140","a141","a142","a143","a144","a145","a146","a147","a148","a149","a150","a151","a152","a153","a154","a155","a156","a157","a158","a159","a160","a161","a162","a163","a164","a165","a166","a167","a168","a169","a170","a171","a172","a173","a174","a175","a176","a177","a178","a179","a180","a181","a182","a183","a184","a185","a186","a187","a188","a189","a190","a191","a192","a193","a194","a195","a196","a197","a198","a199","a200","a201","a202","a203","a204","a205","a206","a207","a208","a209","a210","a211","a212","a213","a214","a215","a216","a217","a218","a219","a220","a221","a222","a223","a224","a225","a226","a227","a228","a229","a230","a231","a232","a233","a234","a235","a236","a237","a238","a239","a240","a241","a242","a243","a244","a245","a246","a247","a248","a249","a250","a251","a252","a253","a254","a255","s30","s31","s32","s33","s34","s35","s36","s37","s38","s39","s40","s41","s42","s43","s44","s45","s46","s47","s48","s49","s50","s51","s52","s53","s54","s55","s56","s57","s58","s59","s60","s61","s62","s63","s64","s65","s66","s67","s68","s69","s70","s71","s72","s73","s74","s75","s76","s77","s78","s79","s80","s81","s82","s83","s84","s85","s86","s87","s88","s89","s90","s91","s92","s93","s94","s95","s96","s97","s98","s99","vcc","scc","memory");
  } else {
    asm volatile(
    "s_lshl_b32 s67, %5, 12\n"
    "s_add_u32 s67, s67, 65536\n"
    "s_mov_b64 s[30:31], %2\n"
    "s_add_u32 s30, s30, s67\n"
    "s_addc_u32 s31, s31, 0\n"
    "s_lshl_b32 s67, %12, 12\n"
    "s_add_u32 s67, s67, 65536\n"
    "s_mov_b64 s[32:33], %2\n"
    "s_add_u32 s32, s32, s67\n"
    "s_addc_u32 s33, s33, 0\n"
    "v_mov_b32 v243, 0\n"
    "global_load_dword v244, v243, s[30:31] sc1\n"
    "global_load_dword v245, v243, s[32:33] sc1\n"
    "v_mbcnt_lo_u32_b32 v255, -1, 0\n"
    "v_mbcnt_hi_u32_b32 v255, -1, v255\n"
    "s_mov_b64 s[36:37], %0\n"
    "s_and_b32 s37, s37, 0xffff\n"
    "s_mov_b32 s38, 2359296\n"
    "s_mov_b32 s39, 0x20000\n"
    "s_lshl_b32 s64, %5, 2\n"
    "s_add_u32 s64, s64, %7\n"
    "s_mul_i32 s64, s64, 36864\n"
    "v_lshrrev_b32 v254, 3, v255\n"
    "v_mul_u32_u24 v254, 0x480, v254\n"
    "v_and_b32 v252, 7, v255\n"
    "v_lshrrev_b32 v251, 4, v255\n"
    "v_xor_b32 v252, v252, v251\n"
    "v_lshl_add_u32 v250, v252, 4, v254\n"
    "v_xor_b32 v252, 4, v252\n"
    "v_lshl_add_u32 v249, v252, 4, v254\n"
    "s_mul_i32 s63, %7, 36864\n"
    "s_add_u32 s63, s63, %8\n"
    "s_add_u32 s52, s64, 1152\n"
    "s_lshl_b32 s65, %15, 7\n"
    "s_add_u32 s64, s64, s65\n"
    "s_add_u32 m0, s63, 0\n"
    "s_add_u32 s65, s64, 0\n"
    "buffer_load_dwordx4 v250, s[36:39], s65 offen lds\n"
    "s_add_u32 m0, s63, 1024\n"
    "s_add_u32 s65, s64, 9216\n"
    "buffer_load_dwordx4 v249, s[36:39], s65 offen lds\n"
    "s_add_u32 m0, s63, 2048\n"
    "s_add_u32 s65, s64, 18432\n"
    "buffer_load_dwordx4 v250, s[36:39], s65 offen lds\n"
    "s_add_u32 m0, s63, 3072\n"
    "s_add_u32 s65, s64, 27648\n"
    "buffer_load_dwordx4 v249, s[36:39], s65 offen lds\n"
    "s_add_u32 s64, s64, 0x80\n"
    "s_cmp_eq_u32 s64, s52\n"
    "s_cselect_b32 s65, 1152, 0\n"
    "s_sub_u32 s64, s64, s65\n"
    "s_add_u32 m0, s63, 4096\n"
    "s_add_u32 s65, s64, 0\n"
    "buffer_load_dwordx4 v250, s[36:39], s65 offen lds\n"
    "s_add_u32 m0, s63, 5120\n"
    "s_add_u32 s65, s64, 9216\n"
    "buffer_load_dwordx4 v249, s[36:39], s65 offen lds\n"
    "s_add_u32 m0, s63, 6144\n"
    "s_add_u32 s65, s64, 18432\n"
    "buffer_load_dwordx4 v250, s[36:39], s65 offen lds\n"
    "s_add_u32 m0, s63, 7168\n"
    "s_add_u32 s65, s64, 27648\n"
    "buffer_load_dwordx4 v249, s[36:39], s65 offen lds\n"
    "v_and_b32 v254, 15, v255\n"
    "v_lshrrev_b32 v253, 4, v255\n"
    "v_bfe_u32 v252, v255, 1, 3\n"
    "v_xor_b32 v252, v252, v253\n"
    "v_lshlrev_b32 v252, 4, v252\n"
    "v_lshl_add_u32 v252, v254, 7, v252\n"
    "v_add_u32 v251, s63, v252\n"
    "v_xor_b32 v252, 64, v251\n"
    "v_mov_b32 v144, 0\n"
    "v_mov_b32 v145, 0\n"
    "v_mov_b32 v146, 0\n"
    "v_mov_b32 v147, 0\n"
    "v_mov_b32 v148, 0\n"
    "v_mov_b32 v149, 0\n"
    "v_mov_b32 v150, 0\n"
    "v_mov_b32 v151, 0\n"
    "s_mul_i32 s67, %6, 1152\n"
    "s_mul_i32 s66, %6, 663552\n"
    "s_mov_b64 s[40:41], %9\n"
    "s_add_u32 s40, s40, s67\n"
    "s_addc_u32 s41, s41, 0\n"
    "s_and_b32 s41, s41, 0xffff\n"
    "s_mov_b32 s42, 5308416\n"
    "s_mov_b32 s43, 0x20000\n"
    "s_mov_b64 s[44:45], %10\n"
    "s_add_u32 s44, s44, s67\n"
    "s_addc_u32 s45, s45, 0\n"
    "s_and_b32 s45, s45, 0xffff\n"
    "s_mov_b32 s46, 5308416\n"
    "s_mov_b32 s47, 0x20000\n"
    "s_mov_b64 s[48:49], %11\n"
    "s_add_u32 s48, s48, s66\n"
    "s_addc_u32 s49, s49, 0\n"
    "s_and_b32 s49, s49, 0xffff\n"
    "s_mov_b32 s50, 5308416\n"
    "s_mov_b32 s51, 0x20000\n"
    "s_and_b32 s67, %5, 7\n"
    "s_lshl_b32 s67, s67, 2\n"
    "s_add_u32 s67, s67, %7\n"
    "s_mul_i32 s68, s67, 165888\n"
    "s_mul_i32 s69, %6, 1152\n"
    "s_add_u32 s68, s68, s69\n"
    "s_add_u32 s68, s68, 0\n"
    "s_mul_i32 s69, %6, 576\n"
    "s_add_u32 s69, s69, s67\n"
    "s_add_u32 s69, s69, 0\n"
    "s_mul_i32 s69, s69, 1152\n"
    "s_mov_b64 s[70:71], %9\n"
    "s_mov_b64 s[72:73], %10\n"
    "s_mov_b64 s[74:75], %11\n"
    "s_add_u32 s70, s70, s68\n"
    "s_addc_u32 s71, s71, 0\n"
    "s_add_u32 s72, s72, s68\n"
    "s_addc_u32 s73, s73, 0\n"
    "s_add_u32 s74, s74, s69\n"
    "s_addc_u32 s75, s75, 0\n"
    "v_cmp_gt_u32 vcc, 18, v255\n"
    "v_cmp_gt_u32 s[76:77], 36, v255\n"
    "v_subrev_u32 v200, 18, v255\n"
    "v_subrev_u32 v201, 36, v255\n"
    "v_min_u32 v201, 17, v201\n"
    "v_cndmask_b32 v200, v200, v255, vcc\n"
    "v_mul_u32_u24 v200, 0x2400, v200\n"
    "v_lshlrev_b32 v202, 7, v201\n"
    "v_cmp_lt_u32 s[68:69], 8, v201\n"
    "v_mov_b32 v203, 35712\n"
    "s_nop 0\n"
    "v_cndmask_b32 v203, 0, v203, s[68:69]\n"
    "v_add_u32 v201, v202, v203\n"
    "v_cndmask_b32 v200, v201, v200, s[76:77]\n"
    "v_mov_b32 v201, s72\n"
    "v_mov_b32 v202, s70\n"
    "v_cndmask_b32 v201, v201, v202, vcc\n"
    "v_mov_b32 v202, s74\n"
    "v_cndmask_b32 v206, v202, v201, s[76:77]\n"
    "v_mov_b32 v201, s73\n"
    "v_mov_b32 v202, s71\n"
    "v_cndmask_b32 v201, v201, v202, vcc\n"
    "v_mov_b32 v202, s75\n"
    "v_cndmask_b32 v207, v202, v201, s[76:77]\n"
    "v_mov_b32 v201, 73728\n"
    "v_mov_b32 v202, 0x80\n"
    "v_cndmask_b32 v205, v201, v202, s[76:77]\n"
    "v_add_co_u32 v206, vcc, v200, v206\n"
    "s_nop 1\n"
    "v_addc_co_u32 v207, vcc, 0, v207, vcc\n"
    "v_lshrrev_b32 v202, 2, v255\n"
    "v_mul_u32_u24 v202, 0x2400, v202\n"
    "v_and_b32 v201, 3, v255\n"
    "v_lshrrev_b32 v203, 4, v255\n"
    "v_and_b32 v204, 2, v203\n"
    "v_xor_b32 v204, v204, v201\n"
    "v_lshl_add_u32 v246, v204, 4, v202\n"
    "v_lshrrev_b32 v202, 3, v255\n"
    "v_mul_u32_u24 v202, 0x480, v202\n"
    "v_and_b32 v200, 7, v255\n"
    "v_lshlrev_b32 v204, 1, v203\n"
    "v_xor_b32 v200, v200, v204\n"
    "v_lshl_add_u32 v247, v200, 4, v202\n"
    "v_bfe_u32 v200, v255, 2, 2\n"
    "v_lshlrev_b32 v201, 3, v201\n"
    "v_add_u32 v201, %8, v201\n"
    "v_lshl_add_u32 v202, v203, 3, v200\n"
    "v_lshl_add_u32 v202, v202, 6, v201\n"
    "v_and_b32 v204, 1, v203\n"
    "v_lshl_add_u32 v240, v204, 5, v202\n"
    "v_lshl_add_u32 v202, v203, 2, v200\n"
    "v_bfe_u32 v204, v202, 1, 2\n"
    "v_lshl_add_u32 v202, v202, 7, v201\n"
    "v_lshl_add_u32 v241, v204, 5, v202\n"
    "s_mov_b32 s54, 73728\n"
    "s_mov_b32 s55, 110592\n"
    "s_mov_b32 s56, 0\n"
    "s_mov_b32 s57, 36864\n"
    "s_mul_i32 s60, %7, 9216\n"
    "s_add_u32 s60, s60, %8\n"
    "s_lshl_b32 s61, %7, 10\n"
    "s_add_u32 s61, s61, %8\n"
    "s_mul_i32 s67, %7, 9\n"
    "s_lshl_b32 s66, %15, 2\n"
    "s_add_u32 s67, s67, s66\n"
    "s_cmp_ge_u32 s67, 36\n"
    "s_cselect_b32 s66, 36, 0\n"
    "s_sub_u32 s67, s67, s66\n"
    "s_mul_i32 s68, s67, 147456\n"
    "s_add_u32 s67, s67, 1\n"
    "s_cmp_ge_u32 s67, 36\n"
    "s_cselect_b32 s66, 36, 0\n"
    "s_sub_u32 s67, s67, s66\n"
    "s_mul_i32 s69, s67, 147456\n"
    "s_add_u32 s67, s67, 1\n"
    "s_cmp_ge_u32 s67, 36\n"
    "s_cselect_b32 s66, 36, 0\n"
    "s_sub_u32 s67, s67, s66\n"
    "s_mul_i32 s70, s67, 147456\n"
    "s_add_u32 s67, s67, 1\n"
    "s_cmp_ge_u32 s67, 36\n"
    "s_cselect_b32 s66, 36, 0\n"
    "s_sub_u32 s67, s67, s66\n"
    "s_mul_i32 s71, s67, 147456\n"
    "s_add_u32 s67, s67, 1\n"
    "s_cmp_ge_u32 s67, 36\n"
    "s_cselect_b32 s66, 36, 0\n"
    "s_sub_u32 s67, s67, s66\n"
    "s_mul_i32 s72, s67, 147456\n"
    "s_add_u32 s67, s67, 1\n"
    "s_cmp_ge_u32 s67, 36\n"
    "s_cselect_b32 s66, 36, 0\n"
    "s_sub_u32 s67, s67, s66\n"
    "s_mul_i32 s73, s67, 147456\n"
    "s_add_u32 s67, s67, 1\n"
    "s_cmp_ge_u32 s67, 36\n"
    "s_cselect_b32 s66, 36, 0\n"
    "s_sub_u32 s67, s67, s66\n"
    "s_mul_i32 s74, s67, 147456\n"
    "s_add_u32 s67, s67, 1\n"
    "s_cmp_ge_u32 s67, 36\n"
    "s_cselect_b32 s66, 36, 0\n"
    "s_sub_u32 s67, s67, s66\n"
    "s_mul_i32 s75, s67, 147456\n"
    "s_add_u32 s67, s67, 1\n"
    "s_cmp_ge_u32 s67, 36\n"
    "s_cselect_b32 s66, 36, 0\n"
    "s_sub_u32 s67, s67, s66\n"
    "s_mul_i32 s76, s67, 147456\n"
    "s_add_u32 s67, s67, 1\n"
    "s_add_u32 s64, s64, 0x80\n"
    "s_cmp_eq_u32 s64, s52\n"
    "s_cselect_b32 s65, 1152, 0\n"
    "s_sub_u32 s64, s64, s65\n"
    "s_add_u32 m0, s63, 8192\n"
    "s_add_u32 s65, s64, 0\n"
    "buffer_load_dwordx4 v250, s[36:39], s65 offen lds\n"
    "s_add_u32 m0, s63, 9216\n"
    "s_add_u32 s65, s64, 9216\n"
    "buffer_load_dwordx4 v249, s[36:39], s65 offen lds\n"
    "s_add_u32 m0, s63, 10240\n"
    "s_add_u32 s65, s64, 18432\n"
    "buffer_load_dwordx4 v250, s[36:39], s65 offen lds\n"
    "s_add_u32 m0, s63, 11264\n"
    "s_add_u32 s65, s64, 27648\n"
    "buffer_load_dwordx4 v249, s[36:39], s65 offen lds\n"
    "s_waitcnt vmcnt(8)\n"
    "ds_read_b128 v[0:3], v251 offset:0\n"
    "ds_read_b128 v[4:7], v251 offset:2048\n"
    "ds_read_b128 v[8:11], v252 offset:0\n"
    "ds_read_b128 v[12:15], v252 offset:2048\n"
    "s_mov_b32 s53, 0x3f803f80\n"
    "s_add_u32 s64, s64, 0x80\n"
    "s_cmp_eq_u32 s64, s52\n"
    "s_cselect_b32 s65, 1152, 0\n"
    "s_sub_u32 s64, s64, s65\n"
    "s_add_u32 m0, s63, 12288\n"
    "s_add_u32 s65, s64, 0\n"
    "buffer_load_dwordx4 v250, s[36:39], s65 offen lds\n"
    "s_add_u32 m0, s63, 13312\n"
    "s_add_u32 s65, s64, 9216\n"
    "buffer_load_dwordx4 v249, s[36:39], s65 offen lds\n"
    "s_add_u32 m0, s63, 14336\n"
    "s_add_u32 s65, s64, 18432\n"
    "buffer_load_dwordx4 v250, s[36:39], s65 offen lds\n"
    "s_add_u32 m0, s63, 15360\n"
    "s_add_u32 s65, s64, 27648\n"
    "buffer_load_dwordx4 v249, s[36:39], s65 offen lds\n"
    "s_waitcnt vmcnt(8)\n"
    "ds_read_b128 v[16:19], v251 offset:4096\n"
    "ds_read_b128 v[20:23], v251 offset:6144\n"
    "ds_read_b128 v[24:27], v252 offset:4096\n"
    "ds_read_b128 v[28:31], v252 offset:6144\n"
    "s_waitcnt lgkmcnt(4)\n"
    "v_dot2c_f32_bf16 v144, s53, v0\n"
    "v_dot2c_f32_bf16 v148, v0, v0\n"
    "v_dot2c_f32_bf16 v145, s53, v1\n"
    "v_dot2c_f32_bf16 v149, v1, v1\n"
    "v_dot2c_f32_bf16 v144, s53, v2\n"
    "v_dot2c_f32_bf16 v148, v2, v2\n"
    "v_dot2c_f32_bf16 v145, s53, v3\n"
    "v_dot2c_f32_bf16 v149, v3, v3\n"
    "v_dot2c_f32_bf16 v146, s53, v4\n"
    "v_dot2c_f32_bf16 v150, v4, v4\n"
    "v_dot2c_f32_bf16 v147, s53, v5\n"
    "v_dot2c_f32_bf16 v151, v5, v5\n"
    "v_dot2c_f32_bf16 v146, s53, v6\n"
    "v_dot2c_f32_bf16 v150, v6, v6\n"
    "v_dot2c_f32_bf16 v147, s53, v7\n"
    "v_dot2c_f32_bf16 v151, v7, v7\n"
    "v_dot2c_f32_bf16 v144, s53, v8\n"
    "v_dot2c_f32_bf16 v148, v8, v8\n"
    "v_dot2c_f32_bf16 v145, s53, v9\n"
    "v_dot2c_f32_bf16 v149, v9, v9\n"
    "v_dot2c_f32_bf16 v144, s53, v10\n"
    "v_dot2c_f32_bf16 v148, v10, v10\n"
    "v_dot2c_f32_bf16 v145, s53, v11\n"
    "v_dot2c_f32_bf16 v149, v11, v11\n"
    "v_dot2c_f32_bf16 v146, s53, v12\n"
    "v_dot2c_f32_bf16 v150, v12, v12\n"
    "v_dot2c_f32_bf16 v147, s53, v13\n"
    "v_dot2c_f32_bf16 v151, v13, v13\n"
    "v_dot2c_f32_bf16 v146, s53, v14\n"
    "v_dot2c_f32_bf16 v150, v14, v14\n"
    "v_dot2c_f32_bf16 v147, s53, v15\n"
    "v_dot2c_f32_bf16 v151, v15, v15\n"
    "s_add_u32 s64, s64, 0x80\n"
    "s_cmp_eq_u32 s64, s52\n"
    "s_cselect_b32 s65, 1152, 0\n"
    "s_sub_u32 s64, s64, s65\n"
    "s_add_u32 m0, s63, 16384\n"
    "s_add_u32 s65, s64, 0\n"
    "buffer_load_dwordx4 v250, s[36:39], s65 offen lds\n"
    "s_add_u32 m0, s63, 17408\n"
    "s_add_u32 s65, s64, 9216\n"
    "buffer_load_dwordx4 v249, s[36:39], s65 offen lds\n"
    "s_add_u32 m0, s63, 18432\n"
    "s_add_u32 s65, s64, 18432\n"
    "buffer_load_dwordx4 v250, s[36:39], s65 offen lds\n"
    "s_add_u32 m0, s63, 19456\n"
    "s_add_u32 s65, s64, 27648\n"
    "buffer_load_dwordx4 v249, s[36:39], s65 offen lds\n"
    "s_waitcnt vmcnt(8)\n"
    "ds_read_b128 v[32:35], v251 offset:8192\n"
    "ds_read_b128 v[36:39], v251 offset:10240\n"
    "ds_read_b128 v[40:43], v252 offset:8192\n"
    "ds_read_b128 v[44:47], v252 offset:10240\n"
    "s_waitcnt lgkmcnt(4)\n"
    "v_dot2c_f32_bf16 v144, s53, v16\n"
    "v_dot2c_f32_bf16 v148, v16, v16\n"
    "v_dot2c_f32_bf16 v145, s53, v17\n"
    "v_dot2c_f32_bf16 v149, v17, v17\n"
    "v_dot2c_f32_bf16 v144, s53, v18\n"
    "v_dot2c_f32_bf16 v148, v18, v18\n"
    "v_dot2c_f32_bf16 v145, s53, v19\n"
    "v_dot2c_f32_bf16 v149, v19, v19\n"
    "v_dot2c_f32_bf16 v146, s53, v20\n"
    "v_dot2c_f32_bf16 v150, v20, v20\n"
    "v_dot2c_f32_bf16 v147, s53, v21\n"
    "v_dot2c_f32_bf16 v151, v21, v21\n"
    "v_dot2c_f32_bf16 v146, s53, v22\n"
    "v_dot2c_f32_bf16 v150, v22, v22\n"
    "v_dot2c_f32_bf16 v147, s53, v23\n"
    "v_dot2c_f32_bf16 v151, v23, v23\n"
    "v_dot2c_f32_bf16 v144, s53, v24\n"
    "v_dot2c_f32_bf16 v148, v24, v24\n"
    "v_dot2c_f32_bf16 v145, s53, v25\n"
    "v_dot2c_f32_bf16 v149, v25, v25\n"
    "v_dot2c_f32_bf16 v144, s53, v26\n"
    "v_dot2c_f32_bf16 v148, v26, v26\n"
    "v_dot2c_f32_bf16 v145, s53, v27\n"
    "v_dot2c_f32_bf16 v149, v27, v27\n"
    "v_dot2c_f32_bf16 v146, s53, v28\n"
    "v_dot2c_f32_bf16 v150, v28, v28\n"
    "v_dot2c_f32_bf16 v147, s53, v29\n"
    "v_dot2c_f32_bf16 v151, v29, v29\n"
    "v_dot2c_f32_bf16 v146, s53, v30\n"
    "v_dot2c_f32_bf16 v150, v30, v30\n"
    "v_dot2c_f32_bf16 v147, s53, v31\n"
    "v_dot2c_f32_bf16 v151, v31, v31\n"
    "s_add_u32 s64, s64, 0x80\n"
    "s_cmp_eq_u32 s64, s52\n"
    "s_cselect_b32 s65, 1152, 0\n"
    "s_sub_u32 s64, s64, s65\n"
    "s_add_u32 m0, s63, 20480\n"
    "s_add_u32 s65, s64, 0\n"
    "buffer_load_dwordx4 v250, s[36:39], s65 offen lds\n"
    "s_add_u32 m0, s63, 21504\n"
    "s_add_u32 s65, s64, 9216\n"
    "buffer_load_dwordx4 v249, s[36:39], s65 offen lds\n"
    "s_add_u32 m0, s63, 22528\n"
    "s_add_u32 s65, s64, 18432\n"
    "buffer_load_dwordx4 v250, s[36:39], s65 offen lds\n"
    "s_add_u32 m0, s63, 23552\n"
    "s_add_u32 s65, s64, 27648\n"
    "buffer_load_dwordx4 v249, s[36:39], s65 offen lds\n"
    "s_waitcnt vmcnt(8)\n"
    "ds_read_b128 v[48:51], v251 offset:12288\n"
    "ds_read_b128 v[52:55], v251 offset:14336\n"
    "ds_read_b128 v[56:59], v252 offset:12288\n"
    "ds_read_b128 v[60:63], v252 offset:14336\n"
    "s_waitcnt lgkmcnt(4)\n"
    "v_dot2c_f32_bf16 v144, s53, v32\n"
    "v_dot2c_f32_bf16 v148, v32, v32\n"
    "v_dot2c_f32_bf16 v145, s53, v33\n"
    "v_dot2c_f32_bf16 v149, v33, v33\n"
    "v_dot2c_f32_bf16 v144, s53, v34\n"
    "v_dot2c_f32_bf16 v148, v34, v34\n"
    "v_dot2c_f32_bf16 v145, s53, v35\n"
    "v_dot2c_f32_bf16 v149, v35, v35\n"
    "v_dot2c_f32_bf16 v146, s53, v36\n"
    "v_dot2c_f32_bf16 v150, v36, v36\n"
    "v_dot2c_f32_bf16 v147, s53, v37\n"
    "v_dot2c_f32_bf16 v151, v37, v37\n"
    "v_dot2c_f32_bf16 v146, s53, v38\n"
    "v_dot2c_f32_bf16 v150, v38, v38\n"
    "v_dot2c_f32_bf16 v147, s53, v39\n"
    "v_dot2c_f32_bf16 v151, v39, v39\n"
    "v_dot2c_f32_bf16 v144, s53, v40\n"
    "v_dot2c_f32_bf16 v148, v40, v40\n"
    "v_dot2c_f32_bf16 v145, s53, v41\n"
    "v_dot2c_f32_bf16 v149, v41, v41\n"
    "v_dot2c_f32_bf16 v144, s53, v42\n"
    "v_dot2c_f32_bf16 v148, v42, v42\n"
    "v_dot2c_f32_bf16 v145, s53, v43\n"
    "v_dot2c_f32_bf16 v149, v43, v43\n"
    "v_dot2c_f32_bf16 v146, s53, v44\n"
    "v_dot2c_f32_bf16 v150, v44, v44\n"
    "v_dot2c_f32_bf16 v147, s53, v45\n"
    "v_dot2c_f32_bf16 v151, v45, v45\n"
    "v_dot2c_f32_bf16 v146, s53, v46\n"
    "v_dot2c_f32_bf16 v150, v46, v46\n"
    "v_dot2c_f32_bf16 v147, s53, v47\n"
    "v_dot2c_f32_bf16 v151, v47, v47\n"
    "s_add_u32 s64, s64, 0x80\n"
    "s_cmp_eq_u32 s64, s52\n"
    "s_cselect_b32 s65, 1152, 0\n"
    "s_sub_u32 s64, s64, s65\n"
    "s_add_u32 m0, s63, 24576\n"
    "s_add_u32 s65, s64, 0\n"
    "buffer_load_dwordx4 v250, s[36:39], s65 offen lds\n"
    "s_add_u32 m0, s63, 25600\n"
    "s_add_u32 s65, s64, 9216\n"
    "buffer_load_dwordx4 v249, s[36:39], s65 offen lds\n"
    "s_add_u32 m0, s63, 26624\n"
    "s_add_u32 s65, s64, 18432\n"
    "buffer_load_dwordx4 v250, s[36:39], s65 offen lds\n"
    "s_add_u32 m0, s63, 27648\n"
    "s_add_u32 s65, s64, 27648\n"
    "buffer_load_dwordx4 v249, s[36:39], s65 offen lds\n"
    "s_waitcnt vmcnt(8)\n"
    "ds_read_b128 v[64:67], v251 offset:16384\n"
    "ds_read_b128 v[68:71], v251 offset:18432\n"
    "ds_read_b128 v[72:75], v252 offset:16384\n"
    "ds_read_b128 v[76:79], v252 offset:18432\n"
    "s_waitcnt lgkmcnt(4)\n"
    "v_dot2c_f32_bf16 v144, s53, v48\n"
    "v_dot2c_f32_bf16 v148, v48, v48\n"
    "v_dot2c_f32_bf16 v145, s53, v49\n"
    "v_dot2c_f32_bf16 v149, v49, v49\n"
    "v_dot2c_f32_bf16 v144, s53, v50\n"
    "v_dot2c_f32_bf16 v148, v50, v50\n"
    "v_dot2c_f32_bf16 v145, s53, v51\n"
    "v_dot2c_f32_bf16 v149, v51, v51\n"
    "v_dot2c_f32_bf16 v146, s53, v52\n"
    "v_dot2c_f32_bf16 v150, v52, v52\n"
    "v_dot2c_f32_bf16 v147, s53, v53\n"
    "v_dot2c_f32_bf16 v151, v53, v53\n"
    "v_dot2c_f32_bf16 v146, s53, v54\n"
    "v_dot2c_f32_bf16 v150, v54, v54\n"
    "v_dot2c_f32_bf16 v147, s53, v55\n"
    "v_dot2c_f32_bf16 v151, v55, v55\n"
    "v_dot2c_f32_bf16 v144, s53, v56\n"
    "v_dot2c_f32_bf16 v148, v56, v56\n"
    "v_dot2c_f32_bf16 v145, s53, v57\n"
    "v_dot2c_f32_bf16 v149, v57, v57\n"
    "v_dot2c_f32_bf16 v144, s53, v58\n"
    "v_dot2c_f32_bf16 v148, v58, v58\n"
    "v_dot2c_f32_bf16 v145, s53, v59\n"
    "v_dot2c_f32_bf16 v149, v59, v59\n"
    "v_dot2c_f32_bf16 v146, s53, v60\n"
    "v_dot2c_f32_bf16 v150, v60, v60\n"
    "v_dot2c_f32_bf16 v147, s53, v61\n"
    "v_dot2c_f32_bf16 v151, v61, v61\n"
    "v_dot2c_f32_bf16 v146, s53, v62\n"
    "v_dot2c_f32_bf16 v150, v62, v62\n"
    "v_dot2c_f32_bf16 v147, s53, v63\n"
    "v_dot2c_f32_bf16 v151, v63, v63\n"
    "s_add_u32 s64, s64, 0x80\n"
    "s_cmp_eq_u32 s64, s52\n"
    "s_cselect_b32 s65, 1152, 0\n"
    "s_sub_u32 s64, s64, s65\n"
    "s_add_u32 m0, s63, 28672\n"
    "s_add_u32 s65, s64, 0\n"
    "buffer_load_dwordx4 v250, s[36:39], s65 offen lds\n"
    "s_add_u32 m0, s63, 29696\n"
    "s_add_u32 s65, s64, 9216\n"
    "buffer_load_dwordx4 v249, s[36:39], s65 offen lds\n"
    "s_add_u32 m0, s63, 30720\n"
    "s_add_u32 s65, s64, 18432\n"
    "buffer_load_dwordx4 v250, s[36:39], s65 offen lds\n"
    "s_add_u32 m0, s63, 31744\n"
    "s_add_u32 s65, s64, 27648\n"
    "buffer_load_dwordx4 v249, s[36:39], s65 offen lds\n"
    "s_waitcnt vmcnt(8)\n"
    "ds_read_b128 v[80:83], v251 offset:20480\n"
    "ds_read_b128 v[84:87], v251 offset:22528\n"
    "ds_read_b128 v[88:91], v252 offset:20480\n"
    "ds_read_b128 v[92:95], v252 offset:22528\n"
    "s_waitcnt lgkmcnt(4)\n"
    "v_dot2c_f32_bf16 v144, s53, v64\n"
    "v_dot2c_f32_bf16 v148, v64, v64\n"
    "v_dot2c_f32_bf16 v145, s53, v65\n"
    "v_dot2c_f32_bf16 v149, v65, v65\n"
    "v_dot2c_f32_bf16 v144, s53, v66\n"
    "v_dot2c_f32_bf16 v148, v66, v66\n"
    "v_dot2c_f32_bf16 v145, s53, v67\n"
    "v_dot2c_f32_bf16 v149, v67, v67\n"
    "v_dot2c_f32_bf16 v146, s53, v68\n"
    "v_dot2c_f32_bf16 v150, v68, v68\n"
    "v_dot2c_f32_bf16 v147, s53, v69\n"
    "v_dot2c_f32_bf16 v151, v69, v69\n"
    "v_dot2c_f32_bf16 v146, s53, v70\n"
    "v_dot2c_f32_bf16 v150, v70, v70\n"
    "v_dot2c_f32_bf16 v147, s53, v71\n"
    "v_dot2c_f32_bf16 v151, v71, v71\n"
    "v_dot2c_f32_bf16 v144, s53, v72\n"
    "v_dot2c_f32_bf16 v148, v72, v72\n"
    "v_dot2c_f32_bf16 v145, s53, v73\n"
    "v_dot2c_f32_bf16 v149, v73, v73\n"
    "v_dot2c_f32_bf16 v144, s53, v74\n"
    "v_dot2c_f32_bf16 v148, v74, v74\n"
    "v_dot2c_f32_bf16 v145, s53, v75\n"
    "v_dot2c_f32_bf16 v149, v75, v75\n"
    "v_dot2c_f32_bf16 v146, s53, v76\n"
    "v_dot2c_f32_bf16 v150, v76, v76\n"
    "v_dot2c_f32_bf16 v147, s53, v77\n"
    "v_dot2c_f32_bf16 v151, v77, v77\n"
    "v_dot2c_f32_bf16 v146, s53, v78\n"
    "v_dot2c_f32_bf16 v150, v78, v78\n"
    "v_dot2c_f32_bf16 v147, s53, v79\n"
    "v_dot2c_f32_bf16 v151, v79, v79\n"
    "s_add_u32 s64, s64, 0x80\n"
    "s_cmp_eq_u32 s64, s52\n"
    "s_cselect_b32 s65, 1152, 0\n"
    "s_sub_u32 s64, s64, s65\n"
    "s_add_u32 m0, s63, 32768\n"
    "s_add_u32 s65, s64, 0\n"
    "buffer_load_dwordx4 v250, s[36:39], s65 offen lds\n"
    "s_add_u32 m0, s63, 33792\n"
    "s_add_u32 s65, s64, 9216\n"
    "buffer_load_dwordx4 v249, s[36:39], s65 offen lds\n"
    "s_add_u32 m0, s63, 34816\n"
    "s_add_u32 s65, s64, 18432\n"
    "buffer_load_dwordx4 v250, s[36:39], s65 offen lds\n"
    "s_add_u32 m0, s63, 35840\n"
    "s_add_u32 s65, s64, 27648\n"
    "buffer_load_dwordx4 v249, s[36:39], s65 offen lds\n"
    "s_getpc_b64 s[96:97]\n"
    "98:\n"
    "s_lshr_b32 s67, %14, 3\n"
    "s_and_b32 s67, s67, 15\n"
    "s_lshl_b32 s67, s67, 2\n"
    "s_add_u32 s67, s67, %7\n"
    "s_lshl_b32 s67, s67, 7\n"
    "v_and_b32 v200, 7, v255\n"
    "v_lshlrev_b32 v200, 13, v200\n"
    "v_add_u32 v200, s67, v200\n"
    "v_min_u32 v200, 99f-98b-4, v200\n"
    "s_add_u32 m0, %8, 147456\n"
    "s_mov_b64 exec, 0xff\n"
    "global_load_lds_dword v200, s[96:97]\n"
    "s_mov_b64 exec, -1\n"
    "s_add_u32 m0, %8, 147456\n"
    "s_mov_b32 exec_hi, 15\n"
    "global_load_lds_dword v[206:207], off\n"
    "s_mov_b32 exec_hi, -1\n"
    "s_waitcnt vmcnt(10)\n"
    "ds_read_b128 v[96:99], v251 offset:24576\n"
    "ds_read_b128 v[100:103], v251 offset:26624\n"
    "ds_read_b128 v[104:107], v252 offset:24576\n"
    "ds_read_b128 v[108:111], v252 offset:26624\n"
    "s_waitcnt lgkmcnt(4)\n"
    "v_dot2c_f32_bf16 v144, s53, v80\n"
    "v_dot2c_f32_bf16 v148, v80, v80\n"
    "v_dot2c_f32_bf16 v145, s53, v81\n"
    "v_dot2c_f32_bf16 v149, v81, v81\n"
    "v_dot2c_f32_bf16 v144, s53, v82\n"
    "v_dot2c_f32_bf16 v148, v82, v82\n"
    "v_dot2c_f32_bf16 v145, s53, v83\n"
    "v_dot2c_f32_bf16 v149, v83, v83\n"
    "v_dot2c_f32_bf16 v146, s53, v84\n"
    "v_dot2c_f32_bf16 v150, v84, v84\n"
    "v_dot2c_f32_bf16 v147, s53, v85\n"
    "v_dot2c_f32_bf16 v151, v85, v85\n"
    "v_dot2c_f32_bf16 v146, s53, v86\n"
    "v_dot2c_f32_bf16 v150, v86, v86\n"
    "v_dot2c_f32_bf16 v147, s53, v87\n"
    "v_dot2c_f32_bf16 v151, v87, v87\n"
    "v_dot2c_f32_bf16 v144, s53, v88\n"
    "v_dot2c_f32_bf16 v148, v88, v88\n"
    "v_dot2c_f32_bf16 v145, s53, v89\n"
    "v_dot2c_f32_bf16 v149, v89, v89\n"
    "v_dot2c_f32_bf16 v144, s53, v90\n"
    "v_dot2c_f32_bf16 v148, v90, v90\n"
    "v_dot2c_f32_bf16 v145, s53, v91\n"
    "v_dot2c_f32_bf16 v149, v91, v91\n"
    "v_dot2c_f32_bf16 v146, s53, v92\n"
    "v_dot2c_f32_bf16 v150, v92, v92\n"
    "v_dot2c_f32_bf16 v147, s53, v93\n"
    "v_dot2c_f32_bf16 v151, v93, v93\n"
    "v_dot2c_f32_bf16 v146, s53, v94\n"
    "v_dot2c_f32_bf16 v150, v94, v94\n"
    "v_dot2c_f32_bf16 v147, s53, v95\n"
    "v_dot2c_f32_bf16 v151, v95, v95\n"
    "s_waitcnt vmcnt(6)\n"
    "ds_read_b128 v[112:115], v251 offset:28672\n"
    "ds_read_b128 v[116:119], v251 offset:30720\n"
    "ds_read_b128 v[120:123], v252 offset:28672\n"
    "ds_read_b128 v[124:127], v252 offset:30720\n"
    "s_waitcnt lgkmcnt(4)\n"
    "v_dot2c_f32_bf16 v144, s53, v96\n"
    "v_dot2c_f32_bf16 v148, v96, v96\n"
    "v_dot2c_f32_bf16 v145, s53, v97\n"
    "v_dot2c_f32_bf16 v149, v97, v97\n"
    "v_dot2c_f32_bf16 v144, s53, v98\n"
    "v_dot2c_f32_bf16 v148, v98, v98\n"
    "v_dot2c_f32_bf16 v145, s53, v99\n"
    "v_dot2c_f32_bf16 v149, v99, v99\n"
    "v_dot2c_f32_bf16 v146, s53, v100\n"
    "v_dot2c_f32_bf16 v150, v100, v100\n"
    "v_dot2c_f32_bf16 v147, s53, v101\n"
    "v_dot2c_f32_bf16 v151, v101, v101\n"
    "v_dot2c_f32_bf16 v146, s53, v102\n"
    "v_dot2c_f32_bf16 v150, v102, v102\n"
    "v_dot2c_f32_bf16 v147, s53, v103\n"
    "v_dot2c_f32_bf16 v151, v103, v103\n"
    "v_dot2c_f32_bf16 v144, s53, v104\n"
    "v_dot2c_f32_bf16 v148, v104, v104\n"
    "v_dot2c_f32_bf16 v145, s53, v105\n"
    "v_dot2c_f32_bf16 v149, v105, v105\n"
    "v_dot2c_f32_bf16 v144, s53, v106\n"
    "v_dot2c_f32_bf16 v148, v106, v106\n"
    "v_dot2c_f32_bf16 v145, s53, v107\n"
    "v_dot2c_f32_bf16 v149, v107, v107\n"
    "v_dot2c_f32_bf16 v146, s53, v108\n"
    "v_dot2c_f32_bf16 v150, v108, v108\n"
    "v_dot2c_f32_bf16 v147, s53, v109\n"
    "v_dot2c_f32_bf16 v151, v109, v109\n"
    "v_dot2c_f32_bf16 v146, s53, v110\n"
    "v_dot2c_f32_bf16 v150, v110, v110\n"
    "v_dot2c_f32_bf16 v147, s53, v111\n"
    "v_dot2c_f32_bf16 v151, v111, v111\n"
    "s_waitcnt vmcnt(2)\n"
    "ds_read_b128 v[128:131], v251 offset:32768\n"
    "ds_read_b128 v[132:135], v251 offset:34816\n"
    "ds_read_b128 v[136:139], v252 offset:32768\n"
    "ds_read_b128 v[140:143], v252 offset:34816\n"
    "s_waitcnt lgkmcnt(4)\n"
    "v_dot2c_f32_bf16 v144, s53, v112\n"
    "v_dot2c_f32_bf16 v148, v112, v112\n"
    "v_dot2c_f32_bf16 v145, s53, v113\n"
    "v_dot2c_f32_bf16 v149, v113, v113\n"
    "v_dot2c_f32_bf16 v144, s53, v114\n"
    "v_dot2c_f32_bf16 v148, v114, v114\n"
    "v_dot2c_f32_bf16 v145, s53, v115\n"
    "v_dot2c_f32_bf16 v149, v115, v115\n"
    "v_dot2c_f32_bf16 v146, s53, v116\n"
    "v_dot2c_f32_bf16 v150, v116, v116\n"
    "v_dot2c_f32_bf16 v147, s53, v117\n"
    "v_dot2c_f32_bf16 v151, v117, v117\n"
    "v_dot2c_f32_bf16 v146, s53, v118\n"
    "v_dot2c_f32_bf16 v150, v118, v118\n"
    "v_dot2c_f32_bf16 v147, s53, v119\n"
    "v_dot2c_f32_bf16 v151, v119, v119\n"
    "v_dot2c_f32_bf16 v144, s53, v120\n"
    "v_dot2c_f32_bf16 v148, v120, v120\n"
    "v_dot2c_f32_bf16 v145, s53, v121\n"
    "v_dot2c_f32_bf16 v149, v121, v121\n"
    "v_dot2c_f32_bf16 v144, s53, v122\n"
    "v_dot2c_f32_bf16 v148, v122, v122\n"
    "v_dot2c_f32_bf16 v145, s53, v123\n"
    "v_dot2c_f32_bf16 v149, v123, v123\n"
    "v_dot2c_f32_bf16 v146, s53, v124\n"
    "v_dot2c_f32_bf16 v150, v124, v124\n"
    "v_dot2c_f32_bf16 v147, s53, v125\n"
    "v_dot2c_f32_bf16 v151, v125, v125\n"
    "v_dot2c_f32_bf16 v146, s53, v126\n"
    "v_dot2c_f32_bf16 v150, v126, v126\n"
    "v_dot2c_f32_bf16 v147, s53, v127\n"
    "v_dot2c_f32_bf16 v151, v127, v127\n"
    "s_waitcnt lgkmcnt(0)\n"
    "s_add_u32 m0, %8, 147456\n"
    "s_nop 0\n"
    "global_load_lds_dword v[206:207], off\n"
    "v_add_co_u32 v206, vcc, v205, v206\n"
    "s_nop 1\n"
    "v_addc_co_u32 v207, vcc, 0, v207, vcc\n"
    "v_readfirstlane_b32 s78, v244\n"
    "v_readfirstlane_b32 s62, v245\n"
    "s_nop 1\n"
    "s_add_u32 s78, s78, 1\n"
    "s_add_u32 s62, s62, 1\n"
    "s_barrier\n"
    "s_cmp_lg_u32 %7, 0\n"
    "s_cbranch_scc1 29f\n"
    "s_lshl_b32 s67, %13, 2\n"
    "v_mov_b32 v201, s62\n"
    "v_mov_b32 v200, s67\n"
    "s_mov_b64 exec, 1\n"
    "global_store_dword v200, v201, s[32:33] offset:192 sc1\n"
    "s_mov_b64 exec, -1\n"
    "29:\n"
    "v_dot2c_f32_bf16 v144, s53, v128\n"
    "v_dot2c_f32_bf16 v148, v128, v128\n"
    "v_dot2c_f32_bf16 v145, s53, v129\n"
    "v_dot2c_f32_bf16 v149, v129, v129\n"
    "v_dot2c_f32_bf16 v144, s53, v130\n"
    "v_dot2c_f32_bf16 v148, v130, v130\n"
    "v_dot2c_f32_bf16 v145, s53, v131\n"
    "v_dot2c_f32_bf16 v149, v131, v131\n"
    "v_dot2c_f32_bf16 v146, s53, v132\n"
    "v_dot2c_f32_bf16 v150, v132, v132\n"
    "v_dot2c_f32_bf16 v147, s53, v133\n"
    "v_dot2c_f32_bf16 v151, v133, v133\n"
    "v_dot2c_f32_bf16 v146, s53, v134\n"
    "v_dot2c_f32_bf16 v150, v134, v134\n"
    "v_dot2c_f32_bf16 v147, s53, v135\n"
    "v_dot2c_f32_bf16 v151, v135, v135\n"
    "v_dot2c_f32_bf16 v144, s53, v136\n"
    "v_dot2c_f32_bf16 v148, v136, v136\n"
    "v_dot2c_f32_bf16 v145, s53, v137\n"
    "v_dot2c_f32_bf16 v149, v137, v137\n"
    "v_dot2c_f32_bf16 v144, s53, v138\n"
    "v_dot2c_f32_bf16 v148, v138, v138\n"
    "v_dot2c_f32_bf16 v145, s53, v139\n"
    "v_dot2c_f32_bf16 v149, v139, v139\n"
    "v_dot2c_f32_bf16 v146, s53, v140\n"
    "v_dot2c_f32_bf16 v150, v140, v140\n"
    "v_dot2c_f32_bf16 v147, s53, v141\n"
    "v_dot2c_f32_bf16 v151, v141, v141\n"
    "v_dot2c_f32_bf16 v146, s53, v142\n"
    "v_dot2c_f32_bf16 v150, v142, v142\n"
    "v_dot2c_f32_bf16 v147, s53, v143\n"
    "v_dot2c_f32_bf16 v151, v143, v143\n"
    "s_mul_i32 s52, %7, 9216\n"
    "s_add_u32 s53, s52, 128\n"
    "s_add_u32 s58, s53, 128\n"
    "s_add_u32 s59, s58, 128\n"
    "s_add_u32 s63, s59, 128\n"
    "s_add_u32 s64, s63, 128\n"
    "s_add_u32 s65, s64, 128\n"
    "s_add_u32 s77, s65, 128\n"
    "s_add_u32 s79, s77, 128\n"
    "s_nop 2\n"
    "v_add_f32 v144, v144, v145\n"
    "v_add_f32 v146, v146, v147\n"
    "v_add_f32 v148, v148, v149\n"
    "v_add_f32 v150, v150, v151\n"
    "v_mov_b32 v145, v144\n"
    "v_mov_b32 v147, v146\n"
    "v_mov_b32 v149, v148\n"
    "v_mov_b32 v151, v150\n"
    "s_nop 1\n"
    "v_permlane16_swap_b32 v144, v145\n"
    "v_permlane16_swap_b32 v146, v147\n"
    "v_permlane16_swap_b32 v148, v149\n"
    "v_permlane16_swap_b32 v150, v151\n"
    "s_nop 1\n"
    "v_add_f32 v144, v144, v145\n"
    "v_add_f32 v146, v146, v147\n"
    "v_add_f32 v148, v148, v149\n"
    "v_add_f32 v150, v150, v151\n"
    "v_mov_b32 v145, v144\n"
    "v_mov_b32 v147, v146\n"
    "v_mov_b32 v149, v148\n"
    "v_mov_b32 v151, v150\n"
    "s_nop 1\n"
    "v_permlane32_swap_b32 v144, v145\n"
    "v_permlane32_swap_b32 v146, v147\n"
    "v_permlane32_swap_b32 v148, v149\n"
    "v_permlane32_swap_b32 v150, v151\n"
    "s_nop 1\n"
    "v_add_f32 v144, v144, v145\n"
    "v_add_f32 v146, v146, v147\n"
    "v_add_f32 v148, v148, v149\n"
    "v_add_f32 v150, v150, v151\n"
    "v_mul_f32 v144, 0x3ae38e39, v144\n"
    "v_mul_f32 v146, 0x3ae38e39, v146\n"
    "v_mul_f32 v148, 0x3ae38e39, v148\n"
    "v_mul_f32 v150, 0x3ae38e39, v150\n"
    "v_fma_f32 v148, -v144, v144, v148\n"
    "v_max_f32 v148, 0, v148\n"
    "v_add_f32 v148, 0x3727c5ac, v148\n"
    "v_rsq_f32 v145, v148\n"
    "v_sqrt_f32 v148, v148\n"
    "v_mov_b32 v149, 0\n"
    "v_mul_f32 v145, 0xbfb8aa3b, v145\n"
    "v_cvt_pk_bf16_f32 v152, v145, v149\n"
    "v_lshlrev_b32 v153, 16, v152\n"
    "v_mul_f32_e64 v154, -v144, v153\n"
    "v_mul_f32 v248, v148, v153\n"
    "v_fma_f32 v150, -v146, v146, v150\n"
    "v_max_f32 v150, 0, v150\n"
    "v_add_f32 v150, 0x3727c5ac, v150\n"
    "v_rsq_f32 v147, v150\n"
    "v_sqrt_f32 v150, v150\n"
    "v_mov_b32 v151, 0\n"
    "v_mul_f32 v147, 0xbfb8aa3b, v147\n"
    "v_cvt_pk_bf16_f32 v155, v147, v151\n"
    "v_lshlrev_b32 v156, 16, v155\n"
    "v_mul_f32_e64 v157, -v146, v156\n"
    "v_mul_f32 v249, v150, v156\n"
    "v_dot2_f32_bf16 v158, v0, v152, v154\n"
    "v_dot2_f32_bf16 v159, v0, v153, v154\n"
    "v_dot2_f32_bf16 v160, v1, v152, v154\n"
    "v_dot2_f32_bf16 v161, v1, v153, v154\n"
    "v_dot2_f32_bf16 v162, v2, v152, v154\n"
    "v_dot2_f32_bf16 v163, v2, v153, v154\n"
    "v_cvt_pk_bf16_f32 v0, v158, v159\n"
    "v_dot2_f32_bf16 v164, v3, v152, v154\n"
    "v_dot2_f32_bf16 v165, v3, v153, v154\n"
    "v_cvt_pk_bf16_f32 v1, v160, v161\n"
    "v_dot2_f32_bf16 v158, v4, v155, v157\n"
    "v_dot2_f32_bf16 v159, v4, v156, v157\n"
    "v_cvt_pk_bf16_f32 v2, v162, v163\n"
    "v_dot2_f32_bf16 v160, v5, v155, v157\n"
    "v_dot2_f32_bf16 v161, v5, v156, v157\n"
    "v_cvt_pk_bf16_f32 v3, v164, v165\n"
    "v_dot2_f32_bf16 v162, v6, v155, v157\n"
    "v_dot2_f32_bf16 v163, v6, v156, v157\n"
    "v_cvt_pk_bf16_f32 v4, v158, v159\n"
    "v_dot2_f32_bf16 v164, v7, v155, v157\n"
    "v_dot2_f32_bf16 v165, v7, v156, v157\n"
    "v_cvt_pk_bf16_f32 v5, v160, v161\n"
    "v_dot2_f32_bf16 v158, v8, v152, v154\n"
    "v_dot2_f32_bf16 v159, v8, v153, v154\n"
    "v_cvt_pk_bf16_f32 v6, v162, v163\n"
    "v_dot2_f32_bf16 v160, v9, v152, v154\n"
    "v_dot2_f32_bf16 v161, v9, v153, v154\n"
    "v_cvt_pk_bf16_f32 v7, v164, v165\n"
    "v_dot2_f32_bf16 v162, v10, v152, v154\n"
    "v_dot2_f32_bf16 v163, v10, v153, v154\n"
    "v_cvt_pk_bf16_f32 v8, v158, v159\n"
    "v_dot2_f32_bf16 v164, v11, v152, v154\n"
    "v_dot2_f32_bf16 v165, v11, v153, v154\n"
    "v_cvt_pk_bf16_f32 v9, v160, v161\n"
    "v_dot2_f32_bf16 v158, v12, v155, v157\n"
    "v_dot2_f32_bf16 v159, v12, v156, v157\n"
    "v_cvt_pk_bf16_f32 v10, v162, v163\n"
    "v_dot2_f32_bf16 v160, v13, v155, v157\n"
    "v_dot2_f32_bf16 v161, v13, v156, v157\n"
    "v_cvt_pk_bf16_f32 v11, v164, v165\n"
    "v_dot2_f32_bf16 v162, v14, v155, v157\n"
    "v_dot2_f32_bf16 v163, v14, v156, v157\n"
    "v_cvt_pk_bf16_f32 v12, v158, v159\n"
    "v_dot2_f32_bf16 v164, v15, v155, v157\n"
    "v_dot2_f32_bf16 v165, v15, v156, v157\n"
    "v_cvt_pk_bf16_f32 v13, v160, v161\n"
    "v_dot2_f32_bf16 v158, v16, v152, v154\n"
    "v_dot2_f32_bf16 v159, v16, v153, v154\n"
    "v_cvt_pk_bf16_f32 v14, v162, v163\n"
    "v_dot2_f32_bf16 v160, v17, v152, v154\n"
    "v_dot2_f32_bf16 v161, v17, v153, v154\n"
    "v_cvt_pk_bf16_f32 v15, v164, v165\n"
    "v_dot2_f32_bf16 v162, v18, v152, v154\n"
    "v_dot2_f32_bf16 v163, v18, v153, v154\n"
    "v_cvt_pk_bf16_f32 v16, v158, v159\n"
    "v_dot2_f32_bf16 v164, v19, v152, v154\n"
    "v_dot2_f32_bf16 v165, v19, v153, v154\n"
    "v_cvt_pk_bf16_f32 v17, v160, v161\n"
    "v_dot2_f32_bf16 v158, v20, v155, v157\n"
    "v_dot2_f32_bf16 v159, v20, v156, v157\n"
    "v_cvt_pk_bf16_f32 v18, v162, v163\n"
    "v_dot2_f32_bf16 v160, v21, v155, v157\n"
    "v_dot2_f32_bf16 v161, v21, v156, v157\n"
    "v_cvt_pk_bf16_f32 v19, v164, v165\n"
    "v_dot2_f32_bf16 v162, v22, v155, v157\n"
    "v_dot2_f32_bf16 v163, v22, v156, v157\n"
    "v_cvt_pk_bf16_f32 v20, v158, v159\n"
    "v_dot2_f32_bf16 v164, v23, v155, v157\n"
    "v_dot2_f32_bf16 v165, v23, v156, v157\n"
    "v_cvt_pk_bf16_f32 v21, v160, v161\n"
    "v_dot2_f32_bf16 v158, v24, v152, v154\n"
    "v_dot2_f32_bf16 v159, v24, v153, v154\n"
    "v_cvt_pk_bf16_f32 v22, v162, v163\n"
    "v_dot2_f32_bf16 v160, v25, v152, v154\n"
    "v_dot2_f32_bf16 v161, v25, v153, v154\n"
    "v_cvt_pk_bf16_f32 v23, v164, v165\n"
    "v_dot2_f32_bf16 v162, v26, v152, v154\n"
    "v_dot2_f32_bf16 v163, v26, v153, v154\n"
    "v_cvt_pk_bf16_f32 v24, v158, v159\n"
    "v_dot2_f32_bf16 v164, v27, v152, v154\n"
    "v_dot2_f32_bf16 v165, v27, v153, v154\n"
    "v_cvt_pk_bf16_f32 v25, v160, v161\n"
    "v_dot2_f32_bf16 v158, v28, v155, v157\n"
    "v_dot2_f32_bf16 v159, v28, v156, v157\n"
    "v_cvt_pk_bf16_f32 v26, v162, v163\n"
    "v_dot2_f32_bf16 v160, v29, v155, v157\n"
    "v_dot2_f32_bf16 v161, v29, v156, v157\n"
    "v_cvt_pk_bf16_f32 v27, v164, v165\n"
    "v_dot2_f32_bf16 v162, v30, v155, v157\n"
    "v_dot2_f32_bf16 v163, v30, v156, v157\n"
    "v_cvt_pk_bf16_f32 v28, v158, v159\n"
    "v_dot2_f32_bf16 v164, v31, v155, v157\n"
    "v_dot2_f32_bf16 v165, v31, v156, v157\n"
    "v_cvt_pk_bf16_f32 v29, v160, v161\n"
    "v_dot2_f32_bf16 v158, v32, v152, v154\n"
    "v_dot2_f32_bf16 v159, v32, v153, v154\n"
    "v_cvt_pk_bf16_f32 v30, v162, v163\n"
    "v_dot2_f32_bf16 v160, v33, v152, v154\n"
    "v_dot2_f32_bf16 v161, v33, v153, v154\n"
    "v_cvt_pk_bf16_f32 v31, v164, v165\n"
    "v_dot2_f32_bf16 v162, v34, v152, v154\n"
    "v_dot2_f32_bf16 v163, v34, v153, v154\n"
    "v_cvt_pk_bf16_f32 v32, v158, v159\n"
    "v_dot2_f32_bf16 v164, v35, v152, v154\n"
    "v_dot2_f32_bf16 v165, v35, v153, v154\n"
    "v_cvt_pk_bf16_f32 v33, v160, v161\n"
    "s_add_u32 m0, s56, s60\n"
    "v_dot2_f32_bf16 v158, v36, v155, v157\n"
    "buffer_load_dwordx4 v246, s[40:43], s68 offen lds\n"
    "v_dot2_f32_bf16 v159, v36, v156, v157\n"
    "v_cvt_pk_bf16_f32 v34, v162, v163\n"
    "v_dot2_f32_bf16 v160, v37, v155, v157\n"
    "v_dot2_f32_bf16 v161, v37, v156, v157\n"
    "v_cvt_pk_bf16_f32 v35, v164, v165\n"
    "v_dot2_f32_bf16 v162, v38, v155, v157\n"
    "v_dot2_f32_bf16 v163, v38, v156, v157\n"
    "v_cvt_pk_bf16_f32 v36, v158, v159\n"
    "v_dot2_f32_bf16 v164, v39, v155, v157\n"
    "v_dot2_f32_bf16 v165, v39, v156, v157\n"
    "v_cvt_pk_bf16_f32 v37, v160, v161\n"
    "s_add_u32 m0, m0, 0x400\n"
    "v_dot2_f32_bf16 v158, v40, v152, v154\n"
    "buffer_load_dwordx4 v246, s[40:43], s69 offen lds\n"
    "v_dot2_f32_bf16 v159, v40, v153, v154\n"
    "v_cvt_pk_bf16_f32 v38, v162, v163\n"
    "v_dot2_f32_bf16 v160, v41, v152, v154\n"
    "v_dot2_f32_bf16 v161, v41, v153, v154\n"
    "v_cvt_pk_bf16_f32 v39, v164, v165\n"
    "v_dot2_f32_bf16 v162, v42, v152, v154\n"
    "v_dot2_f32_bf16 v163, v42, v153, v154\n"
    "v_cvt_pk_bf16_f32 v40, v158, v159\n"
    "v_dot2_f32_bf16 v164, v43, v152, v154\n"
    "v_dot2_f32_bf16 v165, v43, v153, v154\n"
    "v_cvt_pk_bf16_f32 v41, v160, v161\n"
    "s_add_u32 m0, m0, 0x400\n"
    "v_dot2_f32_bf16 v158, v44, v155, v157\n"
    "buffer_load_dwordx4 v246, s[40:43], s70 offen lds\n"
    "v_dot2_f32_bf16 v159, v44, v156, v157\n"
    "v_cvt_pk_bf16_f32 v42, v162, v163\n"
    "v_dot2_f32_bf16 v160, v45, v155, v157\n"
    "v_dot2_f32_bf16 v161, v45, v156, v157\n"
    "v_cvt_pk_bf16_f32 v43, v164, v165\n"
    "v_dot2_f32_bf16 v162, v46, v155, v157\n"
    "v_dot2_f32_bf16 v163, v46, v156, v157\n"
    "v_cvt_pk_bf16_f32 v44, v158, v159\n"
    "v_dot2_f32_bf16 v164, v47, v155, v157\n"
    "v_dot2_f32_bf16 v165, v47, v156, v157\n"
    "v_cvt_pk_bf16_f32 v45, v160, v161\n"
    "s_add_u32 m0, m0, 0x400\n"
    "v_dot2_f32_bf16 v158, v48, v152, v154\n"
    "buffer_load_dwordx4 v246, s[40:43], s71 offen lds\n"
    "v_dot2_f32_bf16 v159, v48, v153, v154\n"
    "v_cvt_pk_bf16_f32 v46, v162, v163\n"
    "v_dot2_f32_bf16 v160, v49, v152, v154\n"
    "v_dot2_f32_bf16 v161, v49, v153, v154\n"
    "v_cvt_pk_bf16_f32 v47, v164, v165\n"
    "v_dot2_f32_bf16 v162, v50, v152, v154\n"
    "v_dot2_f32_bf16 v163, v50, v153, v154\n"
    "v_cvt_pk_bf16_f32 v48, v158, v159\n"
    "v_dot2_f32_bf16 v164, v51, v152, v154\n"
    "v_dot2_f32_bf16 v165, v51, v153, v154\n"
    "v_cvt_pk_bf16_f32 v49, v160, v161\n"
    "s_add_u32 m0, m0, 0x400\n"
    "v_dot2_f32_bf16 v158, v52, v155, v157\n"
    "buffer_load_dwordx4 v246, s[40:43], s72 offen lds\n"
    "v_dot2_f32_bf16 v159, v52, v156, v157\n"
    "v_cvt_pk_bf16_f32 v50, v162, v163\n"
    "v_dot2_f32_bf16 v160, v53, v155, v157\n"
    "v_dot2_f32_bf16 v161, v53, v156, v157\n"
    "v_cvt_pk_bf16_f32 v51, v164, v165\n"
    "v_dot2_f32_bf16 v162, v54, v155, v157\n"
    "v_dot2_f32_bf16 v163, v54, v156, v157\n"
    "v_cvt_pk_bf16_f32 v52, v158, v159\n"
    "v_dot2_f32_bf16 v164, v55, v155, v157\n"
    "v_dot2_f32_bf16 v165, v55, v156, v157\n"
    "v_cvt_pk_bf16_f32 v53, v160, v161\n"
    "s_add_u32 m0, m0, 0x400\n"
    "v_dot2_f32_bf16 v158, v56, v152, v154\n"
    "buffer_load_dwordx4 v246, s[40:43], s73 offen lds\n"
    "v_dot2_f32_bf16 v159, v56, v153, v154\n"
    "v_cvt_pk_bf16_f32 v54, v162, v163\n"
    "v_dot2_f32_bf16 v160, v57, v152, v154\n"
    "v_dot2_f32_bf16 v161, v57, v153, v154\n"
    "v_cvt_pk_bf16_f32 v55, v164, v165\n"
    "v_dot2_f32_bf16 v162, v58, v152, v154\n"
    "v_dot2_f32_bf16 v163, v58, v153, v154\n"
    "v_cvt_pk_bf16_f32 v56, v158, v159\n"
    "v_dot2_f32_bf16 v164, v59, v152, v154\n"
    "v_dot2_f32_bf16 v165, v59, v153, v154\n"
    "v_cvt_pk_bf16_f32 v57, v160, v161\n"
    "s_add_u32 m0, m0, 0x400\n"
    "v_dot2_f32_bf16 v158, v60, v155, v157\n"
    "buffer_load_dwordx4 v246, s[40:43], s74 offen lds\n"
    "v_dot2_f32_bf16 v159, v60, v156, v157\n"
    "v_cvt_pk_bf16_f32 v58, v162, v163\n"
    "v_dot2_f32_bf16 v160, v61, v155, v157\n"
    "v_dot2_f32_bf16 v161, v61, v156, v157\n"
    "v_cvt_pk_bf16_f32 v59, v164, v165\n"
    "v_dot2_f32_bf16 v162, v62, v155, v157\n"
    "v_dot2_f32_bf16 v163, v62, v156, v157\n"
    "v_cvt_pk_bf16_f32 v60, v158, v159\n"
    "v_dot2_f32_bf16 v164, v63, v155, v157\n"
    "v_dot2_f32_bf16 v165, v63, v156, v157\n"
    "v_cvt_pk_bf16_f32 v61, v160, v161\n"
    "s_add_u32 m0, m0, 0x400\n"
    "v_dot2_f32_bf16 v158, v64, v152, v154\n"
    "buffer_load_dwordx4 v246, s[40:43], s75 offen lds\n"
    "v_dot2_f32_bf16 v159, v64, v153, v154\n"
    "v_cvt_pk_bf16_f32 v62, v162, v163\n"
    "v_dot2_f32_bf16 v160, v65, v152, v154\n"
    "v_dot2_f32_bf16 v161, v65, v153, v154\n"
    "v_cvt_pk_bf16_f32 v63, v164, v165\n"
    "v_dot2_f32_bf16 v162, v66, v152, v154\n"
    "v_dot2_f32_bf16 v163, v66, v153, v154\n"
    "v_cvt_pk_bf16_f32 v64, v158, v159\n"
    "v_dot2_f32_bf16 v164, v67, v152, v154\n"
    "v_dot2_f32_bf16 v165, v67, v153, v154\n"
    "v_cvt_pk_bf16_f32 v65, v160, v161\n"
    "s_add_u32 m0, m0, 0x400\n"
    "v_dot2_f32_bf16 v158, v68, v155, v157\n"
    "buffer_load_dwordx4 v246, s[40:43], s76 offen lds\n"
    "v_dot2_f32_bf16 v159, v68, v156, v157\n"
    "v_cvt_pk_bf16_f32 v66, v162, v163\n"
    "v_dot2_f32_bf16 v160, v69, v155, v157\n"
    "v_dot2_f32_bf16 v161, v69, v156, v157\n"
    "v_cvt_pk_bf16_f32 v67, v164, v165\n"
    "v_dot2_f32_bf16 v162, v70, v155, v157\n"
    "v_dot2_f32_bf16 v163, v70, v156, v157\n"
    "v_cvt_pk_bf16_f32 v68, v158, v159\n"
    "v_dot2_f32_bf16 v164, v71, v155, v157\n"
    "v_dot2_f32_bf16 v165, v71, v156, v157\n"
    "v_cvt_pk_bf16_f32 v69, v160, v161\n"
    "s_add_u32 m0, s57, s60\n"
    "v_dot2_f32_bf16 v158, v72, v152, v154\n"
    "buffer_load_dwordx4 v246, s[44:47], s68 offen lds\n"
    "v_dot2_f32_bf16 v159, v72, v153, v154\n"
    "v_cvt_pk_bf16_f32 v70, v162, v163\n"
    "v_dot2_f32_bf16 v160, v73, v152, v154\n"
    "v_dot2_f32_bf16 v161, v73, v153, v154\n"
    "v_cvt_pk_bf16_f32 v71, v164, v165\n"
    "v_dot2_f32_bf16 v162, v74, v152, v154\n"
    "v_dot2_f32_bf16 v163, v74, v153, v154\n"
    "v_cvt_pk_bf16_f32 v72, v158, v159\n"
    "v_dot2_f32_bf16 v164, v75, v152, v154\n"
    "v_dot2_f32_bf16 v165, v75, v153, v154\n"
    "v_cvt_pk_bf16_f32 v73, v160, v161\n"
    "s_add_u32 m0, m0, 0x400\n"
    "v_dot2_f32_bf16 v158, v76, v155, v157\n"
    "buffer_load_dwordx4 v246, s[44:47], s69 offen lds\n"
    "v_dot2_f32_bf16 v159, v76, v156, v157\n"
    "v_cvt_pk_bf16_f32 v74, v162, v163\n"
    "v_dot2_f32_bf16 v160, v77, v155, v157\n"
    "v_dot2_f32_bf16 v161, v77, v156, v157\n"
    "v_cvt_pk_bf16_f32 v75, v164, v165\n"
    "v_dot2_f32_bf16 v162, v78, v155, v157\n"
    "v_dot2_f32_bf16 v163, v78, v156, v157\n"
    "v_cvt_pk_bf16_f32 v76, v158, v159\n"
    "v_dot2_f32_bf16 v164, v79, v155, v157\n"
    "v_dot2_f32_bf16 v165, v79, v156, v157\n"
    "v_cvt_pk_bf16_f32 v77, v160, v161\n"
    "s_add_u32 m0, m0, 0x400\n"
    "v_dot2_f32_bf16 v158, v80, v152, v154\n"
    "buffer_load_dwordx4 v246, s[44:47], s70 offen lds\n"
    "v_dot2_f32_bf16 v159, v80, v153, v154\n"
    "v_cvt_pk_bf16_f32 v78, v162, v163\n"
    "v_dot2_f32_bf16 v160, v81, v152, v154\n"
    "v_dot2_f32_bf16 v161, v81, v153, v154\n"
    "v_cvt_pk_bf16_f32 v79, v164, v165\n"
    "v_dot2_f32_bf16 v162, v82, v152, v154\n"
    "v_dot2_f32_bf16 v163, v82, v153, v154\n"
    "v_cvt_pk_bf16_f32 v80, v158, v159\n"
    "v_dot2_f32_bf16 v164, v83, v152, v154\n"
    "v_dot2_f32_bf16 v165, v83, v153, v154\n"
    "v_cvt_pk_bf16_f32 v81, v160, v161\n"
    "s_add_u32 m0, m0, 0x400\n"
    "v_dot2_f32_bf16 v158, v84, v155, v157\n"
    "buffer_load_dwordx4 v246, s[44:47], s71 offen lds\n"
    "v_dot2_f32_bf16 v159, v84, v156, v157\n"
    "v_cvt_pk_bf16_f32 v82, v162, v163\n"
    "v_dot2_f32_bf16 v160, v85, v155, v157\n"
    "v_dot2_f32_bf16 v161, v85, v156, v157\n"
    "v_cvt_pk_bf16_f32 v83, v164, v165\n"
    "v_dot2_f32_bf16 v162, v86, v155, v157\n"
    "v_dot2_f32_bf16 v163, v86, v156, v157\n"
    "v_cvt_pk_bf16_f32 v84, v158, v159\n"
    "v_dot2_f32_bf16 v164, v87, v155, v157\n"
    "v_dot2_f32_bf16 v165, v87, v156, v157\n"
    "v_cvt_pk_bf16_f32 v85, v160, v161\n"
    "s_add_u32 m0, m0, 0x400\n"
    "v_dot2_f32_bf16 v158, v88, v152, v154\n"
    "buffer_load_dwordx4 v246, s[44:47], s72 offen lds\n"
    "v_dot2_f32_bf16 v159, v88, v153, v154\n"
    "v_cvt_pk_bf16_f32 v86, v162, v163\n"
    "v_dot2_f32_bf16 v160, v89, v152, v154\n"
    "v_dot2_f32_bf16 v161, v89, v153, v154\n"
    "v_cvt_pk_bf16_f32 v87, v164, v165\n"
    "v_dot2_f32_bf16 v162, v90, v152, v154\n"
    "v_dot2_f32_bf16 v163, v90, v153, v154\n"
    "v_cvt_pk_bf16_f32 v88, v158, v159\n"
    "v_dot2_f32_bf16 v164, v91, v152, v154\n"
    "v_dot2_f32_bf16 v165, v91, v153, v154\n"
    "v_cvt_pk_bf16_f32 v89, v160, v161\n"
    "s_add_u32 m0, m0, 0x400\n"
    "v_dot2_f32_bf16 v158, v92, v155, v157\n"
    "buffer_load_dwordx4 v246, s[44:47], s73 offen lds\n"
    "v_dot2_f32_bf16 v159, v92, v156, v157\n"
    "v_cvt_pk_bf16_f32 v90, v162, v163\n"
    "v_dot2_f32_bf16 v160, v93, v155, v157\n"
    "v_dot2_f32_bf16 v161, v93, v156, v157\n"
    "v_cvt_pk_bf16_f32 v91, v164, v165\n"
    "v_dot2_f32_bf16 v162, v94, v155, v157\n"
    "v_dot2_f32_bf16 v163, v94, v156, v157\n"
    "v_cvt_pk_bf16_f32 v92, v158, v159\n"
    "v_dot2_f32_bf16 v164, v95, v155, v157\n"
    "v_dot2_f32_bf16 v165, v95, v156, v157\n"
    "v_cvt_pk_bf16_f32 v93, v160, v161\n"
    "s_add_u32 m0, m0, 0x400\n"
    "v_dot2_f32_bf16 v158, v96, v152, v154\n"
    "buffer_load_dwordx4 v246, s[44:47], s74 offen lds\n"
    "v_dot2_f32_bf16 v159, v96, v153, v154\n"
    "v_cvt_pk_bf16_f32 v94, v162, v163\n"
    "v_dot2_f32_bf16 v160, v97, v152, v154\n"
    "v_dot2_f32_bf16 v161, v97, v153, v154\n"
    "v_cvt_pk_bf16_f32 v95, v164, v165\n"
    "v_dot2_f32_bf16 v162, v98, v152, v154\n"
    "v_dot2_f32_bf16 v163, v98, v153, v154\n"
    "v_cvt_pk_bf16_f32 v96, v158, v159\n"
    "v_dot2_f32_bf16 v164, v99, v152, v154\n"
    "v_dot2_f32_bf16 v165, v99, v153, v154\n"
    "v_cvt_pk_bf16_f32 v97, v160, v161\n"
    "s_add_u32 m0, m0, 0x400\n"
    "v_dot2_f32_bf16 v158, v100, v155, v157\n"
    "buffer_load_dwordx4 v246, s[44:47], s75 offen lds\n"
    "v_dot2_f32_bf16 v159, v100, v156, v157\n"
    "v_cvt_pk_bf16_f32 v98, v162, v163\n"
    "v_dot2_f32_bf16 v160, v101, v155, v157\n"
    "v_dot2_f32_bf16 v161, v101, v156, v157\n"
    "v_cvt_pk_bf16_f32 v99, v164, v165\n"
    "v_dot2_f32_bf16 v162, v102, v155, v157\n"
    "v_dot2_f32_bf16 v163, v102, v156, v157\n"
    "v_cvt_pk_bf16_f32 v100, v158, v159\n"
    "v_dot2_f32_bf16 v164, v103, v155, v157\n"
    "v_dot2_f32_bf16 v165, v103, v156, v157\n"
    "v_cvt_pk_bf16_f32 v101, v160, v161\n"
    "s_add_u32 m0, m0, 0x400\n"
    "v_dot2_f32_bf16 v158, v104, v152, v154\n"
    "buffer_load_dwordx4 v246, s[44:47], s76 offen lds\n"
    "v_dot2_f32_bf16 v159, v104, v153, v154\n"
    "v_cvt_pk_bf16_f32 v102, v162, v163\n"
    "v_dot2_f32_bf16 v160, v105, v152, v154\n"
    "v_dot2_f32_bf16 v161, v105, v153, v154\n"
    "v_cvt_pk_bf16_f32 v103, v164, v165\n"
    "v_dot2_f32_bf16 v162, v106, v152, v154\n"
    "v_dot2_f32_bf16 v163, v106, v153, v154\n"
    "v_cvt_pk_bf16_f32 v104, v158, v159\n"
    "v_dot2_f32_bf16 v164, v107, v152, v154\n"
    "v_dot2_f32_bf16 v165, v107, v153, v154\n"
    "v_cvt_pk_bf16_f32 v105, v160, v161\n"
    "v_dot2_f32_bf16 v158, v108, v155, v157\n"
    "v_dot2_f32_bf16 v159, v108, v156, v157\n"
    "v_cvt_pk_bf16_f32 v106, v162, v163\n"
    "v_dot2_f32_bf16 v160, v109, v155, v157\n"
    "v_dot2_f32_bf16 v161, v109, v156, v157\n"
    "v_cvt_pk_bf16_f32 v107, v164, v165\n"
    "v_dot2_f32_bf16 v162, v110, v155, v157\n"
    "v_dot2_f32_bf16 v163, v110, v156, v157\n"
    "v_cvt_pk_bf16_f32 v108, v158, v159\n"
    "v_dot2_f32_bf16 v164, v111, v155, v157\n"
    "v_dot2_f32_bf16 v165, v111, v156, v157\n"
    "v_cvt_pk_bf16_f32 v109, v160, v161\n"
    "v_dot2_f32_bf16 v158, v112, v152, v154\n"
    "v_dot2_f32_bf16 v159, v112, v153, v154\n"
    "v_cvt_pk_bf16_f32 v110, v162, v163\n"
    "v_dot2_f32_bf16 v160, v113, v152, v154\n"
    "v_dot2_f32_bf16 v161, v113, v153, v154\n"
    "v_cvt_pk_bf16_f32 v111, v164, v165\n"
    "v_dot2_f32_bf16 v162, v114, v152, v154\n"
    "v_dot2_f32_bf16 v163, v114, v153, v154\n"
    "v_cvt_pk_bf16_f32 v112, v158, v159\n"
    "v_dot2_f32_bf16 v164, v115, v152, v154\n"
    "v_dot2_f32_bf16 v165, v115, v153, v154\n"
    "v_cvt_pk_bf16_f32 v113, v160, v161\n"
    "v_dot2_f32_bf16 v158, v116, v155, v157\n"
    "v_dot2_f32_bf16 v159, v116, v156, v157\n"
    "v_cvt_pk_bf16_f32 v114, v162, v163\n"
    "v_dot2_f32_bf16 v160, v117, v155, v157\n"
    "v_dot2_f32_bf16 v161, v117, v156, v157\n"
    "v_cvt_pk_bf16_f32 v115, v164, v165\n"
    "v_dot2_f32_bf16 v162, v118, v155, v157\n"
    "v_dot2_f32_bf16 v163, v118, v156, v157\n"
    "v_cvt_pk_bf16_f32 v116, v158, v159\n"
    "v_dot2_f32_bf16 v164, v119, v155, v157\n"
    "v_dot2_f32_bf16 v165, v119, v156, v157\n"
    "v_cvt_pk_bf16_f32 v117, v160, v161\n"
    "s_add_u32 m0, %8, 147456\n"
    "s_nop 0\n"
    "global_load_lds_dword v[206:207], off\n"
    "v_add_co_u32 v206, vcc, v205, v206\n"
    "s_nop 1\n"
    "v_addc_co_u32 v207, vcc, 0, v207, vcc\n"
    "v_mov_b32 v252, v206\n"
    "v_mov_b32 v253, v207\n"
    "v_dot2_f32_bf16 v158, v120, v152, v154\n"
    "v_dot2_f32_bf16 v159, v120, v153, v154\n"
    "v_cvt_pk_bf16_f32 v118, v162, v163\n"
    "v_dot2_f32_bf16 v160, v121, v152, v154\n"
    "v_dot2_f32_bf16 v161, v121, v153, v154\n"
    "v_cvt_pk_bf16_f32 v119, v164, v165\n"
    "v_dot2_f32_bf16 v162, v122, v152, v154\n"
    "v_dot2_f32_bf16 v163, v122, v153, v154\n"
    "v_cvt_pk_bf16_f32 v120, v158, v159\n"
    "v_dot2_f32_bf16 v164, v123, v152, v154\n"
    "v_dot2_f32_bf16 v165, v123, v153, v154\n"
    "v_cvt_pk_bf16_f32 v121, v160, v161\n"
    "v_dot2_f32_bf16 v158, v124, v155, v157\n"
    "v_dot2_f32_bf16 v159, v124, v156, v157\n"
    "v_cvt_pk_bf16_f32 v122, v162, v163\n"
    "v_dot2_f32_bf16 v160, v125, v155, v157\n"
    "v_dot2_f32_bf16 v161, v125, v156, v157\n"
    "v_cvt_pk_bf16_f32 v123, v164, v165\n"
    "v_dot2_f32_bf16 v162, v126, v155, v157\n"
    "v_dot2_f32_bf16 v163, v126, v156, v157\n"
    "v_cvt_pk_bf16_f32 v124, v158, v159\n"
    "v_dot2_f32_bf16 v164, v127, v155, v157\n"
    "v_dot2_f32_bf16 v165, v127, v156, v157\n"
    "v_cvt_pk_bf16_f32 v125, v160, v161\n"
    "v_dot2_f32_bf16 v158, v128, v152, v154\n"
    "v_dot2_f32_bf16 v159, v128, v153, v154\n"
    "v_cvt_pk_bf16_f32 v126, v162, v163\n"
    "v_dot2_f32_bf16 v160, v129, v152, v154\n"
    "v_dot2_f32_bf16 v161, v129, v153, v154\n"
    "v_cvt_pk_bf16_f32 v127, v164, v165\n"
    "v_dot2_f32_bf16 v162, v130, v152, v154\n"
    "v_dot2_f32_bf16 v163, v130, v153, v154\n"
    "v_cvt_pk_bf16_f32 v128, v158, v159\n"
    "v_dot2_f32_bf16 v164, v131, v152, v154\n"
    "v_dot2_f32_bf16 v165, v131, v153, v154\n"
    "v_cvt_pk_bf16_f32 v129, v160, v161\n"
    "v_dot2_f32_bf16 v158, v132, v155, v157\n"
    "v_dot2_f32_bf16 v159, v132, v156, v157\n"
    "v_cvt_pk_bf16_f32 v130, v162, v163\n"
    "v_dot2_f32_bf16 v160, v133, v155, v157\n"
    "v_dot2_f32_bf16 v161, v133, v156, v157\n"
    "v_cvt_pk_bf16_f32 v131, v164, v165\n"
    "v_dot2_f32_bf16 v162, v134, v155, v157\n"
    "v_dot2_f32_bf16 v163, v134, v156, v157\n"
    "v_cvt_pk_bf16_f32 v132, v158, v159\n"
    "v_dot2_f32_bf16 v164, v135, v155, v157\n"
    "v_dot2_f32_bf16 v165, v135, v156, v157\n"
    "v_cvt_pk_bf16_f32 v133, v160, v161\n"
    "v_dot2_f32_bf16 v158, v136, v152, v154\n"
    "v_dot2_f32_bf16 v159, v136, v153, v154\n"
    "v_cvt_pk_bf16_f32 v134, v162, v163\n"
    "v_dot2_f32_bf16 v160, v137, v152, v154\n"
    "v_dot2_f32_bf16 v161, v137, v153, v154\n"
    "v_cvt_pk_bf16_f32 v135, v164, v165\n"
    "v_dot2_f32_bf16 v162, v138, v152, v154\n"
    "v_dot2_f32_bf16 v163, v138, v153, v154\n"
    "v_cvt_pk_bf16_f32 v136, v158, v159\n"
    "v_dot2_f32_bf16 v164, v139, v152, v154\n"
    "v_dot2_f32_bf16 v165, v139, v153, v154\n"
    "v_cvt_pk_bf16_f32 v137, v160, v161\n"
    "v_dot2_f32_bf16 v158, v140, v155, v157\n"
    "v_dot2_f32_bf16 v159, v140, v156, v157\n"
    "v_cvt_pk_bf16_f32 v138, v162, v163\n"
    "v_dot2_f32_bf16 v160, v141, v155, v157\n"
    "v_dot2_f32_bf16 v161, v141, v156, v157\n"
    "v_cvt_pk_bf16_f32 v139, v164, v165\n"
    "v_dot2_f32_bf16 v162, v142, v155, v157\n"
    "v_dot2_f32_bf16 v163, v142, v156, v157\n"
    "v_cvt_pk_bf16_f32 v140, v158, v159\n"
    "v_dot2_f32_bf16 v164, v143, v155, v157\n"
    "v_dot2_f32_bf16 v165, v143, v156, v157\n"
    "v_cvt_pk_bf16_f32 v141, v160, v161\n"
    "s_nop 2\n"
    "v_cvt_pk_bf16_f32 v142, v162, v163\n"
    "v_cvt_pk_bf16_f32 v143, v164, v165\n"
    "s_add_u32 s44, s44, 64\n"
    "s_addc_u32 s45, s45, 0\n"
    "s_add_u32 s40, s40, 64\n"
    "s_addc_u32 s41, s41, 0\n"
    "v_add_u32 v242, s56, v240\n"
    "v_xor_b32 v243, 32, v242\n"
    "s_waitcnt vmcnt(10)\n"
    "s_barrier\n"
    "ds_read_b64_tr_b16 v[184:185], v242 offset:0\n"
    "ds_read_b64_tr_b16 v[186:187], v242 offset:256\n"
    "ds_read_b64_tr_b16 v[188:189], v242 offset:2048\n"
    "ds_read_b64_tr_b16 v[190:191], v242 offset:2304\n"
    "ds_read_b64_tr_b16 v[192:193], v242 offset:4096\n"
    "ds_read_b64_tr_b16 v[194:195], v242 offset:4352\n"
    "ds_read_b64_tr_b16 v[196:197], v242 offset:6144\n"
    "ds_read_b64_tr_b16 v[198:199], v242 offset:6400\n"
    "s_waitcnt lgkmcnt(4)\n"
    "v_mfma_f32_16x16x32_bf16 v[144:147], v[184:187], v[0:3], 0\n"
    "s_add_u32 m0, s54, s60\n"
    "ds_read_b64_tr_b16 v[200:201], v242 offset:8192\n"
    "ds_read_b64_tr_b16 v[202:203], v242 offset:8448\n"
    "v_mfma_f32_16x16x32_bf16 v[144:147], v[188:191], v[8:11], v[144:147]\n"
    "buffer_load_dwordx4 v246, s[40:43], s68 offen lds\n"
    "v_mfma_f32_16x16x32_bf16 v[148:151], v[184:187], v[4:7], 0\n"
    "ds_read_b64_tr_b16 v[204:205], v242 offset:10240\n"
    "v_mfma_f32_16x16x32_bf16 v[148:151], v[188:191], v[12:15], v[148:151]\n"
    "s_waitcnt lgkmcnt(3)\n"
    "ds_read_b64_tr_b16 v[206:207], v242 offset:10496\n"
    "v_mfma_f32_16x16x32_bf16 v[148:151], v[192:195], v[20:23], v[148:151]\n"
    "s_add_u32 m0, m0, 0x400\n"
    "ds_read_b64_tr_b16 v[184:185], v242 offset:12288\n"
    "ds_read_b64_tr_b16 v[186:187], v242 offset:12544\n"
    "v_mfma_f32_16x16x32_bf16 v[148:151], v[196:199], v[28:31], v[148:151]\n"
    "buffer_load_dwordx4 v246, s[40:43], s69 offen lds\n"
    "v_mfma_f32_16x16x32_bf16 v[144:147], v[192:195], v[16:19], v[144:147]\n"
    "ds_read_b64_tr_b16 v[188:189], v242 offset:14336\n"
    "v_mfma_f32_16x16x32_bf16 v[144:147], v[196:199], v[24:27], v[144:147]\n"
    "s_waitcnt lgkmcnt(3)\n"
    "ds_read_b64_tr_b16 v[190:191], v242 offset:14592\n"
    "v_mfma_f32_16x16x32_bf16 v[144:147], v[200:203], v[32:35], v[144:147]\n"
    "s_add_u32 m0, m0, 0x400\n"
    "ds_read_b64_tr_b16 v[192:193], v242 offset:16384\n"
    "ds_read_b64_tr_b16 v[194:195], v242 offset:16640\n"
    "v_mfma_f32_16x16x32_bf16 v[144:147], v[204:207], v[40:43], v[144:147]\n"
    "buffer_load_dwordx4 v246, s[40:43], s70 offen lds\n"
    "v_mfma_f32_16x16x32_bf16 v[148:151], v[200:203], v[36:39], v[148:151]\n"
    "ds_read_b64_tr_b16 v[196:197], v242 offset:18432\n"
    "v_mfma_f32_16x16x32_bf16 v[148:151], v[204:207], v[44:47], v[148:151]\n"
    "s_waitcnt lgkmcnt(3)\n"
    "ds_read_b64_tr_b16 v[198:199], v242 offset:18688\n"
    "v_mfma_f32_16x16x32_bf16 v[148:151], v[184:187], v[52:55], v[148:151]\n"
    "s_add_u32 m0, m0, 0x400\n"
    "ds_read_b64_tr_b16 v[200:201], v242 offset:20480\n"
    "ds_read_b64_tr_b16 v[202:203], v242 offset:20736\n"
    "v_mfma_f32_16x16x32_bf16 v[148:151], v[188:191], v[60:63], v[148:151]\n"
    "buffer_load_dwordx4 v246, s[40:43], s71 offen lds\n"
    "v_mfma_f32_16x16x32_bf16 v[144:147], v[184:187], v[48:51], v[144:147]\n"
    "ds_read_b64_tr_b16 v[204:205], v242 offset:22528\n"
    "v_mfma_f32_16x16x32_bf16 v[144:147], v[188:191], v[56:59], v[144:147]\n"
    "s_waitcnt lgkmcnt(3)\n"
    "ds_read_b64_tr_b16 v[206:207], v242 offset:22784\n"
    "v_mfma_f32_16x16x32_bf16 v[144:147], v[192:195], v[64:67], v[144:147]\n"
    "s_add_u32 m0, m0, 0x400\n"
    "ds_read_b64_tr_b16 v[184:185], v242 offset:24576\n"
    "ds_read_b64_tr_b16 v[186:187], v242 offset:24832\n"
    "v_mfma_f32_16x16x32_bf16 v[144:147], v[196:199], v[72:75], v[144:147]\n"
    "buffer_load_dwordx4 v246, s[40:43], s72 offen lds\n"
    "v_mfma_f32_16x16x32_bf16 v[148:151], v[192:195], v[68:71], v[148:151]\n"
    "ds_read_b64_tr_b16 v[188:189], v242 offset:26624\n"
    "v_mfma_f32_16x16x32_bf16 v[148:151], v[196:199], v[76:79], v[148:151]\n"
    "s_waitcnt lgkmcnt(3)\n"
    "ds_read_b64_tr_b16 v[190:191], v242 offset:26880\n"
    "v_mfma_f32_16x16x32_bf16 v[148:151], v[200:203], v[84:87], v[148:151]\n"
    "s_add_u32 m0, m0, 0x400\n"
    "ds_read_b64_tr_b16 v[192:193], v242 offset:28672\n"
    "ds_read_b64_tr_b16 v[194:195], v242 offset:28928\n"
    "v_mfma_f32_16x16x32_bf16 v[148:151], v[204:207], v[92:95], v[148:151]\n"
    "buffer_load_dwordx4 v246, s[40:43], s73 offen lds\n"
    "v_mfma_f32_16x16x32_bf16 v[144:147], v[200:203], v[80:83], v[144:147]\n"
    "ds_read_b64_tr_b16 v[196:197], v242 offset:30720\n"
    "v_mfma_f32_16x16x32_bf16 v[144:147], v[204:207], v[88:91], v[144:147]\n"
    "s_waitcnt lgkmcnt(3)\n"
    "ds_read_b64_tr_b16 v[198:199], v242 offset:30976\n"
    "v_mfma_f32_16x16x32_bf16 v[144:147], v[184:187], v[96:99], v[144:147]\n"
    "s_add_u32 m0, m0, 0x400\n"
    "ds_read_b64_tr_b16 v[200:201], v242 offset:32768\n"
    "ds_read_b64_tr_b16 v[202:203], v242 offset:33024\n"
    "v_mfma_f32_16x16x32_bf16 v[144:147], v[188:191], v[104:107], v[144:147]\n"
    "buffer_load_dwordx4 v246, s[40:43], s74 offen lds\n"
    "v_mfma_f32_16x16x32_bf16 v[148:151], v[184:187], v[100:103], v[148:151]\n"
    "ds_read_b64_tr_b16 v[204:205], v242 offset:34816\n"
    "v_mfma_f32_16x16x32_bf16 v[148:151], v[188:191], v[108:111], v[148:151]\n"
    "s_waitcnt lgkmcnt(3)\n"
    "ds_read_b64_tr_b16 v[206:207], v242 offset:35072\n"
    "v_mfma_f32_16x16x32_bf16 v[148:151], v[192:195], v[116:119], v[148:151]\n"
    "s_add_u32 m0, m0, 0x400\n"
    "ds_read_b64_tr_b16 v[184:185], v243 offset:0\n"
    "ds_read_b64_tr_b16 v[186:187], v243 offset:256\n"
    "v_mfma_f32_16x16x32_bf16 v[148:151], v[196:199], v[124:127], v[148:151]\n"
    "buffer_load_dwordx4 v246, s[40:43], s75 offen lds\n"
    "v_mfma_f32_16x16x32_bf16 v[144:147], v[192:195], v[112:115], v[144:147]\n"
    "ds_read_b64_tr_b16 v[188:189], v243 offset:2048\n"
    "v_add_u32 v242, s57, v240\n"
    "v_mfma_f32_16x16x32_bf16 v[144:147], v[196:199], v[120:123], v[144:147]\n"
    "s_waitcnt lgkmcnt(3)\n"
    "ds_read_b64_tr_b16 v[190:191], v243 offset:2304\n"
    "v_mfma_f32_16x16x32_bf16 v[144:147], v[200:203], v[128:131], v[144:147]\n"
    "s_add_u32 m0, m0, 0x400\n"
    "ds_read_b64_tr_b16 v[192:193], v243 offset:4096\n"
    "ds_read_b64_tr_b16 v[194:195], v243 offset:4352\n"
    "v_mfma_f32_16x16x32_bf16 v[144:147], v[204:207], v[136:139], v[144:147]\n"
    "buffer_load_dwordx4 v246, s[40:43], s76 offen lds\n"
    "v_mfma_f32_16x16x32_bf16 v[148:151], v[200:203], v[132:135], v[148:151]\n"
    "ds_read_b64_tr_b16 v[196:197], v243 offset:6144\n"
    "v_mfma_f32_16x16x32_bf16 v[148:151], v[204:207], v[140:143], v[148:151]\n"
    "s_waitcnt lgkmcnt(3)\n"
    "ds_read_b64_tr_b16 v[198:199], v243 offset:6400\n"
    "v_mfma_f32_16x16x32_bf16 v[156:159], v[184:187], v[4:7], 0\n"
    "s_add_u32 s40, s40, 64\n"
    "s_add_u32 m0, s55, s60\n"
    "ds_read_b64_tr_b16 v[200:201], v243 offset:8192\n"
    "ds_read_b64_tr_b16 v[202:203], v243 offset:8448\n"
    "v_mfma_f32_16x16x32_bf16 v[156:159], v[188:191], v[12:15], v[156:159]\n"
    "s_addc_u32 s41, s41, 0\n"
    "buffer_load_dwordx4 v246, s[44:47], s68 offen lds\n"
    "v_mfma_f32_16x16x32_bf16 v[152:155], v[184:187], v[0:3], 0\n"
    "ds_read_b64_tr_b16 v[204:205], v243 offset:10240\n"
    "v_mfma_f32_16x16x32_bf16 v[152:155], v[188:191], v[8:11], v[152:155]\n"
    "s_waitcnt lgkmcnt(3)\n"
    "ds_read_b64_tr_b16 v[206:207], v243 offset:10496\n"
    "v_mfma_f32_16x16x32_bf16 v[152:155], v[192:195], v[16:19], v[152:155]\n"
    "s_add_u32 m0, m0, 0x400\n"
    "ds_read_b64_tr_b16 v[184:185], v243 offset:12288\n"
    "ds_read_b64_tr_b16 v[186:187], v243 offset:12544\n"
    "v_mfma_f32_16x16x32_bf16 v[152:155], v[196:199], v[24:27], v[152:155]\n"
    "buffer_load_dwordx4 v246, s[44:47], s69 offen lds\n"
    "v_mfma_f32_16x16x32_bf16 v[156:159], v[192:195], v[20:23], v[156:159]\n"
    "ds_read_b64_tr_b16 v[188:189], v243 offset:14336\n"
    "ds_read_b64_tr_b16 v[190:191], v243 offset:14592\n"
    "v_mfma_f32_16x16x32_bf16 v[156:159], v[196:199], v[28:31], v[156:159]\n"
    "s_waitcnt lgkmcnt(4)\n"
    "v_exp_f32 v144, v144\n"
    "v_mfma_f32_16x16x32_bf16 v[156:159], v[200:203], v[36:39], v[156:159]\n"
    "s_add_u32 m0, m0, 0x400\n"
    "ds_read_b64_tr_b16 v[192:193], v243 offset:16384\n"
    "ds_read_b64_tr_b16 v[194:195], v243 offset:16640\n"
    "v_mfma_f32_16x16x32_bf16 v[156:159], v[204:207], v[44:47], v[156:159]\n"
    "buffer_load_dwordx4 v246, s[44:47], s70 offen lds\n"
    "v_mfma_f32_16x16x32_bf16 v[152:155], v[200:203], v[32:35], v[152:155]\n"
    "ds_read_b64_tr_b16 v[196:197], v243 offset:18432\n"
    "ds_read_b64_tr_b16 v[198:199], v243 offset:18688\n"
    "v_mfma_f32_16x16x32_bf16 v[152:155], v[204:207], v[40:43], v[152:155]\n"
    "s_waitcnt lgkmcnt(4)\n"
    "v_exp_f32 v145, v145\n"
    "v_mfma_f32_16x16x32_bf16 v[152:155], v[184:187], v[48:51], v[152:155]\n"
    "s_add_u32 m0, m0, 0x400\n"
    "ds_read_b64_tr_b16 v[200:201], v243 offset:20480\n"
    "ds_read_b64_tr_b16 v[202:203], v243 offset:20736\n"
    "v_mfma_f32_16x16x32_bf16 v[152:155], v[188:191], v[56:59], v[152:155]\n"
    "buffer_load_dwordx4 v246, s[44:47], s71 offen lds\n"
    "v_mfma_f32_16x16x32_bf16 v[156:159], v[184:187], v[52:55], v[156:159]\n"
    "ds_read_b64_tr_b16 v[204:205], v243 offset:22528\n"
    "v_fma_f32 v144, v144, v248, v248\n"
    "v_mfma_f32_16x16x32_bf16 v[156:159], v[188:191], v[60:63], v[156:159]\n"
    "s_waitcnt lgkmcnt(3)\n"
    "ds_read_b64_tr_b16 v[206:207], v243 offset:22784\n"
    "v_fma_f32 v145, v145, v248, v248\n"
    "v_mfma_f32_16x16x32_bf16 v[156:159], v[192:195], v[68:71], v[156:159]\n"
    "s_add_u32 m0, m0, 0x400\n"
    "ds_read_b64_tr_b16 v[184:185], v243 offset:24576\n"
    "ds_read_b64_tr_b16 v[186:187], v243 offset:24832\n"
    "v_mfma_f32_16x16x32_bf16 v[156:159], v[196:199], v[76:79], v[156:159]\n"
    "buffer_load_dwordx4 v246, s[44:47], s72 offen lds\n"
    "v_mfma_f32_16x16x32_bf16 v[152:155], v[192:195], v[64:67], v[152:155]\n"
    "ds_read_b64_tr_b16 v[188:189], v243 offset:26624\n"
    "ds_read_b64_tr_b16 v[190:191], v243 offset:26880\n"
    "v_mfma_f32_16x16x32_bf16 v[152:155], v[196:199], v[72:75], v[152:155]\n"
    "s_waitcnt lgkmcnt(4)\n"
    "v_rcp_f32 v144, v144\n"
    "v_mfma_f32_16x16x32_bf16 v[152:155], v[200:203], v[80:83], v[152:155]\n"
    "s_add_u32 m0, m0, 0x400\n"
    "ds_read_b64_tr_b16 v[192:193], v243 offset:28672\n"
    "ds_read_b64_tr_b16 v[194:195], v243 offset:28928\n"
    "v_mfma_f32_16x16x32_bf16 v[152:155], v[204:207], v[88:91], v[152:155]\n"
    "buffer_load_dwordx4 v246, s[44:47], s73 offen lds\n"
    "v_mfma_f32_16x16x32_bf16 v[156:159], v[200:203], v[84:87], v[156:159]\n"
    "ds_read_b64_tr_b16 v[196:197], v243 offset:30720\n"
    "ds_read_b64_tr_b16 v[198:199], v243 offset:30976\n"
    "v_mfma_f32_16x16x32_bf16 v[156:159], v[204:207], v[92:95], v[156:159]\n"
    "s_waitcnt lgkmcnt(4)\n"
    "v_rcp_f32 v145, v145\n"
    "v_mfma_f32_16x16x32_bf16 v[156:159], v[184:187], v[100:103], v[156:159]\n"
    "s_add_u32 m0, m0, 0x400\n"
    "ds_read_b64_tr_b16 v[200:201], v243 offset:32768\n"
    "ds_read_b64_tr_b16 v[202:203], v243 offset:33024\n"
    "v_mfma_f32_16x16x32_bf16 v[156:159], v[188:191], v[108:111], v[156:159]\n"
    "buffer_load_dwordx4 v246, s[44:47], s74 offen lds\n"
    "v_mfma_f32_16x16x32_bf16 v[152:155], v[184:187], v[96:99], v[152:155]\n"
    "ds_read_b64_tr_b16 v[204:205], v243 offset:34816\n"
    "ds_read_b64_tr_b16 v[206:207], v243 offset:35072\n"
    "v_mfma_f32_16x16x32_bf16 v[152:155], v[188:191], v[104:107], v[152:155]\n"
    "s_waitcnt vmcnt(17) lgkmcnt(4)\n"
    "s_barrier\n"
    "v_exp_f32 v146, v146\n"
    "v_mfma_f32_16x16x32_bf16 v[152:155], v[192:195], v[112:115], v[152:155]\n"
    "s_add_u32 m0, m0, 0x400\n"
    "ds_read_b64_tr_b16 v[184:185], v242 offset:0\n"
    "ds_read_b64_tr_b16 v[186:187], v242 offset:256\n"
    "v_mfma_f32_16x16x32_bf16 v[152:155], v[196:199], v[120:123], v[152:155]\n"
    "buffer_load_dwordx4 v246, s[44:47], s75 offen lds\n"
    "v_mfma_f32_16x16x32_bf16 v[156:159], v[192:195], v[116:119], v[156:159]\n"
    "ds_read_b64_tr_b16 v[188:189], v242 offset:2048\n"
    "v_xor_b32 v243, 32, v242\n"
    "v_mfma_f32_16x16x32_bf16 v[156:159], v[196:199], v[124:127], v[156:159]\n"
    "s_waitcnt lgkmcnt(3)\n"
    "s_barrier\n"
    "ds_read_b64_tr_b16 v[190:191], v242 offset:2304\n"
    "v_mfma_f32_16x16x32_bf16 v[156:159], v[200:203], v[132:135], v[156:159]\n"
    "s_add_u32 m0, m0, 0x400\n"
    "ds_read_b64_tr_b16 v[192:193], v242 offset:4096\n"
    "ds_read_b64_tr_b16 v[194:195], v242 offset:4352\n"
    "v_mfma_f32_16x16x32_bf16 v[156:159], v[204:207], v[140:143], v[156:159]\n"
    "buffer_load_dwordx4 v246, s[44:47], s76 offen lds\n"
    "v_mfma_f32_16x16x32_bf16 v[152:155], v[200:203], v[128:131], v[152:155]\n"
    "ds_read_b64_tr_b16 v[196:197], v242 offset:6144\n"
    "ds_read_b64_tr_b16 v[198:199], v242 offset:6400\n"
    "v_mfma_f32_16x16x32_bf16 v[152:155], v[204:207], v[136:139], v[152:155]\n"
    "s_waitcnt lgkmcnt(4)\n"
    "v_exp_f32 v147, v147\n"
    "v_mfma_f32_16x16x32_bf16 v[160:163], v[184:187], v[0:3], 0\n"
    "s_add_u32 s44, s44, 64\n"
    "ds_read_b64_tr_b16 v[200:201], v242 offset:8192\n"
    "v_fma_f32 v146, v146, v248, v248\n"
    "v_mfma_f32_16x16x32_bf16 v[160:163], v[188:191], v[8:11], v[160:163]\n"
    "s_addc_u32 s45, s45, 0\n"
    "ds_read_b64_tr_b16 v[202:203], v242 offset:8448\n"
    "v_fma_f32 v147, v147, v248, v248\n"
    "v_mfma_f32_16x16x32_bf16 v[164:167], v[184:187], v[4:7], 0\n"
    "s_add_u32 m0, s56, s61\n"
    "ds_read_b64_tr_b16 v[204:205], v242 offset:10240\n"
    "ds_read_b64_tr_b16 v[206:207], v242 offset:10496\n"
    "v_mfma_f32_16x16x32_bf16 v[164:167], v[188:191], v[12:15], v[164:167]\n"
    "s_waitcnt lgkmcnt(4)\n"
    "buffer_load_dwordx4 v247, s[48:51], s52 offen lds\n"
    "v_mfma_f32_16x16x32_bf16 v[164:167], v[192:195], v[20:23], v[164:167]\n"
    "ds_read_b64_tr_b16 v[184:185], v242 offset:12288\n"
    "ds_read_b64_tr_b16 v[186:187], v242 offset:12544\n"
    "v_mfma_f32_16x16x32_bf16 v[164:167], v[196:199], v[28:31], v[164:167]\n"
    "v_rcp_f32 v146, v146\n"
    "v_mfma_f32_16x16x32_bf16 v[160:163], v[192:195], v[16:19], v[160:163]\n"
    "ds_read_b64_tr_b16 v[188:189], v242 offset:14336\n"
    "ds_read_b64_tr_b16 v[190:191], v242 offset:14592\n"
    "v_mfma_f32_16x16x32_bf16 v[160:163], v[196:199], v[24:27], v[160:163]\n"
    "s_waitcnt lgkmcnt(4)\n"
    "v_rcp_f32 v147, v147\n"
    "v_mfma_f32_16x16x32_bf16 v[160:163], v[200:203], v[32:35], v[160:163]\n"
    "ds_read_b64_tr_b16 v[192:193], v242 offset:16384\n"
    "ds_read_b64_tr_b16 v[194:195], v242 offset:16640\n"
    "v_mfma_f32_16x16x32_bf16 v[160:163], v[204:207], v[40:43], v[160:163]\n"
    "v_exp_f32 v148, v148\n"
    "v_mfma_f32_16x16x32_bf16 v[164:167], v[200:203], v[36:39], v[164:167]\n"
    "s_add_u32 m0, m0, 0x1000\n"
    "ds_read_b64_tr_b16 v[196:197], v242 offset:18432\n"
    "ds_read_b64_tr_b16 v[198:199], v242 offset:18688\n"
    "v_mfma_f32_16x16x32_bf16 v[164:167], v[204:207], v[44:47], v[164:167]\n"
    "s_waitcnt lgkmcnt(4)\n"
    "buffer_load_dwordx4 v247, s[48:51], s53 offen lds\n"
    "v_mfma_f32_16x16x32_bf16 v[164:167], v[184:187], v[52:55], v[164:167]\n"
    "ds_read_b64_tr_b16 v[200:201], v242 offset:20480\n"
    "ds_read_b64_tr_b16 v[202:203], v242 offset:20736\n"
    "v_mfma_f32_16x16x32_bf16 v[164:167], v[188:191], v[60:63], v[164:167]\n"
    "v_exp_f32 v149, v149\n"
    "v_mfma_f32_16x16x32_bf16 v[160:163], v[184:187], v[48:51], v[160:163]\n"
    "ds_read_b64_tr_b16 v[204:205], v242 offset:22528\n"
    "v_fma_f32 v148, v148, v249, v249\n"
    "v_mfma_f32_16x16x32_bf16 v[160:163], v[188:191], v[56:59], v[160:163]\n"
    "s_waitcnt lgkmcnt(3)\n"
    "ds_read_b64_tr_b16 v[206:207], v242 offset:22784\n"
    "v_fma_f32 v149, v149, v249, v249\n"
    "v_mfma_f32_16x16x32_bf16 v[160:163], v[192:195], v[64:67], v[160:163]\n"
    "ds_read_b64_tr_b16 v[184:185], v242 offset:24576\n"
    "ds_read_b64_tr_b16 v[186:187], v242 offset:24832\n"
    "v_mfma_f32_16x16x32_bf16 v[160:163], v[196:199], v[72:75], v[160:163]\n"
    "v_rcp_f32 v148, v148\n"
    "v_mfma_f32_16x16x32_bf16 v[164:167], v[192:195], v[68:71], v[164:167]\n"
    "s_add_u32 m0, m0, 0x1000\n"
    "ds_read_b64_tr_b16 v[188:189], v242 offset:26624\n"
    "ds_read_b64_tr_b16 v[190:191], v242 offset:26880\n"
    "v_mfma_f32_16x16x32_bf16 v[164:167], v[196:199], v[76:79], v[164:167]\n"
    "s_waitcnt lgkmcnt(4)\n"
    "buffer_load_dwordx4 v247, s[48:51], s58 offen lds\n"
    "v_mfma_f32_16x16x32_bf16 v[164:167], v[200:203], v[84:87], v[164:167]\n"
    "ds_read_b64_tr_b16 v[192:193], v242 offset:28672\n"
    "ds_read_b64_tr_b16 v[194:195], v242 offset:28928\n"
    "v_mfma_f32_16x16x32_bf16 v[164:167], v[204:207], v[92:95], v[164:167]\n"
    "v_rcp_f32 v149, v149\n"
    "v_mfma_f32_16x16x32_bf16 v[160:163], v[200:203], v[80:83], v[160:163]\n"
    "ds_read_b64_tr_b16 v[196:197], v242 offset:30720\n"
    "ds_read_b64_tr_b16 v[198:199], v242 offset:30976\n"
    "v_mfma_f32_16x16x32_bf16 v[160:163], v[204:207], v[88:91], v[160:163]\n"
    "s_waitcnt lgkmcnt(4)\n"
    "v_exp_f32 v150, v150\n"
    "v_mfma_f32_16x16x32_bf16 v[160:163], v[184:187], v[96:99], v[160:163]\n"
    "ds_read_b64_tr_b16 v[200:201], v242 offset:32768\n"
    "ds_read_b64_tr_b16 v[202:203], v242 offset:33024\n"
    "v_mfma_f32_16x16x32_bf16 v[160:163], v[188:191], v[104:107], v[160:163]\n"
    "v_exp_f32 v151, v151\n"
    "v_mfma_f32_16x16x32_bf16 v[164:167], v[184:187], v[100:103], v[164:167]\n"
    "s_add_u32 m0, m0, 0x1000\n"
    "ds_read_b64_tr_b16 v[204:205], v242 offset:34816\n"
    "ds_read_b64_tr_b16 v[206:207], v242 offset:35072\n"
    "v_mfma_f32_16x16x32_bf16 v[164:167], v[188:191], v[108:111], v[164:167]\n"
    "s_waitcnt lgkmcnt(4)\n"
    "buffer_load_dwordx4 v247, s[48:51], s59 offen lds\n"
    "v_mfma_f32_16x16x32_bf16 v[164:167], v[192:195], v[116:119], v[164:167]\n"
    "ds_read_b64_tr_b16 v[184:185], v243 offset:0\n"
    "v_add_u32 v242, s54, v240\n"
    "v_mfma_f32_16x16x32_bf16 v[164:167], v[196:199], v[124:127], v[164:167]\n"
    "ds_read_b64_tr_b16 v[186:187], v243 offset:256\n"
    "v_mfma_f32_16x16x32_bf16 v[160:163], v[192:195], v[112:115], v[160:163]\n"
    "ds_read_b64_tr_b16 v[188:189], v243 offset:2048\n"
    "v_fma_f32 v150, v150, v249, v249\n"
    "v_mfma_f32_16x16x32_bf16 v[160:163], v[196:199], v[120:123], v[160:163]\n"
    "s_waitcnt lgkmcnt(3)\n"
    "ds_read_b64_tr_b16 v[190:191], v243 offset:2304\n"
    "v_fma_f32 v151, v151, v249, v249\n"
    "v_mfma_f32_16x16x32_bf16 v[160:163], v[200:203], v[128:131], v[160:163]\n"
    "ds_read_b64_tr_b16 v[192:193], v243 offset:4096\n"
    "ds_read_b64_tr_b16 v[194:195], v243 offset:4352\n"
    "v_mfma_f32_16x16x32_bf16 v[160:163], v[204:207], v[136:139], v[160:163]\n"
    "v_rcp_f32 v150, v150\n"
    "v_mfma_f32_16x16x32_bf16 v[164:167], v[200:203], v[132:135], v[164:167]\n"
    "s_add_u32 m0, m0, 0x1000\n"
    "ds_read_b64_tr_b16 v[196:197], v243 offset:6144\n"
    "ds_read_b64_tr_b16 v[198:199], v243 offset:6400\n"
    "v_mfma_f32_16x16x32_bf16 v[164:167], v[204:207], v[140:143], v[164:167]\n"
    "s_waitcnt lgkmcnt(4)\n"
    "buffer_load_dwordx4 v247, s[48:51], s63 offen lds\n"
    "v_mfma_f32_16x16x32_bf16 v[172:175], v[184:187], v[4:7], 0\n"
    "ds_read_b64_tr_b16 v[200:201], v243 offset:8192\n"
    "ds_read_b64_tr_b16 v[202:203], v243 offset:8448\n"
    "v_mfma_f32_16x16x32_bf16 v[172:175], v[188:191], v[12:15], v[172:175]\n"
    "v_rcp_f32 v151, v151\n"
    "v_mfma_f32_16x16x32_bf16 v[168:171], v[184:187], v[0:3], 0\n"
    "ds_read_b64_tr_b16 v[204:205], v243 offset:10240\n"
    "ds_read_b64_tr_b16 v[206:207], v243 offset:10496\n"
    "v_mfma_f32_16x16x32_bf16 v[168:171], v[188:191], v[8:11], v[168:171]\n"
    "s_waitcnt lgkmcnt(4)\n"
    "v_exp_f32 v152, v152\n"
    "v_mfma_f32_16x16x32_bf16 v[168:171], v[192:195], v[16:19], v[168:171]\n"
    "ds_read_b64_tr_b16 v[184:185], v243 offset:12288\n"
    "ds_read_b64_tr_b16 v[186:187], v243 offset:12544\n"
    "v_mfma_f32_16x16x32_bf16 v[168:171], v[196:199], v[24:27], v[168:171]\n"
    "v_exp_f32 v153, v153\n"
    "v_mfma_f32_16x16x32_bf16 v[172:175], v[192:195], v[20:23], v[172:175]\n"
    "s_add_u32 m0, m0, 0x1000\n"
    "ds_read_b64_tr_b16 v[188:189], v243 offset:14336\n"
    "ds_read_b64_tr_b16 v[190:191], v243 offset:14592\n"
    "v_mfma_f32_16x16x32_bf16 v[172:175], v[196:199], v[28:31], v[172:175]\n"
    "s_waitcnt lgkmcnt(4)\n"
    "buffer_load_dwordx4 v247, s[48:51], s64 offen lds\n"
    "v_mfma_f32_16x16x32_bf16 v[172:175], v[200:203], v[36:39], v[172:175]\n"
    "ds_read_b64_tr_b16 v[192:193], v243 offset:16384\n"
    "v_fma_f32 v152, v152, v248, v248\n"
    "v_mfma_f32_16x16x32_bf16 v[172:175], v[204:207], v[44:47], v[172:175]\n"
    "ds_read_b64_tr_b16 v[194:195], v243 offset:16640\n"
    "v_fma_f32 v153, v153, v248, v248\n"
    "v_mfma_f32_16x16x32_bf16 v[168:171], v[200:203], v[32:35], v[168:171]\n"
    "ds_read_b64_tr_b16 v[196:197], v243 offset:18432\n"
    "ds_read_b64_tr_b16 v[198:199], v243 offset:18688\n"
    "v_mfma_f32_16x16x32_bf16 v[168:171], v[204:207], v[40:43], v[168:171]\n"
    "s_waitcnt lgkmcnt(4)\n"
    "v_rcp_f32 v152, v152\n"
    "v_mfma_f32_16x16x32_bf16 v[168:171], v[184:187], v[48:51], v[168:171]\n"
    "ds_read_b64_tr_b16 v[200:201], v243 offset:20480\n"
    "ds_read_b64_tr_b16 v[202:203], v243 offset:20736\n"
    "v_mfma_f32_16x16x32_bf16 v[168:171], v[188:191], v[56:59], v[168:171]\n"
    "v_rcp_f32 v153, v153\n"
    "v_mfma_f32_16x16x32_bf16 v[172:175], v[184:187], v[52:55], v[172:175]\n"
    "s_add_u32 m0, m0, 0x1000\n"
    "ds_read_b64_tr_b16 v[204:205], v243 offset:22528\n"
    "ds_read_b64_tr_b16 v[206:207], v243 offset:22784\n"
    "v_mfma_f32_16x16x32_bf16 v[172:175], v[188:191], v[60:63], v[172:175]\n"
    "s_waitcnt lgkmcnt(4)\n"
    "buffer_load_dwordx4 v247, s[48:51], s65 offen lds\n"
    "v_mfma_f32_16x16x32_bf16 v[172:175], v[192:195], v[68:71], v[172:175]\n"
    "ds_read_b64_tr_b16 v[184:185], v243 offset:24576\n"
    "ds_read_b64_tr_b16 v[186:187], v243 offset:24832\n"
    "v_mfma_f32_16x16x32_bf16 v[172:175], v[196:199], v[76:79], v[172:175]\n"
    "v_exp_f32 v154, v154\n"
    "v_mfma_f32_16x16x32_bf16 v[168:171], v[192:195], v[64:67], v[168:171]\n"
    "ds_read_b64_tr_b16 v[188:189], v243 offset:26624\n"
    "ds_read_b64_tr_b16 v[190:191], v243 offset:26880\n"
    "v_mfma_f32_16x16x32_bf16 v[168:171], v[196:199], v[72:75], v[168:171]\n"
    "s_waitcnt lgkmcnt(4)\n"
    "v_exp_f32 v155, v155\n"
    "v_mfma_f32_16x16x32_bf16 v[168:171], v[200:203], v[80:83], v[168:171]\n"
    "ds_read_b64_tr_b16 v[192:193], v243 offset:28672\n"
    "v_fma_f32 v154, v154, v248, v248\n"
    "v_mfma_f32_16x16x32_bf16 v[168:171], v[204:207], v[88:91], v[168:171]\n"
    "ds_read_b64_tr_b16 v[194:195], v243 offset:28928\n"
    "v_fma_f32 v155, v155, v248, v248\n"
    "v_mfma_f32_16x16x32_bf16 v[172:175], v[200:203], v[84:87], v[172:175]\n"
    "s_add_u32 m0, m0, 0x1000\n"
    "ds_read_b64_tr_b16 v[196:197], v243 offset:30720\n"
    "ds_read_b64_tr_b16 v[198:199], v243 offset:30976\n"
    "v_mfma_f32_16x16x32_bf16 v[172:175], v[204:207], v[92:95], v[172:175]\n"
    "s_waitcnt lgkmcnt(4)\n"
    "buffer_load_dwordx4 v247, s[48:51], s77 offen lds\n"
    "v_mfma_f32_16x16x32_bf16 v[172:175], v[184:187], v[100:103], v[172:175]\n"
    "ds_read_b64_tr_b16 v[200:201], v243 offset:32768\n"
    "ds_read_b64_tr_b16 v[202:203], v243 offset:33024\n"
    "v_mfma_f32_16x16x32_bf16 v[172:175], v[188:191], v[108:111], v[172:175]\n"
    "v_rcp_f32 v154, v154\n"
    "v_mfma_f32_16x16x32_bf16 v[168:171], v[184:187], v[96:99], v[168:171]\n"
    "ds_read_b64_tr_b16 v[204:205], v243 offset:34816\n"
    "ds_read_b64_tr_b16 v[206:207], v243 offset:35072\n"
    "v_mfma_f32_16x16x32_bf16 v[168:171], v[188:191], v[104:107], v[168:171]\n"
    "s_waitcnt vmcnt(17) lgkmcnt(4)\n"
    "s_barrier\n"
    "v_rcp_f32 v155, v155\n"
    "v_mfma_f32_16x16x32_bf16 v[168:171], v[192:195], v[112:115], v[168:171]\n"
    "ds_read_b64_tr_b16 v[184:185], v242 offset:0\n"
    "ds_read_b64_tr_b16 v[186:187], v242 offset:256\n"
    "v_mfma_f32_16x16x32_bf16 v[168:171], v[196:199], v[120:123], v[168:171]\n"
    "v_exp_f32 v156, v156\n"
    "v_mfma_f32_16x16x32_bf16 v[172:175], v[192:195], v[116:119], v[172:175]\n"
    "s_add_u32 m0, m0, 0x1000\n"
    "ds_read_b64_tr_b16 v[188:189], v242 offset:2048\n"
    "ds_read_b64_tr_b16 v[190:191], v242 offset:2304\n"
    "v_mfma_f32_16x16x32_bf16 v[172:175], v[196:199], v[124:127], v[172:175]\n"
    "s_waitcnt vmcnt(8) lgkmcnt(4)\n"
    "s_barrier\n"
    "buffer_load_dwordx4 v247, s[48:51], s79 offen lds\n"
    "v_mfma_f32_16x16x32_bf16 v[172:175], v[200:203], v[132:135], v[172:175]\n"
    "s_add_u32 s48, s48, 36864\n"
    "ds_read_b64_tr_b16 v[192:193], v242 offset:4096\n"
    "ds_read_b64_tr_b16 v[194:195], v242 offset:4352\n"
    "v_mfma_f32_16x16x32_bf16 v[172:175], v[204:207], v[140:143], v[172:175]\n"
    "s_addc_u32 s49, s49, 0\n"
    "v_exp_f32 v157, v157\n"
    "v_mfma_f32_16x16x32_bf16 v[168:171], v[200:203], v[128:131], v[168:171]\n"
    "ds_read_b64_tr_b16 v[196:197], v242 offset:6144\n"
    "v_fma_f32 v156, v156, v249, v249\n"
    "v_fma_f32 v157, v157, v249, v249\n"
    "v_rcp_f32 v156, v156\n"
    "v_rcp_f32 v157, v157\n"
    "v_exp_f32 v158, v158\n"
    "v_exp_f32 v159, v159\n"
    "v_fma_f32 v158, v158, v249, v249\n"
    "v_fma_f32 v159, v159, v249, v249\n"
    "v_rcp_f32 v158, v158\n"
    "v_mfma_f32_16x16x32_bf16 v[168:171], v[204:207], v[136:139], v[168:171]\n"
    "s_waitcnt lgkmcnt(3)\n"
    "ds_read_b64_tr_b16 v[198:199], v242 offset:6400\n"
    "v_rcp_f32 v159, v159\n"
    "v_mul_f32 v160, v144, v160\n"
    "v_mul_f32 v161, v145, v161\n"
    "v_mul_f32 v162, v146, v162\n"
    "v_mul_f32 v163, v147, v163\n"
    "v_mul_f32 v164, v148, v164\n"
    "v_mul_f32 v165, v149, v165\n"
    "v_mul_f32 v166, v150, v166\n"
    "v_mul_f32 v167, v151, v167\n"
    "v_mfma_f32_16x16x32_bf16 v[144:147], v[184:187], v[0:3], 0\n"
    "ds_read_b64_tr_b16 v[200:201], v242 offset:8192\n"
    "v_xor_b32 v243, 32, v242\n"
    "v_mfma_f32_16x16x32_bf16 v[144:147], v[188:191], v[8:11], v[144:147]\n"
    "ds_read_b64_tr_b16 v[202:203], v242 offset:8448\n"
    "v_mfma_f32_16x16x32_bf16 v[148:151], v[184:187], v[4:7], 0\n"
    "s_add_u32 m0, s57, s60\n"
    "ds_read_b64_tr_b16 v[204:205], v242 offset:10240\n"
    "ds_read_b64_tr_b16 v[206:207], v242 offset:10496\n"
    "v_mfma_f32_16x16x32_bf16 v[148:151], v[188:191], v[12:15], v[148:151]\n"
    "s_waitcnt lgkmcnt(4)\n"
    "buffer_load_dwordx4 v246, s[40:43], s68 offen lds\n"
    "v_mfma_f32_16x16x32_bf16 v[148:151], v[192:195], v[20:23], v[148:151]\n"
    "ds_read_b64_tr_b16 v[184:185], v242 offset:12288\n"
    "v_mfma_f32_16x16x32_bf16 v[148:151], v[196:199], v[28:31], v[148:151]\n"
    "ds_read_b64_tr_b16 v[186:187], v242 offset:12544\n"
    "v_mfma_f32_16x16x32_bf16 v[144:147], v[192:195], v[16:19], v[144:147]\n"
    "ds_read_b64_tr_b16 v[188:189], v242 offset:14336\n"
    "v_mfma_f32_16x16x32_bf16 v[144:147], v[196:199], v[24:27], v[144:147]\n"
    "s_waitcnt lgkmcnt(3)\n"
    "ds_read_b64_tr_b16 v[190:191], v242 offset:14592\n"
    "v_mfma_f32_16x16x32_bf16 v[144:147], v[200:203], v[32:35], v[144:147]\n"
    "ds_read_b64_tr_b16 v[192:193], v242 offset:16384\n"
    "v_mul_f32 v168, v152, v168\n"
    "v_mfma_f32_16x16x32_bf16 v[144:147], v[204:207], v[40:43], v[144:147]\n"
    "ds_read_b64_tr_b16 v[194:195], v242 offset:16640\n"
    "v_mul_f32 v169, v153, v169\n"
    "v_mfma_f32_16x16x32_bf16 v[148:151], v[200:203], v[36:39], v[148:151]\n"
    "s_add_u32 m0, m0, 0x400\n"
    "ds_read_b64_tr_b16 v[196:197], v242 offset:18432\n"
    "ds_read_b64_tr_b16 v[198:199], v242 offset:18688\n"
    "v_mfma_f32_16x16x32_bf16 v[148:151], v[204:207], v[44:47], v[148:151]\n"
    "s_waitcnt lgkmcnt(4)\n"
    "buffer_load_dwordx4 v246, s[40:43], s69 offen lds\n"
    "v_mfma_f32_16x16x32_bf16 v[148:151], v[184:187], v[52:55], v[148:151]\n"
    "ds_read_b64_tr_b16 v[200:201], v242 offset:20480\n"
    "v_mul_f32 v170, v154, v170\n"
    "v_mfma_f32_16x16x32_bf16 v[148:151], v[188:191], v[60:63], v[148:151]\n"
    "ds_read_b64_tr_b16 v[202:203], v242 offset:20736\n"
    "v_mul_f32 v171, v155, v171\n"
    "v_mfma_f32_16x16x32_bf16 v[144:147], v[184:187], v[48:51], v[144:147]\n"
    "ds_read_b64_tr_b16 v[204:205], v242 offset:22528\n"
    "v_mul_f32 v172, v156, v172\n"
    "v_mfma_f32_16x16x32_bf16 v[144:147], v[188:191], v[56:59], v[144:147]\n"
    "s_waitcnt lgkmcnt(3)\n"
    "ds_read_b64_tr_b16 v[206:207], v242 offset:22784\n"
    "v_mul_f32 v173, v157, v173\n"
    "v_mfma_f32_16x16x32_bf16 v[144:147], v[192:195], v[64:67], v[144:147]\n"
    "ds_read_b64_tr_b16 v[184:185], v242 offset:24576\n"
    "v_mul_f32 v174, v158, v174\n"
    "v_mfma_f32_16x16x32_bf16 v[144:147], v[196:199], v[72:75], v[144:147]\n"
    "ds_read_b64_tr_b16 v[186:187], v242 offset:24832\n"
    "v_mul_f32 v175, v159, v175\n"
    "v_mfma_f32_16x16x32_bf16 v[148:151], v[192:195], v[68:71], v[148:151]\n"
    "s_add_u32 m0, m0, 0x400\n"
    "ds_read_b64_tr_b16 v[188:189], v242 offset:26624\n"
    "ds_read_b64_tr_b16 v[190:191], v242 offset:26880\n"
    "v_mfma_f32_16x16x32_bf16 v[148:151], v[196:199], v[76:79], v[148:151]\n"
    "s_waitcnt lgkmcnt(4)\n"
    "buffer_load_dwordx4 v246, s[40:43], s70 offen lds\n"
    "v_mfma_f32_16x16x32_bf16 v[148:151], v[200:203], v[84:87], v[148:151]\n"
    "ds_read_b64_tr_b16 v[192:193], v242 offset:28672\n"
    "v_cvt_pk_bf16_f32 v176, v160, v161\n"
    "v_mfma_f32_16x16x32_bf16 v[148:151], v[204:207], v[92:95], v[148:151]\n"
    "ds_read_b64_tr_b16 v[194:195], v242 offset:28928\n"
    "v_cvt_pk_bf16_f32 v177, v162, v163\n"
    "v_mfma_f32_16x16x32_bf16 v[144:147], v[200:203], v[80:83], v[144:147]\n"
    "ds_read_b64_tr_b16 v[196:197], v242 offset:30720\n"
    "v_cvt_pk_bf16_f32 v180, v164, v165\n"
    "v_mfma_f32_16x16x32_bf16 v[144:147], v[204:207], v[88:91], v[144:147]\n"
    "s_waitcnt lgkmcnt(3)\n"
    "ds_read_b64_tr_b16 v[198:199], v242 offset:30976\n"
    "v_cvt_pk_bf16_f32 v181, v166, v167\n"
    "v_mfma_f32_16x16x32_bf16 v[144:147], v[184:187], v[96:99], v[144:147]\n"
    "ds_read_b64_tr_b16 v[200:201], v242 offset:32768\n"
    "v_cvt_pk_bf16_f32 v178, v168, v169\n"
    "v_mfma_f32_16x16x32_bf16 v[144:147], v[188:191], v[104:107], v[144:147]\n"
    "ds_read_b64_tr_b16 v[202:203], v242 offset:33024\n"
    "v_cvt_pk_bf16_f32 v179, v170, v171\n"
    "v_mfma_f32_16x16x32_bf16 v[148:151], v[184:187], v[100:103], v[148:151]\n"
    "s_add_u32 m0, m0, 0x400\n"
    "ds_read_b64_tr_b16 v[204:205], v242 offset:34816\n"
    "ds_read_b64_tr_b16 v[206:207], v242 offset:35072\n"
    "v_mfma_f32_16x16x32_bf16 v[148:151], v[188:191], v[108:111], v[148:151]\n"
    "s_waitcnt lgkmcnt(4)\n"
    "buffer_load_dwordx4 v246, s[40:43], s71 offen lds\n"
    "v_mfma_f32_16x16x32_bf16 v[148:151], v[192:195], v[116:119], v[148:151]\n"
    "ds_read_b64_tr_b16 v[184:185], v243 offset:0\n"
    "v_add_u32 v242, s55, v240\n"
    "v_mfma_f32_16x16x32_bf16 v[148:151], v[196:199], v[124:127], v[148:151]\n"
    "ds_read_b64_tr_b16 v[186:187], v243 offset:256\n"
    "v_mfma_f32_16x16x32_bf16 v[144:147], v[192:195], v[112:115], v[144:147]\n"
    "ds_read_b64_tr_b16 v[188:189], v243 offset:2048\n"
    "v_cvt_pk_bf16_f32 v182, v172, v173\n"
    "v_mfma_f32_16x16x32_bf16 v[144:147], v[196:199], v[120:123], v[144:147]\n"
    "s_waitcnt lgkmcnt(3)\n"
    "ds_read_b64_tr_b16 v[190:191], v243 offset:2304\n"
    "v_cvt_pk_bf16_f32 v183, v174, v175\n"
    "v_mfma_f32_16x16x32_bf16 v[144:147], v[200:203], v[128:131], v[144:147]\n"
    "ds_read_b64_tr_b16 v[192:193], v243 offset:4096\n"
    "v_mfma_f32_16x16x32_bf16 v[144:147], v[204:207], v[136:139], v[144:147]\n"
    "ds_read_b64_tr_b16 v[194:195], v243 offset:4352\n"
    "v_mfma_f32_16x16x32_bf16 v[148:151], v[200:203], v[132:135], v[148:151]\n"
    "s_add_u32 m0, m0, 0x400\n"
    "ds_read_b64_tr_b16 v[196:197], v243 offset:6144\n"
    "ds_read_b64_tr_b16 v[198:199], v243 offset:6400\n"
    "v_mfma_f32_16x16x32_bf16 v[148:151], v[204:207], v[140:143], v[148:151]\n"
    "s_waitcnt lgkmcnt(4)\n"
    "buffer_load_dwordx4 v246, s[40:43], s72 offen lds\n"
    "v_mfma_f32_16x16x32_bf16 v[156:159], v[184:187], v[4:7], 0\n"
    "ds_read_b64_tr_b16 v[200:201], v243 offset:8192\n"
    "v_mfma_f32_16x16x32_bf16 v[156:159], v[188:191], v[12:15], v[156:159]\n"
    "ds_read_b64_tr_b16 v[202:203], v243 offset:8448\n"
    "v_mfma_f32_16x16x32_bf16 v[152:155], v[184:187], v[0:3], 0\n"
    "ds_read_b64_tr_b16 v[204:205], v243 offset:10240\n"
    "v_mfma_f32_16x16x32_bf16 v[152:155], v[188:191], v[8:11], v[152:155]\n"
    "s_waitcnt lgkmcnt(3)\n"
    "ds_read_b64_tr_b16 v[206:207], v243 offset:10496\n"
    "v_mfma_f32_16x16x32_bf16 v[152:155], v[192:195], v[16:19], v[152:155]\n"
    "ds_read_b64_tr_b16 v[184:185], v243 offset:12288\n"
    "ds_read_b64_tr_b16 v[186:187], v243 offset:12544\n"
    "v_mfma_f32_16x16x32_bf16 v[152:155], v[196:199], v[24:27], v[152:155]\n"
    "v_exp_f32 v144, v144\n"
    "v_mfma_f32_16x16x32_bf16 v[156:159], v[192:195], v[20:23], v[156:159]\n"
    "s_add_u32 m0, m0, 0x400\n"
    "ds_read_b64_tr_b16 v[188:189], v243 offset:14336\n"
    "ds_read_b64_tr_b16 v[190:191], v243 offset:14592\n"
    "v_mfma_f32_16x16x32_bf16 v[156:159], v[196:199], v[28:31], v[156:159]\n"
    "s_waitcnt lgkmcnt(4)\n"
    "buffer_load_dwordx4 v246, s[40:43], s73 offen lds\n"
    "v_mfma_f32_16x16x32_bf16 v[156:159], v[200:203], v[36:39], v[156:159]\n"
    "ds_read_b64_tr_b16 v[192:193], v243 offset:16384\n"
    "ds_read_b64_tr_b16 v[194:195], v243 offset:16640\n"
    "v_mfma_f32_16x16x32_bf16 v[156:159], v[204:207], v[44:47], v[156:159]\n"
    "v_exp_f32 v145, v145\n"
    "v_mfma_f32_16x16x32_bf16 v[152:155], v[200:203], v[32:35], v[152:155]\n"
    "ds_read_b64_tr_b16 v[196:197], v243 offset:18432\n"
    "v_fma_f32 v144, v144, v248, v248\n"
    "v_mfma_f32_16x16x32_bf16 v[152:155], v[204:207], v[40:43], v[152:155]\n"
    "s_waitcnt lgkmcnt(3)\n"
    "ds_read_b64_tr_b16 v[198:199], v243 offset:18688\n"
    "v_fma_f32 v145, v145, v248, v248\n"
    "v_mfma_f32_16x16x32_bf16 v[152:155], v[184:187], v[48:51], v[152:155]\n"
    "ds_read_b64_tr_b16 v[200:201], v243 offset:20480\n"
    "ds_read_b64_tr_b16 v[202:203], v243 offset:20736\n"
    "v_mfma_f32_16x16x32_bf16 v[152:155], v[188:191], v[56:59], v[152:155]\n"
    "v_rcp_f32 v144, v144\n"
    "v_mfma_f32_16x16x32_bf16 v[156:159], v[184:187], v[52:55], v[156:159]\n"
    "s_add_u32 m0, m0, 0x400\n"
    "ds_read_b64_tr_b16 v[204:205], v243 offset:22528\n"
    "ds_read_b64_tr_b16 v[206:207], v243 offset:22784\n"
    "v_mfma_f32_16x16x32_bf16 v[156:159], v[188:191], v[60:63], v[156:159]\n"
    "s_waitcnt lgkmcnt(4)\n"
    "buffer_load_dwordx4 v246, s[40:43], s74 offen lds\n"
    "v_mfma_f32_16x16x32_bf16 v[156:159], v[192:195], v[68:71], v[156:159]\n"
    "ds_read_b64_tr_b16 v[184:185], v243 offset:24576\n"
    "ds_read_b64_tr_b16 v[186:187], v243 offset:24832\n"
    "v_mfma_f32_16x16x32_bf16 v[156:159], v[196:199], v[76:79], v[156:159]\n"
    "v_rcp_f32 v145, v145\n"
    "v_mfma_f32_16x16x32_bf16 v[152:155], v[192:195], v[64:67], v[152:155]\n"
    "ds_read_b64_tr_b16 v[188:189], v243 offset:26624\n"
    "ds_read_b64_tr_b16 v[190:191], v243 offset:26880\n"
    "v_mfma_f32_16x16x32_bf16 v[152:155], v[196:199], v[72:75], v[152:155]\n"
    "s_waitcnt lgkmcnt(4)\n"
    "v_exp_f32 v146, v146\n"
    "v_mfma_f32_16x16x32_bf16 v[152:155], v[200:203], v[80:83], v[152:155]\n"
    "ds_read_b64_tr_b16 v[192:193], v243 offset:28672\n"
    "ds_read_b64_tr_b16 v[194:195], v243 offset:28928\n"
    "v_mfma_f32_16x16x32_bf16 v[152:155], v[204:207], v[88:91], v[152:155]\n"
    "v_exp_f32 v147, v147\n"
    "v_mfma_f32_16x16x32_bf16 v[156:159], v[200:203], v[84:87], v[156:159]\n"
    "s_add_u32 m0, m0, 0x400\n"
    "ds_read_b64_tr_b16 v[196:197], v243 offset:30720\n"
    "ds_read_b64_tr_b16 v[198:199], v243 offset:30976\n"
    "v_mfma_f32_16x16x32_bf16 v[156:159], v[204:207], v[92:95], v[156:159]\n"
    "s_waitcnt lgkmcnt(4)\n"
    "buffer_load_dwordx4 v246, s[40:43], s75 offen lds\n"
    "v_mfma_f32_16x16x32_bf16 v[156:159], v[184:187], v[100:103], v[156:159]\n"
    "ds_read_b64_tr_b16 v[200:201], v243 offset:32768\n"
    "v_fma_f32 v146, v146, v248, v248\n"
    "v_mfma_f32_16x16x32_bf16 v[156:159], v[188:191], v[108:111], v[156:159]\n"
    "ds_read_b64_tr_b16 v[202:203], v243 offset:33024\n"
    "v_fma_f32 v147, v147, v248, v248\n"
    "v_mfma_f32_16x16x32_bf16 v[152:155], v[184:187], v[96:99], v[152:155]\n"
    "ds_read_b64_tr_b16 v[204:205], v243 offset:34816\n"
    "ds_read_b64_tr_b16 v[206:207], v243 offset:35072\n"
    "v_mfma_f32_16x16x32_bf16 v[152:155], v[188:191], v[104:107], v[152:155]\n"
    "s_waitcnt lgkmcnt(4)\n"
    "v_rcp_f32 v146, v146\n"
    "v_mfma_f32_16x16x32_bf16 v[152:155], v[192:195], v[112:115], v[152:155]\n"
    "ds_read_b64_tr_b16 v[184:185], v242 offset:0\n"
    "v_xor_b32 v243, 32, v242\n"
    "v_mfma_f32_16x16x32_bf16 v[152:155], v[196:199], v[120:123], v[152:155]\n"
    "ds_read_b64_tr_b16 v[186:187], v242 offset:256\n"
    "v_mfma_f32_16x16x32_bf16 v[156:159], v[192:195], v[116:119], v[156:159]\n"
    "s_add_u32 m0, m0, 0x400\n"
    "ds_read_b64_tr_b16 v[188:189], v242 offset:2048\n"
    "ds_read_b64_tr_b16 v[190:191], v242 offset:2304\n"
    "v_mfma_f32_16x16x32_bf16 v[156:159], v[196:199], v[124:127], v[156:159]\n"
    "s_waitcnt vmcnt(8) lgkmcnt(4)\n"
    "s_barrier\n"
    "buffer_load_dwordx4 v246, s[40:43], s76 offen lds\n"
    "v_mfma_f32_16x16x32_bf16 v[156:159], v[200:203], v[132:135], v[156:159]\n"
    "s_add_u32 s40, s40, 64\n"
    "ds_read_b64_tr_b16 v[192:193], v242 offset:4096\n"
    "ds_read_b64_tr_b16 v[194:195], v242 offset:4352\n"
    "v_mfma_f32_16x16x32_bf16 v[156:159], v[204:207], v[140:143], v[156:159]\n"
    "s_addc_u32 s41, s41, 0\n"
    "v_rcp_f32 v147, v147\n"
    "v_mfma_f32_16x16x32_bf16 v[152:155], v[200:203], v[128:131], v[152:155]\n"
    "s_add_u32 m0, %8, 147456\n"
    "ds_read_b64_tr_b16 v[196:197], v242 offset:6144\n"
    "ds_read_b64_tr_b16 v[198:199], v242 offset:6400\n"
    "v_mfma_f32_16x16x32_bf16 v[152:155], v[204:207], v[136:139], v[152:155]\n"
    "s_waitcnt lgkmcnt(4)\n"
    "global_load_lds_dword v[252:253], off\n"
    "v_mfma_f32_16x16x32_bf16 v[160:163], v[184:187], v[0:3], 0\n"
    "ds_read_b64_tr_b16 v[200:201], v242 offset:8192\n"
    "ds_read_b64_tr_b16 v[202:203], v242 offset:8448\n"
    "v_mfma_f32_16x16x32_bf16 v[160:163], v[188:191], v[8:11], v[160:163]\n"
    "v_exp_f32 v148, v148\n"
    "v_mfma_f32_16x16x32_bf16 v[164:167], v[184:187], v[4:7], 0\n"
    "s_add_u32 m0, s54, s60\n"
    "ds_read_b64_tr_b16 v[204:205], v242 offset:10240\n"
    "ds_read_b64_tr_b16 v[206:207], v242 offset:10496\n"
    "v_mfma_f32_16x16x32_bf16 v[164:167], v[188:191], v[12:15], v[164:167]\n"
    "s_waitcnt lgkmcnt(4)\n"
    "buffer_load_dwordx4 v246, s[44:47], s68 offen lds\n"
    "v_mfma_f32_16x16x32_bf16 v[164:167], v[192:195], v[20:23], v[164:167]\n"
    "ds_read_b64_tr_b16 v[184:185], v242 offset:12288\n"
    "ds_read_b64_tr_b16 v[186:187], v242 offset:12544\n"
    "v_mfma_f32_16x16x32_bf16 v[164:167], v[196:199], v[28:31], v[164:167]\n"
    "v_exp_f32 v149, v149\n"
    "v_mfma_f32_16x16x32_bf16 v[160:163], v[192:195], v[16:19], v[160:163]\n"
    "ds_read_b64_tr_b16 v[188:189], v242 offset:14336\n"
    "v_fma_f32 v148, v148, v249, v249\n"
    "v_mfma_f32_16x16x32_bf16 v[160:163], v[196:199], v[24:27], v[160:163]\n"
    "s_waitcnt lgkmcnt(3)\n"
    "ds_read_b64_tr_b16 v[190:191], v242 offset:14592\n"
    "v_fma_f32 v149, v149, v249, v249\n"
    "v_mfma_f32_16x16x32_bf16 v[160:163], v[200:203], v[32:35], v[160:163]\n"
    "ds_read_b64_tr_b16 v[192:193], v242 offset:16384\n"
    "ds_read_b64_tr_b16 v[194:195], v242 offset:16640\n"
    "v_mfma_f32_16x16x32_bf16 v[160:163], v[204:207], v[40:43], v[160:163]\n"
    "v_rcp_f32 v148, v148\n"
    "v_mfma_f32_16x16x32_bf16 v[164:167], v[200:203], v[36:39], v[164:167]\n"
    "s_add_u32 m0, m0, 0x400\n"
    "ds_read_b64_tr_b16 v[196:197], v242 offset:18432\n"
    "ds_read_b64_tr_b16 v[198:199], v242 offset:18688\n"
    "v_mfma_f32_16x16x32_bf16 v[164:167], v[204:207], v[44:47], v[164:167]\n"
    "s_waitcnt lgkmcnt(4)\n"
    "buffer_load_dwordx4 v246, s[44:47], s69 offen lds\n"
    "v_mfma_f32_16x16x32_bf16 v[164:167], v[184:187], v[52:55], v[164:167]\n"
    "ds_read_b64_tr_b16 v[200:201], v242 offset:20480\n"
    "ds_read_b64_tr_b16 v[202:203], v242 offset:20736\n"
    "v_mfma_f32_16x16x32_bf16 v[164:167], v[188:191], v[60:63], v[164:167]\n"
    "v_rcp_f32 v149, v149\n"
    "v_mfma_f32_16x16x32_bf16 v[160:163], v[184:187], v[48:51], v[160:163]\n"
    "ds_read_b64_tr_b16 v[204:205], v242 offset:22528\n"
    "ds_read_b64_tr_b16 v[206:207], v242 offset:22784\n"
    "v_mfma_f32_16x16x32_bf16 v[160:163], v[188:191], v[56:59], v[160:163]\n"
    "s_waitcnt lgkmcnt(4)\n"
    "v_exp_f32 v150, v150\n"
    "v_mfma_f32_16x16x32_bf16 v[160:163], v[192:195], v[64:67], v[160:163]\n"
    "ds_read_b64_tr_b16 v[184:185], v242 offset:24576\n"
    "ds_read_b64_tr_b16 v[186:187], v242 offset:24832\n"
    "v_mfma_f32_16x16x32_bf16 v[160:163], v[196:199], v[72:75], v[160:163]\n"
    "v_exp_f32 v151, v151\n"
    "v_mfma_f32_16x16x32_bf16 v[164:167], v[192:195], v[68:71], v[164:167]\n"
    "s_add_u32 m0, m0, 0x400\n"
    "ds_read_b64_tr_b16 v[188:189], v242 offset:26624\n"
    "ds_read_b64_tr_b16 v[190:191], v242 offset:26880\n"
    "v_mfma_f32_16x16x32_bf16 v[164:167], v[196:199], v[76:79], v[164:167]\n"
    "s_waitcnt lgkmcnt(4)\n"
    "buffer_load_dwordx4 v246, s[44:47], s70 offen lds\n"
    "v_mfma_f32_16x16x32_bf16 v[164:167], v[200:203], v[84:87], v[164:167]\n"
    "ds_read_b64_tr_b16 v[192:193], v242 offset:28672\n"
    "v_fma_f32 v150, v150, v249, v249\n"
    "v_mfma_f32_16x16x32_bf16 v[164:167], v[204:207], v[92:95], v[164:167]\n"
    "ds_read_b64_tr_b16 v[194:195], v242 offset:28928\n"
    "v_fma_f32 v151, v151, v249, v249\n"
    "v_mfma_f32_16x16x32_bf16 v[160:163], v[200:203], v[80:83], v[160:163]\n"
    "ds_read_b64_tr_b16 v[196:197], v242 offset:30720\n"
    "ds_read_b64_tr_b16 v[198:199], v242 offset:30976\n"
    "v_mfma_f32_16x16x32_bf16 v[160:163], v[204:207], v[88:91], v[160:163]\n"
    "s_waitcnt lgkmcnt(4)\n"
    "v_rcp_f32 v150, v150\n"
    "v_mfma_f32_16x16x32_bf16 v[160:163], v[184:187], v[96:99], v[160:163]\n"
    "ds_read_b64_tr_b16 v[200:201], v242 offset:32768\n"
    "ds_read_b64_tr_b16 v[202:203], v242 offset:33024\n"
    "v_mfma_f32_16x16x32_bf16 v[160:163], v[188:191], v[104:107], v[160:163]\n"
    "v_rcp_f32 v151, v151\n"
    "v_mfma_f32_16x16x32_bf16 v[164:167], v[184:187], v[100:103], v[164:167]\n"
    "s_add_u32 m0, m0, 0x400\n"
    "ds_read_b64_tr_b16 v[204:205], v242 offset:34816\n"
    "ds_read_b64_tr_b16 v[206:207], v242 offset:35072\n"
    "v_mfma_f32_16x16x32_bf16 v[164:167], v[188:191], v[108:111], v[164:167]\n"
    "s_waitcnt lgkmcnt(4)\n"
    "buffer_load_dwordx4 v246, s[44:47], s71 offen lds\n"
    "v_mfma_f32_16x16x32_bf16 v[164:167], v[192:195], v[116:119], v[164:167]\n"
    "ds_read_b64_tr_b16 v[184:185], v243 offset:0\n"
    "v_add_u32 v242, s56, v241\n"
    "v_mfma_f32_16x16x32_bf16 v[164:167], v[196:199], v[124:127], v[164:167]\n"
    "ds_read_b64_tr_b16 v[186:187], v243 offset:256\n"
    "v_xor_b32 v244, 64, v242\n"
    "v_mfma_f32_16x16x32_bf16 v[160:163], v[192:195], v[112:115], v[160:163]\n"
    "ds_read_b64_tr_b16 v[188:189], v243 offset:2048\n"
    "v_xor_b32 v245, 0x60, v242\n"
    "v_mfma_f32_16x16x32_bf16 v[160:163], v[196:199], v[120:123], v[160:163]\n"
    "s_waitcnt lgkmcnt(3)\n"
    "ds_read_b64_tr_b16 v[190:191], v243 offset:2304\n"
    "v_mfma_f32_16x16x32_bf16 v[160:163], v[200:203], v[128:131], v[160:163]\n"
    "ds_read_b64_tr_b16 v[192:193], v243 offset:4096\n"
    "ds_read_b64_tr_b16 v[194:195], v243 offset:4352\n"
    "v_mfma_f32_16x16x32_bf16 v[160:163], v[204:207], v[136:139], v[160:163]\n"
    "v_exp_f32 v152, v152\n"
    "v_mfma_f32_16x16x32_bf16 v[164:167], v[200:203], v[132:135], v[164:167]\n"
    "s_add_u32 m0, m0, 0x400\n"
    "ds_read_b64_tr_b16 v[196:197], v243 offset:6144\n"
    "ds_read_b64_tr_b16 v[198:199], v243 offset:6400\n"
    "v_mfma_f32_16x16x32_bf16 v[164:167], v[204:207], v[140:143], v[164:167]\n"
    "s_waitcnt lgkmcnt(4)\n"
    "buffer_load_dwordx4 v246, s[44:47], s72 offen lds\n"
    "v_mfma_f32_16x16x32_bf16 v[172:175], v[184:187], v[4:7], 0\n"
    "ds_read_b64_tr_b16 v[200:201], v243 offset:8192\n"
    "ds_read_b64_tr_b16 v[202:203], v243 offset:8448\n"
    "v_mfma_f32_16x16x32_bf16 v[172:175], v[188:191], v[12:15], v[172:175]\n"
    "v_exp_f32 v153, v153\n"
    "v_mfma_f32_16x16x32_bf16 v[168:171], v[184:187], v[0:3], 0\n"
    "ds_read_b64_tr_b16 v[204:205], v243 offset:10240\n"
    "v_fma_f32 v152, v152, v248, v248\n"
    "v_mfma_f32_16x16x32_bf16 v[168:171], v[188:191], v[8:11], v[168:171]\n"
    "s_waitcnt lgkmcnt(3)\n"
    "ds_read_b64_tr_b16 v[206:207], v243 offset:10496\n"
    "v_fma_f32 v153, v153, v248, v248\n"
    "v_mfma_f32_16x16x32_bf16 v[168:171], v[192:195], v[16:19], v[168:171]\n"
    "ds_read_b64_tr_b16 v[184:185], v243 offset:12288\n"
    "ds_read_b64_tr_b16 v[186:187], v243 offset:12544\n"
    "v_mfma_f32_16x16x32_bf16 v[168:171], v[196:199], v[24:27], v[168:171]\n"
    "v_rcp_f32 v152, v152\n"
    "v_mfma_f32_16x16x32_bf16 v[172:175], v[192:195], v[20:23], v[172:175]\n"
    "s_add_u32 m0, m0, 0x400\n"
    "ds_read_b64_tr_b16 v[188:189], v243 offset:14336\n"
    "ds_read_b64_tr_b16 v[190:191], v243 offset:14592\n"
    "v_mfma_f32_16x16x32_bf16 v[172:175], v[196:199], v[28:31], v[172:175]\n"
    "s_waitcnt lgkmcnt(4)\n"
    "buffer_load_dwordx4 v246, s[44:47], s73 offen lds\n"
    "v_mfma_f32_16x16x32_bf16 v[172:175], v[200:203], v[36:39], v[172:175]\n"
    "ds_read_b64_tr_b16 v[192:193], v243 offset:16384\n"
    "ds_read_b64_tr_b16 v[194:195], v243 offset:16640\n"
    "v_mfma_f32_16x16x32_bf16 v[172:175], v[204:207], v[44:47], v[172:175]\n"
    "v_rcp_f32 v153, v153\n"
    "v_mfma_f32_16x16x32_bf16 v[168:171], v[200:203], v[32:35], v[168:171]\n"
    "ds_read_b64_tr_b16 v[196:197], v243 offset:18432\n"
    "ds_read_b64_tr_b16 v[198:199], v243 offset:18688\n"
    "v_mfma_f32_16x16x32_bf16 v[168:171], v[204:207], v[40:43], v[168:171]\n"
    "s_waitcnt lgkmcnt(4)\n"
    "v_exp_f32 v154, v154\n"
    "v_mfma_f32_16x16x32_bf16 v[168:171], v[184:187], v[48:51], v[168:171]\n"
    "ds_read_b64_tr_b16 v[200:201], v243 offset:20480\n"
    "ds_read_b64_tr_b16 v[202:203], v243 offset:20736\n"
    "v_mfma_f32_16x16x32_bf16 v[168:171], v[188:191], v[56:59], v[168:171]\n"
    "v_exp_f32 v155, v155\n"
    "v_mfma_f32_16x16x32_bf16 v[172:175], v[184:187], v[52:55], v[172:175]\n"
    "s_add_u32 m0, m0, 0x400\n"
    "ds_read_b64_tr_b16 v[204:205], v243 offset:22528\n"
    "ds_read_b64_tr_b16 v[206:207], v243 offset:22784\n"
    "v_mfma_f32_16x16x32_bf16 v[172:175], v[188:191], v[60:63], v[172:175]\n"
    "s_waitcnt lgkmcnt(4)\n"
    "buffer_load_dwordx4 v246, s[44:47], s74 offen lds\n"
    "v_mfma_f32_16x16x32_bf16 v[172:175], v[192:195], v[68:71], v[172:175]\n"
    "ds_read_b64_tr_b16 v[184:185], v243 offset:24576\n"
    "v_fma_f32 v154, v154, v248, v248\n"
    "v_mfma_f32_16x16x32_bf16 v[172:175], v[196:199], v[76:79], v[172:175]\n"
    "ds_read_b64_tr_b16 v[186:187], v243 offset:24832\n"
    "v_fma_f32 v155, v155, v248, v248\n"
    "v_mfma_f32_16x16x32_bf16 v[168:171], v[192:195], v[64:67], v[168:171]\n"
    "ds_read_b64_tr_b16 v[188:189], v243 offset:26624\n"
    "ds_read_b64_tr_b16 v[190:191], v243 offset:26880\n"
    "v_mfma_f32_16x16x32_bf16 v[168:171], v[196:199], v[72:75], v[168:171]\n"
    "s_waitcnt lgkmcnt(4)\n"
    "v_rcp_f32 v154, v154\n"
    "v_mfma_f32_16x16x32_bf16 v[168:171], v[200:203], v[80:83], v[168:171]\n"
    "ds_read_b64_tr_b16 v[192:193], v243 offset:28672\n"
    "ds_read_b64_tr_b16 v[194:195], v243 offset:28928\n"
    "v_mfma_f32_16x16x32_bf16 v[168:171], v[204:207], v[88:91], v[168:171]\n"
    "v_rcp_f32 v155, v155\n"
    "v_mfma_f32_16x16x32_bf16 v[172:175], v[200:203], v[84:87], v[172:175]\n"
    "s_add_u32 m0, m0, 0x400\n"
    "ds_read_b64_tr_b16 v[196:197], v243 offset:30720\n"
    "ds_read_b64_tr_b16 v[198:199], v243 offset:30976\n"
    "v_mfma_f32_16x16x32_bf16 v[172:175], v[204:207], v[92:95], v[172:175]\n"
    "s_waitcnt lgkmcnt(4)\n"
    "buffer_load_dwordx4 v246, s[44:47], s75 offen lds\n"
    "v_mfma_f32_16x16x32_bf16 v[172:175], v[184:187], v[100:103], v[172:175]\n"
    "ds_read_b64_tr_b16 v[200:201], v243 offset:32768\n"
    "ds_read_b64_tr_b16 v[202:203], v243 offset:33024\n"
    "v_mfma_f32_16x16x32_bf16 v[172:175], v[188:191], v[108:111], v[172:175]\n"
    "v_exp_f32 v156, v156\n"
    "v_mfma_f32_16x16x32_bf16 v[168:171], v[184:187], v[96:99], v[168:171]\n"
    "ds_read_b64_tr_b16 v[204:205], v243 offset:34816\n"
    "ds_read_b64_tr_b16 v[206:207], v243 offset:35072\n"
    "v_mfma_f32_16x16x32_bf16 v[168:171], v[188:191], v[104:107], v[168:171]\n"
    "s_waitcnt lgkmcnt(4)\n"
    "v_exp_f32 v157, v157\n"
    "v_mfma_f32_16x16x32_bf16 v[168:171], v[192:195], v[112:115], v[168:171]\n"
    "ds_read_b64_tr_b16 v[184:185], v242 offset:0\n"
    "v_xor_b32 v243, 32, v242\n"
    "v_mfma_f32_16x16x32_bf16 v[168:171], v[196:199], v[120:123], v[168:171]\n"
    "ds_read_b64_tr_b16 v[186:187], v242 offset:2048\n"
    "v_mfma_f32_16x16x32_bf16 v[172:175], v[192:195], v[116:119], v[172:175]\n"
    "s_add_u32 m0, m0, 0x400\n"
    "ds_read_b64_tr_b16 v[188:189], v243 offset:0\n"
    "ds_read_b64_tr_b16 v[190:191], v243 offset:2048\n"
    "v_mfma_f32_16x16x32_bf16 v[172:175], v[196:199], v[124:127], v[172:175]\n"
    "s_waitcnt vmcnt(9) lgkmcnt(4)\n"
    "s_barrier\n"
    "buffer_load_dwordx4 v246, s[44:47], s76 offen lds\n"
    "v_mfma_f32_16x16x32_bf16 v[172:175], v[200:203], v[132:135], v[172:175]\n"
    "s_add_u32 s44, s44, 64\n"
    "ds_read_b64_tr_b16 v[192:193], v244 offset:0\n"
    "v_fma_f32 v156, v156, v249, v249\n"
    "v_mfma_f32_16x16x32_bf16 v[172:175], v[204:207], v[140:143], v[172:175]\n"
    "s_addc_u32 s45, s45, 0\n"
    "ds_read_b64_tr_b16 v[194:195], v244 offset:2048\n"
    "v_fma_f32 v157, v157, v249, v249\n"
    "v_mfma_f32_16x16x32_bf16 v[168:171], v[200:203], v[128:131], v[168:171]\n"
    "ds_read_b64_tr_b16 v[196:197], v245 offset:0\n"
    "ds_read_b64_tr_b16 v[198:199], v245 offset:2048\n"
    "v_mfma_f32_16x16x32_bf16 v[168:171], v[204:207], v[136:139], v[168:171]\n"
    "s_waitcnt lgkmcnt(4)\n"
    "v_rcp_f32 v156, v156\n"
    "v_mfma_f32_16x16x32_bf16 a[0:3], v[184:187], v[176:179], 0\n"
    "ds_read_b64_tr_b16 v[200:201], v242 offset:4096\n"
    "ds_read_b64_tr_b16 v[202:203], v242 offset:6144\n"
    "v_mfma_f32_16x16x32_bf16 a[4:7], v[188:191], v[176:179], 0\n"
    "v_rcp_f32 v157, v157\n"
    "v_mfma_f32_16x16x32_bf16 a[8:11], v[184:187], v[180:183], 0\n"
    "s_add_u32 m0, s55, s61\n"
    "ds_read_b64_tr_b16 v[204:205], v243 offset:4096\n"
    "ds_read_b64_tr_b16 v[206:207], v243 offset:6144\n"
    "v_mfma_f32_16x16x32_bf16 a[12:15], v[188:191], v[180:183], 0\n"
    "s_waitcnt lgkmcnt(4)\n"
    "buffer_load_dwordx4 v247, s[48:51], s52 offen lds\n"
    "v_mfma_f32_16x16x32_bf16 a[24:27], v[192:195], v[180:183], 0\n"
    "ds_read_b64_tr_b16 v[184:185], v244 offset:4096\n"
    "ds_read_b64_tr_b16 v[186:187], v244 offset:6144\n"
    "v_mfma_f32_16x16x32_bf16 a[28:31], v[196:199], v[180:183], 0\n"
    "v_exp_f32 v158, v158\n"
    "v_mfma_f32_16x16x32_bf16 a[16:19], v[192:195], v[176:179], 0\n"
    "ds_read_b64_tr_b16 v[188:189], v245 offset:4096\n"
    "ds_read_b64_tr_b16 v[190:191], v245 offset:6144\n"
    "v_mfma_f32_16x16x32_bf16 a[20:23], v[196:199], v[176:179], 0\n"
    "s_waitcnt lgkmcnt(4)\n"
    "v_exp_f32 v159, v159\n"
    "v_mfma_f32_16x16x32_bf16 a[32:35], v[200:203], v[176:179], 0\n"
    "ds_read_b64_tr_b16 v[192:193], v242 offset:8192\n"
    "v_fma_f32 v158, v158, v249, v249\n"
    "v_mfma_f32_16x16x32_bf16 a[36:39], v[204:207], v[176:179], 0\n"
    "ds_read_b64_tr_b16 v[194:195], v242 offset:10240\n"
    "v_fma_f32 v159, v159, v249, v249\n"
    "v_mfma_f32_16x16x32_bf16 a[40:43], v[200:203], v[180:183], 0\n"
    "s_add_u32 m0, m0, 0x1000\n"
    "ds_read_b64_tr_b16 v[196:197], v243 offset:8192\n"
    "ds_read_b64_tr_b16 v[198:199], v243 offset:10240\n"
    "v_mfma_f32_16x16x32_bf16 a[44:47], v[204:207], v[180:183], 0\n"
    "s_waitcnt lgkmcnt(4)\n"
    "buffer_load_dwordx4 v247, s[48:51], s53 offen lds\n"
    "v_mfma_f32_16x16x32_bf16 a[56:59], v[184:187], v[180:183], 0\n"
    "ds_read_b64_tr_b16 v[200:201], v244 offset:8192\n"
    "ds_read_b64_tr_b16 v[202:203], v244 offset:10240\n"
    "v_mfma_f32_16x16x32_bf16 a[60:63], v[188:191], v[180:183], 0\n"
    "v_rcp_f32 v158, v158\n"
    "v_mfma_f32_16x16x32_bf16 a[48:51], v[184:187], v[176:179], 0\n"
    "ds_read_b64_tr_b16 v[204:205], v245 offset:8192\n"
    "ds_read_b64_tr_b16 v[206:207], v245 offset:10240\n"
    "v_mfma_f32_16x16x32_bf16 a[52:55], v[188:191], v[176:179], 0\n"
    "s_waitcnt lgkmcnt(4)\n"
    "v_rcp_f32 v159, v159\n"
    "v_mfma_f32_16x16x32_bf16 a[64:67], v[192:195], v[176:179], 0\n"
    "ds_read_b64_tr_b16 v[184:185], v242 offset:12288\n"
    "v_mul_f32 v160, v144, v160\n"
    "v_mfma_f32_16x16x32_bf16 a[68:71], v[196:199], v[176:179], 0\n"
    "ds_read_b64_tr_b16 v[186:187], v242 offset:14336\n"
    "v_mul_f32 v161, v145, v161\n"
    "v_mfma_f32_16x16x32_bf16 a[72:75], v[192:195], v[180:183], 0\n"
    "s_add_u32 m0, m0, 0x1000\n"
    "ds_read_b64_tr_b16 v[188:189], v243 offset:12288\n"
    "ds_read_b64_tr_b16 v[190:191], v243 offset:14336\n"
    "v_mfma_f32_16x16x32_bf16 a[76:79], v[196:199], v[180:183], 0\n"
    "s_waitcnt lgkmcnt(4)\n"
    "buffer_load_dwordx4 v247, s[48:51], s58 offen lds\n"
    "v_mfma_f32_16x16x32_bf16 a[88:91], v[200:203], v[180:183], 0\n"
    "ds_read_b64_tr_b16 v[192:193], v244 offset:12288\n"
    "v_mul_f32 v162, v146, v162\n"
    "v_mfma_f32_16x16x32_bf16 a[92:95], v[204:207], v[180:183], 0\n"
    "ds_read_b64_tr_b16 v[194:195], v244 offset:14336\n"
    "v_mul_f32 v163, v147, v163\n"
    "v_mfma_f32_16x16x32_bf16 a[80:83], v[200:203], v[176:179], 0\n"
    "ds_read_b64_tr_b16 v[196:197], v245 offset:12288\n"
    "v_mul_f32 v164, v148, v164\n"
    "v_mfma_f32_16x16x32_bf16 a[84:87], v[204:207], v[176:179], 0\n"
    "s_waitcnt lgkmcnt(3)\n"
    "ds_read_b64_tr_b16 v[198:199], v245 offset:14336\n"
    "v_mul_f32 v165, v149, v165\n"
    "v_mfma_f32_16x16x32_bf16 a[96:99], v[184:187], v[176:179], 0\n"
    "ds_read_b64_tr_b16 v[200:201], v242 offset:16384\n"
    "v_mul_f32 v166, v150, v166\n"
    "v_mfma_f32_16x16x32_bf16 a[100:103], v[188:191], v[176:179], 0\n"
    "ds_read_b64_tr_b16 v[202:203], v242 offset:18432\n"
    "v_mul_f32 v167, v151, v167\n"
    "v_mfma_f32_16x16x32_bf16 a[104:107], v[184:187], v[180:183], 0\n"
    "s_add_u32 m0, m0, 0x1000\n"
    "ds_read_b64_tr_b16 v[204:205], v243 offset:16384\n"
    "ds_read_b64_tr_b16 v[206:207], v243 offset:18432\n"
    "v_mfma_f32_16x16x32_bf16 a[108:111], v[188:191], v[180:183], 0\n"
    "s_waitcnt lgkmcnt(4)\n"
    "buffer_load_dwordx4 v247, s[48:51], s59 offen lds\n"
    "v_mfma_f32_16x16x32_bf16 a[120:123], v[192:195], v[180:183], 0\n"
    "ds_read_b64_tr_b16 v[184:185], v244 offset:16384\n"
    "v_mul_f32 v168, v152, v168\n"
    "v_mfma_f32_16x16x32_bf16 a[124:127], v[196:199], v[180:183], 0\n"
    "ds_read_b64_tr_b16 v[186:187], v244 offset:18432\n"
    "v_mul_f32 v169, v153, v169\n"
    "v_mfma_f32_16x16x32_bf16 a[112:115], v[192:195], v[176:179], 0\n"
    "ds_read_b64_tr_b16 v[188:189], v245 offset:16384\n"
    "v_mul_f32 v170, v154, v170\n"
    "v_mfma_f32_16x16x32_bf16 a[116:119], v[196:199], v[176:179], 0\n"
    "s_waitcnt lgkmcnt(3)\n"
    "ds_read_b64_tr_b16 v[190:191], v245 offset:18432\n"
    "v_mul_f32 v171, v155, v171\n"
    "v_mfma_f32_16x16x32_bf16 a[128:131], v[200:203], v[176:179], 0\n"
    "ds_read_b64_tr_b16 v[192:193], v242 offset:20480\n"
    "v_mul_f32 v172, v156, v172\n"
    "v_mfma_f32_16x16x32_bf16 a[132:135], v[204:207], v[176:179], 0\n"
    "ds_read_b64_tr_b16 v[194:195], v242 offset:22528\n"
    "v_mul_f32 v173, v157, v173\n"
    "v_mfma_f32_16x16x32_bf16 a[136:139], v[200:203], v[180:183], 0\n"
    "s_add_u32 m0, m0, 0x1000\n"
    "ds_read_b64_tr_b16 v[196:197], v243 offset:20480\n"
    "ds_read_b64_tr_b16 v[198:199], v243 offset:22528\n"
    "v_mfma_f32_16x16x32_bf16 a[140:143], v[204:207], v[180:183], 0\n"
    "s_waitcnt lgkmcnt(4)\n"
    "buffer_load_dwordx4 v247, s[48:51], s63 offen lds\n"
    "v_mfma_f32_16x16x32_bf16 a[152:155], v[184:187], v[180:183], 0\n"
    "ds_read_b64_tr_b16 v[200:201], v244 offset:20480\n"
    "v_mul_f32 v174, v158, v174\n"
    "v_mfma_f32_16x16x32_bf16 a[156:159], v[188:191], v[180:183], 0\n"
    "ds_read_b64_tr_b16 v[202:203], v244 offset:22528\n"
    "v_mul_f32 v175, v159, v175\n"
    "v_mfma_f32_16x16x32_bf16 a[144:147], v[184:187], v[176:179], 0\n"
    "ds_read_b64_tr_b16 v[204:205], v245 offset:20480\n"
    "v_mfma_f32_16x16x32_bf16 a[148:151], v[188:191], v[176:179], 0\n"
    "s_waitcnt lgkmcnt(3)\n"
    "ds_read_b64_tr_b16 v[206:207], v245 offset:22528\n"
    "v_mfma_f32_16x16x32_bf16 a[160:163], v[192:195], v[176:179], 0\n"
    "ds_read_b64_tr_b16 v[184:185], v242 offset:24576\n"
    "v_mfma_f32_16x16x32_bf16 a[164:167], v[196:199], v[176:179], 0\n"
    "ds_read_b64_tr_b16 v[186:187], v242 offset:26624\n"
    "v_mfma_f32_16x16x32_bf16 a[168:171], v[192:195], v[180:183], 0\n"
    "s_add_u32 m0, m0, 0x1000\n"
    "ds_read_b64_tr_b16 v[188:189], v243 offset:24576\n"
    "ds_read_b64_tr_b16 v[190:191], v243 offset:26624\n"
    "v_mfma_f32_16x16x32_bf16 a[172:175], v[196:199], v[180:183], 0\n"
    "s_waitcnt lgkmcnt(4)\n"
    "buffer_load_dwordx4 v247, s[48:51], s64 offen lds\n"
    "v_mfma_f32_16x16x32_bf16 a[184:187], v[200:203], v[180:183], 0\n"
    "ds_read_b64_tr_b16 v[192:193], v244 offset:24576\n"
    "v_mfma_f32_16x16x32_bf16 a[188:191], v[204:207], v[180:183], 0\n"
    "ds_read_b64_tr_b16 v[194:195], v244 offset:26624\n"
    "v_mfma_f32_16x16x32_bf16 a[176:179], v[200:203], v[176:179], 0\n"
    "ds_read_b64_tr_b16 v[196:197], v245 offset:24576\n"
    "v_mfma_f32_16x16x32_bf16 a[180:183], v[204:207], v[176:179], 0\n"
    "s_waitcnt lgkmcnt(3)\n"
    "ds_read_b64_tr_b16 v[198:199], v245 offset:26624\n"
    "v_mfma_f32_16x16x32_bf16 a[192:195], v[184:187], v[176:179], 0\n"
    "ds_read_b64_tr_b16 v[200:201], v242 offset:28672\n"
    "v_mfma_f32_16x16x32_bf16 a[196:199], v[188:191], v[176:179], 0\n"
    "ds_read_b64_tr_b16 v[202:203], v242 offset:30720\n"
    "v_mfma_f32_16x16x32_bf16 a[200:203], v[184:187], v[180:183], 0\n"
    "s_add_u32 m0, m0, 0x1000\n"
    "ds_read_b64_tr_b16 v[204:205], v243 offset:28672\n"
    "ds_read_b64_tr_b16 v[206:207], v243 offset:30720\n"
    "v_mfma_f32_16x16x32_bf16 a[204:207], v[188:191], v[180:183], 0\n"
    "s_waitcnt lgkmcnt(4)\n"
    "buffer_load_dwordx4 v247, s[48:51], s65 offen lds\n"
    "v_mfma_f32_16x16x32_bf16 a[216:219], v[192:195], v[180:183], 0\n"
    "ds_read_b64_tr_b16 v[184:185], v244 offset:28672\n"
    "v_mfma_f32_16x16x32_bf16 a[220:223], v[196:199], v[180:183], 0\n"
    "ds_read_b64_tr_b16 v[186:187], v244 offset:30720\n"
    "v_mfma_f32_16x16x32_bf16 a[208:211], v[192:195], v[176:179], 0\n"
    "ds_read_b64_tr_b16 v[188:189], v245 offset:28672\n"
    "v_mfma_f32_16x16x32_bf16 a[212:215], v[196:199], v[176:179], 0\n"
    "s_waitcnt lgkmcnt(3)\n"
    "ds_read_b64_tr_b16 v[190:191], v245 offset:30720\n"
    "v_mfma_f32_16x16x32_bf16 a[224:227], v[200:203], v[176:179], 0\n"
    "ds_read_b64_tr_b16 v[192:193], v242 offset:32768\n"
    "v_mfma_f32_16x16x32_bf16 a[228:231], v[204:207], v[176:179], 0\n"
    "ds_read_b64_tr_b16 v[194:195], v242 offset:34816\n"
    "v_mfma_f32_16x16x32_bf16 a[232:235], v[200:203], v[180:183], 0\n"
    "s_add_u32 m0, m0, 0x1000\n"
    "ds_read_b64_tr_b16 v[196:197], v243 offset:32768\n"
    "ds_read_b64_tr_b16 v[198:199], v243 offset:34816\n"
    "v_mfma_f32_16x16x32_bf16 a[236:239], v[204:207], v[180:183], 0\n"
    "s_waitcnt lgkmcnt(4)\n"
    "buffer_load_dwordx4 v247, s[48:51], s77 offen lds\n"
    "v_mfma_f32_16x16x32_bf16 a[248:251], v[184:187], v[180:183], 0\n"
    "ds_read_b64_tr_b16 v[200:201], v244 offset:32768\n"
    "v_add_u32 v242, s57, v240\n"
    "v_mfma_f32_16x16x32_bf16 a[252:255], v[188:191], v[180:183], 0\n"
    "ds_read_b64_tr_b16 v[202:203], v244 offset:34816\n"
    "v_xor_b32 v243, 32, v242\n"
    "v_mfma_f32_16x16x32_bf16 a[240:243], v[184:187], v[176:179], 0\n"
    "ds_read_b64_tr_b16 v[204:205], v245 offset:32768\n"
    "v_mfma_f32_16x16x32_bf16 a[244:247], v[188:191], v[176:179], 0\n"
    "s_waitcnt lgkmcnt(3)\n"
    "ds_read_b64_tr_b16 v[206:207], v245 offset:34816\n"
    "v_mfma_f32_16x16x32_bf16 v[208:211], v[192:195], v[176:179], 0\n"
    "s_mov_b32 s67, s56\n"
    "ds_read_b64_tr_b16 v[184:185], v242 offset:0\n"
    "v_mfma_f32_16x16x32_bf16 v[212:215], v[196:199], v[176:179], 0\n"
    "s_mov_b32 s56, s55\n"
    "ds_read_b64_tr_b16 v[186:187], v242 offset:256\n"
    "v_mfma_f32_16x16x32_bf16 v[216:219], v[192:195], v[180:183], 0\n"
    "s_add_u32 m0, m0, 0x1000\n"
    "ds_read_b64_tr_b16 v[188:189], v242 offset:2048\n"
    "ds_read_b64_tr_b16 v[190:191], v242 offset:2304\n"
    "v_mfma_f32_16x16x32_bf16 v[220:223], v[196:199], v[180:183], 0\n"
    "s_mov_b32 s55, s54\n"
    "s_waitcnt vmcnt(8) lgkmcnt(4)\n"
    "s_barrier\n"
    "buffer_load_dwordx4 v247, s[48:51], s79 offen lds\n"
    "v_mfma_f32_16x16x32_bf16 v[232:235], v[200:203], v[180:183], 0\n"
    "s_add_u32 s48, s48, 36864\n"
    "ds_read_b64_tr_b16 v[192:193], v242 offset:4096\n"
    "v_mfma_f32_16x16x32_bf16 v[236:239], v[204:207], v[180:183], 0\n"
    "s_addc_u32 s49, s49, 0\n"
    "ds_read_b64_tr_b16 v[194:195], v242 offset:4352\n"
    "v_mfma_f32_16x16x32_bf16 v[224:227], v[200:203], v[176:179], 0\n"
    "s_mov_b32 s54, s57\n"
    "ds_read_b64_tr_b16 v[196:197], v242 offset:6144\n"
    "v_mfma_f32_16x16x32_bf16 v[228:231], v[204:207], v[176:179], 0\n"
    "s_mov_b32 s57, s67\n"
    "s_waitcnt lgkmcnt(3)\n"
    "ds_read_b64_tr_b16 v[198:199], v242 offset:6400\n"
    "s_mov_b32 s66, 7\n"
    "1:\n"
    "v_mfma_f32_16x16x32_bf16 v[144:147], v[184:187], v[0:3], 0\n"
    "ds_read_b64_tr_b16 v[200:201], v242 offset:8192\n"
    "v_cvt_pk_bf16_f32 v176, v160, v161\n"
    "v_mfma_f32_16x16x32_bf16 v[144:147], v[188:191], v[8:11], v[144:147]\n"
    "ds_read_b64_tr_b16 v[202:203], v242 offset:8448\n"
    "v_cvt_pk_bf16_f32 v177, v162, v163\n"
    "v_mfma_f32_16x16x32_bf16 v[148:151], v[184:187], v[4:7], 0\n"
    "s_add_u32 m0, s57, s60\n"
    "ds_read_b64_tr_b16 v[204:205], v242 offset:10240\n"
    "ds_read_b64_tr_b16 v[206:207], v242 offset:10496\n"
    "v_mfma_f32_16x16x32_bf16 v[148:151], v[188:191], v[12:15], v[148:151]\n"
    "s_waitcnt lgkmcnt(4)\n"
    "buffer_load_dwordx4 v246, s[40:43], s68 offen lds\n"
    "v_mfma_f32_16x16x32_bf16 v[148:151], v[192:195], v[20:23], v[148:151]\n"
    "ds_read_b64_tr_b16 v[184:185], v242 offset:12288\n"
    "v_cvt_pk_bf16_f32 v180, v164, v165\n"
    "v_mfma_f32_16x16x32_bf16 v[148:151], v[196:199], v[28:31], v[148:151]\n"
    "ds_read_b64_tr_b16 v[186:187], v242 offset:12544\n"
    "v_cvt_pk_bf16_f32 v181, v166, v167\n"
    "v_mfma_f32_16x16x32_bf16 v[144:147], v[192:195], v[16:19], v[144:147]\n"
    "ds_read_b64_tr_b16 v[188:189], v242 offset:14336\n"
    "v_cvt_pk_bf16_f32 v178, v168, v169\n"
    "v_mfma_f32_16x16x32_bf16 v[144:147], v[196:199], v[24:27], v[144:147]\n"
    "s_waitcnt lgkmcnt(3)\n"
    "ds_read_b64_tr_b16 v[190:191], v242 offset:14592\n"
    "v_cvt_pk_bf16_f32 v179, v170, v171\n"
    "v_mfma_f32_16x16x32_bf16 v[144:147], v[200:203], v[32:35], v[144:147]\n"
    "ds_read_b64_tr_b16 v[192:193], v242 offset:16384\n"
    "v_cvt_pk_bf16_f32 v182, v172, v173\n"
    "v_mfma_f32_16x16x32_bf16 v[144:147], v[204:207], v[40:43], v[144:147]\n"
    "ds_read_b64_tr_b16 v[194:195], v242 offset:16640\n"
    "v_cvt_pk_bf16_f32 v183, v174, v175\n"
    "v_mfma_f32_16x16x32_bf16 v[148:151], v[200:203], v[36:39], v[148:151]\n"
    "s_add_u32 m0, m0, 0x400\n"
    "ds_read_b64_tr_b16 v[196:197], v242 offset:18432\n"
    "ds_read_b64_tr_b16 v[198:199], v242 offset:18688\n"
    "v_mfma_f32_16x16x32_bf16 v[148:151], v[204:207], v[44:47], v[148:151]\n"
    "s_waitcnt lgkmcnt(4)\n"
    "buffer_load_dwordx4 v246, s[40:43], s69 offen lds\n"
    "v_mfma_f32_16x16x32_bf16 v[148:151], v[184:187], v[52:55], v[148:151]\n"
    "ds_read_b64_tr_b16 v[200:201], v242 offset:20480\n"
    "v_mfma_f32_16x16x32_bf16 v[148:151], v[188:191], v[60:63], v[148:151]\n"
    "ds_read_b64_tr_b16 v[202:203], v242 offset:20736\n"
    "v_mfma_f32_16x16x32_bf16 v[144:147], v[184:187], v[48:51], v[144:147]\n"
    "ds_read_b64_tr_b16 v[204:205], v242 offset:22528\n"
    "v_mfma_f32_16x16x32_bf16 v[144:147], v[188:191], v[56:59], v[144:147]\n"
    "s_waitcnt lgkmcnt(3)\n"
    "ds_read_b64_tr_b16 v[206:207], v242 offset:22784\n"
    "v_mfma_f32_16x16x32_bf16 v[144:147], v[192:195], v[64:67], v[144:147]\n"
    "ds_read_b64_tr_b16 v[184:185], v242 offset:24576\n"
    "v_mfma_f32_16x16x32_bf16 v[144:147], v[196:199], v[72:75], v[144:147]\n"
    "ds_read_b64_tr_b16 v[186:187], v242 offset:24832\n"
    "v_mfma_f32_16x16x32_bf16 v[148:151], v[192:195], v[68:71], v[148:151]\n"
    "s_add_u32 m0, m0, 0x400\n"
    "ds_read_b64_tr_b16 v[188:189], v242 offset:26624\n"
    "ds_read_b64_tr_b16 v[190:191], v242 offset:26880\n"
    "v_mfma_f32_16x16x32_bf16 v[148:151], v[196:199], v[76:79], v[148:151]\n"
    "s_waitcnt lgkmcnt(4)\n"
    "buffer_load_dwordx4 v246, s[40:43], s70 offen lds\n"
    "v_mfma_f32_16x16x32_bf16 v[148:151], v[200:203], v[84:87], v[148:151]\n"
    "ds_read_b64_tr_b16 v[192:193], v242 offset:28672\n"
    "v_mfma_f32_16x16x32_bf16 v[148:151], v[204:207], v[92:95], v[148:151]\n"
    "ds_read_b64_tr_b16 v[194:195], v242 offset:28928\n"
    "v_mfma_f32_16x16x32_bf16 v[144:147], v[200:203], v[80:83], v[144:147]\n"
    "ds_read_b64_tr_b16 v[196:197], v242 offset:30720\n"
    "v_mfma_f32_16x16x32_bf16 v[144:147], v[204:207], v[88:91], v[144:147]\n"
    "s_waitcnt lgkmcnt(3)\n"
    "ds_read_b64_tr_b16 v[198:199], v242 offset:30976\n"
    "v_mfma_f32_16x16x32_bf16 v[144:147], v[184:187], v[96:99], v[144:147]\n"
    "ds_read_b64_tr_b16 v[200:201], v242 offset:32768\n"
    "v_mfma_f32_16x16x32_bf16 v[144:147], v[188:191], v[104:107], v[144:147]\n"
    "ds_read_b64_tr_b16 v[202:203], v242 offset:33024\n"
    "v_mfma_f32_16x16x32_bf16 v[148:151], v[184:187], v[100:103], v[148:151]\n"
    "s_add_u32 m0, m0, 0x400\n"
    "ds_read_b64_tr_b16 v[204:205], v242 offset:34816\n"
    "ds_read_b64_tr_b16 v[206:207], v242 offset:35072\n"
    "v_mfma_f32_16x16x32_bf16 v[148:151], v[188:191], v[108:111], v[148:151]\n"
    "s_waitcnt lgkmcnt(4)\n"
    "buffer_load_dwordx4 v246, s[40:43], s71 offen lds\n"
    "v_mfma_f32_16x16x32_bf16 v[148:151], v[192:195], v[116:119], v[148:151]\n"
    "ds_read_b64_tr_b16 v[184:185], v243 offset:0\n"
    "v_add_u32 v242, s55, v240\n"
    "v_mfma_f32_16x16x32_bf16 v[148:151], v[196:199], v[124:127], v[148:151]\n"
    "ds_read_b64_tr_b16 v[186:187], v243 offset:256\n"
    "v_mfma_f32_16x16x32_bf16 v[144:147], v[192:195], v[112:115], v[144:147]\n"
    "ds_read_b64_tr_b16 v[188:189], v243 offset:2048\n"
    "v_mfma_f32_16x16x32_bf16 v[144:147], v[196:199], v[120:123], v[144:147]\n"
    "s_waitcnt lgkmcnt(3)\n"
    "ds_read_b64_tr_b16 v[190:191], v243 offset:2304\n"
    "v_mfma_f32_16x16x32_bf16 v[144:147], v[200:203], v[128:131], v[144:147]\n"
    "ds_read_b64_tr_b16 v[192:193], v243 offset:4096\n"
    "v_mfma_f32_16x16x32_bf16 v[144:147], v[204:207], v[136:139], v[144:147]\n"
    "ds_read_b64_tr_b16 v[194:195], v243 offset:4352\n"
    "v_mfma_f32_16x16x32_bf16 v[148:151], v[200:203], v[132:135], v[148:151]\n"
    "s_add_u32 m0, m0, 0x400\n"
    "ds_read_b64_tr_b16 v[196:197], v243 offset:6144\n"
    "ds_read_b64_tr_b16 v[198:199], v243 offset:6400\n"
    "v_mfma_f32_16x16x32_bf16 v[148:151], v[204:207], v[140:143], v[148:151]\n"
    "s_waitcnt lgkmcnt(4)\n"
    "buffer_load_dwordx4 v246, s[40:43], s72 offen lds\n"
    "v_mfma_f32_16x16x32_bf16 v[156:159], v[184:187], v[4:7], 0\n"
    "ds_read_b64_tr_b16 v[200:201], v243 offset:8192\n"
    "v_mfma_f32_16x16x32_bf16 v[156:159], v[188:191], v[12:15], v[156:159]\n"
    "ds_read_b64_tr_b16 v[202:203], v243 offset:8448\n"
    "v_mfma_f32_16x16x32_bf16 v[152:155], v[184:187], v[0:3], 0\n"
    "ds_read_b64_tr_b16 v[204:205], v243 offset:10240\n"
    "v_mfma_f32_16x16x32_bf16 v[152:155], v[188:191], v[8:11], v[152:155]\n"
    "s_waitcnt lgkmcnt(3)\n"
    "ds_read_b64_tr_b16 v[206:207], v243 offset:10496\n"
    "v_mfma_f32_16x16x32_bf16 v[152:155], v[192:195], v[16:19], v[152:155]\n"
    "ds_read_b64_tr_b16 v[184:185], v243 offset:12288\n"
    "ds_read_b64_tr_b16 v[186:187], v243 offset:12544\n"
    "v_mfma_f32_16x16x32_bf16 v[152:155], v[196:199], v[24:27], v[152:155]\n"
    "v_exp_f32 v144, v144\n"
    "v_mfma_f32_16x16x32_bf16 v[156:159], v[192:195], v[20:23], v[156:159]\n"
    "s_add_u32 m0, m0, 0x400\n"
    "ds_read_b64_tr_b16 v[188:189], v243 offset:14336\n"
    "ds_read_b64_tr_b16 v[190:191], v243 offset:14592\n"
    "v_mfma_f32_16x16x32_bf16 v[156:159], v[196:199], v[28:31], v[156:159]\n"
    "s_waitcnt lgkmcnt(4)\n"
    "buffer_load_dwordx4 v246, s[40:43], s73 offen lds\n"
    "v_mfma_f32_16x16x32_bf16 v[156:159], v[200:203], v[36:39], v[156:159]\n"
    "ds_read_b64_tr_b16 v[192:193], v243 offset:16384\n"
    "ds_read_b64_tr_b16 v[194:195], v243 offset:16640\n"
    "v_mfma_f32_16x16x32_bf16 v[156:159], v[204:207], v[44:47], v[156:159]\n"
    "v_exp_f32 v145, v145\n"
    "v_mfma_f32_16x16x32_bf16 v[152:155], v[200:203], v[32:35], v[152:155]\n"
    "ds_read_b64_tr_b16 v[196:197], v243 offset:18432\n"
    "v_fma_f32 v144, v144, v248, v248\n"
    "v_mfma_f32_16x16x32_bf16 v[152:155], v[204:207], v[40:43], v[152:155]\n"
    "s_waitcnt lgkmcnt(3)\n"
    "ds_read_b64_tr_b16 v[198:199], v243 offset:18688\n"
    "v_fma_f32 v145, v145, v248, v248\n"
    "v_mfma_f32_16x16x32_bf16 v[152:155], v[184:187], v[48:51], v[152:155]\n"
    "ds_read_b64_tr_b16 v[200:201], v243 offset:20480\n"
    "ds_read_b64_tr_b16 v[202:203], v243 offset:20736\n"
    "v_mfma_f32_16x16x32_bf16 v[152:155], v[188:191], v[56:59], v[152:155]\n"
    "v_rcp_f32 v144, v144\n"
    "v_mfma_f32_16x16x32_bf16 v[156:159], v[184:187], v[52:55], v[156:159]\n"
    "s_add_u32 m0, m0, 0x400\n"
    "ds_read_b64_tr_b16 v[204:205], v243 offset:22528\n"
    "ds_read_b64_tr_b16 v[206:207], v243 offset:22784\n"
    "v_mfma_f32_16x16x32_bf16 v[156:159], v[188:191], v[60:63], v[156:159]\n"
    "s_waitcnt lgkmcnt(4)\n"
    "buffer_load_dwordx4 v246, s[40:43], s74 offen lds\n"
    "v_mfma_f32_16x16x32_bf16 v[156:159], v[192:195], v[68:71], v[156:159]\n"
    "ds_read_b64_tr_b16 v[184:185], v243 offset:24576\n"
    "ds_read_b64_tr_b16 v[186:187], v243 offset:24832\n"
    "v_mfma_f32_16x16x32_bf16 v[156:159], v[196:199], v[76:79], v[156:159]\n"
    "v_rcp_f32 v145, v145\n"
    "v_mfma_f32_16x16x32_bf16 v[152:155], v[192:195], v[64:67], v[152:155]\n"
    "ds_read_b64_tr_b16 v[188:189], v243 offset:26624\n"
    "ds_read_b64_tr_b16 v[190:191], v243 offset:26880\n"
    "v_mfma_f32_16x16x32_bf16 v[152:155], v[196:199], v[72:75], v[152:155]\n"
    "s_waitcnt lgkmcnt(4)\n"
    "v_exp_f32 v146, v146\n"
    "v_mfma_f32_16x16x32_bf16 v[152:155], v[200:203], v[80:83], v[152:155]\n"
    "ds_read_b64_tr_b16 v[192:193], v243 offset:28672\n"
    "ds_read_b64_tr_b16 v[194:195], v243 offset:28928\n"
    "v_mfma_f32_16x16x32_bf16 v[152:155], v[204:207], v[88:91], v[152:155]\n"
    "v_exp_f32 v147, v147\n"
    "v_mfma_f32_16x16x32_bf16 v[156:159], v[200:203], v[84:87], v[156:159]\n"
    "s_add_u32 m0, m0, 0x400\n"
    "ds_read_b64_tr_b16 v[196:197], v243 offset:30720\n"
    "ds_read_b64_tr_b16 v[198:199], v243 offset:30976\n"
    "v_mfma_f32_16x16x32_bf16 v[156:159], v[204:207], v[92:95], v[156:159]\n"
    "s_waitcnt lgkmcnt(4)\n"
    "buffer_load_dwordx4 v246, s[40:43], s75 offen lds\n"
    "v_mfma_f32_16x16x32_bf16 v[156:159], v[184:187], v[100:103], v[156:159]\n"
    "ds_read_b64_tr_b16 v[200:201], v243 offset:32768\n"
    "v_fma_f32 v146, v146, v248, v248\n"
    "v_mfma_f32_16x16x32_bf16 v[156:159], v[188:191], v[108:111], v[156:159]\n"
    "ds_read_b64_tr_b16 v[202:203], v243 offset:33024\n"
    "v_fma_f32 v147, v147, v248, v248\n"
    "v_mfma_f32_16x16x32_bf16 v[152:155], v[184:187], v[96:99], v[152:155]\n"
    "ds_read_b64_tr_b16 v[204:205], v243 offset:34816\n"
    "ds_read_b64_tr_b16 v[206:207], v243 offset:35072\n"
    "v_mfma_f32_16x16x32_bf16 v[152:155], v[188:191], v[104:107], v[152:155]\n"
    "s_waitcnt lgkmcnt(4)\n"
    "v_rcp_f32 v146, v146\n"
    "v_mfma_f32_16x16x32_bf16 v[152:155], v[192:195], v[112:115], v[152:155]\n"
    "ds_read_b64_tr_b16 v[184:185], v242 offset:0\n"
    "v_xor_b32 v243, 32, v242\n"
    "v_mfma_f32_16x16x32_bf16 v[152:155], v[196:199], v[120:123], v[152:155]\n"
    "ds_read_b64_tr_b16 v[186:187], v242 offset:256\n"
    "v_mfma_f32_16x16x32_bf16 v[156:159], v[192:195], v[116:119], v[156:159]\n"
    "s_add_u32 m0, m0, 0x400\n"
    "ds_read_b64_tr_b16 v[188:189], v242 offset:2048\n"
    "ds_read_b64_tr_b16 v[190:191], v242 offset:2304\n"
    "v_mfma_f32_16x16x32_bf16 v[156:159], v[196:199], v[124:127], v[156:159]\n"
    "s_waitcnt vmcnt(8) lgkmcnt(4)\n"
    "s_barrier\n"
    "buffer_load_dwordx4 v246, s[40:43], s76 offen lds\n"
    "v_mfma_f32_16x16x32_bf16 v[156:159], v[200:203], v[132:135], v[156:159]\n"
    "s_add_u32 s40, s40, 64\n"
    "ds_read_b64_tr_b16 v[192:193], v242 offset:4096\n"
    "ds_read_b64_tr_b16 v[194:195], v242 offset:4352\n"
    "v_mfma_f32_16x16x32_bf16 v[156:159], v[204:207], v[140:143], v[156:159]\n"
    "s_addc_u32 s41, s41, 0\n"
    "v_rcp_f32 v147, v147\n"
    "v_mfma_f32_16x16x32_bf16 v[152:155], v[200:203], v[128:131], v[152:155]\n"
    "ds_read_b64_tr_b16 v[196:197], v242 offset:6144\n"
    "ds_read_b64_tr_b16 v[198:199], v242 offset:6400\n"
    "v_mfma_f32_16x16x32_bf16 v[152:155], v[204:207], v[136:139], v[152:155]\n"
    "s_waitcnt lgkmcnt(4)\n"
    "v_exp_f32 v148, v148\n"
    "v_mfma_f32_16x16x32_bf16 v[160:163], v[184:187], v[0:3], 0\n"
    "ds_read_b64_tr_b16 v[200:201], v242 offset:8192\n"
    "ds_read_b64_tr_b16 v[202:203], v242 offset:8448\n"
    "v_mfma_f32_16x16x32_bf16 v[160:163], v[188:191], v[8:11], v[160:163]\n"
    "v_exp_f32 v149, v149\n"
    "v_mfma_f32_16x16x32_bf16 v[164:167], v[184:187], v[4:7], 0\n"
    "s_add_u32 m0, s54, s60\n"
    "ds_read_b64_tr_b16 v[204:205], v242 offset:10240\n"
    "ds_read_b64_tr_b16 v[206:207], v242 offset:10496\n"
    "v_mfma_f32_16x16x32_bf16 v[164:167], v[188:191], v[12:15], v[164:167]\n"
    "s_waitcnt lgkmcnt(4)\n"
    "buffer_load_dwordx4 v246, s[44:47], s68 offen lds\n"
    "v_mfma_f32_16x16x32_bf16 v[164:167], v[192:195], v[20:23], v[164:167]\n"
    "ds_read_b64_tr_b16 v[184:185], v242 offset:12288\n"
    "v_fma_f32 v148, v148, v249, v249\n"
    "v_mfma_f32_16x16x32_bf16 v[164:167], v[196:199], v[28:31], v[164:167]\n"
    "ds_read_b64_tr_b16 v[186:187], v242 offset:12544\n"
    "v_fma_f32 v149, v149, v249, v249\n"
    "v_mfma_f32_16x16x32_bf16 v[160:163], v[192:195], v[16:19], v[160:163]\n"
    "ds_read_b64_tr_b16 v[188:189], v242 offset:14336\n"
    "ds_read_b64_tr_b16 v[190:191], v242 offset:14592\n"
    "v_mfma_f32_16x16x32_bf16 v[160:163], v[196:199], v[24:27], v[160:163]\n"
    "s_waitcnt lgkmcnt(4)\n"
    "v_rcp_f32 v148, v148\n"
    "v_mfma_f32_16x16x32_bf16 v[160:163], v[200:203], v[32:35], v[160:163]\n"
    "ds_read_b64_tr_b16 v[192:193], v242 offset:16384\n"
    "ds_read_b64_tr_b16 v[194:195], v242 offset:16640\n"
    "v_mfma_f32_16x16x32_bf16 v[160:163], v[204:207], v[40:43], v[160:163]\n"
    "v_rcp_f32 v149, v149\n"
    "v_mfma_f32_16x16x32_bf16 v[164:167], v[200:203], v[36:39], v[164:167]\n"
    "s_add_u32 m0, m0, 0x400\n"
    "ds_read_b64_tr_b16 v[196:197], v242 offset:18432\n"
    "ds_read_b64_tr_b16 v[198:199], v242 offset:18688\n"
    "v_mfma_f32_16x16x32_bf16 v[164:167], v[204:207], v[44:47], v[164:167]\n"
    "s_waitcnt lgkmcnt(4)\n"
    "buffer_load_dwordx4 v246, s[44:47], s69 offen lds\n"
    "v_mfma_f32_16x16x32_bf16 v[164:167], v[184:187], v[52:55], v[164:167]\n"
    "ds_read_b64_tr_b16 v[200:201], v242 offset:20480\n"
    "ds_read_b64_tr_b16 v[202:203], v242 offset:20736\n"
    "v_mfma_f32_16x16x32_bf16 v[164:167], v[188:191], v[60:63], v[164:167]\n"
    "v_exp_f32 v150, v150\n"
    "v_mfma_f32_16x16x32_bf16 v[160:163], v[184:187], v[48:51], v[160:163]\n"
    "ds_read_b64_tr_b16 v[204:205], v242 offset:22528\n"
    "ds_read_b64_tr_b16 v[206:207], v242 offset:22784\n"
    "v_mfma_f32_16x16x32_bf16 v[160:163], v[188:191], v[56:59], v[160:163]\n"
    "s_waitcnt lgkmcnt(4)\n"
    "v_exp_f32 v151, v151\n"
    "v_mfma_f32_16x16x32_bf16 v[160:163], v[192:195], v[64:67], v[160:163]\n"
    "ds_read_b64_tr_b16 v[184:185], v242 offset:24576\n"
    "v_fma_f32 v150, v150, v249, v249\n"
    "v_mfma_f32_16x16x32_bf16 v[160:163], v[196:199], v[72:75], v[160:163]\n"
    "ds_read_b64_tr_b16 v[186:187], v242 offset:24832\n"
    "v_fma_f32 v151, v151, v249, v249\n"
    "v_mfma_f32_16x16x32_bf16 v[164:167], v[192:195], v[68:71], v[164:167]\n"
    "s_add_u32 m0, m0, 0x400\n"
    "ds_read_b64_tr_b16 v[188:189], v242 offset:26624\n"
    "ds_read_b64_tr_b16 v[190:191], v242 offset:26880\n"
    "v_mfma_f32_16x16x32_bf16 v[164:167], v[196:199], v[76:79], v[164:167]\n"
    "s_waitcnt lgkmcnt(4)\n"
    "buffer_load_dwordx4 v246, s[44:47], s70 offen lds\n"
    "v_mfma_f32_16x16x32_bf16 v[164:167], v[200:203], v[84:87], v[164:167]\n"
    "ds_read_b64_tr_b16 v[192:193], v242 offset:28672\n"
    "ds_read_b64_tr_b16 v[194:195], v242 offset:28928\n"
    "v_mfma_f32_16x16x32_bf16 v[164:167], v[204:207], v[92:95], v[164:167]\n"
    "v_rcp_f32 v150, v150\n"
    "v_mfma_f32_16x16x32_bf16 v[160:163], v[200:203], v[80:83], v[160:163]\n"
    "ds_read_b64_tr_b16 v[196:197], v242 offset:30720\n"
    "ds_read_b64_tr_b16 v[198:199], v242 offset:30976\n"
    "v_mfma_f32_16x16x32_bf16 v[160:163], v[204:207], v[88:91], v[160:163]\n"
    "s_waitcnt lgkmcnt(4)\n"
    "v_rcp_f32 v151, v151\n"
    "v_mfma_f32_16x16x32_bf16 v[160:163], v[184:187], v[96:99], v[160:163]\n"
    "ds_read_b64_tr_b16 v[200:201], v242 offset:32768\n"
    "ds_read_b64_tr_b16 v[202:203], v242 offset:33024\n"
    "v_mfma_f32_16x16x32_bf16 v[160:163], v[188:191], v[104:107], v[160:163]\n"
    "v_exp_f32 v152, v152\n"
    "v_mfma_f32_16x16x32_bf16 v[164:167], v[184:187], v[100:103], v[164:167]\n"
    "s_add_u32 m0, m0, 0x400\n"
    "ds_read_b64_tr_b16 v[204:205], v242 offset:34816\n"
    "ds_read_b64_tr_b16 v[206:207], v242 offset:35072\n"
    "v_mfma_f32_16x16x32_bf16 v[164:167], v[188:191], v[108:111], v[164:167]\n"
    "s_waitcnt lgkmcnt(4)\n"
    "buffer_load_dwordx4 v246, s[44:47], s71 offen lds\n"
    "v_mfma_f32_16x16x32_bf16 v[164:167], v[192:195], v[116:119], v[164:167]\n"
    "ds_read_b64_tr_b16 v[184:185], v243 offset:0\n"
    "v_add_u32 v242, s56, v241\n"
    "v_mfma_f32_16x16x32_bf16 v[164:167], v[196:199], v[124:127], v[164:167]\n"
    "ds_read_b64_tr_b16 v[186:187], v243 offset:256\n"
    "v_xor_b32 v244, 64, v242\n"
    "v_mfma_f32_16x16x32_bf16 v[160:163], v[192:195], v[112:115], v[160:163]\n"
    "ds_read_b64_tr_b16 v[188:189], v243 offset:2048\n"
    "v_xor_b32 v245, 0x60, v242\n"
    "v_mfma_f32_16x16x32_bf16 v[160:163], v[196:199], v[120:123], v[160:163]\n"
    "s_waitcnt lgkmcnt(3)\n"
    "ds_read_b64_tr_b16 v[190:191], v243 offset:2304\n"
    "v_mfma_f32_16x16x32_bf16 v[160:163], v[200:203], v[128:131], v[160:163]\n"
    "ds_read_b64_tr_b16 v[192:193], v243 offset:4096\n"
    "ds_read_b64_tr_b16 v[194:195], v243 offset:4352\n"
    "v_mfma_f32_16x16x32_bf16 v[160:163], v[204:207], v[136:139], v[160:163]\n"
    "v_exp_f32 v153, v153\n"
    "v_mfma_f32_16x16x32_bf16 v[164:167], v[200:203], v[132:135], v[164:167]\n"
    "s_add_u32 m0, m0, 0x400\n"
    "ds_read_b64_tr_b16 v[196:197], v243 offset:6144\n"
    "ds_read_b64_tr_b16 v[198:199], v243 offset:6400\n"
    "v_mfma_f32_16x16x32_bf16 v[164:167], v[204:207], v[140:143], v[164:167]\n"
    "s_waitcnt lgkmcnt(4)\n"
    "buffer_load_dwordx4 v246, s[44:47], s72 offen lds\n"
    "v_mfma_f32_16x16x32_bf16 v[172:175], v[184:187], v[4:7], 0\n"
    "ds_read_b64_tr_b16 v[200:201], v243 offset:8192\n"
    "v_fma_f32 v152, v152, v248, v248\n"
    "v_mfma_f32_16x16x32_bf16 v[172:175], v[188:191], v[12:15], v[172:175]\n"
    "ds_read_b64_tr_b16 v[202:203], v243 offset:8448\n"
    "v_fma_f32 v153, v153, v248, v248\n"
    "v_mfma_f32_16x16x32_bf16 v[168:171], v[184:187], v[0:3], 0\n"
    "ds_read_b64_tr_b16 v[204:205], v243 offset:10240\n"
    "ds_read_b64_tr_b16 v[206:207], v243 offset:10496\n"
    "v_mfma_f32_16x16x32_bf16 v[168:171], v[188:191], v[8:11], v[168:171]\n"
    "s_waitcnt lgkmcnt(4)\n"
    "v_rcp_f32 v152, v152\n"
    "v_mfma_f32_16x16x32_bf16 v[168:171], v[192:195], v[16:19], v[168:171]\n"
    "ds_read_b64_tr_b16 v[184:185], v243 offset:12288\n"
    "ds_read_b64_tr_b16 v[186:187], v243 offset:12544\n"
    "v_mfma_f32_16x16x32_bf16 v[168:171], v[196:199], v[24:27], v[168:171]\n"
    "v_rcp_f32 v153, v153\n"
    "v_mfma_f32_16x16x32_bf16 v[172:175], v[192:195], v[20:23], v[172:175]\n"
    "s_add_u32 m0, m0, 0x400\n"
    "ds_read_b64_tr_b16 v[188:189], v243 offset:14336\n"
    "ds_read_b64_tr_b16 v[190:191], v243 offset:14592\n"
    "v_mfma_f32_16x16x32_bf16 v[172:175], v[196:199], v[28:31], v[172:175]\n"
    "s_waitcnt lgkmcnt(4)\n"
    "buffer_load_dwordx4 v246, s[44:47], s73 offen lds\n"
    "v_mfma_f32_16x16x32_bf16 v[172:175], v[200:203], v[36:39], v[172:175]\n"
    "ds_read_b64_tr_b16 v[192:193], v243 offset:16384\n"
    "ds_read_b64_tr_b16 v[194:195], v243 offset:16640\n"
    "v_mfma_f32_16x16x32_bf16 v[172:175], v[204:207], v[44:47], v[172:175]\n"
    "v_exp_f32 v154, v154\n"
    "v_mfma_f32_16x16x32_bf16 v[168:171], v[200:203], v[32:35], v[168:171]\n"
    "ds_read_b64_tr_b16 v[196:197], v243 offset:18432\n"
    "ds_read_b64_tr_b16 v[198:199], v243 offset:18688\n"
    "v_mfma_f32_16x16x32_bf16 v[168:171], v[204:207], v[40:43], v[168:171]\n"
    "s_waitcnt lgkmcnt(4)\n"
    "v_exp_f32 v155, v155\n"
    "v_mfma_f32_16x16x32_bf16 v[168:171], v[184:187], v[48:51], v[168:171]\n"
    "ds_read_b64_tr_b16 v[200:201], v243 offset:20480\n"
    "v_fma_f32 v154, v154, v248, v248\n"
    "v_mfma_f32_16x16x32_bf16 v[168:171], v[188:191], v[56:59], v[168:171]\n"
    "ds_read_b64_tr_b16 v[202:203], v243 offset:20736\n"
    "v_fma_f32 v155, v155, v248, v248\n"
    "v_mfma_f32_16x16x32_bf16 v[172:175], v[184:187], v[52:55], v[172:175]\n"
    "s_add_u32 m0, m0, 0x400\n"
    "ds_read_b64_tr_b16 v[204:205], v243 offset:22528\n"
    "ds_read_b64_tr_b16 v[206:207], v243 offset:22784\n"
    "v_mfma_f32_16x16x32_bf16 v[172:175], v[188:191], v[60:63], v[172:175]\n"
    "s_waitcnt lgkmcnt(4)\n"
    "buffer_load_dwordx4 v246, s[44:47], s74 offen lds\n"
    "v_mfma_f32_16x16x32_bf16 v[172:175], v[192:195], v[68:71], v[172:175]\n"
    "ds_read_b64_tr_b16 v[184:185], v243 offset:24576\n"
    "ds_read_b64_tr_b16 v[186:187], v243 offset:24832\n"
    "v_mfma_f32_16x16x32_bf16 v[172:175], v[196:199], v[76:79], v[172:175]\n"
    "v_rcp_f32 v154, v154\n"
    "v_mfma_f32_16x16x32_bf16 v[168:171], v[192:195], v[64:67], v[168:171]\n"
    "ds_read_b64_tr_b16 v[188:189], v243 offset:26624\n"
    "ds_read_b64_tr_b16 v[190:191], v243 offset:26880\n"
    "v_mfma_f32_16x16x32_bf16 v[168:171], v[196:199], v[72:75], v[168:171]\n"
    "s_waitcnt lgkmcnt(4)\n"
    "v_rcp_f32 v155, v155\n"
    "v_mfma_f32_16x16x32_bf16 v[168:171], v[200:203], v[80:83], v[168:171]\n"
    "ds_read_b64_tr_b16 v[192:193], v243 offset:28672\n"
    "ds_read_b64_tr_b16 v[194:195], v243 offset:28928\n"
    "v_mfma_f32_16x16x32_bf16 v[168:171], v[204:207], v[88:91], v[168:171]\n"
    "v_exp_f32 v156, v156\n"
    "v_mfma_f32_16x16x32_bf16 v[172:175], v[200:203], v[84:87], v[172:175]\n"
    "s_add_u32 m0, m0, 0x400\n"
    "ds_read_b64_tr_b16 v[196:197], v243 offset:30720\n"
    "ds_read_b64_tr_b16 v[198:199], v243 offset:30976\n"
    "v_mfma_f32_16x16x32_bf16 v[172:175], v[204:207], v[92:95], v[172:175]\n"
    "s_waitcnt lgkmcnt(4)\n"
    "buffer_load_dwordx4 v246, s[44:47], s75 offen lds\n"
    "v_mfma_f32_16x16x32_bf16 v[172:175], v[184:187], v[100:103], v[172:175]\n"
    "ds_read_b64_tr_b16 v[200:201], v243 offset:32768\n"
    "ds_read_b64_tr_b16 v[202:203], v243 offset:33024\n"
    "v_mfma_f32_16x16x32_bf16 v[172:175], v[188:191], v[108:111], v[172:175]\n"
    "v_exp_f32 v157, v157\n"
    "v_mfma_f32_16x16x32_bf16 v[168:171], v[184:187], v[96:99], v[168:171]\n"
    "ds_read_b64_tr_b16 v[204:205], v243 offset:34816\n"
    "v_fma_f32 v156, v156, v249, v249\n"
    "v_mfma_f32_16x16x32_bf16 v[168:171], v[188:191], v[104:107], v[168:171]\n"
    "s_waitcnt lgkmcnt(3)\n"
    "ds_read_b64_tr_b16 v[206:207], v243 offset:35072\n"
    "v_fma_f32 v157, v157, v249, v249\n"
    "v_mfma_f32_16x16x32_bf16 v[168:171], v[192:195], v[112:115], v[168:171]\n"
    "ds_read_b64_tr_b16 v[184:185], v242 offset:0\n"
    "v_xor_b32 v243, 32, v242\n"
    "v_mfma_f32_16x16x32_bf16 v[168:171], v[196:199], v[120:123], v[168:171]\n"
    "ds_read_b64_tr_b16 v[186:187], v242 offset:2048\n"
    "v_mfma_f32_16x16x32_bf16 v[172:175], v[192:195], v[116:119], v[172:175]\n"
    "s_add_u32 m0, m0, 0x400\n"
    "ds_read_b64_tr_b16 v[188:189], v243 offset:0\n"
    "ds_read_b64_tr_b16 v[190:191], v243 offset:2048\n"
    "v_mfma_f32_16x16x32_bf16 v[172:175], v[196:199], v[124:127], v[172:175]\n"
    "s_waitcnt vmcnt(8) lgkmcnt(4)\n"
    "s_barrier\n"
    "buffer_load_dwordx4 v246, s[44:47], s76 offen lds\n"
    "v_mfma_f32_16x16x32_bf16 v[172:175], v[200:203], v[132:135], v[172:175]\n"
    "s_add_u32 s44, s44, 64\n"
    "ds_read_b64_tr_b16 v[192:193], v244 offset:0\n"
    "ds_read_b64_tr_b16 v[194:195], v244 offset:2048\n"
    "v_mfma_f32_16x16x32_bf16 v[172:175], v[204:207], v[140:143], v[172:175]\n"
    "s_addc_u32 s45, s45, 0\n"
    "v_rcp_f32 v156, v156\n"
    "v_mfma_f32_16x16x32_bf16 v[168:171], v[200:203], v[128:131], v[168:171]\n"
    "ds_read_b64_tr_b16 v[196:197], v245 offset:0\n"
    "ds_read_b64_tr_b16 v[198:199], v245 offset:2048\n"
    "v_mfma_f32_16x16x32_bf16 v[168:171], v[204:207], v[136:139], v[168:171]\n"
    "s_waitcnt lgkmcnt(4)\n"
    "v_rcp_f32 v157, v157\n"
    "v_mfma_f32_16x16x32_bf16 a[0:3], v[184:187], v[176:179], a[0:3]\n"
    "ds_read_b64_tr_b16 v[200:201], v242 offset:4096\n"
    "ds_read_b64_tr_b16 v[202:203], v242 offset:6144\n"
    "v_mfma_f32_16x16x32_bf16 a[4:7], v[188:191], v[176:179], a[4:7]\n"
    "v_exp_f32 v158, v158\n"
    "v_mfma_f32_16x16x32_bf16 a[8:11], v[184:187], v[180:183], a[8:11]\n"
    "s_add_u32 m0, s55, s61\n"
    "ds_read_b64_tr_b16 v[204:205], v243 offset:4096\n"
    "ds_read_b64_tr_b16 v[206:207], v243 offset:6144\n"
    "v_mfma_f32_16x16x32_bf16 a[12:15], v[188:191], v[180:183], a[12:15]\n"
    "s_waitcnt lgkmcnt(4)\n"
    "buffer_load_dwordx4 v247, s[48:51], s52 offen lds\n"
    "v_mfma_f32_16x16x32_bf16 a[24:27], v[192:195], v[180:183], a[24:27]\n"
    "ds_read_b64_tr_b16 v[184:185], v244 offset:4096\n"
    "ds_read_b64_tr_b16 v[186:187], v244 offset:6144\n"
    "v_mfma_f32_16x16x32_bf16 a[28:31], v[196:199], v[180:183], a[28:31]\n"
    "v_exp_f32 v159, v159\n"
    "v_mfma_f32_16x16x32_bf16 a[16:19], v[192:195], v[176:179], a[16:19]\n"
    "ds_read_b64_tr_b16 v[188:189], v245 offset:4096\n"
    "v_fma_f32 v158, v158, v249, v249\n"
    "v_mfma_f32_16x16x32_bf16 a[20:23], v[196:199], v[176:179], a[20:23]\n"
    "s_waitcnt lgkmcnt(3)\n"
    "ds_read_b64_tr_b16 v[190:191], v245 offset:6144\n"
    "v_fma_f32 v159, v159, v249, v249\n"
    "v_mfma_f32_16x16x32_bf16 a[32:35], v[200:203], v[176:179], a[32:35]\n"
    "ds_read_b64_tr_b16 v[192:193], v242 offset:8192\n"
    "ds_read_b64_tr_b16 v[194:195], v242 offset:10240\n"
    "v_mfma_f32_16x16x32_bf16 a[36:39], v[204:207], v[176:179], a[36:39]\n"
    "v_rcp_f32 v158, v158\n"
    "v_mfma_f32_16x16x32_bf16 a[40:43], v[200:203], v[180:183], a[40:43]\n"
    "s_add_u32 m0, m0, 0x1000\n"
    "ds_read_b64_tr_b16 v[196:197], v243 offset:8192\n"
    "ds_read_b64_tr_b16 v[198:199], v243 offset:10240\n"
    "v_mfma_f32_16x16x32_bf16 a[44:47], v[204:207], v[180:183], a[44:47]\n"
    "s_waitcnt lgkmcnt(4)\n"
    "buffer_load_dwordx4 v247, s[48:51], s53 offen lds\n"
    "v_mfma_f32_16x16x32_bf16 a[56:59], v[184:187], v[180:183], a[56:59]\n"
    "ds_read_b64_tr_b16 v[200:201], v244 offset:8192\n"
    "ds_read_b64_tr_b16 v[202:203], v244 offset:10240\n"
    "v_mfma_f32_16x16x32_bf16 a[60:63], v[188:191], v[180:183], a[60:63]\n"
    "v_rcp_f32 v159, v159\n"
    "v_mfma_f32_16x16x32_bf16 a[48:51], v[184:187], v[176:179], a[48:51]\n"
    "ds_read_b64_tr_b16 v[204:205], v245 offset:8192\n"
    "v_mul_f32 v160, v144, v160\n"
    "v_mfma_f32_16x16x32_bf16 a[52:55], v[188:191], v[176:179], a[52:55]\n"
    "s_waitcnt lgkmcnt(3)\n"
    "ds_read_b64_tr_b16 v[206:207], v245 offset:10240\n"
    "v_mul_f32 v161, v145, v161\n"
    "v_mfma_f32_16x16x32_bf16 a[64:67], v[192:195], v[176:179], a[64:67]\n"
    "ds_read_b64_tr_b16 v[184:185], v242 offset:12288\n"
    "v_mul_f32 v162, v146, v162\n"
    "v_mfma_f32_16x16x32_bf16 a[68:71], v[196:199], v[176:179], a[68:71]\n"
    "ds_read_b64_tr_b16 v[186:187], v242 offset:14336\n"
    "v_mul_f32 v163, v147, v163\n"
    "v_mfma_f32_16x16x32_bf16 a[72:75], v[192:195], v[180:183], a[72:75]\n"
    "s_add_u32 m0, m0, 0x1000\n"
    "ds_read_b64_tr_b16 v[188:189], v243 offset:12288\n"
    "ds_read_b64_tr_b16 v[190:191], v243 offset:14336\n"
    "v_mfma_f32_16x16x32_bf16 a[76:79], v[196:199], v[180:183], a[76:79]\n"
    "s_waitcnt lgkmcnt(4)\n"
    "buffer_load_dwordx4 v247, s[48:51], s58 offen lds\n"
    "v_mfma_f32_16x16x32_bf16 a[88:91], v[200:203], v[180:183], a[88:91]\n"
    "ds_read_b64_tr_b16 v[192:193], v244 offset:12288\n"
    "v_mul_f32 v164, v148, v164\n"
    "v_mfma_f32_16x16x32_bf16 a[92:95], v[204:207], v[180:183], a[92:95]\n"
    "ds_read_b64_tr_b16 v[194:195], v244 offset:14336\n"
    "v_mul_f32 v165, v149, v165\n"
    "v_mfma_f32_16x16x32_bf16 a[80:83], v[200:203], v[176:179], a[80:83]\n"
    "ds_read_b64_tr_b16 v[196:197], v245 offset:12288\n"
    "v_mul_f32 v166, v150, v166\n"
    "v_mfma_f32_16x16x32_bf16 a[84:87], v[204:207], v[176:179], a[84:87]\n"
    "s_waitcnt lgkmcnt(3)\n"
    "ds_read_b64_tr_b16 v[198:199], v245 offset:14336\n"
    "v_mul_f32 v167, v151, v167\n"
    "v_mfma_f32_16x16x32_bf16 a[96:99], v[184:187], v[176:179], a[96:99]\n"
    "ds_read_b64_tr_b16 v[200:201], v242 offset:16384\n"
    "v_mul_f32 v168, v152, v168\n"
    "v_mfma_f32_16x16x32_bf16 a[100:103], v[188:191], v[176:179], a[100:103]\n"
    "ds_read_b64_tr_b16 v[202:203], v242 offset:18432\n"
    "v_mul_f32 v169, v153, v169\n"
    "v_mfma_f32_16x16x32_bf16 a[104:107], v[184:187], v[180:183], a[104:107]\n"
    "s_add_u32 m0, m0, 0x1000\n"
    "ds_read_b64_tr_b16 v[204:205], v243 offset:16384\n"
    "ds_read_b64_tr_b16 v[206:207], v243 offset:18432\n"
    "v_mfma_f32_16x16x32_bf16 a[108:111], v[188:191], v[180:183], a[108:111]\n"
    "s_waitcnt lgkmcnt(4)\n"
    "buffer_load_dwordx4 v247, s[48:51], s59 offen lds\n"
    "v_mfma_f32_16x16x32_bf16 a[120:123], v[192:195], v[180:183], a[120:123]\n"
    "ds_read_b64_tr_b16 v[184:185], v244 offset:16384\n"
    "v_mul_f32 v170, v154, v170\n"
    "v_mfma_f32_16x16x32_bf16 a[124:127], v[196:199], v[180:183], a[124:127]\n"
    "ds_read_b64_tr_b16 v[186:187], v244 offset:18432\n"
    "v_mul_f32 v171, v155, v171\n"
    "v_mfma_f32_16x16x32_bf16 a[112:115], v[192:195], v[176:179], a[112:115]\n"
    "ds_read_b64_tr_b16 v[188:189], v245 offset:16384\n"
    "v_mul_f32 v172, v156, v172\n"
    "v_mfma_f32_16x16x32_bf16 a[116:119], v[196:199], v[176:179], a[116:119]\n"
    "s_waitcnt lgkmcnt(3)\n"
    "ds_read_b64_tr_b16 v[190:191], v245 offset:18432\n"
    "v_mul_f32 v173, v157, v173\n"
    "v_mfma_f32_16x16x32_bf16 a[128:131], v[200:203], v[176:179], a[128:131]\n"
    "ds_read_b64_tr_b16 v[192:193], v242 offset:20480\n"
    "v_mul_f32 v174, v158, v174\n"
    "v_mfma_f32_16x16x32_bf16 a[132:135], v[204:207], v[176:179], a[132:135]\n"
    "ds_read_b64_tr_b16 v[194:195], v242 offset:22528\n"
    "v_mul_f32 v175, v159, v175\n"
    "v_mfma_f32_16x16x32_bf16 a[136:139], v[200:203], v[180:183], a[136:139]\n"
    "s_add_u32 m0, m0, 0x1000\n"
    "ds_read_b64_tr_b16 v[196:197], v243 offset:20480\n"
    "ds_read_b64_tr_b16 v[198:199], v243 offset:22528\n"
    "v_mfma_f32_16x16x32_bf16 a[140:143], v[204:207], v[180:183], a[140:143]\n"
    "s_waitcnt lgkmcnt(4)\n"
    "buffer_load_dwordx4 v247, s[48:51], s63 offen lds\n"
    "v_mfma_f32_16x16x32_bf16 a[152:155], v[184:187], v[180:183], a[152:155]\n"
    "ds_read_b64_tr_b16 v[200:201], v244 offset:20480\n"
    "v_mfma_f32_16x16x32_bf16 a[156:159], v[188:191], v[180:183], a[156:159]\n"
    "ds_read_b64_tr_b16 v[202:203], v244 offset:22528\n"
    "v_mfma_f32_16x16x32_bf16 a[144:147], v[184:187], v[176:179], a[144:147]\n"
    "ds_read_b64_tr_b16 v[204:205], v245 offset:20480\n"
    "v_mfma_f32_16x16x32_bf16 a[148:151], v[188:191], v[176:179], a[148:151]\n"
    "s_waitcnt lgkmcnt(3)\n"
    "ds_read_b64_tr_b16 v[206:207], v245 offset:22528\n"
    "v_mfma_f32_16x16x32_bf16 a[160:163], v[192:195], v[176:179], a[160:163]\n"
    "ds_read_b64_tr_b16 v[184:185], v242 offset:24576\n"
    "v_mfma_f32_16x16x32_bf16 a[164:167], v[196:199], v[176:179], a[164:167]\n"
    "ds_read_b64_tr_b16 v[186:187], v242 offset:26624\n"
    "v_mfma_f32_16x16x32_bf16 a[168:171], v[192:195], v[180:183], a[168:171]\n"
    "s_add_u32 m0, m0, 0x1000\n"
    "ds_read_b64_tr_b16 v[188:189], v243 offset:24576\n"
    "ds_read_b64_tr_b16 v[190:191], v243 offset:26624\n"
    "v_mfma_f32_16x16x32_bf16 a[172:175], v[196:199], v[180:183], a[172:175]\n"
    "s_waitcnt lgkmcnt(4)\n"
    "buffer_load_dwordx4 v247, s[48:51], s64 offen lds\n"
    "v_mfma_f32_16x16x32_bf16 a[184:187], v[200:203], v[180:183], a[184:187]\n"
    "ds_read_b64_tr_b16 v[192:193], v244 offset:24576\n"
    "v_mfma_f32_16x16x32_bf16 a[188:191], v[204:207], v[180:183], a[188:191]\n"
    "ds_read_b64_tr_b16 v[194:195], v244 offset:26624\n"
    "v_mfma_f32_16x16x32_bf16 a[176:179], v[200:203], v[176:179], a[176:179]\n"
    "ds_read_b64_tr_b16 v[196:197], v245 offset:24576\n"
    "v_mfma_f32_16x16x32_bf16 a[180:183], v[204:207], v[176:179], a[180:183]\n"
    "s_waitcnt lgkmcnt(3)\n"
    "ds_read_b64_tr_b16 v[198:199], v245 offset:26624\n"
    "v_mfma_f32_16x16x32_bf16 a[192:195], v[184:187], v[176:179], a[192:195]\n"
    "ds_read_b64_tr_b16 v[200:201], v242 offset:28672\n"
    "v_mfma_f32_16x16x32_bf16 a[196:199], v[188:191], v[176:179], a[196:199]\n"
    "ds_read_b64_tr_b16 v[202:203], v242 offset:30720\n"
    "v_mfma_f32_16x16x32_bf16 a[200:203], v[184:187], v[180:183], a[200:203]\n"
    "s_add_u32 m0, m0, 0x1000\n"
    "ds_read_b64_tr_b16 v[204:205], v243 offset:28672\n"
    "ds_read_b64_tr_b16 v[206:207], v243 offset:30720\n"
    "v_mfma_f32_16x16x32_bf16 a[204:207], v[188:191], v[180:183], a[204:207]\n"
    "s_waitcnt lgkmcnt(4)\n"
    "buffer_load_dwordx4 v247, s[48:51], s65 offen lds\n"
    "v_mfma_f32_16x16x32_bf16 a[216:219], v[192:195], v[180:183], a[216:219]\n"
    "ds_read_b64_tr_b16 v[184:185], v244 offset:28672\n"
    "v_mfma_f32_16x16x32_bf16 a[220:223], v[196:199], v[180:183], a[220:223]\n"
    "ds_read_b64_tr_b16 v[186:187], v244 offset:30720\n"
    "v_mfma_f32_16x16x32_bf16 a[208:211], v[192:195], v[176:179], a[208:211]\n"
    "ds_read_b64_tr_b16 v[188:189], v245 offset:28672\n"
    "v_mfma_f32_16x16x32_bf16 a[212:215], v[196:199], v[176:179], a[212:215]\n"
    "s_waitcnt lgkmcnt(3)\n"
    "ds_read_b64_tr_b16 v[190:191], v245 offset:30720\n"
    "v_mfma_f32_16x16x32_bf16 a[224:227], v[200:203], v[176:179], a[224:227]\n"
    "ds_read_b64_tr_b16 v[192:193], v242 offset:32768\n"
    "v_mfma_f32_16x16x32_bf16 a[228:231], v[204:207], v[176:179], a[228:231]\n"
    "ds_read_b64_tr_b16 v[194:195], v242 offset:34816\n"
    "v_mfma_f32_16x16x32_bf16 a[232:235], v[200:203], v[180:183], a[232:235]\n"
    "s_add_u32 m0, m0, 0x1000\n"
    "ds_read_b64_tr_b16 v[196:197], v243 offset:32768\n"
    "ds_read_b64_tr_b16 v[198:199], v243 offset:34816\n"
    "v_mfma_f32_16x16x32_bf16 a[236:239], v[204:207], v[180:183], a[236:239]\n"
    "s_waitcnt lgkmcnt(4)\n"
    "buffer_load_dwordx4 v247, s[48:51], s77 offen lds\n"
    "v_mfma_f32_16x16x32_bf16 a[248:251], v[184:187], v[180:183], a[248:251]\n"
    "ds_read_b64_tr_b16 v[200:201], v244 offset:32768\n"
    "v_add_u32 v242, s57, v240\n"
    "v_mfma_f32_16x16x32_bf16 a[252:255], v[188:191], v[180:183], a[252:255]\n"
    "ds_read_b64_tr_b16 v[202:203], v244 offset:34816\n"
    "v_xor_b32 v243, 32, v242\n"
    "v_mfma_f32_16x16x32_bf16 a[240:243], v[184:187], v[176:179], a[240:243]\n"
    "ds_read_b64_tr_b16 v[204:205], v245 offset:32768\n"
    "v_mfma_f32_16x16x32_bf16 a[244:247], v[188:191], v[176:179], a[244:247]\n"
    "s_waitcnt lgkmcnt(3)\n"
    "ds_read_b64_tr_b16 v[206:207], v245 offset:34816\n"
    "v_mfma_f32_16x16x32_bf16 v[208:211], v[192:195], v[176:179], v[208:211]\n"
    "s_mov_b32 s67, s56\n"
    "ds_read_b64_tr_b16 v[184:185], v242 offset:0\n"
    "v_mfma_f32_16x16x32_bf16 v[212:215], v[196:199], v[176:179], v[212:215]\n"
    "s_mov_b32 s56, s55\n"
    "ds_read_b64_tr_b16 v[186:187], v242 offset:256\n"
    "v_mfma_f32_16x16x32_bf16 v[216:219], v[192:195], v[180:183], v[216:219]\n"
    "s_add_u32 m0, m0, 0x1000\n"
    "ds_read_b64_tr_b16 v[188:189], v242 offset:2048\n"
    "ds_read_b64_tr_b16 v[190:191], v242 offset:2304\n"
    "v_mfma_f32_16x16x32_bf16 v[220:223], v[196:199], v[180:183], v[220:223]\n"
    "s_mov_b32 s55, s54\n"
    "s_waitcnt vmcnt(8) lgkmcnt(4)\n"
    "s_barrier\n"
    "buffer_load_dwordx4 v247, s[48:51], s79 offen lds\n"
    "v_mfma_f32_16x16x32_bf16 v[232:235], v[200:203], v[180:183], v[232:235]\n"
    "s_add_u32 s48, s48, 36864\n"
    "ds_read_b64_tr_b16 v[192:193], v242 offset:4096\n"
    "v_mfma_f32_16x16x32_bf16 v[236:239], v[204:207], v[180:183], v[236:239]\n"
    "s_addc_u32 s49, s49, 0\n"
    "ds_read_b64_tr_b16 v[194:195], v242 offset:4352\n"
    "v_mfma_f32_16x16x32_bf16 v[224:227], v[200:203], v[176:179], v[224:227]\n"
    "s_mov_b32 s54, s57\n"
    "ds_read_b64_tr_b16 v[196:197], v242 offset:6144\n"
    "v_mfma_f32_16x16x32_bf16 v[228:231], v[204:207], v[176:179], v[228:231]\n"
    "s_mov_b32 s57, s67\n"
    "s_waitcnt lgkmcnt(3)\n"
    "ds_read_b64_tr_b16 v[198:199], v242 offset:6400\n"
    "v_mfma_f32_16x16x32_bf16 v[144:147], v[184:187], v[0:3], 0\n"
    "ds_read_b64_tr_b16 v[200:201], v242 offset:8192\n"
    "v_cvt_pk_bf16_f32 v176, v160, v161\n"
    "v_mfma_f32_16x16x32_bf16 v[144:147], v[188:191], v[8:11], v[144:147]\n"
    "ds_read_b64_tr_b16 v[202:203], v242 offset:8448\n"
    "v_cvt_pk_bf16_f32 v177, v162, v163\n"
    "v_mfma_f32_16x16x32_bf16 v[148:151], v[184:187], v[4:7], 0\n"
    "s_add_u32 m0, s57, s60\n"
    "ds_read_b64_tr_b16 v[204:205], v242 offset:10240\n"
    "ds_read_b64_tr_b16 v[206:207], v242 offset:10496\n"
    "v_mfma_f32_16x16x32_bf16 v[148:151], v[188:191], v[12:15], v[148:151]\n"
    "s_waitcnt lgkmcnt(4)\n"
    "buffer_load_dwordx4 v246, s[40:43], s68 offen lds\n"
    "v_mfma_f32_16x16x32_bf16 v[148:151], v[192:195], v[20:23], v[148:151]\n"
    "ds_read_b64_tr_b16 v[184:185], v242 offset:12288\n"
    "v_cvt_pk_bf16_f32 v180, v164, v165\n"
    "v_mfma_f32_16x16x32_bf16 v[148:151], v[196:199], v[28:31], v[148:151]\n"
    "ds_read_b64_tr_b16 v[186:187], v242 offset:12544\n"
    "v_cvt_pk_bf16_f32 v181, v166, v167\n"
    "v_mfma_f32_16x16x32_bf16 v[144:147], v[192:195], v[16:19], v[144:147]\n"
    "ds_read_b64_tr_b16 v[188:189], v242 offset:14336\n"
    "v_cvt_pk_bf16_f32 v178, v168, v169\n"
    "v_mfma_f32_16x16x32_bf16 v[144:147], v[196:199], v[24:27], v[144:147]\n"
    "s_waitcnt lgkmcnt(3)\n"
    "ds_read_b64_tr_b16 v[190:191], v242 offset:14592\n"
    "v_cvt_pk_bf16_f32 v179, v170, v171\n"
    "v_mfma_f32_16x16x32_bf16 v[144:147], v[200:203], v[32:35], v[144:147]\n"
    "ds_read_b64_tr_b16 v[192:193], v242 offset:16384\n"
    "v_cvt_pk_bf16_f32 v182, v172, v173\n"
    "v_mfma_f32_16x16x32_bf16 v[144:147], v[204:207], v[40:43], v[144:147]\n"
    "ds_read_b64_tr_b16 v[194:195], v242 offset:16640\n"
    "v_cvt_pk_bf16_f32 v183, v174, v175\n"
    "v_mfma_f32_16x16x32_bf16 v[148:151], v[200:203], v[36:39], v[148:151]\n"
    "s_add_u32 m0, m0, 0x400\n"
    "ds_read_b64_tr_b16 v[196:197], v242 offset:18432\n"
    "ds_read_b64_tr_b16 v[198:199], v242 offset:18688\n"
    "v_mfma_f32_16x16x32_bf16 v[148:151], v[204:207], v[44:47], v[148:151]\n"
    "s_waitcnt lgkmcnt(4)\n"
    "buffer_load_dwordx4 v246, s[40:43], s69 offen lds\n"
    "v_mfma_f32_16x16x32_bf16 v[148:151], v[184:187], v[52:55], v[148:151]\n"
    "ds_read_b64_tr_b16 v[200:201], v242 offset:20480\n"
    "v_mfma_f32_16x16x32_bf16 v[148:151], v[188:191], v[60:63], v[148:151]\n"
    "ds_read_b64_tr_b16 v[202:203], v242 offset:20736\n"
    "v_mfma_f32_16x16x32_bf16 v[144:147], v[184:187], v[48:51], v[144:147]\n"
    "ds_read_b64_tr_b16 v[204:205], v242 offset:22528\n"
    "v_mfma_f32_16x16x32_bf16 v[144:147], v[188:191], v[56:59], v[144:147]\n"
    "s_waitcnt lgkmcnt(3)\n"
    "ds_read_b64_tr_b16 v[206:207], v242 offset:22784\n"
    "v_mfma_f32_16x16x32_bf16 v[144:147], v[192:195], v[64:67], v[144:147]\n"
    "ds_read_b64_tr_b16 v[184:185], v242 offset:24576\n"
    "v_mfma_f32_16x16x32_bf16 v[144:147], v[196:199], v[72:75], v[144:147]\n"
    "ds_read_b64_tr_b16 v[186:187], v242 offset:24832\n"
    "v_mfma_f32_16x16x32_bf16 v[148:151], v[192:195], v[68:71], v[148:151]\n"
    "s_add_u32 m0, m0, 0x400\n"
    "ds_read_b64_tr_b16 v[188:189], v242 offset:26624\n"
    "ds_read_b64_tr_b16 v[190:191], v242 offset:26880\n"
    "v_mfma_f32_16x16x32_bf16 v[148:151], v[196:199], v[76:79], v[148:151]\n"
    "s_waitcnt lgkmcnt(4)\n"
    "buffer_load_dwordx4 v246, s[40:43], s70 offen lds\n"
    "v_mfma_f32_16x16x32_bf16 v[148:151], v[200:203], v[84:87], v[148:151]\n"
    "ds_read_b64_tr_b16 v[192:193], v242 offset:28672\n"
    "v_mfma_f32_16x16x32_bf16 v[148:151], v[204:207], v[92:95], v[148:151]\n"
    "ds_read_b64_tr_b16 v[194:195], v242 offset:28928\n"
    "v_mfma_f32_16x16x32_bf16 v[144:147], v[200:203], v[80:83], v[144:147]\n"
    "ds_read_b64_tr_b16 v[196:197], v242 offset:30720\n"
    "v_mfma_f32_16x16x32_bf16 v[144:147], v[204:207], v[88:91], v[144:147]\n"
    "s_waitcnt lgkmcnt(3)\n"
    "ds_read_b64_tr_b16 v[198:199], v242 offset:30976\n"
    "v_mfma_f32_16x16x32_bf16 v[144:147], v[184:187], v[96:99], v[144:147]\n"
    "ds_read_b64_tr_b16 v[200:201], v242 offset:32768\n"
    "v_mfma_f32_16x16x32_bf16 v[144:147], v[188:191], v[104:107], v[144:147]\n"
    "ds_read_b64_tr_b16 v[202:203], v242 offset:33024\n"
    "v_mfma_f32_16x16x32_bf16 v[148:151], v[184:187], v[100:103], v[148:151]\n"
    "s_add_u32 m0, m0, 0x400\n"
    "ds_read_b64_tr_b16 v[204:205], v242 offset:34816\n"
    "ds_read_b64_tr_b16 v[206:207], v242 offset:35072\n"
    "v_mfma_f32_16x16x32_bf16 v[148:151], v[188:191], v[108:111], v[148:151]\n"
    "s_waitcnt lgkmcnt(4)\n"
    "buffer_load_dwordx4 v246, s[40:43], s71 offen lds\n"
    "v_mfma_f32_16x16x32_bf16 v[148:151], v[192:195], v[116:119], v[148:151]\n"
    "ds_read_b64_tr_b16 v[184:185], v243 offset:0\n"
    "v_add_u32 v242, s55, v240\n"
    "v_mfma_f32_16x16x32_bf16 v[148:151], v[196:199], v[124:127], v[148:151]\n"
    "ds_read_b64_tr_b16 v[186:187], v243 offset:256\n"
    "v_mfma_f32_16x16x32_bf16 v[144:147], v[192:195], v[112:115], v[144:147]\n"
    "ds_read_b64_tr_b16 v[188:189], v243 offset:2048\n"
    "v_mfma_f32_16x16x32_bf16 v[144:147], v[196:199], v[120:123], v[144:147]\n"
    "s_waitcnt lgkmcnt(3)\n"
    "ds_read_b64_tr_b16 v[190:191], v243 offset:2304\n"
    "v_mfma_f32_16x16x32_bf16 v[144:147], v[200:203], v[128:131], v[144:147]\n"
    "ds_read_b64_tr_b16 v[192:193], v243 offset:4096\n"
    "v_mfma_f32_16x16x32_bf16 v[144:147], v[204:207], v[136:139], v[144:147]\n"
    "ds_read_b64_tr_b16 v[194:195], v243 offset:4352\n"
    "v_mfma_f32_16x16x32_bf16 v[148:151], v[200:203], v[132:135], v[148:151]\n"
    "s_add_u32 m0, m0, 0x400\n"
    "ds_read_b64_tr_b16 v[196:197], v243 offset:6144\n"
    "ds_read_b64_tr_b16 v[198:199], v243 offset:6400\n"
    "v_mfma_f32_16x16x32_bf16 v[148:151], v[204:207], v[140:143], v[148:151]\n"
    "s_waitcnt lgkmcnt(4)\n"
    "buffer_load_dwordx4 v246, s[40:43], s72 offen lds\n"
    "v_mfma_f32_16x16x32_bf16 v[156:159], v[184:187], v[4:7], 0\n"
    "ds_read_b64_tr_b16 v[200:201], v243 offset:8192\n"
    "v_mfma_f32_16x16x32_bf16 v[156:159], v[188:191], v[12:15], v[156:159]\n"
    "ds_read_b64_tr_b16 v[202:203], v243 offset:8448\n"
    "v_mfma_f32_16x16x32_bf16 v[152:155], v[184:187], v[0:3], 0\n"
    "ds_read_b64_tr_b16 v[204:205], v243 offset:10240\n"
    "v_mfma_f32_16x16x32_bf16 v[152:155], v[188:191], v[8:11], v[152:155]\n"
    "s_waitcnt lgkmcnt(3)\n"
    "ds_read_b64_tr_b16 v[206:207], v243 offset:10496\n"
    "v_mfma_f32_16x16x32_bf16 v[152:155], v[192:195], v[16:19], v[152:155]\n"
    "ds_read_b64_tr_b16 v[184:185], v243 offset:12288\n"
    "ds_read_b64_tr_b16 v[186:187], v243 offset:12544\n"
    "v_mfma_f32_16x16x32_bf16 v[152:155], v[196:199], v[24:27], v[152:155]\n"
    "v_exp_f32 v144, v144\n"
    "v_mfma_f32_16x16x32_bf16 v[156:159], v[192:195], v[20:23], v[156:159]\n"
    "s_add_u32 m0, m0, 0x400\n"
    "ds_read_b64_tr_b16 v[188:189], v243 offset:14336\n"
    "ds_read_b64_tr_b16 v[190:191], v243 offset:14592\n"
    "v_mfma_f32_16x16x32_bf16 v[156:159], v[196:199], v[28:31], v[156:159]\n"
    "s_waitcnt lgkmcnt(4)\n"
    "buffer_load_dwordx4 v246, s[40:43], s73 offen lds\n"
    "v_mfma_f32_16x16x32_bf16 v[156:159], v[200:203], v[36:39], v[156:159]\n"
    "ds_read_b64_tr_b16 v[192:193], v243 offset:16384\n"
    "ds_read_b64_tr_b16 v[194:195], v243 offset:16640\n"
    "v_mfma_f32_16x16x32_bf16 v[156:159], v[204:207], v[44:47], v[156:159]\n"
    "v_exp_f32 v145, v145\n"
    "v_mfma_f32_16x16x32_bf16 v[152:155], v[200:203], v[32:35], v[152:155]\n"
    "ds_read_b64_tr_b16 v[196:197], v243 offset:18432\n"
    "v_fma_f32 v144, v144, v248, v248\n"
    "v_mfma_f32_16x16x32_bf16 v[152:155], v[204:207], v[40:43], v[152:155]\n"
    "s_waitcnt lgkmcnt(3)\n"
    "ds_read_b64_tr_b16 v[198:199], v243 offset:18688\n"
    "v_fma_f32 v145, v145, v248, v248\n"
    "v_mfma_f32_16x16x32_bf16 v[152:155], v[184:187], v[48:51], v[152:155]\n"
    "ds_read_b64_tr_b16 v[200:201], v243 offset:20480\n"
    "ds_read_b64_tr_b16 v[202:203], v243 offset:20736\n"
    "v_mfma_f32_16x16x32_bf16 v[152:155], v[188:191], v[56:59], v[152:155]\n"
    "v_rcp_f32 v144, v144\n"
    "v_mfma_f32_16x16x32_bf16 v[156:159], v[184:187], v[52:55], v[156:159]\n"
    "s_add_u32 m0, m0, 0x400\n"
    "ds_read_b64_tr_b16 v[204:205], v243 offset:22528\n"
    "ds_read_b64_tr_b16 v[206:207], v243 offset:22784\n"
    "v_mfma_f32_16x16x32_bf16 v[156:159], v[188:191], v[60:63], v[156:159]\n"
    "s_waitcnt lgkmcnt(4)\n"
    "buffer_load_dwordx4 v246, s[40:43], s74 offen lds\n"
    "v_mfma_f32_16x16x32_bf16 v[156:159], v[192:195], v[68:71], v[156:159]\n"
    "ds_read_b64_tr_b16 v[184:185], v243 offset:24576\n"
    "ds_read_b64_tr_b16 v[186:187], v243 offset:24832\n"
    "v_mfma_f32_16x16x32_bf16 v[156:159], v[196:199], v[76:79], v[156:159]\n"
    "v_rcp_f32 v145, v145\n"
    "v_mfma_f32_16x16x32_bf16 v[152:155], v[192:195], v[64:67], v[152:155]\n"
    "ds_read_b64_tr_b16 v[188:189], v243 offset:26624\n"
    "ds_read_b64_tr_b16 v[190:191], v243 offset:26880\n"
    "v_mfma_f32_16x16x32_bf16 v[152:155], v[196:199], v[72:75], v[152:155]\n"
    "s_waitcnt lgkmcnt(4)\n"
    "v_exp_f32 v146, v146\n"
    "v_mfma_f32_16x16x32_bf16 v[152:155], v[200:203], v[80:83], v[152:155]\n"
    "ds_read_b64_tr_b16 v[192:193], v243 offset:28672\n"
    "ds_read_b64_tr_b16 v[194:195], v243 offset:28928\n"
    "v_mfma_f32_16x16x32_bf16 v[152:155], v[204:207], v[88:91], v[152:155]\n"
    "v_exp_f32 v147, v147\n"
    "v_mfma_f32_16x16x32_bf16 v[156:159], v[200:203], v[84:87], v[156:159]\n"
    "s_add_u32 m0, m0, 0x400\n"
    "ds_read_b64_tr_b16 v[196:197], v243 offset:30720\n"
    "ds_read_b64_tr_b16 v[198:199], v243 offset:30976\n"
    "v_mfma_f32_16x16x32_bf16 v[156:159], v[204:207], v[92:95], v[156:159]\n"
    "s_waitcnt lgkmcnt(4)\n"
    "buffer_load_dwordx4 v246, s[40:43], s75 offen lds\n"
    "v_mfma_f32_16x16x32_bf16 v[156:159], v[184:187], v[100:103], v[156:159]\n"
    "ds_read_b64_tr_b16 v[200:201], v243 offset:32768\n"
    "v_fma_f32 v146, v146, v248, v248\n"
    "v_mfma_f32_16x16x32_bf16 v[156:159], v[188:191], v[108:111], v[156:159]\n"
    "ds_read_b64_tr_b16 v[202:203], v243 offset:33024\n"
    "v_fma_f32 v147, v147, v248, v248\n"
    "v_mfma_f32_16x16x32_bf16 v[152:155], v[184:187], v[96:99], v[152:155]\n"
    "ds_read_b64_tr_b16 v[204:205], v243 offset:34816\n"
    "ds_read_b64_tr_b16 v[206:207], v243 offset:35072\n"
    "v_mfma_f32_16x16x32_bf16 v[152:155], v[188:191], v[104:107], v[152:155]\n"
    "s_waitcnt lgkmcnt(4)\n"
    "v_rcp_f32 v146, v146\n"
    "v_mfma_f32_16x16x32_bf16 v[152:155], v[192:195], v[112:115], v[152:155]\n"
    "ds_read_b64_tr_b16 v[184:185], v242 offset:0\n"
    "v_xor_b32 v243, 32, v242\n"
    "v_mfma_f32_16x16x32_bf16 v[152:155], v[196:199], v[120:123], v[152:155]\n"
    "ds_read_b64_tr_b16 v[186:187], v242 offset:256\n"
    "v_mfma_f32_16x16x32_bf16 v[156:159], v[192:195], v[116:119], v[156:159]\n"
    "s_add_u32 m0, m0, 0x400\n"
    "ds_read_b64_tr_b16 v[188:189], v242 offset:2048\n"
    "ds_read_b64_tr_b16 v[190:191], v242 offset:2304\n"
    "v_mfma_f32_16x16x32_bf16 v[156:159], v[196:199], v[124:127], v[156:159]\n"
    "s_waitcnt vmcnt(8) lgkmcnt(4)\n"
    "s_barrier\n"
    "buffer_load_dwordx4 v246, s[40:43], s76 offen lds\n"
    "v_mfma_f32_16x16x32_bf16 v[156:159], v[200:203], v[132:135], v[156:159]\n"
    "s_add_u32 s40, s40, 64\n"
    "ds_read_b64_tr_b16 v[192:193], v242 offset:4096\n"
    "ds_read_b64_tr_b16 v[194:195], v242 offset:4352\n"
    "v_mfma_f32_16x16x32_bf16 v[156:159], v[204:207], v[140:143], v[156:159]\n"
    "s_addc_u32 s41, s41, 0\n"
    "v_rcp_f32 v147, v147\n"
    "v_mfma_f32_16x16x32_bf16 v[152:155], v[200:203], v[128:131], v[152:155]\n"
    "ds_read_b64_tr_b16 v[196:197], v242 offset:6144\n"
    "ds_read_b64_tr_b16 v[198:199], v242 offset:6400\n"
    "v_mfma_f32_16x16x32_bf16 v[152:155], v[204:207], v[136:139], v[152:155]\n"
    "s_waitcnt lgkmcnt(4)\n"
    "v_exp_f32 v148, v148\n"
    "v_mfma_f32_16x16x32_bf16 v[160:163], v[184:187], v[0:3], 0\n"
    "ds_read_b64_tr_b16 v[200:201], v242 offset:8192\n"
    "ds_read_b64_tr_b16 v[202:203], v242 offset:8448\n"
    "v_mfma_f32_16x16x32_bf16 v[160:163], v[188:191], v[8:11], v[160:163]\n"
    "v_exp_f32 v149, v149\n"
    "v_mfma_f32_16x16x32_bf16 v[164:167], v[184:187], v[4:7], 0\n"
    "s_add_u32 m0, s54, s60\n"
    "ds_read_b64_tr_b16 v[204:205], v242 offset:10240\n"
    "ds_read_b64_tr_b16 v[206:207], v242 offset:10496\n"
    "v_mfma_f32_16x16x32_bf16 v[164:167], v[188:191], v[12:15], v[164:167]\n"
    "s_waitcnt lgkmcnt(4)\n"
    "buffer_load_dwordx4 v246, s[44:47], s68 offen lds\n"
    "v_mfma_f32_16x16x32_bf16 v[164:167], v[192:195], v[20:23], v[164:167]\n"
    "ds_read_b64_tr_b16 v[184:185], v242 offset:12288\n"
    "v_fma_f32 v148, v148, v249, v249\n"
    "v_mfma_f32_16x16x32_bf16 v[164:167], v[196:199], v[28:31], v[164:167]\n"
    "ds_read_b64_tr_b16 v[186:187], v242 offset:12544\n"
    "v_fma_f32 v149, v149, v249, v249\n"
    "v_mfma_f32_16x16x32_bf16 v[160:163], v[192:195], v[16:19], v[160:163]\n"
    "ds_read_b64_tr_b16 v[188:189], v242 offset:14336\n"
    "ds_read_b64_tr_b16 v[190:191], v242 offset:14592\n"
    "v_mfma_f32_16x16x32_bf16 v[160:163], v[196:199], v[24:27], v[160:163]\n"
    "s_waitcnt lgkmcnt(4)\n"
    "v_rcp_f32 v148, v148\n"
    "v_mfma_f32_16x16x32_bf16 v[160:163], v[200:203], v[32:35], v[160:163]\n"
    "ds_read_b64_tr_b16 v[192:193], v242 offset:16384\n"
    "ds_read_b64_tr_b16 v[194:195], v242 offset:16640\n"
    "v_mfma_f32_16x16x32_bf16 v[160:163], v[204:207], v[40:43], v[160:163]\n"
    "v_rcp_f32 v149, v149\n"
    "v_mfma_f32_16x16x32_bf16 v[164:167], v[200:203], v[36:39], v[164:167]\n"
    "s_add_u32 m0, m0, 0x400\n"
    "ds_read_b64_tr_b16 v[196:197], v242 offset:18432\n"
    "ds_read_b64_tr_b16 v[198:199], v242 offset:18688\n"
    "v_mfma_f32_16x16x32_bf16 v[164:167], v[204:207], v[44:47], v[164:167]\n"
    "s_waitcnt lgkmcnt(4)\n"
    "buffer_load_dwordx4 v246, s[44:47], s69 offen lds\n"
    "v_mfma_f32_16x16x32_bf16 v[164:167], v[184:187], v[52:55], v[164:167]\n"
    "ds_read_b64_tr_b16 v[200:201], v242 offset:20480\n"
    "ds_read_b64_tr_b16 v[202:203], v242 offset:20736\n"
    "v_mfma_f32_16x16x32_bf16 v[164:167], v[188:191], v[60:63], v[164:167]\n"
    "v_exp_f32 v150, v150\n"
    "v_mfma_f32_16x16x32_bf16 v[160:163], v[184:187], v[48:51], v[160:163]\n"
    "ds_read_b64_tr_b16 v[204:205], v242 offset:22528\n"
    "ds_read_b64_tr_b16 v[206:207], v242 offset:22784\n"
    "v_mfma_f32_16x16x32_bf16 v[160:163], v[188:191], v[56:59], v[160:163]\n"
    "s_waitcnt lgkmcnt(4)\n"
    "v_exp_f32 v151, v151\n"
    "v_mfma_f32_16x16x32_bf16 v[160:163], v[192:195], v[64:67], v[160:163]\n"
    "ds_read_b64_tr_b16 v[184:185], v242 offset:24576\n"
    "v_fma_f32 v150, v150, v249, v249\n"
    "v_mfma_f32_16x16x32_bf16 v[160:163], v[196:199], v[72:75], v[160:163]\n"
    "ds_read_b64_tr_b16 v[186:187], v242 offset:24832\n"
    "v_fma_f32 v151, v151, v249, v249\n"
    "v_mfma_f32_16x16x32_bf16 v[164:167], v[192:195], v[68:71], v[164:167]\n"
    "s_add_u32 m0, m0, 0x400\n"
    "ds_read_b64_tr_b16 v[188:189], v242 offset:26624\n"
    "ds_read_b64_tr_b16 v[190:191], v242 offset:26880\n"
    "v_mfma_f32_16x16x32_bf16 v[164:167], v[196:199], v[76:79], v[164:167]\n"
    "s_waitcnt lgkmcnt(4)\n"
    "buffer_load_dwordx4 v246, s[44:47], s70 offen lds\n"
    "v_mfma_f32_16x16x32_bf16 v[164:167], v[200:203], v[84:87], v[164:167]\n"
    "ds_read_b64_tr_b16 v[192:193], v242 offset:28672\n"
    "ds_read_b64_tr_b16 v[194:195], v242 offset:28928\n"
    "v_mfma_f32_16x16x32_bf16 v[164:167], v[204:207], v[92:95], v[164:167]\n"
    "v_rcp_f32 v150, v150\n"
    "v_mfma_f32_16x16x32_bf16 v[160:163], v[200:203], v[80:83], v[160:163]\n"
    "ds_read_b64_tr_b16 v[196:197], v242 offset:30720\n"
    "ds_read_b64_tr_b16 v[198:199], v242 offset:30976\n"
    "v_mfma_f32_16x16x32_bf16 v[160:163], v[204:207], v[88:91], v[160:163]\n"
    "s_waitcnt lgkmcnt(4)\n"
    "v_rcp_f32 v151, v151\n"
    "v_mfma_f32_16x16x32_bf16 v[160:163], v[184:187], v[96:99], v[160:163]\n"
    "ds_read_b64_tr_b16 v[200:201], v242 offset:32768\n"
    "ds_read_b64_tr_b16 v[202:203], v242 offset:33024\n"
    "v_mfma_f32_16x16x32_bf16 v[160:163], v[188:191], v[104:107], v[160:163]\n"
    "v_exp_f32 v152, v152\n"
    "v_mfma_f32_16x16x32_bf16 v[164:167], v[184:187], v[100:103], v[164:167]\n"
    "s_add_u32 m0, m0, 0x400\n"
    "ds_read_b64_tr_b16 v[204:205], v242 offset:34816\n"
    "ds_read_b64_tr_b16 v[206:207], v242 offset:35072\n"
    "v_mfma_f32_16x16x32_bf16 v[164:167], v[188:191], v[108:111], v[164:167]\n"
    "s_waitcnt lgkmcnt(4)\n"
    "buffer_load_dwordx4 v246, s[44:47], s71 offen lds\n"
    "v_mfma_f32_16x16x32_bf16 v[164:167], v[192:195], v[116:119], v[164:167]\n"
    "ds_read_b64_tr_b16 v[184:185], v243 offset:0\n"
    "v_add_u32 v242, s56, v241\n"
    "v_mfma_f32_16x16x32_bf16 v[164:167], v[196:199], v[124:127], v[164:167]\n"
    "ds_read_b64_tr_b16 v[186:187], v243 offset:256\n"
    "v_xor_b32 v244, 64, v242\n"
    "v_mfma_f32_16x16x32_bf16 v[160:163], v[192:195], v[112:115], v[160:163]\n"
    "ds_read_b64_tr_b16 v[188:189], v243 offset:2048\n"
    "v_xor_b32 v245, 0x60, v242\n"
    "v_mfma_f32_16x16x32_bf16 v[160:163], v[196:199], v[120:123], v[160:163]\n"
    "s_waitcnt lgkmcnt(3)\n"
    "ds_read_b64_tr_b16 v[190:191], v243 offset:2304\n"
    "v_mfma_f32_16x16x32_bf16 v[160:163], v[200:203], v[128:131], v[160:163]\n"
    "ds_read_b64_tr_b16 v[192:193], v243 offset:4096\n"
    "ds_read_b64_tr_b16 v[194:195], v243 offset:4352\n"
    "v_mfma_f32_16x16x32_bf16 v[160:163], v[204:207], v[136:139], v[160:163]\n"
    "v_exp_f32 v153, v153\n"
    "v_mfma_f32_16x16x32_bf16 v[164:167], v[200:203], v[132:135], v[164:167]\n"
    "s_add_u32 m0, m0, 0x400\n"
    "ds_read_b64_tr_b16 v[196:197], v243 offset:6144\n"
    "ds_read_b64_tr_b16 v[198:199], v243 offset:6400\n"
    "v_mfma_f32_16x16x32_bf16 v[164:167], v[204:207], v[140:143], v[164:167]\n"
    "s_waitcnt lgkmcnt(4)\n"
    "buffer_load_dwordx4 v246, s[44:47], s72 offen lds\n"
    "v_mfma_f32_16x16x32_bf16 v[172:175], v[184:187], v[4:7], 0\n"
    "ds_read_b64_tr_b16 v[200:201], v243 offset:8192\n"
    "v_fma_f32 v152, v152, v248, v248\n"
    "v_mfma_f32_16x16x32_bf16 v[172:175], v[188:191], v[12:15], v[172:175]\n"
    "ds_read_b64_tr_b16 v[202:203], v243 offset:8448\n"
    "v_fma_f32 v153, v153, v248, v248\n"
    "v_mfma_f32_16x16x32_bf16 v[168:171], v[184:187], v[0:3], 0\n"
    "ds_read_b64_tr_b16 v[204:205], v243 offset:10240\n"
    "ds_read_b64_tr_b16 v[206:207], v243 offset:10496\n"
    "v_mfma_f32_16x16x32_bf16 v[168:171], v[188:191], v[8:11], v[168:171]\n"
    "s_waitcnt lgkmcnt(4)\n"
    "v_rcp_f32 v152, v152\n"
    "v_mfma_f32_16x16x32_bf16 v[168:171], v[192:195], v[16:19], v[168:171]\n"
    "ds_read_b64_tr_b16 v[184:185], v243 offset:12288\n"
    "ds_read_b64_tr_b16 v[186:187], v243 offset:12544\n"
    "v_mfma_f32_16x16x32_bf16 v[168:171], v[196:199], v[24:27], v[168:171]\n"
    "v_rcp_f32 v153, v153\n"
    "v_mfma_f32_16x16x32_bf16 v[172:175], v[192:195], v[20:23], v[172:175]\n"
    "s_add_u32 m0, m0, 0x400\n"
    "ds_read_b64_tr_b16 v[188:189], v243 offset:14336\n"
    "ds_read_b64_tr_b16 v[190:191], v243 offset:14592\n"
    "v_mfma_f32_16x16x32_bf16 v[172:175], v[196:199], v[28:31], v[172:175]\n"
    "s_waitcnt lgkmcnt(4)\n"
    "buffer_load_dwordx4 v246, s[44:47], s73 offen lds\n"
    "v_mfma_f32_16x16x32_bf16 v[172:175], v[200:203], v[36:39], v[172:175]\n"
    "ds_read_b64_tr_b16 v[192:193], v243 offset:16384\n"
    "ds_read_b64_tr_b16 v[194:195], v243 offset:16640\n"
    "v_mfma_f32_16x16x32_bf16 v[172:175], v[204:207], v[44:47], v[172:175]\n"
    "v_exp_f32 v154, v154\n"
    "v_mfma_f32_16x16x32_bf16 v[168:171], v[200:203], v[32:35], v[168:171]\n"
    "ds_read_b64_tr_b16 v[196:197], v243 offset:18432\n"
    "ds_read_b64_tr_b16 v[198:199], v243 offset:18688\n"
    "v_mfma_f32_16x16x32_bf16 v[168:171], v[204:207], v[40:43], v[168:171]\n"
    "s_waitcnt lgkmcnt(4)\n"
    "v_exp_f32 v155, v155\n"
    "v_mfma_f32_16x16x32_bf16 v[168:171], v[184:187], v[48:51], v[168:171]\n"
    "ds_read_b64_tr_b16 v[200:201], v243 offset:20480\n"
    "v_fma_f32 v154, v154, v248, v248\n"
    "v_mfma_f32_16x16x32_bf16 v[168:171], v[188:191], v[56:59], v[168:171]\n"
    "ds_read_b64_tr_b16 v[202:203], v243 offset:20736\n"
    "v_fma_f32 v155, v155, v248, v248\n"
    "v_mfma_f32_16x16x32_bf16 v[172:175], v[184:187], v[52:55], v[172:175]\n"
    "s_add_u32 m0, m0, 0x400\n"
    "ds_read_b64_tr_b16 v[204:205], v243 offset:22528\n"
    "ds_read_b64_tr_b16 v[206:207], v243 offset:22784\n"
    "v_mfma_f32_16x16x32_bf16 v[172:175], v[188:191], v[60:63], v[172:175]\n"
    "s_waitcnt lgkmcnt(4)\n"
    "buffer_load_dwordx4 v246, s[44:47], s74 offen lds\n"
    "v_mfma_f32_16x16x32_bf16 v[172:175], v[192:195], v[68:71], v[172:175]\n"
    "ds_read_b64_tr_b16 v[184:185], v243 offset:24576\n"
    "ds_read_b64_tr_b16 v[186:187], v243 offset:24832\n"
    "v_mfma_f32_16x16x32_bf16 v[172:175], v[196:199], v[76:79], v[172:175]\n"
    "v_rcp_f32 v154, v154\n"
    "v_mfma_f32_16x16x32_bf16 v[168:171], v[192:195], v[64:67], v[168:171]\n"
    "ds_read_b64_tr_b16 v[188:189], v243 offset:26624\n"
    "ds_read_b64_tr_b16 v[190:191], v243 offset:26880\n"
    "v_mfma_f32_16x16x32_bf16 v[168:171], v[196:199], v[72:75], v[168:171]\n"
    "s_waitcnt lgkmcnt(4)\n"
    "v_rcp_f32 v155, v155\n"
    "v_mfma_f32_16x16x32_bf16 v[168:171], v[200:203], v[80:83], v[168:171]\n"
    "ds_read_b64_tr_b16 v[192:193], v243 offset:28672\n"
    "ds_read_b64_tr_b16 v[194:195], v243 offset:28928\n"
    "v_mfma_f32_16x16x32_bf16 v[168:171], v[204:207], v[88:91], v[168:171]\n"
    "v_exp_f32 v156, v156\n"
    "v_mfma_f32_16x16x32_bf16 v[172:175], v[200:203], v[84:87], v[172:175]\n"
    "s_add_u32 m0, m0, 0x400\n"
    "ds_read_b64_tr_b16 v[196:197], v243 offset:30720\n"
    "ds_read_b64_tr_b16 v[198:199], v243 offset:30976\n"
    "v_mfma_f32_16x16x32_bf16 v[172:175], v[204:207], v[92:95], v[172:175]\n"
    "s_waitcnt lgkmcnt(4)\n"
    "buffer_load_dwordx4 v246, s[44:47], s75 offen lds\n"
    "v_mfma_f32_16x16x32_bf16 v[172:175], v[184:187], v[100:103], v[172:175]\n"
    "ds_read_b64_tr_b16 v[200:201], v243 offset:32768\n"
    "ds_read_b64_tr_b16 v[202:203], v243 offset:33024\n"
    "v_mfma_f32_16x16x32_bf16 v[172:175], v[188:191], v[108:111], v[172:175]\n"
    "v_exp_f32 v157, v157\n"
    "v_mfma_f32_16x16x32_bf16 v[168:171], v[184:187], v[96:99], v[168:171]\n"
    "ds_read_b64_tr_b16 v[204:205], v243 offset:34816\n"
    "v_fma_f32 v156, v156, v249, v249\n"
    "v_mfma_f32_16x16x32_bf16 v[168:171], v[188:191], v[104:107], v[168:171]\n"
    "s_waitcnt lgkmcnt(3)\n"
    "ds_read_b64_tr_b16 v[206:207], v243 offset:35072\n"
    "v_fma_f32 v157, v157, v249, v249\n"
    "v_mfma_f32_16x16x32_bf16 v[168:171], v[192:195], v[112:115], v[168:171]\n"
    "ds_read_b64_tr_b16 v[184:185], v242 offset:0\n"
    "v_xor_b32 v243, 32, v242\n"
    "v_mfma_f32_16x16x32_bf16 v[168:171], v[196:199], v[120:123], v[168:171]\n"
    "ds_read_b64_tr_b16 v[186:187], v242 offset:2048\n"
    "v_mfma_f32_16x16x32_bf16 v[172:175], v[192:195], v[116:119], v[172:175]\n"
    "s_add_u32 m0, m0, 0x400\n"
    "ds_read_b64_tr_b16 v[188:189], v243 offset:0\n"
    "ds_read_b64_tr_b16 v[190:191], v243 offset:2048\n"
    "v_mfma_f32_16x16x32_bf16 v[172:175], v[196:199], v[124:127], v[172:175]\n"
    "s_waitcnt vmcnt(8) lgkmcnt(4)\n"
    "s_barrier\n"
    "buffer_load_dwordx4 v246, s[44:47], s76 offen lds\n"
    "v_mfma_f32_16x16x32_bf16 v[172:175], v[200:203], v[132:135], v[172:175]\n"
    "s_add_u32 s44, s44, 64\n"
    "ds_read_b64_tr_b16 v[192:193], v244 offset:0\n"
    "ds_read_b64_tr_b16 v[194:195], v244 offset:2048\n"
    "v_mfma_f32_16x16x32_bf16 v[172:175], v[204:207], v[140:143], v[172:175]\n"
    "s_addc_u32 s45, s45, 0\n"
    "v_rcp_f32 v156, v156\n"
    "v_mfma_f32_16x16x32_bf16 v[168:171], v[200:203], v[128:131], v[168:171]\n"
    "ds_read_b64_tr_b16 v[196:197], v245 offset:0\n"
    "ds_read_b64_tr_b16 v[198:199], v245 offset:2048\n"
    "v_mfma_f32_16x16x32_bf16 v[168:171], v[204:207], v[136:139], v[168:171]\n"
    "s_waitcnt lgkmcnt(4)\n"
    "v_rcp_f32 v157, v157\n"
    "v_mfma_f32_16x16x32_bf16 a[0:3], v[184:187], v[176:179], a[0:3]\n"
    "ds_read_b64_tr_b16 v[200:201], v242 offset:4096\n"
    "ds_read_b64_tr_b16 v[202:203], v242 offset:6144\n"
    "v_mfma_f32_16x16x32_bf16 a[4:7], v[188:191], v[176:179], a[4:7]\n"
    "v_exp_f32 v158, v158\n"
    "v_mfma_f32_16x16x32_bf16 a[8:11], v[184:187], v[180:183], a[8:11]\n"
    "s_add_u32 m0, s55, s61\n"
    "ds_read_b64_tr_b16 v[204:205], v243 offset:4096\n"
    "ds_read_b64_tr_b16 v[206:207], v243 offset:6144\n"
    "v_mfma_f32_16x16x32_bf16 a[12:15], v[188:191], v[180:183], a[12:15]\n"
    "s_waitcnt lgkmcnt(4)\n"
    "buffer_load_dwordx4 v247, s[48:51], s52 offen lds\n"
    "v_mfma_f32_16x16x32_bf16 a[24:27], v[192:195], v[180:183], a[24:27]\n"
    "ds_read_b64_tr_b16 v[184:185], v244 offset:4096\n"
    "ds_read_b64_tr_b16 v[186:187], v244 offset:6144\n"
    "v_mfma_f32_16x16x32_bf16 a[28:31], v[196:199], v[180:183], a[28:31]\n"
    "v_exp_f32 v159, v159\n"
    "v_mfma_f32_16x16x32_bf16 a[16:19], v[192:195], v[176:179], a[16:19]\n"
    "ds_read_b64_tr_b16 v[188:189], v245 offset:4096\n"
    "v_fma_f32 v158, v158, v249, v249\n"
    "v_mfma_f32_16x16x32_bf16 a[20:23], v[196:199], v[176:179], a[20:23]\n"
    "s_waitcnt lgkmcnt(3)\n"
    "ds_read_b64_tr_b16 v[190:191], v245 offset:6144\n"
    "v_fma_f32 v159, v159, v249, v249\n"
    "v_mfma_f32_16x16x32_bf16 a[32:35], v[200:203], v[176:179], a[32:35]\n"
    "ds_read_b64_tr_b16 v[192:193], v242 offset:8192\n"
    "ds_read_b64_tr_b16 v[194:195], v242 offset:10240\n"
    "v_mfma_f32_16x16x32_bf16 a[36:39], v[204:207], v[176:179], a[36:39]\n"
    "v_rcp_f32 v158, v158\n"
    "v_mfma_f32_16x16x32_bf16 a[40:43], v[200:203], v[180:183], a[40:43]\n"
    "s_add_u32 m0, m0, 0x1000\n"
    "ds_read_b64_tr_b16 v[196:197], v243 offset:8192\n"
    "ds_read_b64_tr_b16 v[198:199], v243 offset:10240\n"
    "v_mfma_f32_16x16x32_bf16 a[44:47], v[204:207], v[180:183], a[44:47]\n"
    "s_waitcnt lgkmcnt(4)\n"
    "buffer_load_dwordx4 v247, s[48:51], s53 offen lds\n"
    "v_mfma_f32_16x16x32_bf16 a[56:59], v[184:187], v[180:183], a[56:59]\n"
    "ds_read_b64_tr_b16 v[200:201], v244 offset:8192\n"
    "ds_read_b64_tr_b16 v[202:203], v244 offset:10240\n"
    "v_mfma_f32_16x16x32_bf16 a[60:63], v[188:191], v[180:183], a[60:63]\n"
    "v_rcp_f32 v159, v159\n"
    "v_mfma_f32_16x16x32_bf16 a[48:51], v[184:187], v[176:179], a[48:51]\n"
    "ds_read_b64_tr_b16 v[204:205], v245 offset:8192\n"
    "v_mul_f32 v160, v144, v160\n"
    "v_mfma_f32_16x16x32_bf16 a[52:55], v[188:191], v[176:179], a[52:55]\n"
    "s_waitcnt lgkmcnt(3)\n"
    "ds_read_b64_tr_b16 v[206:207], v245 offset:10240\n"
    "v_mul_f32 v161, v145, v161\n"
    "v_mfma_f32_16x16x32_bf16 a[64:67], v[192:195], v[176:179], a[64:67]\n"
    "ds_read_b64_tr_b16 v[184:185], v242 offset:12288\n"
    "v_mul_f32 v162, v146, v162\n"
    "v_mfma_f32_16x16x32_bf16 a[68:71], v[196:199], v[176:179], a[68:71]\n"
    "ds_read_b64_tr_b16 v[186:187], v242 offset:14336\n"
    "v_mul_f32 v163, v147, v163\n"
    "v_mfma_f32_16x16x32_bf16 a[72:75], v[192:195], v[180:183], a[72:75]\n"
    "s_add_u32 m0, m0, 0x1000\n"
    "ds_read_b64_tr_b16 v[188:189], v243 offset:12288\n"
    "ds_read_b64_tr_b16 v[190:191], v243 offset:14336\n"
    "v_mfma_f32_16x16x32_bf16 a[76:79], v[196:199], v[180:183], a[76:79]\n"
    "s_waitcnt lgkmcnt(4)\n"
    "buffer_load_dwordx4 v247, s[48:51], s58 offen lds\n"
    "v_mfma_f32_16x16x32_bf16 a[88:91], v[200:203], v[180:183], a[88:91]\n"
    "ds_read_b64_tr_b16 v[192:193], v244 offset:12288\n"
    "v_mul_f32 v164, v148, v164\n"
    "v_mfma_f32_16x16x32_bf16 a[92:95], v[204:207], v[180:183], a[92:95]\n"
    "ds_read_b64_tr_b16 v[194:195], v244 offset:14336\n"
    "v_mul_f32 v165, v149, v165\n"
    "v_mfma_f32_16x16x32_bf16 a[80:83], v[200:203], v[176:179], a[80:83]\n"
    "ds_read_b64_tr_b16 v[196:197], v245 offset:12288\n"
    "v_mul_f32 v166, v150, v166\n"
    "v_mfma_f32_16x16x32_bf16 a[84:87], v[204:207], v[176:179], a[84:87]\n"
    "s_waitcnt lgkmcnt(3)\n"
    "ds_read_b64_tr_b16 v[198:199], v245 offset:14336\n"
    "v_mul_f32 v167, v151, v167\n"
    "v_mfma_f32_16x16x32_bf16 a[96:99], v[184:187], v[176:179], a[96:99]\n"
    "ds_read_b64_tr_b16 v[200:201], v242 offset:16384\n"
    "v_mul_f32 v168, v152, v168\n"
    "v_mfma_f32_16x16x32_bf16 a[100:103], v[188:191], v[176:179], a[100:103]\n"
    "ds_read_b64_tr_b16 v[202:203], v242 offset:18432\n"
    "v_mul_f32 v169, v153, v169\n"
    "v_mfma_f32_16x16x32_bf16 a[104:107], v[184:187], v[180:183], a[104:107]\n"
    "s_add_u32 m0, m0, 0x1000\n"
    "ds_read_b64_tr_b16 v[204:205], v243 offset:16384\n"
    "ds_read_b64_tr_b16 v[206:207], v243 offset:18432\n"
    "v_mfma_f32_16x16x32_bf16 a[108:111], v[188:191], v[180:183], a[108:111]\n"
    "s_waitcnt lgkmcnt(4)\n"
    "buffer_load_dwordx4 v247, s[48:51], s59 offen lds\n"
    "v_mfma_f32_16x16x32_bf16 a[120:123], v[192:195], v[180:183], a[120:123]\n"
    "ds_read_b64_tr_b16 v[184:185], v244 offset:16384\n"
    "v_mul_f32 v170, v154, v170\n"
    "v_mfma_f32_16x16x32_bf16 a[124:127], v[196:199], v[180:183], a[124:127]\n"
    "ds_read_b64_tr_b16 v[186:187], v244 offset:18432\n"
    "v_mul_f32 v171, v155, v171\n"
    "v_mfma_f32_16x16x32_bf16 a[112:115], v[192:195], v[176:179], a[112:115]\n"
    "ds_read_b64_tr_b16 v[188:189], v245 offset:16384\n"
    "v_mul_f32 v172, v156, v172\n"
    "v_mfma_f32_16x16x32_bf16 a[116:119], v[196:199], v[176:179], a[116:119]\n"
    "s_waitcnt lgkmcnt(3)\n"
    "ds_read_b64_tr_b16 v[190:191], v245 offset:18432\n"
    "v_mul_f32 v173, v157, v173\n"
    "v_mfma_f32_16x16x32_bf16 a[128:131], v[200:203], v[176:179], a[128:131]\n"
    "ds_read_b64_tr_b16 v[192:193], v242 offset:20480\n"
    "v_mul_f32 v174, v158, v174\n"
    "v_mfma_f32_16x16x32_bf16 a[132:135], v[204:207], v[176:179], a[132:135]\n"
    "ds_read_b64_tr_b16 v[194:195], v242 offset:22528\n"
    "v_mul_f32 v175, v159, v175\n"
    "v_mfma_f32_16x16x32_bf16 a[136:139], v[200:203], v[180:183], a[136:139]\n"
    "s_add_u32 m0, m0, 0x1000\n"
    "ds_read_b64_tr_b16 v[196:197], v243 offset:20480\n"
    "ds_read_b64_tr_b16 v[198:199], v243 offset:22528\n"
    "v_mfma_f32_16x16x32_bf16 a[140:143], v[204:207], v[180:183], a[140:143]\n"
    "s_waitcnt lgkmcnt(4)\n"
    "buffer_load_dwordx4 v247, s[48:51], s63 offen lds\n"
    "v_mfma_f32_16x16x32_bf16 a[152:155], v[184:187], v[180:183], a[152:155]\n"
    "ds_read_b64_tr_b16 v[200:201], v244 offset:20480\n"
    "v_mfma_f32_16x16x32_bf16 a[156:159], v[188:191], v[180:183], a[156:159]\n"
    "ds_read_b64_tr_b16 v[202:203], v244 offset:22528\n"
    "v_mfma_f32_16x16x32_bf16 a[144:147], v[184:187], v[176:179], a[144:147]\n"
    "ds_read_b64_tr_b16 v[204:205], v245 offset:20480\n"
    "v_mfma_f32_16x16x32_bf16 a[148:151], v[188:191], v[176:179], a[148:151]\n"
    "s_waitcnt lgkmcnt(3)\n"
    "ds_read_b64_tr_b16 v[206:207], v245 offset:22528\n"
    "v_mfma_f32_16x16x32_bf16 a[160:163], v[192:195], v[176:179], a[160:163]\n"
    "ds_read_b64_tr_b16 v[184:185], v242 offset:24576\n"
    "v_mfma_f32_16x16x32_bf16 a[164:167], v[196:199], v[176:179], a[164:167]\n"
    "ds_read_b64_tr_b16 v[186:187], v242 offset:26624\n"
    "v_mfma_f32_16x16x32_bf16 a[168:171], v[192:195], v[180:183], a[168:171]\n"
    "s_add_u32 m0, m0, 0x1000\n"
    "ds_read_b64_tr_b16 v[188:189], v243 offset:24576\n"
    "ds_read_b64_tr_b16 v[190:191], v243 offset:26624\n"
    "v_mfma_f32_16x16x32_bf16 a[172:175], v[196:199], v[180:183], a[172:175]\n"
    "s_waitcnt lgkmcnt(4)\n"
    "buffer_load_dwordx4 v247, s[48:51], s64 offen lds\n"
    "v_mfma_f32_16x16x32_bf16 a[184:187], v[200:203], v[180:183], a[184:187]\n"
    "ds_read_b64_tr_b16 v[192:193], v244 offset:24576\n"
    "v_mfma_f32_16x16x32_bf16 a[188:191], v[204:207], v[180:183], a[188:191]\n"
    "ds_read_b64_tr_b16 v[194:195], v244 offset:26624\n"
    "v_mfma_f32_16x16x32_bf16 a[176:179], v[200:203], v[176:179], a[176:179]\n"
    "ds_read_b64_tr_b16 v[196:197], v245 offset:24576\n"
    "v_mfma_f32_16x16x32_bf16 a[180:183], v[204:207], v[176:179], a[180:183]\n"
    "s_waitcnt lgkmcnt(3)\n"
    "ds_read_b64_tr_b16 v[198:199], v245 offset:26624\n"
    "v_mfma_f32_16x16x32_bf16 a[192:195], v[184:187], v[176:179], a[192:195]\n"
    "ds_read_b64_tr_b16 v[200:201], v242 offset:28672\n"
    "v_mfma_f32_16x16x32_bf16 a[196:199], v[188:191], v[176:179], a[196:199]\n"
    "ds_read_b64_tr_b16 v[202:203], v242 offset:30720\n"
    "v_mfma_f32_16x16x32_bf16 a[200:203], v[184:187], v[180:183], a[200:203]\n"
    "s_add_u32 m0, m0, 0x1000\n"
    "ds_read_b64_tr_b16 v[204:205], v243 offset:28672\n"
    "ds_read_b64_tr_b16 v[206:207], v243 offset:30720\n"
    "v_mfma_f32_16x16x32_bf16 a[204:207], v[188:191], v[180:183], a[204:207]\n"
    "s_waitcnt lgkmcnt(4)\n"
    "buffer_load_dwordx4 v247, s[48:51], s65 offen lds\n"
    "v_mfma_f32_16x16x32_bf16 a[216:219], v[192:195], v[180:183], a[216:219]\n"
    "ds_read_b64_tr_b16 v[184:185], v244 offset:28672\n"
    "v_mfma_f32_16x16x32_bf16 a[220:223], v[196:199], v[180:183], a[220:223]\n"
    "ds_read_b64_tr_b16 v[186:187], v244 offset:30720\n"
    "v_mfma_f32_16x16x32_bf16 a[208:211], v[192:195], v[176:179], a[208:211]\n"
    "ds_read_b64_tr_b16 v[188:189], v245 offset:28672\n"
    "v_mfma_f32_16x16x32_bf16 a[212:215], v[196:199], v[176:179], a[212:215]\n"
    "s_waitcnt lgkmcnt(3)\n"
    "ds_read_b64_tr_b16 v[190:191], v245 offset:30720\n"
    "v_mfma_f32_16x16x32_bf16 a[224:227], v[200:203], v[176:179], a[224:227]\n"
    "ds_read_b64_tr_b16 v[192:193], v242 offset:32768\n"
    "v_mfma_f32_16x16x32_bf16 a[228:231], v[204:207], v[176:179], a[228:231]\n"
    "ds_read_b64_tr_b16 v[194:195], v242 offset:34816\n"
    "v_mfma_f32_16x16x32_bf16 a[232:235], v[200:203], v[180:183], a[232:235]\n"
    "s_add_u32 m0, m0, 0x1000\n"
    "ds_read_b64_tr_b16 v[196:197], v243 offset:32768\n"
    "ds_read_b64_tr_b16 v[198:199], v243 offset:34816\n"
    "v_mfma_f32_16x16x32_bf16 a[236:239], v[204:207], v[180:183], a[236:239]\n"
    "s_waitcnt lgkmcnt(4)\n"
    "buffer_load_dwordx4 v247, s[48:51], s77 offen lds\n"
    "v_mfma_f32_16x16x32_bf16 a[248:251], v[184:187], v[180:183], a[248:251]\n"
    "s_sub_u32 s66, s66, 1\n"
    "ds_read_b64_tr_b16 v[200:201], v244 offset:32768\n"
    "v_add_u32 v242, s57, v240\n"
    "v_mfma_f32_16x16x32_bf16 a[252:255], v[188:191], v[180:183], a[252:255]\n"
    "ds_read_b64_tr_b16 v[202:203], v244 offset:34816\n"
    "v_xor_b32 v243, 32, v242\n"
    "v_mfma_f32_16x16x32_bf16 a[240:243], v[184:187], v[176:179], a[240:243]\n"
    "ds_read_b64_tr_b16 v[204:205], v245 offset:32768\n"
    "v_mfma_f32_16x16x32_bf16 a[244:247], v[188:191], v[176:179], a[244:247]\n"
    "s_waitcnt lgkmcnt(3)\n"
    "ds_read_b64_tr_b16 v[206:207], v245 offset:34816\n"
    "v_mfma_f32_16x16x32_bf16 v[208:211], v[192:195], v[176:179], v[208:211]\n"
    "s_mov_b32 s67, s56\n"
    "ds_read_b64_tr_b16 v[184:185], v242 offset:0\n"
    "v_mfma_f32_16x16x32_bf16 v[212:215], v[196:199], v[176:179], v[212:215]\n"
    "s_mov_b32 s56, s55\n"
    "ds_read_b64_tr_b16 v[186:187], v242 offset:256\n"
    "v_mfma_f32_16x16x32_bf16 v[216:219], v[192:195], v[180:183], v[216:219]\n"
    "s_add_u32 m0, m0, 0x1000\n"
    "ds_read_b64_tr_b16 v[188:189], v242 offset:2048\n"
    "ds_read_b64_tr_b16 v[190:191], v242 offset:2304\n"
    "v_mfma_f32_16x16x32_bf16 v[220:223], v[196:199], v[180:183], v[220:223]\n"
    "s_mov_b32 s55, s54\n"
    "s_waitcnt vmcnt(8) lgkmcnt(4)\n"
    "s_barrier\n"
    "buffer_load_dwordx4 v247, s[48:51], s79 offen lds\n"
    "v_mfma_f32_16x16x32_bf16 v[232:235], v[200:203], v[180:183], v[232:235]\n"
    "s_add_u32 s48, s48, 36864\n"
    "ds_read_b64_tr_b16 v[192:193], v242 offset:4096\n"
    "v_mfma_f32_16x16x32_bf16 v[236:239], v[204:207], v[180:183], v[236:239]\n"
    "s_addc_u32 s49, s49, 0\n"
    "ds_read_b64_tr_b16 v[194:195], v242 offset:4352\n"
    "v_mfma_f32_16x16x32_bf16 v[224:227], v[200:203], v[176:179], v[224:227]\n"
    "s_mov_b32 s54, s57\n"
    "s_cmp_lg_u32 s66, 0\n"
    "ds_read_b64_tr_b16 v[196:197], v242 offset:6144\n"
    "v_mfma_f32_16x16x32_bf16 v[228:231], v[204:207], v[176:179], v[228:231]\n"
    "s_mov_b32 s57, s67\n"
    "s_waitcnt lgkmcnt(3)\n"
    "ds_read_b64_tr_b16 v[198:199], v242 offset:6400\n"
    "s_cbranch_scc1 1b\n"
    "v_mfma_f32_16x16x32_bf16 v[144:147], v[184:187], v[0:3], 0\n"
    "ds_read_b64_tr_b16 v[200:201], v242 offset:8192\n"
    "v_cvt_pk_bf16_f32 v176, v160, v161\n"
    "v_mfma_f32_16x16x32_bf16 v[144:147], v[188:191], v[8:11], v[144:147]\n"
    "ds_read_b64_tr_b16 v[202:203], v242 offset:8448\n"
    "v_cvt_pk_bf16_f32 v177, v162, v163\n"
    "v_mfma_f32_16x16x32_bf16 v[148:151], v[184:187], v[4:7], 0\n"
    "s_add_u32 m0, s57, s60\n"
    "ds_read_b64_tr_b16 v[204:205], v242 offset:10240\n"
    "ds_read_b64_tr_b16 v[206:207], v242 offset:10496\n"
    "v_mfma_f32_16x16x32_bf16 v[148:151], v[188:191], v[12:15], v[148:151]\n"
    "s_waitcnt lgkmcnt(4)\n"
    "buffer_load_dwordx4 v246, s[40:43], s68 offen lds\n"
    "v_mfma_f32_16x16x32_bf16 v[148:151], v[192:195], v[20:23], v[148:151]\n"
    "ds_read_b64_tr_b16 v[184:185], v242 offset:12288\n"
    "v_cvt_pk_bf16_f32 v180, v164, v165\n"
    "v_mfma_f32_16x16x32_bf16 v[148:151], v[196:199], v[28:31], v[148:151]\n"
    "ds_read_b64_tr_b16 v[186:187], v242 offset:12544\n"
    "v_cvt_pk_bf16_f32 v181, v166, v167\n"
    "v_mfma_f32_16x16x32_bf16 v[144:147], v[192:195], v[16:19], v[144:147]\n"
    "ds_read_b64_tr_b16 v[188:189], v242 offset:14336\n"
    "v_cvt_pk_bf16_f32 v178, v168, v169\n"
    "v_mfma_f32_16x16x32_bf16 v[144:147], v[196:199], v[24:27], v[144:147]\n"
    "s_waitcnt lgkmcnt(3)\n"
    "ds_read_b64_tr_b16 v[190:191], v242 offset:14592\n"
    "v_cvt_pk_bf16_f32 v179, v170, v171\n"
    "v_mfma_f32_16x16x32_bf16 v[144:147], v[200:203], v[32:35], v[144:147]\n"
    "ds_read_b64_tr_b16 v[192:193], v242 offset:16384\n"
    "v_cvt_pk_bf16_f32 v182, v172, v173\n"
    "v_mfma_f32_16x16x32_bf16 v[144:147], v[204:207], v[40:43], v[144:147]\n"
    "ds_read_b64_tr_b16 v[194:195], v242 offset:16640\n"
    "v_cvt_pk_bf16_f32 v183, v174, v175\n"
    "v_mfma_f32_16x16x32_bf16 v[148:151], v[200:203], v[36:39], v[148:151]\n"
    "s_add_u32 m0, m0, 0x400\n"
    "ds_read_b64_tr_b16 v[196:197], v242 offset:18432\n"
    "ds_read_b64_tr_b16 v[198:199], v242 offset:18688\n"
    "v_mfma_f32_16x16x32_bf16 v[148:151], v[204:207], v[44:47], v[148:151]\n"
    "s_waitcnt lgkmcnt(4)\n"
    "buffer_load_dwordx4 v246, s[40:43], s69 offen lds\n"
    "v_mfma_f32_16x16x32_bf16 v[148:151], v[184:187], v[52:55], v[148:151]\n"
    "ds_read_b64_tr_b16 v[200:201], v242 offset:20480\n"
    "v_mfma_f32_16x16x32_bf16 v[148:151], v[188:191], v[60:63], v[148:151]\n"
    "ds_read_b64_tr_b16 v[202:203], v242 offset:20736\n"
    "v_mfma_f32_16x16x32_bf16 v[144:147], v[184:187], v[48:51], v[144:147]\n"
    "ds_read_b64_tr_b16 v[204:205], v242 offset:22528\n"
    "v_mfma_f32_16x16x32_bf16 v[144:147], v[188:191], v[56:59], v[144:147]\n"
    "s_waitcnt lgkmcnt(3)\n"
    "ds_read_b64_tr_b16 v[206:207], v242 offset:22784\n"
    "v_mfma_f32_16x16x32_bf16 v[144:147], v[192:195], v[64:67], v[144:147]\n"
    "ds_read_b64_tr_b16 v[184:185], v242 offset:24576\n"
    "v_mfma_f32_16x16x32_bf16 v[144:147], v[196:199], v[72:75], v[144:147]\n"
    "ds_read_b64_tr_b16 v[186:187], v242 offset:24832\n"
    "v_mfma_f32_16x16x32_bf16 v[148:151], v[192:195], v[68:71], v[148:151]\n"
    "s_add_u32 m0, m0, 0x400\n"
    "ds_read_b64_tr_b16 v[188:189], v242 offset:26624\n"
    "ds_read_b64_tr_b16 v[190:191], v242 offset:26880\n"
    "v_mfma_f32_16x16x32_bf16 v[148:151], v[196:199], v[76:79], v[148:151]\n"
    "s_waitcnt lgkmcnt(4)\n"
    "buffer_load_dwordx4 v246, s[40:43], s70 offen lds\n"
    "v_mfma_f32_16x16x32_bf16 v[148:151], v[200:203], v[84:87], v[148:151]\n"
    "ds_read_b64_tr_b16 v[192:193], v242 offset:28672\n"
    "v_mfma_f32_16x16x32_bf16 v[148:151], v[204:207], v[92:95], v[148:151]\n"
    "ds_read_b64_tr_b16 v[194:195], v242 offset:28928\n"
    "v_mfma_f32_16x16x32_bf16 v[144:147], v[200:203], v[80:83], v[144:147]\n"
    "ds_read_b64_tr_b16 v[196:197], v242 offset:30720\n"
    "v_mfma_f32_16x16x32_bf16 v[144:147], v[204:207], v[88:91], v[144:147]\n"
    "s_waitcnt lgkmcnt(3)\n"
    "ds_read_b64_tr_b16 v[198:199], v242 offset:30976\n"
    "v_mfma_f32_16x16x32_bf16 v[144:147], v[184:187], v[96:99], v[144:147]\n"
    "ds_read_b64_tr_b16 v[200:201], v242 offset:32768\n"
    "v_mfma_f32_16x16x32_bf16 v[144:147], v[188:191], v[104:107], v[144:147]\n"
    "ds_read_b64_tr_b16 v[202:203], v242 offset:33024\n"
    "v_mfma_f32_16x16x32_bf16 v[148:151], v[184:187], v[100:103], v[148:151]\n"
    "s_add_u32 m0, m0, 0x400\n"
    "ds_read_b64_tr_b16 v[204:205], v242 offset:34816\n"
    "ds_read_b64_tr_b16 v[206:207], v242 offset:35072\n"
    "v_mfma_f32_16x16x32_bf16 v[148:151], v[188:191], v[108:111], v[148:151]\n"
    "s_waitcnt lgkmcnt(4)\n"
    "buffer_load_dwordx4 v246, s[40:43], s71 offen lds\n"
    "v_mfma_f32_16x16x32_bf16 v[148:151], v[192:195], v[116:119], v[148:151]\n"
    "ds_read_b64_tr_b16 v[184:185], v243 offset:0\n"
    "v_add_u32 v242, s55, v240\n"
    "v_mfma_f32_16x16x32_bf16 v[148:151], v[196:199], v[124:127], v[148:151]\n"
    "ds_read_b64_tr_b16 v[186:187], v243 offset:256\n"
    "v_mfma_f32_16x16x32_bf16 v[144:147], v[192:195], v[112:115], v[144:147]\n"
    "ds_read_b64_tr_b16 v[188:189], v243 offset:2048\n"
    "v_mfma_f32_16x16x32_bf16 v[144:147], v[196:199], v[120:123], v[144:147]\n"
    "s_waitcnt lgkmcnt(3)\n"
    "ds_read_b64_tr_b16 v[190:191], v243 offset:2304\n"
    "v_mfma_f32_16x16x32_bf16 v[144:147], v[200:203], v[128:131], v[144:147]\n"
    "ds_read_b64_tr_b16 v[192:193], v243 offset:4096\n"
    "v_mfma_f32_16x16x32_bf16 v[144:147], v[204:207], v[136:139], v[144:147]\n"
    "ds_read_b64_tr_b16 v[194:195], v243 offset:4352\n"
    "v_mfma_f32_16x16x32_bf16 v[148:151], v[200:203], v[132:135], v[148:151]\n"
    "s_add_u32 m0, m0, 0x400\n"
    "ds_read_b64_tr_b16 v[196:197], v243 offset:6144\n"
    "ds_read_b64_tr_b16 v[198:199], v243 offset:6400\n"
    "v_mfma_f32_16x16x32_bf16 v[148:151], v[204:207], v[140:143], v[148:151]\n"
    "s_waitcnt lgkmcnt(4)\n"
    "buffer_load_dwordx4 v246, s[40:43], s72 offen lds\n"
    "v_mfma_f32_16x16x32_bf16 v[156:159], v[184:187], v[4:7], 0\n"
    "ds_read_b64_tr_b16 v[200:201], v243 offset:8192\n"
    "v_mfma_f32_16x16x32_bf16 v[156:159], v[188:191], v[12:15], v[156:159]\n"
    "ds_read_b64_tr_b16 v[202:203], v243 offset:8448\n"
    "v_mfma_f32_16x16x32_bf16 v[152:155], v[184:187], v[0:3], 0\n"
    "ds_read_b64_tr_b16 v[204:205], v243 offset:10240\n"
    "v_mfma_f32_16x16x32_bf16 v[152:155], v[188:191], v[8:11], v[152:155]\n"
    "s_waitcnt lgkmcnt(3)\n"
    "ds_read_b64_tr_b16 v[206:207], v243 offset:10496\n"
    "v_mfma_f32_16x16x32_bf16 v[152:155], v[192:195], v[16:19], v[152:155]\n"
    "ds_read_b64_tr_b16 v[184:185], v243 offset:12288\n"
    "ds_read_b64_tr_b16 v[186:187], v243 offset:12544\n"
    "v_mfma_f32_16x16x32_bf16 v[152:155], v[196:199], v[24:27], v[152:155]\n"
    "v_exp_f32 v144, v144\n"
    "v_mfma_f32_16x16x32_bf16 v[156:159], v[192:195], v[20:23], v[156:159]\n"
    "s_add_u32 m0, m0, 0x400\n"
    "ds_read_b64_tr_b16 v[188:189], v243 offset:14336\n"
    "ds_read_b64_tr_b16 v[190:191], v243 offset:14592\n"
    "v_mfma_f32_16x16x32_bf16 v[156:159], v[196:199], v[28:31], v[156:159]\n"
    "s_waitcnt lgkmcnt(4)\n"
    "buffer_load_dwordx4 v246, s[40:43], s73 offen lds\n"
    "v_mfma_f32_16x16x32_bf16 v[156:159], v[200:203], v[36:39], v[156:159]\n"
    "ds_read_b64_tr_b16 v[192:193], v243 offset:16384\n"
    "ds_read_b64_tr_b16 v[194:195], v243 offset:16640\n"
    "v_mfma_f32_16x16x32_bf16 v[156:159], v[204:207], v[44:47], v[156:159]\n"
    "v_exp_f32 v145, v145\n"
    "v_mfma_f32_16x16x32_bf16 v[152:155], v[200:203], v[32:35], v[152:155]\n"
    "ds_read_b64_tr_b16 v[196:197], v243 offset:18432\n"
    "v_fma_f32 v144, v144, v248, v248\n"
    "v_mfma_f32_16x16x32_bf16 v[152:155], v[204:207], v[40:43], v[152:155]\n"
    "s_waitcnt lgkmcnt(3)\n"
    "ds_read_b64_tr_b16 v[198:199], v243 offset:18688\n"
    "v_fma_f32 v145, v145, v248, v248\n"
    "v_mfma_f32_16x16x32_bf16 v[152:155], v[184:187], v[48:51], v[152:155]\n"
    "ds_read_b64_tr_b16 v[200:201], v243 offset:20480\n"
    "ds_read_b64_tr_b16 v[202:203], v243 offset:20736\n"
    "v_mfma_f32_16x16x32_bf16 v[152:155], v[188:191], v[56:59], v[152:155]\n"
    "v_rcp_f32 v144, v144\n"
    "v_mfma_f32_16x16x32_bf16 v[156:159], v[184:187], v[52:55], v[156:159]\n"
    "s_add_u32 m0, m0, 0x400\n"
    "ds_read_b64_tr_b16 v[204:205], v243 offset:22528\n"
    "ds_read_b64_tr_b16 v[206:207], v243 offset:22784\n"
    "v_mfma_f32_16x16x32_bf16 v[156:159], v[188:191], v[60:63], v[156:159]\n"
    "s_waitcnt lgkmcnt(4)\n"
    "buffer_load_dwordx4 v246, s[40:43], s74 offen lds\n"
    "v_mfma_f32_16x16x32_bf16 v[156:159], v[192:195], v[68:71], v[156:159]\n"
    "ds_read_b64_tr_b16 v[184:185], v243 offset:24576\n"
    "ds_read_b64_tr_b16 v[186:187], v243 offset:24832\n"
    "v_mfma_f32_16x16x32_bf16 v[156:159], v[196:199], v[76:79], v[156:159]\n"
    "v_rcp_f32 v145, v145\n"
    "v_mfma_f32_16x16x32_bf16 v[152:155], v[192:195], v[64:67], v[152:155]\n"
    "ds_read_b64_tr_b16 v[188:189], v243 offset:26624\n"
    "ds_read_b64_tr_b16 v[190:191], v243 offset:26880\n"
    "v_mfma_f32_16x16x32_bf16 v[152:155], v[196:199], v[72:75], v[152:155]\n"
    "s_waitcnt lgkmcnt(4)\n"
    "v_exp_f32 v146, v146\n"
    "v_mfma_f32_16x16x32_bf16 v[152:155], v[200:203], v[80:83], v[152:155]\n"
    "ds_read_b64_tr_b16 v[192:193], v243 offset:28672\n"
    "ds_read_b64_tr_b16 v[194:195], v243 offset:28928\n"
    "v_mfma_f32_16x16x32_bf16 v[152:155], v[204:207], v[88:91], v[152:155]\n"
    "v_exp_f32 v147, v147\n"
    "v_mfma_f32_16x16x32_bf16 v[156:159], v[200:203], v[84:87], v[156:159]\n"
    "s_add_u32 m0, m0, 0x400\n"
    "ds_read_b64_tr_b16 v[196:197], v243 offset:30720\n"
    "ds_read_b64_tr_b16 v[198:199], v243 offset:30976\n"
    "v_mfma_f32_16x16x32_bf16 v[156:159], v[204:207], v[92:95], v[156:159]\n"
    "s_waitcnt lgkmcnt(4)\n"
    "buffer_load_dwordx4 v246, s[40:43], s75 offen lds\n"
    "v_mfma_f32_16x16x32_bf16 v[156:159], v[184:187], v[100:103], v[156:159]\n"
    "ds_read_b64_tr_b16 v[200:201], v243 offset:32768\n"
    "v_fma_f32 v146, v146, v248, v248\n"
    "v_mfma_f32_16x16x32_bf16 v[156:159], v[188:191], v[108:111], v[156:159]\n"
    "ds_read_b64_tr_b16 v[202:203], v243 offset:33024\n"
    "v_fma_f32 v147, v147, v248, v248\n"
    "v_mfma_f32_16x16x32_bf16 v[152:155], v[184:187], v[96:99], v[152:155]\n"
    "ds_read_b64_tr_b16 v[204:205], v243 offset:34816\n"
    "ds_read_b64_tr_b16 v[206:207], v243 offset:35072\n"
    "v_mfma_f32_16x16x32_bf16 v[152:155], v[188:191], v[104:107], v[152:155]\n"
    "s_waitcnt lgkmcnt(4)\n"
    "v_rcp_f32 v146, v146\n"
    "v_mfma_f32_16x16x32_bf16 v[152:155], v[192:195], v[112:115], v[152:155]\n"
    "ds_read_b64_tr_b16 v[184:185], v242 offset:0\n"
    "v_xor_b32 v243, 32, v242\n"
    "v_mfma_f32_16x16x32_bf16 v[152:155], v[196:199], v[120:123], v[152:155]\n"
    "ds_read_b64_tr_b16 v[186:187], v242 offset:256\n"
    "v_mfma_f32_16x16x32_bf16 v[156:159], v[192:195], v[116:119], v[156:159]\n"
    "s_add_u32 m0, m0, 0x400\n"
    "ds_read_b64_tr_b16 v[188:189], v242 offset:2048\n"
    "ds_read_b64_tr_b16 v[190:191], v242 offset:2304\n"
    "v_mfma_f32_16x16x32_bf16 v[156:159], v[196:199], v[124:127], v[156:159]\n"
    "s_waitcnt vmcnt(8) lgkmcnt(4)\n"
    "s_barrier\n"
    "buffer_load_dwordx4 v246, s[40:43], s76 offen lds\n"
    "v_mfma_f32_16x16x32_bf16 v[156:159], v[200:203], v[132:135], v[156:159]\n"
    "s_add_u32 s40, s40, 64\n"
    "ds_read_b64_tr_b16 v[192:193], v242 offset:4096\n"
    "ds_read_b64_tr_b16 v[194:195], v242 offset:4352\n"
    "v_mfma_f32_16x16x32_bf16 v[156:159], v[204:207], v[140:143], v[156:159]\n"
    "s_addc_u32 s41, s41, 0\n"
    "v_rcp_f32 v147, v147\n"
    "v_mfma_f32_16x16x32_bf16 v[152:155], v[200:203], v[128:131], v[152:155]\n"
    "ds_read_b64_tr_b16 v[196:197], v242 offset:6144\n"
    "ds_read_b64_tr_b16 v[198:199], v242 offset:6400\n"
    "v_mfma_f32_16x16x32_bf16 v[152:155], v[204:207], v[136:139], v[152:155]\n"
    "s_waitcnt lgkmcnt(4)\n"
    "v_exp_f32 v148, v148\n"
    "v_mfma_f32_16x16x32_bf16 v[160:163], v[184:187], v[0:3], 0\n"
    "ds_read_b64_tr_b16 v[200:201], v242 offset:8192\n"
    "ds_read_b64_tr_b16 v[202:203], v242 offset:8448\n"
    "v_mfma_f32_16x16x32_bf16 v[160:163], v[188:191], v[8:11], v[160:163]\n"
    "v_exp_f32 v149, v149\n"
    "v_mfma_f32_16x16x32_bf16 v[164:167], v[184:187], v[4:7], 0\n"
    "s_add_u32 m0, s54, s60\n"
    "ds_read_b64_tr_b16 v[204:205], v242 offset:10240\n"
    "ds_read_b64_tr_b16 v[206:207], v242 offset:10496\n"
    "v_mfma_f32_16x16x32_bf16 v[164:167], v[188:191], v[12:15], v[164:167]\n"
    "s_waitcnt lgkmcnt(4)\n"
    "buffer_load_dwordx4 v246, s[44:47], s68 offen lds\n"
    "v_mfma_f32_16x16x32_bf16 v[164:167], v[192:195], v[20:23], v[164:167]\n"
    "ds_read_b64_tr_b16 v[184:185], v242 offset:12288\n"
    "v_fma_f32 v148, v148, v249, v249\n"
    "v_mfma_f32_16x16x32_bf16 v[164:167], v[196:199], v[28:31], v[164:167]\n"
    "ds_read_b64_tr_b16 v[186:187], v242 offset:12544\n"
    "v_fma_f32 v149, v149, v249, v249\n"
    "v_mfma_f32_16x16x32_bf16 v[160:163], v[192:195], v[16:19], v[160:163]\n"
    "ds_read_b64_tr_b16 v[188:189], v242 offset:14336\n"
    "ds_read_b64_tr_b16 v[190:191], v242 offset:14592\n"
    "v_mfma_f32_16x16x32_bf16 v[160:163], v[196:199], v[24:27], v[160:163]\n"
    "s_waitcnt lgkmcnt(4)\n"
    "v_rcp_f32 v148, v148\n"
    "v_mfma_f32_16x16x32_bf16 v[160:163], v[200:203], v[32:35], v[160:163]\n"
    "ds_read_b64_tr_b16 v[192:193], v242 offset:16384\n"
    "ds_read_b64_tr_b16 v[194:195], v242 offset:16640\n"
    "v_mfma_f32_16x16x32_bf16 v[160:163], v[204:207], v[40:43], v[160:163]\n"
    "v_rcp_f32 v149, v149\n"
    "v_mfma_f32_16x16x32_bf16 v[164:167], v[200:203], v[36:39], v[164:167]\n"
    "s_add_u32 m0, m0, 0x400\n"
    "ds_read_b64_tr_b16 v[196:197], v242 offset:18432\n"
    "ds_read_b64_tr_b16 v[198:199], v242 offset:18688\n"
    "v_mfma_f32_16x16x32_bf16 v[164:167], v[204:207], v[44:47], v[164:167]\n"
    "s_waitcnt lgkmcnt(4)\n"
    "buffer_load_dwordx4 v246, s[44:47], s69 offen lds\n"
    "v_mfma_f32_16x16x32_bf16 v[164:167], v[184:187], v[52:55], v[164:167]\n"
    "ds_read_b64_tr_b16 v[200:201], v242 offset:20480\n"
    "ds_read_b64_tr_b16 v[202:203], v242 offset:20736\n"
    "v_mfma_f32_16x16x32_bf16 v[164:167], v[188:191], v[60:63], v[164:167]\n"
    "v_exp_f32 v150, v150\n"
    "v_mfma_f32_16x16x32_bf16 v[160:163], v[184:187], v[48:51], v[160:163]\n"
    "ds_read_b64_tr_b16 v[204:205], v242 offset:22528\n"
    "ds_read_b64_tr_b16 v[206:207], v242 offset:22784\n"
    "v_mfma_f32_16x16x32_bf16 v[160:163], v[188:191], v[56:59], v[160:163]\n"
    "s_waitcnt lgkmcnt(4)\n"
    "v_exp_f32 v151, v151\n"
    "v_mfma_f32_16x16x32_bf16 v[160:163], v[192:195], v[64:67], v[160:163]\n"
    "ds_read_b64_tr_b16 v[184:185], v242 offset:24576\n"
    "v_fma_f32 v150, v150, v249, v249\n"
    "v_mfma_f32_16x16x32_bf16 v[160:163], v[196:199], v[72:75], v[160:163]\n"
    "ds_read_b64_tr_b16 v[186:187], v242 offset:24832\n"
    "v_fma_f32 v151, v151, v249, v249\n"
    "v_mfma_f32_16x16x32_bf16 v[164:167], v[192:195], v[68:71], v[164:167]\n"
    "s_add_u32 m0, m0, 0x400\n"
    "ds_read_b64_tr_b16 v[188:189], v242 offset:26624\n"
    "ds_read_b64_tr_b16 v[190:191], v242 offset:26880\n"
    "v_mfma_f32_16x16x32_bf16 v[164:167], v[196:199], v[76:79], v[164:167]\n"
    "s_waitcnt lgkmcnt(4)\n"
    "buffer_load_dwordx4 v246, s[44:47], s70 offen lds\n"
    "v_mfma_f32_16x16x32_bf16 v[164:167], v[200:203], v[84:87], v[164:167]\n"
    "ds_read_b64_tr_b16 v[192:193], v242 offset:28672\n"
    "ds_read_b64_tr_b16 v[194:195], v242 offset:28928\n"
    "v_mfma_f32_16x16x32_bf16 v[164:167], v[204:207], v[92:95], v[164:167]\n"
    "v_rcp_f32 v150, v150\n"
    "v_mfma_f32_16x16x32_bf16 v[160:163], v[200:203], v[80:83], v[160:163]\n"
    "ds_read_b64_tr_b16 v[196:197], v242 offset:30720\n"
    "ds_read_b64_tr_b16 v[198:199], v242 offset:30976\n"
    "v_mfma_f32_16x16x32_bf16 v[160:163], v[204:207], v[88:91], v[160:163]\n"
    "s_waitcnt lgkmcnt(4)\n"
    "v_rcp_f32 v151, v151\n"
    "v_mfma_f32_16x16x32_bf16 v[160:163], v[184:187], v[96:99], v[160:163]\n"
    "ds_read_b64_tr_b16 v[200:201], v242 offset:32768\n"
    "ds_read_b64_tr_b16 v[202:203], v242 offset:33024\n"
    "v_mfma_f32_16x16x32_bf16 v[160:163], v[188:191], v[104:107], v[160:163]\n"
    "v_exp_f32 v152, v152\n"
    "v_mfma_f32_16x16x32_bf16 v[164:167], v[184:187], v[100:103], v[164:167]\n"
    "s_add_u32 m0, m0, 0x400\n"
    "ds_read_b64_tr_b16 v[204:205], v242 offset:34816\n"
    "ds_read_b64_tr_b16 v[206:207], v242 offset:35072\n"
    "v_mfma_f32_16x16x32_bf16 v[164:167], v[188:191], v[108:111], v[164:167]\n"
    "s_waitcnt lgkmcnt(4)\n"
    "buffer_load_dwordx4 v246, s[44:47], s71 offen lds\n"
    "v_mfma_f32_16x16x32_bf16 v[164:167], v[192:195], v[116:119], v[164:167]\n"
    "ds_read_b64_tr_b16 v[184:185], v243 offset:0\n"
    "v_add_u32 v242, s57, v240\n"
    "v_mfma_f32_16x16x32_bf16 v[164:167], v[196:199], v[124:127], v[164:167]\n"
    "ds_read_b64_tr_b16 v[186:187], v243 offset:256\n"
    "v_mfma_f32_16x16x32_bf16 v[160:163], v[192:195], v[112:115], v[160:163]\n"
    "ds_read_b64_tr_b16 v[188:189], v243 offset:2048\n"
    "ds_read_b64_tr_b16 v[190:191], v243 offset:2304\n"
    "v_mfma_f32_16x16x32_bf16 v[160:163], v[196:199], v[120:123], v[160:163]\n"
    "s_waitcnt lgkmcnt(4)\n"
    "v_exp_f32 v153, v153\n"
    "v_mfma_f32_16x16x32_bf16 v[160:163], v[200:203], v[128:131], v[160:163]\n"
    "ds_read_b64_tr_b16 v[192:193], v243 offset:4096\n"
    "v_fma_f32 v152, v152, v248, v248\n"
    "v_mfma_f32_16x16x32_bf16 v[160:163], v[204:207], v[136:139], v[160:163]\n"
    "ds_read_b64_tr_b16 v[194:195], v243 offset:4352\n"
    "v_fma_f32 v153, v153, v248, v248\n"
    "v_mfma_f32_16x16x32_bf16 v[164:167], v[200:203], v[132:135], v[164:167]\n"
    "s_add_u32 m0, m0, 0x400\n"
    "ds_read_b64_tr_b16 v[196:197], v243 offset:6144\n"
    "ds_read_b64_tr_b16 v[198:199], v243 offset:6400\n"
    "v_mfma_f32_16x16x32_bf16 v[164:167], v[204:207], v[140:143], v[164:167]\n"
    "s_waitcnt lgkmcnt(4)\n"
    "buffer_load_dwordx4 v246, s[44:47], s72 offen lds\n"
    "v_mfma_f32_16x16x32_bf16 v[172:175], v[184:187], v[4:7], 0\n"
    "ds_read_b64_tr_b16 v[200:201], v243 offset:8192\n"
    "ds_read_b64_tr_b16 v[202:203], v243 offset:8448\n"
    "v_mfma_f32_16x16x32_bf16 v[172:175], v[188:191], v[12:15], v[172:175]\n"
    "v_rcp_f32 v152, v152\n"
    "v_mfma_f32_16x16x32_bf16 v[168:171], v[184:187], v[0:3], 0\n"
    "ds_read_b64_tr_b16 v[204:205], v243 offset:10240\n"
    "ds_read_b64_tr_b16 v[206:207], v243 offset:10496\n"
    "v_mfma_f32_16x16x32_bf16 v[168:171], v[188:191], v[8:11], v[168:171]\n"
    "s_waitcnt lgkmcnt(4)\n"
    "v_rcp_f32 v153, v153\n"
    "v_mfma_f32_16x16x32_bf16 v[168:171], v[192:195], v[16:19], v[168:171]\n"
    "ds_read_b64_tr_b16 v[184:185], v243 offset:12288\n"
    "ds_read_b64_tr_b16 v[186:187], v243 offset:12544\n"
    "v_mfma_f32_16x16x32_bf16 v[168:171], v[196:199], v[24:27], v[168:171]\n"
    "v_exp_f32 v154, v154\n"
    "v_mfma_f32_16x16x32_bf16 v[172:175], v[192:195], v[20:23], v[172:175]\n"
    "s_add_u32 m0, m0, 0x400\n"
    "ds_read_b64_tr_b16 v[188:189], v243 offset:14336\n"
    "ds_read_b64_tr_b16 v[190:191], v243 offset:14592\n"
    "v_mfma_f32_16x16x32_bf16 v[172:175], v[196:199], v[28:31], v[172:175]\n"
    "s_waitcnt lgkmcnt(4)\n"
    "buffer_load_dwordx4 v246, s[44:47], s73 offen lds\n"
    "v_mfma_f32_16x16x32_bf16 v[172:175], v[200:203], v[36:39], v[172:175]\n"
    "ds_read_b64_tr_b16 v[192:193], v243 offset:16384\n"
    "ds_read_b64_tr_b16 v[194:195], v243 offset:16640\n"
    "v_mfma_f32_16x16x32_bf16 v[172:175], v[204:207], v[44:47], v[172:175]\n"
    "v_exp_f32 v155, v155\n"
    "v_mfma_f32_16x16x32_bf16 v[168:171], v[200:203], v[32:35], v[168:171]\n"
    "ds_read_b64_tr_b16 v[196:197], v243 offset:18432\n"
    "v_fma_f32 v154, v154, v248, v248\n"
    "v_mfma_f32_16x16x32_bf16 v[168:171], v[204:207], v[40:43], v[168:171]\n"
    "s_waitcnt lgkmcnt(3)\n"
    "ds_read_b64_tr_b16 v[198:199], v243 offset:18688\n"
    "v_fma_f32 v155, v155, v248, v248\n"
    "v_mfma_f32_16x16x32_bf16 v[168:171], v[184:187], v[48:51], v[168:171]\n"
    "ds_read_b64_tr_b16 v[200:201], v243 offset:20480\n"
    "ds_read_b64_tr_b16 v[202:203], v243 offset:20736\n"
    "v_mfma_f32_16x16x32_bf16 v[168:171], v[188:191], v[56:59], v[168:171]\n"
    "v_rcp_f32 v154, v154\n"
    "v_mfma_f32_16x16x32_bf16 v[172:175], v[184:187], v[52:55], v[172:175]\n"
    "s_add_u32 m0, m0, 0x400\n"
    "ds_read_b64_tr_b16 v[204:205], v243 offset:22528\n"
    "ds_read_b64_tr_b16 v[206:207], v243 offset:22784\n"
    "v_mfma_f32_16x16x32_bf16 v[172:175], v[188:191], v[60:63], v[172:175]\n"
    "s_waitcnt lgkmcnt(4)\n"
    "buffer_load_dwordx4 v246, s[44:47], s74 offen lds\n"
    "v_mfma_f32_16x16x32_bf16 v[172:175], v[192:195], v[68:71], v[172:175]\n"
    "ds_read_b64_tr_b16 v[184:185], v243 offset:24576\n"
    "ds_read_b64_tr_b16 v[186:187], v243 offset:24832\n"
    "v_mfma_f32_16x16x32_bf16 v[172:175], v[196:199], v[76:79], v[172:175]\n"
    "v_rcp_f32 v155, v155\n"
    "v_mfma_f32_16x16x32_bf16 v[168:171], v[192:195], v[64:67], v[168:171]\n"
    "ds_read_b64_tr_b16 v[188:189], v243 offset:26624\n"
    "ds_read_b64_tr_b16 v[190:191], v243 offset:26880\n"
    "v_mfma_f32_16x16x32_bf16 v[168:171], v[196:199], v[72:75], v[168:171]\n"
    "s_waitcnt lgkmcnt(4)\n"
    "v_exp_f32 v156, v156\n"
    "v_mfma_f32_16x16x32_bf16 v[168:171], v[200:203], v[80:83], v[168:171]\n"
    "ds_read_b64_tr_b16 v[192:193], v243 offset:28672\n"
    "ds_read_b64_tr_b16 v[194:195], v243 offset:28928\n"
    "v_mfma_f32_16x16x32_bf16 v[168:171], v[204:207], v[88:91], v[168:171]\n"
    "v_exp_f32 v157, v157\n"
    "v_mfma_f32_16x16x32_bf16 v[172:175], v[200:203], v[84:87], v[172:175]\n"
    "s_add_u32 m0, m0, 0x400\n"
    "ds_read_b64_tr_b16 v[196:197], v243 offset:30720\n"
    "ds_read_b64_tr_b16 v[198:199], v243 offset:30976\n"
    "v_mfma_f32_16x16x32_bf16 v[172:175], v[204:207], v[92:95], v[172:175]\n"
    "s_waitcnt lgkmcnt(4)\n"
    "buffer_load_dwordx4 v246, s[44:47], s75 offen lds\n"
    "v_mfma_f32_16x16x32_bf16 v[172:175], v[184:187], v[100:103], v[172:175]\n"
    "ds_read_b64_tr_b16 v[200:201], v243 offset:32768\n"
    "v_fma_f32 v156, v156, v249, v249\n"
    "v_mfma_f32_16x16x32_bf16 v[172:175], v[188:191], v[108:111], v[172:175]\n"
    "ds_read_b64_tr_b16 v[202:203], v243 offset:33024\n"
    "v_fma_f32 v157, v157, v249, v249\n"
    "v_mfma_f32_16x16x32_bf16 v[168:171], v[184:187], v[96:99], v[168:171]\n"
    "ds_read_b64_tr_b16 v[204:205], v243 offset:34816\n"
    "ds_read_b64_tr_b16 v[206:207], v243 offset:35072\n"
    "v_mfma_f32_16x16x32_bf16 v[168:171], v[188:191], v[104:107], v[168:171]\n"
    "s_waitcnt lgkmcnt(4)\n"
    "v_rcp_f32 v156, v156\n"
    "v_mfma_f32_16x16x32_bf16 v[168:171], v[192:195], v[112:115], v[168:171]\n"
    "ds_read_b64_tr_b16 v[184:185], v242 offset:0\n"
    "ds_read_b64_tr_b16 v[186:187], v242 offset:256\n"
    "v_mfma_f32_16x16x32_bf16 v[168:171], v[196:199], v[120:123], v[168:171]\n"
    "v_rcp_f32 v157, v157\n"
    "v_mfma_f32_16x16x32_bf16 v[172:175], v[192:195], v[116:119], v[172:175]\n"
    "s_add_u32 m0, m0, 0x400\n"
    "ds_read_b64_tr_b16 v[188:189], v242 offset:2048\n"
    "ds_read_b64_tr_b16 v[190:191], v242 offset:2304\n"
    "v_mfma_f32_16x16x32_bf16 v[172:175], v[196:199], v[124:127], v[172:175]\n"
    "s_waitcnt vmcnt(8) lgkmcnt(4)\n"
    "s_barrier\n"
    "buffer_load_dwordx4 v246, s[44:47], s76 offen lds\n"
    "v_mfma_f32_16x16x32_bf16 v[172:175], v[200:203], v[132:135], v[172:175]\n"
    "s_add_u32 s44, s44, 64\n"
    "ds_read_b64_tr_b16 v[192:193], v242 offset:4096\n"
    "ds_read_b64_tr_b16 v[194:195], v242 offset:4352\n"
    "v_mfma_f32_16x16x32_bf16 v[172:175], v[204:207], v[140:143], v[172:175]\n"
    "s_addc_u32 s45, s45, 0\n"
    "v_exp_f32 v158, v158\n"
    "v_mfma_f32_16x16x32_bf16 v[168:171], v[200:203], v[128:131], v[168:171]\n"
    "ds_read_b64_tr_b16 v[196:197], v242 offset:6144\n"
    "ds_read_b64_tr_b16 v[198:199], v242 offset:6400\n"
    "v_mfma_f32_16x16x32_bf16 v[168:171], v[204:207], v[136:139], v[168:171]\n"
    "s_waitcnt lgkmcnt(4)\n"
    "v_exp_f32 v159, v159\n"
    "v_fma_f32 v158, v158, v249, v249\n"
    "v_fma_f32 v159, v159, v249, v249\n"
    "v_rcp_f32 v158, v158\n"
    "v_rcp_f32 v159, v159\n"
    "v_mul_f32 v160, v144, v160\n"
    "v_mul_f32 v161, v145, v161\n"
    "v_mul_f32 v162, v146, v162\n"
    "v_mul_f32 v163, v147, v163\n"
    "v_mul_f32 v164, v148, v164\n"
    "v_mul_f32 v165, v149, v165\n"
    "v_mul_f32 v166, v150, v166\n"
    "v_mul_f32 v167, v151, v167\n"
    "95:\n"
    "v_mfma_f32_16x16x32_bf16 v[144:147], v[184:187], v[0:3], 0\n"
    "s_mul_i32 s34, %7, 3072\n"
    "ds_read_b64_tr_b16 v[200:201], v242 offset:8192\n"
    "v_xor_b32 v243, 32, v242\n"
    "v_mfma_f32_16x16x32_bf16 v[144:147], v[188:191], v[8:11], v[144:147]\n"
    "s_add_u32 s34, s34, %8\n"
    "ds_read_b64_tr_b16 v[202:203], v242 offset:8448\n"
    "v_mfma_f32_16x16x32_bf16 v[148:151], v[184:187], v[4:7], 0\n"
    "s_add_u32 m0, s55, s61\n"
    "ds_read_b64_tr_b16 v[204:205], v242 offset:10240\n"
    "ds_read_b64_tr_b16 v[206:207], v242 offset:10496\n"
    "v_mfma_f32_16x16x32_bf16 v[148:151], v[188:191], v[12:15], v[148:151]\n"
    "s_add_u32 s34, s34, 148480\n"
    "s_waitcnt lgkmcnt(4)\n"
    "buffer_load_dwordx4 v247, s[48:51], s52 offen lds\n"
    "v_mfma_f32_16x16x32_bf16 v[148:151], v[192:195], v[20:23], v[148:151]\n"
    "s_mov_b64 s[40:41], %3\n"
    "ds_read_b64_tr_b16 v[184:185], v242 offset:12288\n"
    "v_lshlrev_b32 v254, 4, v255\n"
    "v_mfma_f32_16x16x32_bf16 v[148:151], v[196:199], v[28:31], v[148:151]\n"
    "ds_read_b64_tr_b16 v[186:187], v242 offset:12544\n"
    "v_mfma_f32_16x16x32_bf16 v[144:147], v[192:195], v[16:19], v[144:147]\n"
    "ds_read_b64_tr_b16 v[188:189], v242 offset:14336\n"
    "v_add_u32 v244, s34, v254\n"
    "v_mfma_f32_16x16x32_bf16 v[144:147], v[196:199], v[24:27], v[144:147]\n"
    "s_waitcnt lgkmcnt(3)\n"
    "ds_read_b64_tr_b16 v[190:191], v242 offset:14592\n"
    "v_mfma_f32_16x16x32_bf16 v[144:147], v[200:203], v[32:35], v[144:147]\n"
    "ds_read_b64_tr_b16 v[192:193], v242 offset:16384\n"
    "v_mul_f32 v168, v152, v168\n"
    "v_mfma_f32_16x16x32_bf16 v[144:147], v[204:207], v[40:43], v[144:147]\n"
    "ds_read_b64_tr_b16 v[194:195], v242 offset:16640\n"
    "v_mul_f32 v169, v153, v169\n"
    "v_mfma_f32_16x16x32_bf16 v[148:151], v[200:203], v[36:39], v[148:151]\n"
    "s_add_u32 m0, m0, 0x1000\n"
    "ds_read_b64_tr_b16 v[196:197], v242 offset:18432\n"
    "ds_read_b64_tr_b16 v[198:199], v242 offset:18688\n"
    "v_mfma_f32_16x16x32_bf16 v[148:151], v[204:207], v[44:47], v[148:151]\n"
    "s_waitcnt lgkmcnt(4)\n"
    "buffer_load_dwordx4 v247, s[48:51], s53 offen lds\n"
    "v_mfma_f32_16x16x32_bf16 v[148:151], v[184:187], v[52:55], v[148:151]\n"
    "s_and_b32 s41, s41, 0xffff\n"
    "ds_read_b64_tr_b16 v[200:201], v242 offset:20480\n"
    "v_mul_f32 v170, v154, v170\n"
    "v_mfma_f32_16x16x32_bf16 v[148:151], v[188:191], v[60:63], v[148:151]\n"
    "ds_read_b64_tr_b16 v[202:203], v242 offset:20736\n"
    "v_mul_f32 v171, v155, v171\n"
    "v_mfma_f32_16x16x32_bf16 v[144:147], v[184:187], v[48:51], v[144:147]\n"
    "ds_read_b64_tr_b16 v[204:205], v242 offset:22528\n"
    "v_mul_f32 v172, v156, v172\n"
    "v_mfma_f32_16x16x32_bf16 v[144:147], v[188:191], v[56:59], v[144:147]\n"
    "s_waitcnt lgkmcnt(3)\n"
    "ds_read_b64_tr_b16 v[206:207], v242 offset:22784\n"
    "v_mul_f32 v173, v157, v173\n"
    "v_mfma_f32_16x16x32_bf16 v[144:147], v[192:195], v[64:67], v[144:147]\n"
    "ds_read_b64_tr_b16 v[184:185], v242 offset:24576\n"
    "v_mul_f32 v174, v158, v174\n"
    "v_mfma_f32_16x16x32_bf16 v[144:147], v[196:199], v[72:75], v[144:147]\n"
    "ds_read_b64_tr_b16 v[186:187], v242 offset:24832\n"
    "v_mul_f32 v175, v159, v175\n"
    "v_mfma_f32_16x16x32_bf16 v[148:151], v[192:195], v[68:71], v[148:151]\n"
    "s_add_u32 m0, m0, 0x1000\n"
    "ds_read_b64_tr_b16 v[188:189], v242 offset:26624\n"
    "ds_read_b64_tr_b16 v[190:191], v242 offset:26880\n"
    "v_mfma_f32_16x16x32_bf16 v[148:151], v[196:199], v[76:79], v[148:151]\n"
    "s_waitcnt lgkmcnt(4)\n"
    "buffer_load_dwordx4 v247, s[48:51], s58 offen lds\n"
    "v_mfma_f32_16x16x32_bf16 v[148:151], v[200:203], v[84:87], v[148:151]\n"
    "s_mov_b32 s42, 663552\n"
    "ds_read_b64_tr_b16 v[192:193], v242 offset:28672\n"
    "v_cvt_pk_bf16_f32 v250, v160, v161\n"
    "v_mfma_f32_16x16x32_bf16 v[148:151], v[204:207], v[92:95], v[148:151]\n"
    "ds_read_b64_tr_b16 v[194:195], v242 offset:28928\n"
    "v_cvt_pk_bf16_f32 v251, v162, v163\n"
    "v_mfma_f32_16x16x32_bf16 v[144:147], v[200:203], v[80:83], v[144:147]\n"
    "ds_read_b64_tr_b16 v[196:197], v242 offset:30720\n"
    "v_cvt_pk_bf16_f32 v252, v168, v169\n"
    "v_mfma_f32_16x16x32_bf16 v[144:147], v[204:207], v[88:91], v[144:147]\n"
    "s_waitcnt lgkmcnt(3)\n"
    "ds_read_b64_tr_b16 v[198:199], v242 offset:30976\n"
    "v_cvt_pk_bf16_f32 v253, v170, v171\n"
    "v_mfma_f32_16x16x32_bf16 v[144:147], v[184:187], v[96:99], v[144:147]\n"
    "ds_read_b64_tr_b16 v[200:201], v242 offset:32768\n"
    "ds_write_b128 v244, v[250:253] offset:0\n"
    "v_mfma_f32_16x16x32_bf16 v[144:147], v[188:191], v[104:107], v[144:147]\n"
    "ds_read_b64_tr_b16 v[202:203], v242 offset:33024\n"
    "v_mfma_f32_16x16x32_bf16 v[148:151], v[184:187], v[100:103], v[148:151]\n"
    "s_add_u32 m0, m0, 0x1000\n"
    "ds_read_b64_tr_b16 v[204:205], v242 offset:34816\n"
    "ds_read_b64_tr_b16 v[206:207], v242 offset:35072\n"
    "v_mfma_f32_16x16x32_bf16 v[148:151], v[188:191], v[108:111], v[148:151]\n"
    "s_waitcnt lgkmcnt(5)\n"
    "buffer_load_dwordx4 v247, s[48:51], s59 offen lds\n"
    "v_mfma_f32_16x16x32_bf16 v[148:151], v[192:195], v[116:119], v[148:151]\n"
    "s_mul_i32 s69, %7, 165888\n"
    "ds_read_b64_tr_b16 v[184:185], v243 offset:0\n"
    "v_add_u32 v242, s54, v240\n"
    "v_mfma_f32_16x16x32_bf16 v[148:151], v[196:199], v[124:127], v[148:151]\n"
    "ds_read_b64_tr_b16 v[186:187], v243 offset:256\n"
    "v_mfma_f32_16x16x32_bf16 v[144:147], v[192:195], v[112:115], v[144:147]\n"
    "ds_read_b64_tr_b16 v[188:189], v243 offset:2048\n"
    "v_cvt_pk_bf16_f32 v250, v164, v165\n"
    "v_mfma_f32_16x16x32_bf16 v[144:147], v[196:199], v[120:123], v[144:147]\n"
    "s_waitcnt lgkmcnt(3)\n"
    "ds_read_b64_tr_b16 v[190:191], v243 offset:2304\n"
    "v_cvt_pk_bf16_f32 v251, v166, v167\n"
    "v_mfma_f32_16x16x32_bf16 v[144:147], v[200:203], v[128:131], v[144:147]\n"
    "ds_read_b64_tr_b16 v[192:193], v243 offset:4096\n"
    "v_cvt_pk_bf16_f32 v252, v172, v173\n"
    "v_mfma_f32_16x16x32_bf16 v[144:147], v[204:207], v[136:139], v[144:147]\n"
    "ds_read_b64_tr_b16 v[194:195], v243 offset:4352\n"
    "v_cvt_pk_bf16_f32 v253, v174, v175\n"
    "v_mfma_f32_16x16x32_bf16 v[148:151], v[200:203], v[132:135], v[148:151]\n"
    "s_add_u32 m0, m0, 0x1000\n"
    "ds_read_b64_tr_b16 v[196:197], v243 offset:6144\n"
    "ds_read_b64_tr_b16 v[198:199], v243 offset:6400\n"
    "v_mfma_f32_16x16x32_bf16 v[148:151], v[204:207], v[140:143], v[148:151]\n"
    "s_waitcnt lgkmcnt(4)\n"
    "buffer_load_dwordx4 v247, s[48:51], s63 offen lds\n"
    "v_mfma_f32_16x16x32_bf16 v[156:159], v[184:187], v[4:7], 0\n"
    "s_lshl_b32 s70, %13, 1\n"
    "ds_read_b64_tr_b16 v[200:201], v243 offset:8192\n"
    "ds_write_b128 v244, v[250:253] offset:1024\n"
    "v_mfma_f32_16x16x32_bf16 v[156:159], v[188:191], v[12:15], v[156:159]\n"
    "ds_read_b64_tr_b16 v[202:203], v243 offset:8448\n"
    "v_mfma_f32_16x16x32_bf16 v[152:155], v[184:187], v[0:3], 0\n"
    "ds_read_b64_tr_b16 v[204:205], v243 offset:10240\n"
    "v_mfma_f32_16x16x32_bf16 v[152:155], v[188:191], v[8:11], v[152:155]\n"
    "s_waitcnt lgkmcnt(4)\n"
    "ds_read_b64_tr_b16 v[206:207], v243 offset:10496\n"
    "v_mfma_f32_16x16x32_bf16 v[152:155], v[192:195], v[16:19], v[152:155]\n"
    "ds_read_b64_tr_b16 v[184:185], v243 offset:12288\n"
    "v_lshrrev_b32 v250, 2, v255\n"
    "v_mfma_f32_16x16x32_bf16 v[152:155], v[196:199], v[24:27], v[152:155]\n"
    "ds_read_b64_tr_b16 v[186:187], v243 offset:12544\n"
    "v_and_b32 v251, 3, v255\n"
    "v_mfma_f32_16x16x32_bf16 v[156:159], v[192:195], v[20:23], v[156:159]\n"
    "s_add_u32 m0, m0, 0x1000\n"
    "ds_read_b64_tr_b16 v[188:189], v243 offset:14336\n"
    "ds_read_b64_tr_b16 v[190:191], v243 offset:14592\n"
    "v_mfma_f32_16x16x32_bf16 v[156:159], v[196:199], v[28:31], v[156:159]\n"
    "s_waitcnt lgkmcnt(4)\n"
    "buffer_load_dwordx4 v247, s[48:51], s64 offen lds\n"
    "v_mfma_f32_16x16x32_bf16 v[156:159], v[200:203], v[36:39], v[156:159]\n"
    "s_add_u32 s71, s70, 1\n"
    "ds_read_b64_tr_b16 v[192:193], v243 offset:16384\n"
    "v_mul_u32_u24 v250, 0x480, v250\n"
    "v_mfma_f32_16x16x32_bf16 v[156:159], v[204:207], v[44:47], v[156:159]\n"
    "ds_read_b64_tr_b16 v[194:195], v243 offset:16640\n"
    "v_bfe_u32 v252, v255, 4, 1\n"
    "v_mfma_f32_16x16x32_bf16 v[152:155], v[200:203], v[32:35], v[152:155]\n"
    "ds_read_b64_tr_b16 v[196:197], v243 offset:18432\n"
    "v_lshlrev_b32 v252, 1, v252\n"
    "v_mfma_f32_16x16x32_bf16 v[152:155], v[204:207], v[40:43], v[152:155]\n"
    "s_waitcnt lgkmcnt(3)\n"
    "ds_read_b64_tr_b16 v[198:199], v243 offset:18688\n"
    "v_mfma_f32_16x16x32_bf16 v[152:155], v[184:187], v[48:51], v[152:155]\n"
    "ds_read_b64_tr_b16 v[200:201], v243 offset:20480\n"
    "v_xor_b32 v251, v251, v252\n"
    "v_mfma_f32_16x16x32_bf16 v[152:155], v[188:191], v[56:59], v[152:155]\n"
    "ds_read_b64_tr_b16 v[202:203], v243 offset:20736\n"
    "v_mfma_f32_16x16x32_bf16 v[156:159], v[184:187], v[52:55], v[156:159]\n"
    "s_add_u32 m0, m0, 0x1000\n"
    "ds_read_b64_tr_b16 v[204:205], v243 offset:22528\n"
    "ds_read_b64_tr_b16 v[206:207], v243 offset:22784\n"
    "v_mfma_f32_16x16x32_bf16 v[156:159], v[188:191], v[60:63], v[156:159]\n"
    "s_waitcnt lgkmcnt(4)\n"
    "buffer_load_dwordx4 v247, s[48:51], s65 offen lds\n"
    "v_mfma_f32_16x16x32_bf16 v[156:159], v[192:195], v[68:71], v[156:159]\n"
    "s_lshl_b32 s68, s70, 6\n"
    "ds_read_b64_tr_b16 v[184:185], v243 offset:24576\n"
    "v_lshl_add_u32 v250, v251, 4, v250\n"
    "v_mfma_f32_16x16x32_bf16 v[156:159], v[196:199], v[76:79], v[156:159]\n"
    "ds_read_b64_tr_b16 v[186:187], v243 offset:24832\n"
    "v_mfma_f32_16x16x32_bf16 v[152:155], v[192:195], v[64:67], v[152:155]\n"
    "ds_read_b64_tr_b16 v[188:189], v243 offset:26624\n"
    "ds_read_b64_tr_b16 v[190:191], v243 offset:26880\n"
    "v_mfma_f32_16x16x32_bf16 v[152:155], v[196:199], v[72:75], v[152:155]\n"
    "s_waitcnt lgkmcnt(4)\n"
    "v_exp_f32 v144, v144\n"
    "v_mfma_f32_16x16x32_bf16 v[152:155], v[200:203], v[80:83], v[152:155]\n"
    "ds_read_b64_tr_b16 v[192:193], v243 offset:28672\n"
    "ds_read_b64_tr_b16 v[194:195], v243 offset:28928\n"
    "v_mfma_f32_16x16x32_bf16 v[152:155], v[204:207], v[88:91], v[152:155]\n"
    "v_exp_f32 v145, v145\n"
    "v_mfma_f32_16x16x32_bf16 v[156:159], v[200:203], v[84:87], v[156:159]\n"
    "s_add_u32 m0, m0, 0x1000\n"
    "ds_read_b64_tr_b16 v[196:197], v243 offset:30720\n"
    "ds_read_b64_tr_b16 v[198:199], v243 offset:30976\n"
    "v_mfma_f32_16x16x32_bf16 v[156:159], v[204:207], v[92:95], v[156:159]\n"
    "s_waitcnt lgkmcnt(4)\n"
    "buffer_load_dwordx4 v247, s[48:51], s77 offen lds\n"
    "v_mfma_f32_16x16x32_bf16 v[156:159], v[184:187], v[100:103], v[156:159]\n"
    "s_add_u32 s68, s68, s69\n"
    "ds_read_b64_tr_b16 v[200:201], v243 offset:32768\n"
    "v_fma_f32 v144, v144, v248, v248\n"
    "v_mfma_f32_16x16x32_bf16 v[156:159], v[188:191], v[108:111], v[156:159]\n"
    "ds_read_b64_tr_b16 v[202:203], v243 offset:33024\n"
    "v_fma_f32 v145, v145, v248, v248\n"
    "v_mfma_f32_16x16x32_bf16 v[152:155], v[184:187], v[96:99], v[152:155]\n"
    "ds_read_b64_tr_b16 v[204:205], v243 offset:34816\n"
    "ds_read_b64_tr_b16 v[206:207], v243 offset:35072\n"
    "v_mfma_f32_16x16x32_bf16 v[152:155], v[188:191], v[104:107], v[152:155]\n"
    "s_waitcnt lgkmcnt(4)\n"
    "v_rcp_f32 v144, v144\n"
    "v_mfma_f32_16x16x32_bf16 v[152:155], v[192:195], v[112:115], v[152:155]\n"
    "ds_read_b64_tr_b16 v[184:185], v242 offset:0\n"
    "v_xor_b32 v243, 32, v242\n"
    "v_mfma_f32_16x16x32_bf16 v[152:155], v[196:199], v[120:123], v[152:155]\n"
    "ds_read_b64_tr_b16 v[186:187], v242 offset:256\n"
    "v_mfma_f32_16x16x32_bf16 v[156:159], v[192:195], v[116:119], v[156:159]\n"
    "s_add_u32 m0, m0, 0x1000\n"
    "ds_read_b64_tr_b16 v[188:189], v242 offset:2048\n"
    "ds_read_b64_tr_b16 v[190:191], v242 offset:2304\n"
    "v_mfma_f32_16x16x32_bf16 v[156:159], v[196:199], v[124:127], v[156:159]\n"
    "s_waitcnt vmcnt(8) lgkmcnt(4)\n"
    "s_barrier\n"
    "buffer_load_dwordx4 v247, s[48:51], s79 offen lds\n"
    "v_mfma_f32_16x16x32_bf16 v[156:159], v[200:203], v[132:135], v[156:159]\n"
    "s_add_u32 s48, s48, 36864\n"
    "ds_read_b64_tr_b16 v[192:193], v242 offset:4096\n"
    "ds_read_b64_tr_b16 v[194:195], v242 offset:4352\n"
    "v_mfma_f32_16x16x32_bf16 v[156:159], v[204:207], v[140:143], v[156:159]\n"
    "s_addc_u32 s49, s49, 0\n"
    "v_rcp_f32 v145, v145\n"
    "v_mfma_f32_16x16x32_bf16 v[152:155], v[200:203], v[128:131], v[152:155]\n"
    "ds_read_b64_tr_b16 v[196:197], v242 offset:6144\n"
    "ds_read_b64_tr_b16 v[198:199], v242 offset:6400\n"
    "v_mfma_f32_16x16x32_bf16 v[152:155], v[204:207], v[136:139], v[152:155]\n"
    "s_waitcnt lgkmcnt(4)\n"
    "v_exp_f32 v146, v146\n"
    "v_mfma_f32_16x16x32_bf16 v[160:163], v[184:187], v[0:3], 0\n"
    "s_add_u32 m0, s57, s61\n"
    "ds_read_b64_tr_b16 v[200:201], v242 offset:8192\n"
    "ds_read_b64_tr_b16 v[202:203], v242 offset:8448\n"
    "v_mfma_f32_16x16x32_bf16 v[160:163], v[188:191], v[8:11], v[160:163]\n"
    "buffer_load_dwordx4 v247, s[48:51], s52 offen lds\n"
    "v_mfma_f32_16x16x32_bf16 v[164:167], v[184:187], v[4:7], 0\n"
    "ds_read_b64_tr_b16 v[204:205], v242 offset:10240\n"
    "ds_read_b64_tr_b16 v[206:207], v242 offset:10496\n"
    "v_mfma_f32_16x16x32_bf16 v[164:167], v[188:191], v[12:15], v[164:167]\n"
    "s_waitcnt lgkmcnt(4)\n"
    "v_exp_f32 v147, v147\n"
    "v_mfma_f32_16x16x32_bf16 v[164:167], v[192:195], v[20:23], v[164:167]\n"
    "s_lshl_b32 s67, %6, 4\n"
    "ds_read_b64_tr_b16 v[184:185], v242 offset:12288\n"
    "v_fma_f32 v146, v146, v248, v248\n"
    "v_mfma_f32_16x16x32_bf16 v[164:167], v[196:199], v[28:31], v[164:167]\n"
    "ds_read_b64_tr_b16 v[186:187], v242 offset:12544\n"
    "v_fma_f32 v147, v147, v248, v248\n"
    "v_mfma_f32_16x16x32_bf16 v[160:163], v[192:195], v[16:19], v[160:163]\n"
    "s_add_u32 m0, m0, 0x1000\n"
    "ds_read_b64_tr_b16 v[188:189], v242 offset:14336\n"
    "ds_read_b64_tr_b16 v[190:191], v242 offset:14592\n"
    "v_mfma_f32_16x16x32_bf16 v[160:163], v[196:199], v[24:27], v[160:163]\n"
    "s_waitcnt lgkmcnt(4)\n"
    "buffer_load_dwordx4 v247, s[48:51], s53 offen lds\n"
    "v_mfma_f32_16x16x32_bf16 v[160:163], v[200:203], v[32:35], v[160:163]\n"
    "ds_read_b64_tr_b16 v[192:193], v242 offset:16384\n"
    "ds_read_b64_tr_b16 v[194:195], v242 offset:16640\n"
    "v_mfma_f32_16x16x32_bf16 v[160:163], v[204:207], v[40:43], v[160:163]\n"
    "v_rcp_f32 v146, v146\n"
    "v_mfma_f32_16x16x32_bf16 v[164:167], v[200:203], v[36:39], v[164:167]\n"
    "s_add_u32 s67, s67, %5\n"
    "ds_read_b64_tr_b16 v[196:197], v242 offset:18432\n"
    "ds_read_b64_tr_b16 v[198:199], v242 offset:18688\n"
    "v_mfma_f32_16x16x32_bf16 v[164:167], v[204:207], v[44:47], v[164:167]\n"
    "s_waitcnt lgkmcnt(4)\n"
    "v_rcp_f32 v147, v147\n"
    "v_mfma_f32_16x16x32_bf16 v[164:167], v[184:187], v[52:55], v[164:167]\n"
    "s_add_u32 m0, m0, 0x1000\n"
    "ds_read_b64_tr_b16 v[200:201], v242 offset:20480\n"
    "ds_read_b64_tr_b16 v[202:203], v242 offset:20736\n"
    "v_mfma_f32_16x16x32_bf16 v[164:167], v[188:191], v[60:63], v[164:167]\n"
    "buffer_load_dwordx4 v247, s[48:51], s58 offen lds\n"
    "v_mfma_f32_16x16x32_bf16 v[160:163], v[184:187], v[48:51], v[160:163]\n"
    "ds_read_b64_tr_b16 v[204:205], v242 offset:22528\n"
    "ds_read_b64_tr_b16 v[206:207], v242 offset:22784\n"
    "v_mfma_f32_16x16x32_bf16 v[160:163], v[188:191], v[56:59], v[160:163]\n"
    "s_waitcnt lgkmcnt(4)\n"
    "v_exp_f32 v148, v148\n"
    "v_mfma_f32_16x16x32_bf16 v[160:163], v[192:195], v[64:67], v[160:163]\n"
    "s_lshl_b32 s67, s67, 2\n"
    "ds_read_b64_tr_b16 v[184:185], v242 offset:24576\n"
    "ds_read_b64_tr_b16 v[186:187], v242 offset:24832\n"
    "v_mfma_f32_16x16x32_bf16 v[160:163], v[196:199], v[72:75], v[160:163]\n"
    "v_exp_f32 v149, v149\n"
    "v_mfma_f32_16x16x32_bf16 v[164:167], v[192:195], v[68:71], v[164:167]\n"
    "s_add_u32 m0, m0, 0x1000\n"
    "ds_read_b64_tr_b16 v[188:189], v242 offset:26624\n"
    "ds_read_b64_tr_b16 v[190:191], v242 offset:26880\n"
    "v_mfma_f32_16x16x32_bf16 v[164:167], v[196:199], v[76:79], v[164:167]\n"
    "s_waitcnt lgkmcnt(4)\n"
    "buffer_load_dwordx4 v247, s[48:51], s59 offen lds\n"
    "v_mfma_f32_16x16x32_bf16 v[164:167], v[200:203], v[84:87], v[164:167]\n"
    "ds_read_b64_tr_b16 v[192:193], v242 offset:28672\n"
    "v_fma_f32 v148, v148, v249, v249\n"
    "v_mfma_f32_16x16x32_bf16 v[164:167], v[204:207], v[92:95], v[164:167]\n"
    "ds_read_b64_tr_b16 v[194:195], v242 offset:28928\n"
    "v_fma_f32 v149, v149, v249, v249\n"
    "v_mfma_f32_16x16x32_bf16 v[160:163], v[200:203], v[80:83], v[160:163]\n"
    "s_add_u32 s67, s67, %7\n"
    "ds_read_b64_tr_b16 v[196:197], v242 offset:30720\n"
    "ds_read_b64_tr_b16 v[198:199], v242 offset:30976\n"
    "v_mfma_f32_16x16x32_bf16 v[160:163], v[204:207], v[88:91], v[160:163]\n"
    "s_waitcnt lgkmcnt(4)\n"
    "v_rcp_f32 v148, v148\n"
    "v_mfma_f32_16x16x32_bf16 v[160:163], v[184:187], v[96:99], v[160:163]\n"
    "s_add_u32 m0, m0, 0x1000\n"
    "ds_read_b64_tr_b16 v[200:201], v242 offset:32768\n"
    "ds_read_b64_tr_b16 v[202:203], v242 offset:33024\n"
    "v_mfma_f32_16x16x32_bf16 v[160:163], v[188:191], v[104:107], v[160:163]\n"
    "buffer_load_dwordx4 v247, s[48:51], s63 offen lds\n"
    "v_mfma_f32_16x16x32_bf16 v[164:167], v[184:187], v[100:103], v[164:167]\n"
    "ds_read_b64_tr_b16 v[204:205], v242 offset:34816\n"
    "ds_read_b64_tr_b16 v[206:207], v242 offset:35072\n"
    "v_mfma_f32_16x16x32_bf16 v[164:167], v[188:191], v[108:111], v[164:167]\n"
    "s_waitcnt lgkmcnt(4)\n"
    "v_rcp_f32 v149, v149\n"
    "v_mfma_f32_16x16x32_bf16 v[164:167], v[192:195], v[116:119], v[164:167]\n"
    "s_mul_i32 s67, s67, 36864\n"
    "ds_read_b64_tr_b16 v[184:185], v243 offset:0\n"
    "v_add_u32 v242, s56, v241\n"
    "v_mfma_f32_16x16x32_bf16 v[164:167], v[196:199], v[124:127], v[164:167]\n"
    "ds_read_b64_tr_b16 v[186:187], v243 offset:256\n"
    "v_mfma_f32_16x16x32_bf16 v[160:163], v[192:195], v[112:115], v[160:163]\n"
    "s_add_u32 m0, m0, 0x1000\n"
    "ds_read_b64_tr_b16 v[188:189], v243 offset:2048\n"
    "ds_read_b64_tr_b16 v[190:191], v243 offset:2304\n"
    "v_mfma_f32_16x16x32_bf16 v[160:163], v[196:199], v[120:123], v[160:163]\n"
    "s_waitcnt lgkmcnt(4)\n"
    "buffer_load_dwordx4 v247, s[48:51], s64 offen lds\n"
    "v_mfma_f32_16x16x32_bf16 v[160:163], v[200:203], v[128:131], v[160:163]\n"
    "ds_read_b64_tr_b16 v[192:193], v243 offset:4096\n"
    "ds_read_b64_tr_b16 v[194:195], v243 offset:4352\n"
    "v_mfma_f32_16x16x32_bf16 v[160:163], v[204:207], v[136:139], v[160:163]\n"
    "v_exp_f32 v150, v150\n"
    "v_mfma_f32_16x16x32_bf16 v[164:167], v[200:203], v[132:135], v[164:167]\n"
    "s_add_u32 s67, s67, 1048576\n"
    "ds_read_b64_tr_b16 v[196:197], v243 offset:6144\n"
    "ds_read_b64_tr_b16 v[198:199], v243 offset:6400\n"
    "v_mfma_f32_16x16x32_bf16 v[164:167], v[204:207], v[140:143], v[164:167]\n"
    "s_waitcnt lgkmcnt(4)\n"
    "v_exp_f32 v151, v151\n"
    "v_mfma_f32_16x16x32_bf16 v[172:175], v[184:187], v[4:7], 0\n"
    "s_add_u32 m0, m0, 0x1000\n"
    "ds_read_b64_tr_b16 v[200:201], v243 offset:8192\n"
    "ds_read_b64_tr_b16 v[202:203], v243 offset:8448\n"
    "v_mfma_f32_16x16x32_bf16 v[172:175], v[188:191], v[12:15], v[172:175]\n"
    "buffer_load_dwordx4 v247, s[48:51], s65 offen lds\n"
    "v_mfma_f32_16x16x32_bf16 v[168:171], v[184:187], v[0:3], 0\n"
    "ds_read_b64_tr_b16 v[204:205], v243 offset:10240\n"
    "v_fma_f32 v150, v150, v249, v249\n"
    "v_mfma_f32_16x16x32_bf16 v[168:171], v[188:191], v[8:11], v[168:171]\n"
    "s_waitcnt lgkmcnt(3)\n"
    "ds_read_b64_tr_b16 v[206:207], v243 offset:10496\n"
    "v_fma_f32 v151, v151, v249, v249\n"
    "v_mfma_f32_16x16x32_bf16 v[168:171], v[192:195], v[16:19], v[168:171]\n"
    "s_mov_b64 s[52:53], %2\n"
    "ds_read_b64_tr_b16 v[184:185], v243 offset:12288\n"
    "ds_read_b64_tr_b16 v[186:187], v243 offset:12544\n"
    "v_mfma_f32_16x16x32_bf16 v[168:171], v[196:199], v[24:27], v[168:171]\n"
    "v_rcp_f32 v150, v150\n"
    "v_mfma_f32_16x16x32_bf16 v[172:175], v[192:195], v[20:23], v[172:175]\n"
    "s_add_u32 m0, m0, 0x1000\n"
    "ds_read_b64_tr_b16 v[188:189], v243 offset:14336\n"
    "ds_read_b64_tr_b16 v[190:191], v243 offset:14592\n"
    "v_mfma_f32_16x16x32_bf16 v[172:175], v[196:199], v[28:31], v[172:175]\n"
    "s_waitcnt lgkmcnt(4)\n"
    "buffer_load_dwordx4 v247, s[48:51], s77 offen lds\n"
    "v_mfma_f32_16x16x32_bf16 v[172:175], v[200:203], v[36:39], v[172:175]\n"
    "ds_read_b64_tr_b16 v[192:193], v243 offset:16384\n"
    "ds_read_b128 v[24:27], v244 offset:0\n"
    "v_mfma_f32_16x16x32_bf16 v[172:175], v[204:207], v[44:47], v[172:175]\n"
    "ds_read_b64_tr_b16 v[194:195], v243 offset:16640\n"
    "ds_read_b128 v[28:31], v244 offset:1024\n"
    "v_mfma_f32_16x16x32_bf16 v[168:171], v[200:203], v[32:35], v[168:171]\n"
    "ds_read_b64_tr_b16 v[196:197], v243 offset:18432\n"
    "ds_read_b64_tr_b16 v[198:199], v243 offset:18688\n"
    "v_mfma_f32_16x16x32_bf16 v[168:171], v[204:207], v[40:43], v[168:171]\n"
    "s_waitcnt lgkmcnt(6)\n"
    "v_rcp_f32 v151, v151\n"
    "v_mfma_f32_16x16x32_bf16 v[168:171], v[184:187], v[48:51], v[168:171]\n"
    "s_add_u32 m0, m0, 0x1000\n"
    "ds_read_b64_tr_b16 v[200:201], v243 offset:20480\n"
    "ds_read_b64_tr_b16 v[202:203], v243 offset:20736\n"
    "v_mfma_f32_16x16x32_bf16 v[168:171], v[188:191], v[56:59], v[168:171]\n"
    "buffer_load_dwordx4 v247, s[48:51], s79 offen lds\n"
    "v_mfma_f32_16x16x32_bf16 v[172:175], v[184:187], v[52:55], v[172:175]\n"
    "ds_read_b64_tr_b16 v[204:205], v243 offset:22528\n"
    "v_xor_b32 v244, 64, v242\n"
    "v_mfma_f32_16x16x32_bf16 v[172:175], v[188:191], v[60:63], v[172:175]\n"
    "s_waitcnt lgkmcnt(3)\n"
    "ds_read_b64_tr_b16 v[206:207], v243 offset:22784\n"
    "v_xor_b32 v245, 0x60, v242\n"
    "v_mfma_f32_16x16x32_bf16 v[172:175], v[192:195], v[68:71], v[172:175]\n"
    "s_add_u32 s48, s48, 36864\n"
    "s_add_u32 s52, s52, s67\n"
    "ds_read_b64_tr_b16 v[184:185], v243 offset:24576\n"
    "v_add_u32 v20, s55, v241\n"
    "v_mfma_f32_16x16x32_bf16 v[172:175], v[196:199], v[76:79], v[172:175]\n"
    "s_addc_u32 s53, s53, 0\n"
    "s_addc_u32 s49, s49, 0\n"
    "ds_read_b64_tr_b16 v[186:187], v243 offset:24832\n"
    "v_xor_b32 v21, 32, v20\n"
    "v_mfma_f32_16x16x32_bf16 v[168:171], v[192:195], v[64:67], v[168:171]\n"
    "s_lshl_b32 s77, %12, 2\n"
    "ds_read_b64_tr_b16 v[188:189], v243 offset:26624\n"
    "v_xor_b32 v22, 64, v20\n"
    "v_mfma_f32_16x16x32_bf16 v[168:171], v[196:199], v[72:75], v[168:171]\n"
    "s_add_u32 s77, s77, %7\n"
    "s_waitcnt lgkmcnt(3)\n"
    "ds_read_b64_tr_b16 v[190:191], v243 offset:26880\n"
    "v_xor_b32 v23, 0x60, v20\n"
    "v_mfma_f32_16x16x32_bf16 v[168:171], v[200:203], v[80:83], v[168:171]\n"
    "s_mul_i32 s77, s77, 36864\n"
    "ds_read_b64_tr_b16 v[192:193], v243 offset:28672\n"
    "v_add_u32 v8, s57, v241\n"
    "v_mfma_f32_16x16x32_bf16 v[168:171], v[204:207], v[88:91], v[168:171]\n"
    "s_add_u32 s77, s77, 18432\n"
    "ds_read_b64_tr_b16 v[194:195], v243 offset:28928\n"
    "v_xor_b32 v9, 32, v8\n"
    "v_mfma_f32_16x16x32_bf16 v[172:175], v[200:203], v[84:87], v[172:175]\n"
    "s_lshl_b32 s63, s70, 6\n"
    "ds_read_b64_tr_b16 v[196:197], v243 offset:30720\n"
    "v_xor_b32 v10, 64, v8\n"
    "v_mfma_f32_16x16x32_bf16 v[172:175], v[204:207], v[92:95], v[172:175]\n"
    "s_add_u32 s63, s63, s77\n"
    "s_waitcnt lgkmcnt(3)\n"
    "ds_read_b64_tr_b16 v[198:199], v243 offset:30976\n"
    "v_xor_b32 v11, 0x60, v8\n"
    "v_mfma_f32_16x16x32_bf16 v[172:175], v[184:187], v[100:103], v[172:175]\n"
    "s_add_u32 s64, s63, 64\n"
    "ds_read_b64_tr_b16 v[200:201], v243 offset:32768\n"
    "ds_read_b64_tr_b16 v[202:203], v243 offset:33024\n"
    "v_mfma_f32_16x16x32_bf16 v[172:175], v[188:191], v[108:111], v[172:175]\n"
    "s_lshr_b32 s79, %13, 2\n"
    "v_exp_f32 v152, v152\n"
    "v_mfma_f32_16x16x32_bf16 v[168:171], v[184:187], v[96:99], v[168:171]\n"
    "s_add_u32 s79, s79, 16\n"
    "ds_read_b64_tr_b16 v[204:205], v243 offset:34816\n"
    "ds_read_b64_tr_b16 v[206:207], v243 offset:35072\n"
    "v_mfma_f32_16x16x32_bf16 v[168:171], v[188:191], v[104:107], v[168:171]\n"
    "s_lshl_b32 s65, s79, 6\n"
    "s_waitcnt vmcnt(9) lgkmcnt(4)\n"
    "s_barrier\n"
    "v_exp_f32 v153, v153\n"
    "v_mfma_f32_16x16x32_bf16 v[168:171], v[192:195], v[112:115], v[168:171]\n"
    "s_add_u32 s65, s65, s77\n"
    "ds_read_b64_tr_b16 v[184:185], v242 offset:0\n"
    "v_xor_b32 v243, 32, v242\n"
    "v_mfma_f32_16x16x32_bf16 v[168:171], v[196:199], v[120:123], v[168:171]\n"
    "ds_read_b64_tr_b16 v[186:187], v242 offset:2048\n"
    "v_mfma_f32_16x16x32_bf16 v[172:175], v[192:195], v[116:119], v[172:175]\n"
    "ds_read_b64_tr_b16 v[188:189], v20 offset:0\n"
    "v_fma_f32 v152, v152, v248, v248\n"
    "v_mfma_f32_16x16x32_bf16 v[172:175], v[196:199], v[124:127], v[172:175]\n"
    "s_waitcnt lgkmcnt(3)\n"
    "s_barrier\n"
    "ds_read_b64_tr_b16 v[190:191], v20 offset:2048\n"
    "v_fma_f32 v153, v153, v248, v248\n"
    "s_add_u32 m0, s34, 0\n"
    "s_nop 0\n"
    "buffer_load_dwordx4 v250, s[36:39], s63 offen lds\n"
    "v_mfma_f32_16x16x32_bf16 v[172:175], v[200:203], v[132:135], v[172:175]\n"
    "ds_read_b64_tr_b16 v[192:193], v243 offset:0\n"
    "ds_read_b64_tr_b16 v[194:195], v243 offset:2048\n"
    "v_mfma_f32_16x16x32_bf16 v[172:175], v[204:207], v[140:143], v[172:175]\n"
    "v_rcp_f32 v152, v152\n"
    "s_add_u32 m0, s34, 1024\n"
    "s_nop 0\n"
    "buffer_load_dwordx4 v250, s[36:39], s64 offen lds\n"
    "v_mfma_f32_16x16x32_bf16 v[168:171], v[200:203], v[128:131], v[168:171]\n"
    "ds_read_b64_tr_b16 v[196:197], v21 offset:0\n"
    "ds_read_b64_tr_b16 v[198:199], v21 offset:2048\n"
    "v_mfma_f32_16x16x32_bf16 v[168:171], v[204:207], v[136:139], v[168:171]\n"
    "s_waitcnt lgkmcnt(4)\n"
    "v_rcp_f32 v153, v153\n"
    "v_exp_f32 v154, v154\n"
    "v_exp_f32 v155, v155\n"
    "v_fma_f32 v154, v154, v248, v248\n"
    "v_fma_f32 v155, v155, v248, v248\n"
    "v_rcp_f32 v154, v154\n"
    "v_rcp_f32 v155, v155\n"
    "v_exp_f32 v156, v156\n"
    "v_exp_f32 v157, v157\n"
    "v_fma_f32 v156, v156, v249, v249\n"
    "v_fma_f32 v157, v157, v249, v249\n"
    "v_rcp_f32 v156, v156\n"
    "v_rcp_f32 v157, v157\n"
    "v_exp_f32 v158, v158\n"
    "v_exp_f32 v159, v159\n"
    "v_fma_f32 v158, v158, v249, v249\n"
    "v_fma_f32 v159, v159, v249, v249\n"
    "v_rcp_f32 v158, v158\n"
    "v_rcp_f32 v159, v159\n"
    "s_add_u32 m0, s34, 2048\n"
    "s_nop 0\n"
    "buffer_load_dwordx4 v250, s[36:39], s65 offen lds\n"
    "v_mfma_f32_16x16x32_bf16 a[0:3], v[184:187], v[176:179], a[0:3]\n"
    "ds_read_b64_tr_b16 v[200:201], v244 offset:0\n"
    "v_mul_f32 v160, v144, v160\n"
    "v_mfma_f32_16x16x32_bf16 a[0:3], v[188:191], v[24:27], a[0:3]\n"
    "ds_read_b64_tr_b16 v[202:203], v244 offset:2048\n"
    "v_mul_f32 v161, v145, v161\n"
    "v_mfma_f32_16x16x32_bf16 a[8:11], v[184:187], v[180:183], a[8:11]\n"
    "s_add_u32 m0, s54, s60\n"
    "ds_read_b64_tr_b16 v[204:205], v22 offset:0\n"
    "ds_read_b64_tr_b16 v[206:207], v22 offset:2048\n"
    "v_mfma_f32_16x16x32_bf16 a[8:11], v[188:191], v[28:31], a[8:11]\n"
    "s_waitcnt lgkmcnt(4)\n"
    "buffer_load_dwordx4 v250, s[40:43], s68 offen lds\n"
    "v_mfma_f32_16x16x32_bf16 a[12:15], v[192:195], v[180:183], a[12:15]\n"
    "s_add_u32 s68, s68, 18432\n"
    "ds_read_b64_tr_b16 v[184:185], v245 offset:0\n"
    "v_mul_f32 v162, v146, v162\n"
    "v_mfma_f32_16x16x32_bf16 a[12:15], v[196:199], v[28:31], a[12:15]\n"
    "ds_read_b64_tr_b16 v[186:187], v245 offset:2048\n"
    "v_mul_f32 v163, v147, v163\n"
    "v_mfma_f32_16x16x32_bf16 a[4:7], v[192:195], v[176:179], a[4:7]\n"
    "s_add_u32 m0, m0, 0x400\n"
    "ds_read_b64_tr_b16 v[188:189], v23 offset:0\n"
    "ds_read_b64_tr_b16 v[190:191], v23 offset:2048\n"
    "v_mfma_f32_16x16x32_bf16 a[4:7], v[196:199], v[24:27], a[4:7]\n"
    "s_waitcnt lgkmcnt(4)\n"
    "buffer_load_dwordx4 v250, s[40:43], s68 offen lds\n"
    "v_mfma_f32_16x16x32_bf16 a[16:19], v[200:203], v[176:179], a[16:19]\n"
    "s_add_u32 s68, s68, 18432\n"
    "ds_read_b64_tr_b16 v[192:193], v242 offset:4096\n"
    "v_mul_f32 v164, v148, v164\n"
    "v_mfma_f32_16x16x32_bf16 a[16:19], v[204:207], v[24:27], a[16:19]\n"
    "ds_read_b64_tr_b16 v[194:195], v242 offset:6144\n"
    "v_mul_f32 v165, v149, v165\n"
    "v_mfma_f32_16x16x32_bf16 a[24:27], v[200:203], v[180:183], a[24:27]\n"
    "s_add_u32 m0, m0, 0x400\n"
    "ds_read_b64_tr_b16 v[196:197], v20 offset:4096\n"
    "ds_read_b64_tr_b16 v[198:199], v20 offset:6144\n"
    "v_mfma_f32_16x16x32_bf16 a[24:27], v[204:207], v[28:31], a[24:27]\n"
    "s_waitcnt vmcnt(5) lgkmcnt(4)\n"
    "s_barrier\n"
    "buffer_load_dwordx4 v250, s[40:43], s68 offen lds\n"
    "v_mfma_f32_16x16x32_bf16 a[28:31], v[184:187], v[180:183], a[28:31]\n"
    "s_add_u32 s68, s68, 18432\n"
    "ds_read_b64_tr_b16 v[200:201], v243 offset:4096\n"
    "v_mul_f32 v166, v150, v166\n"
    "v_mfma_f32_16x16x32_bf16 a[28:31], v[188:191], v[28:31], a[28:31]\n"
    "ds_read_b64_tr_b16 v[202:203], v243 offset:6144\n"
    "v_mul_f32 v167, v151, v167\n"
    "v_mfma_f32_16x16x32_bf16 a[20:23], v[184:187], v[176:179], a[20:23]\n"
    "s_add_u32 m0, m0, 0x400\n"
    "ds_read_b64_tr_b16 v[204:205], v21 offset:4096\n"
    "ds_read_b64_tr_b16 v[206:207], v21 offset:6144\n"
    "v_mfma_f32_16x16x32_bf16 a[20:23], v[188:191], v[24:27], a[20:23]\n"
    "s_waitcnt lgkmcnt(4)\n"
    "buffer_load_dwordx4 v250, s[40:43], s68 offen lds\n"
    "v_mfma_f32_16x16x32_bf16 a[32:35], v[192:195], v[176:179], a[32:35]\n"
    "s_add_u32 s68, s68, 18432\n"
    "ds_read_b64_tr_b16 v[184:185], v8 offset:0\n"
    "v_mul_f32 v168, v152, v168\n"
    "v_mfma_f32_16x16x32_bf16 a[32:35], v[196:199], v[24:27], a[32:35]\n"
    "ds_read_b64_tr_b16 v[186:187], v8 offset:2048\n"
    "v_mul_f32 v169, v153, v169\n"
    "v_mfma_f32_16x16x32_bf16 a[40:43], v[192:195], v[180:183], a[40:43]\n"
    "s_add_u32 m0, m0, 0x400\n"
    "ds_read_b64_tr_b16 v[188:189], v9 offset:0\n"
    "ds_read_b64_tr_b16 v[190:191], v9 offset:2048\n"
    "v_mfma_f32_16x16x32_bf16 a[40:43], v[196:199], v[28:31], a[40:43]\n"
    "s_waitcnt lgkmcnt(4)\n"
    "buffer_load_dwordx4 v250, s[40:43], s68 offen lds\n"
    "v_mfma_f32_16x16x32_bf16 a[44:47], v[200:203], v[180:183], a[44:47]\n"
    "s_add_u32 s68, s68, 18432\n"
    "ds_read_b64_tr_b16 v[192:193], v10 offset:0\n"
    "v_mul_f32 v170, v154, v170\n"
    "v_mul_f32 v171, v155, v171\n"
    "v_mul_f32 v172, v156, v172\n"
    "v_mul_f32 v173, v157, v173\n"
    "v_mul_f32 v174, v158, v174\n"
    "v_mul_f32 v175, v159, v175\n"
    "v_cvt_pk_bf16_f32 v0, v160, v161\n"
    "v_mfma_f32_16x16x32_bf16 a[44:47], v[204:207], v[28:31], a[44:47]\n"
    "ds_read_b64_tr_b16 v[194:195], v10 offset:2048\n"
    "v_cvt_pk_bf16_f32 v1, v162, v163\n"
    "v_cvt_pk_bf16_f32 v4, v164, v165\n"
    "v_cvt_pk_bf16_f32 v5, v166, v167\n"
    "v_cvt_pk_bf16_f32 v2, v168, v169\n"
    "v_cvt_pk_bf16_f32 v3, v170, v171\n"
    "v_cvt_pk_bf16_f32 v6, v172, v173\n"
    "v_cvt_pk_bf16_f32 v7, v174, v175\n"
    "v_mfma_f32_16x16x32_bf16 a[36:39], v[200:203], v[176:179], a[36:39]\n"
    "s_add_u32 m0, m0, 0x400\n"
    "ds_read_b64_tr_b16 v[196:197], v11 offset:0\n"
    "ds_read_b64_tr_b16 v[198:199], v11 offset:2048\n"
    "v_mfma_f32_16x16x32_bf16 a[36:39], v[204:207], v[24:27], a[36:39]\n"
    "s_waitcnt lgkmcnt(4)\n"
    "buffer_load_dwordx4 v250, s[40:43], s68 offen lds\n"
    "v_mfma_f32_16x16x32_bf16 a[0:3], v[184:187], v[0:3], a[0:3]\n"
    "s_add_u32 s68, s68, 18432\n"
    "ds_read_b64_tr_b16 v[200:201], v8 offset:4096\n"
    "v_mfma_f32_16x16x32_bf16 a[4:7], v[188:191], v[0:3], a[4:7]\n"
    "ds_read_b64_tr_b16 v[202:203], v8 offset:6144\n"
    "v_mfma_f32_16x16x32_bf16 a[8:11], v[184:187], v[4:7], a[8:11]\n"
    "s_add_u32 m0, m0, 0x400\n"
    "ds_read_b64_tr_b16 v[204:205], v9 offset:4096\n"
    "ds_read_b64_tr_b16 v[206:207], v9 offset:6144\n"
    "v_mfma_f32_16x16x32_bf16 a[12:15], v[188:191], v[4:7], a[12:15]\n"
    "s_waitcnt lgkmcnt(4)\n"
    "buffer_load_dwordx4 v250, s[40:43], s68 offen lds\n"
    "v_mfma_f32_16x16x32_bf16 a[24:27], v[192:195], v[4:7], a[24:27]\n"
    "s_add_u32 s68, s68, 18432\n"
    "ds_read_b64_tr_b16 v[184:185], v244 offset:4096\n"
    "v_mfma_f32_16x16x32_bf16 a[28:31], v[196:199], v[4:7], a[28:31]\n"
    "ds_read_b64_tr_b16 v[186:187], v244 offset:6144\n"
    "v_mfma_f32_16x16x32_bf16 a[16:19], v[192:195], v[0:3], a[16:19]\n"
    "s_add_u32 m0, m0, 0x400\n"
    "ds_read_b64_tr_b16 v[188:189], v22 offset:4096\n"
    "ds_read_b64_tr_b16 v[190:191], v22 offset:6144\n"
    "v_mfma_f32_16x16x32_bf16 a[20:23], v[196:199], v[0:3], a[20:23]\n"
    "s_waitcnt lgkmcnt(4)\n"
    "buffer_load_dwordx4 v250, s[40:43], s68 offen lds\n"
    "v_accvgpr_read_b32 v32, a0\n"
    "v_accvgpr_read_b32 v33, a1\n"
    "v_accvgpr_read_b32 v34, a2\n"
    "v_accvgpr_read_b32 v35, a3\n"
    "v_accvgpr_read_b32 v36, a4\n"
    "v_accvgpr_read_b32 v37, a5\n"
    "v_accvgpr_read_b32 v38, a6\n"
    "v_mfma_f32_16x16x32_bf16 a[32:35], v[200:203], v[0:3], a[32:35]\n"
    "s_add_u32 s68, s68, 18432\n"
    "ds_read_b64_tr_b16 v[192:193], v10 offset:4096\n"
    "v_accvgpr_read_b32 v39, a7\n"
    "v_accvgpr_read_b32 v40, a8\n"
    "v_accvgpr_read_b32 v41, a9\n"
    "v_accvgpr_read_b32 v42, a10\n"
    "v_accvgpr_read_b32 v43, a11\n"
    "v_accvgpr_read_b32 v44, a12\n"
    "v_accvgpr_read_b32 v45, a13\n"
    "v_mfma_f32_16x16x32_bf16 a[36:39], v[204:207], v[0:3], a[36:39]\n"
    "ds_read_b64_tr_b16 v[194:195], v10 offset:6144\n"
    "v_accvgpr_read_b32 v46, a14\n"
    "v_accvgpr_read_b32 v47, a15\n"
    "v_cvt_pk_bf16_f32 v48, v32, v33\n"
    "v_cvt_pk_bf16_f32 v49, v34, v35\n"
    "v_cvt_pk_bf16_f32 v50, v36, v37\n"
    "v_cvt_pk_bf16_f32 v51, v38, v39\n"
    "v_cvt_pk_bf16_f32 v52, v40, v41\n"
    "v_mfma_f32_16x16x32_bf16 a[40:43], v[200:203], v[4:7], a[40:43]\n"
    "s_add_u32 m0, m0, 0x400\n"
    "ds_read_b64_tr_b16 v[196:197], v245 offset:4096\n"
    "ds_read_b64_tr_b16 v[198:199], v245 offset:6144\n"
    "v_cvt_pk_bf16_f32 v53, v42, v43\n"
    "v_cvt_pk_bf16_f32 v54, v44, v45\n"
    "v_cvt_pk_bf16_f32 v55, v46, v47\n"
    "global_store_dwordx4 v254, v[48:51], s[52:53] offset:0 sc1\n"
    "global_store_dwordx4 v254, v[52:55], s[52:53] offset:1024 sc1\n"
    "v_mfma_f32_16x16x32_bf16 a[44:47], v[204:207], v[4:7], a[44:47]\n"
    "s_waitcnt lgkmcnt(4)\n"
    "buffer_load_dwordx4 v250, s[40:43], s68 offen lds\n"
    "v_accvgpr_read_b32 v56, a16\n"
    "v_accvgpr_read_b32 v57, a17\n"
    "v_accvgpr_read_b32 v58, a18\n"
    "v_accvgpr_read_b32 v59, a19\n"
    "v_accvgpr_read_b32 v60, a20\n"
    "v_accvgpr_read_b32 v61, a21\n"
    "v_accvgpr_read_b32 v62, a22\n"
    "v_accvgpr_read_b32 v63, a23\n"
    "v_accvgpr_read_b32 v64, a24\n"
    "v_accvgpr_read_b32 v65, a25\n"
    "v_accvgpr_read_b32 v66, a26\n"
    "v_accvgpr_read_b32 v67, a27\n"
    "v_accvgpr_read_b32 v68, a28\n"
    "v_mfma_f32_16x16x32_bf16 a[56:59], v[184:187], v[180:183], a[56:59]\n"
    "ds_read_b64_tr_b16 v[200:201], v23 offset:4096\n"
    "v_accvgpr_read_b32 v69, a29\n"
    "v_accvgpr_read_b32 v70, a30\n"
    "v_accvgpr_read_b32 v71, a31\n"
    "v_cvt_pk_bf16_f32 v72, v56, v57\n"
    "v_cvt_pk_bf16_f32 v73, v58, v59\n"
    "v_cvt_pk_bf16_f32 v74, v60, v61\n"
    "v_cvt_pk_bf16_f32 v75, v62, v63\n"
    "v_cvt_pk_bf16_f32 v76, v64, v65\n"
    "v_cvt_pk_bf16_f32 v77, v66, v67\n"
    "v_cvt_pk_bf16_f32 v78, v68, v69\n"
    "v_cvt_pk_bf16_f32 v79, v70, v71\n"
    "global_store_dwordx4 v254, v[72:75], s[52:53] offset:2048 sc1\n"
    "global_store_dwordx4 v254, v[76:79], s[52:53] offset:3072 sc1\n"
    "s_add_u32 s52, s52, 0x1000\n"
    "s_addc_u32 s53, s53, 0\n"
    "v_mfma_f32_16x16x32_bf16 a[56:59], v[188:191], v[28:31], a[56:59]\n"
    "ds_read_b64_tr_b16 v[202:203], v23 offset:6144\n"
    "v_accvgpr_read_b32 v80, a32\n"
    "v_accvgpr_read_b32 v81, a33\n"
    "v_accvgpr_read_b32 v82, a34\n"
    "v_mfma_f32_16x16x32_bf16 a[48:51], v[184:187], v[176:179], a[48:51]\n"
    "ds_read_b64_tr_b16 v[204:205], v11 offset:4096\n"
    "v_accvgpr_read_b32 v83, a35\n"
    "v_accvgpr_read_b32 v84, a36\n"
    "v_accvgpr_read_b32 v85, a37\n"
    "v_mfma_f32_16x16x32_bf16 a[48:51], v[188:191], v[24:27], a[48:51]\n"
    "s_waitcnt lgkmcnt(3)\n"
    "ds_read_b64_tr_b16 v[206:207], v11 offset:6144\n"
    "v_accvgpr_read_b32 v86, a38\n"
    "v_accvgpr_read_b32 v87, a39\n"
    "v_accvgpr_read_b32 v88, a40\n"
    "v_mfma_f32_16x16x32_bf16 a[48:51], v[192:195], v[0:3], a[48:51]\n"
    "ds_read_b64_tr_b16 v[184:185], v242 offset:8192\n"
    "v_accvgpr_read_b32 v89, a41\n"
    "v_accvgpr_read_b32 v90, a42\n"
    "v_accvgpr_read_b32 v91, a43\n"
    "v_mfma_f32_16x16x32_bf16 a[52:55], v[196:199], v[176:179], a[52:55]\n"
    "ds_read_b64_tr_b16 v[186:187], v242 offset:10240\n"
    "v_accvgpr_read_b32 v92, a44\n"
    "v_accvgpr_read_b32 v93, a45\n"
    "v_accvgpr_read_b32 v94, a46\n"
    "v_mfma_f32_16x16x32_bf16 a[56:59], v[192:195], v[4:7], a[56:59]\n"
    "ds_read_b64_tr_b16 v[188:189], v20 offset:8192\n"
    "v_accvgpr_read_b32 v95, a47\n"
    "v_cvt_pk_bf16_f32 v96, v80, v81\n"
    "v_cvt_pk_bf16_f32 v97, v82, v83\n"
    "v_mfma_f32_16x16x32_bf16 a[60:63], v[196:199], v[180:183], a[60:63]\n"
    "s_waitcnt lgkmcnt(3)\n"
    "ds_read_b64_tr_b16 v[190:191], v20 offset:10240\n"
    "v_cvt_pk_bf16_f32 v98, v84, v85\n"
    "v_cvt_pk_bf16_f32 v99, v86, v87\n"
    "v_cvt_pk_bf16_f32 v100, v88, v89\n"
    "v_mfma_f32_16x16x32_bf16 a[60:63], v[200:203], v[28:31], a[60:63]\n"
    "ds_read_b64_tr_b16 v[192:193], v8 offset:8192\n"
    "v_cvt_pk_bf16_f32 v101, v90, v91\n"
    "v_cvt_pk_bf16_f32 v102, v92, v93\n"
    "v_cvt_pk_bf16_f32 v103, v94, v95\n"
    "v_mfma_f32_16x16x32_bf16 a[60:63], v[204:207], v[4:7], a[60:63]\n"
    "ds_read_b64_tr_b16 v[194:195], v8 offset:10240\n"
    "global_store_dwordx4 v254, v[96:99], s[52:53] offset:0 sc1\n"
    "global_store_dwordx4 v254, v[100:103], s[52:53] offset:1024 sc1\n"
    "v_mfma_f32_16x16x32_bf16 a[52:55], v[200:203], v[24:27], a[52:55]\n"
    "ds_read_b64_tr_b16 v[196:197], v243 offset:8192\n"
    "v_mfma_f32_16x16x32_bf16 a[52:55], v[204:207], v[0:3], a[52:55]\n"
    "s_waitcnt lgkmcnt(3)\n"
    "ds_read_b64_tr_b16 v[198:199], v243 offset:10240\n"
    "v_mfma_f32_16x16x32_bf16 a[64:67], v[184:187], v[176:179], a[64:67]\n"
    "ds_read_b64_tr_b16 v[200:201], v21 offset:8192\n"
    "v_mfma_f32_16x16x32_bf16 a[64:67], v[188:191], v[24:27], a[64:67]\n"
    "ds_read_b64_tr_b16 v[202:203], v21 offset:10240\n"
    "v_accvgpr_read_b32 v104, a48\n"
    "v_accvgpr_read_b32 v105, a49\n"
    "v_accvgpr_read_b32 v106, a50\n"
    "v_mfma_f32_16x16x32_bf16 a[72:75], v[184:187], v[180:183], a[72:75]\n"
    "ds_read_b64_tr_b16 v[204:205], v9 offset:8192\n"
    "v_accvgpr_read_b32 v107, a51\n"
    "v_accvgpr_read_b32 v108, a52\n"
    "v_accvgpr_read_b32 v109, a53\n"
    "v_mfma_f32_16x16x32_bf16 a[72:75], v[188:191], v[28:31], a[72:75]\n"
    "s_waitcnt lgkmcnt(3)\n"
    "ds_read_b64_tr_b16 v[206:207], v9 offset:10240\n"
    "v_accvgpr_read_b32 v110, a54\n"
    "v_accvgpr_read_b32 v111, a55\n"
    "v_accvgpr_read_b32 v112, a56\n"
    "v_mfma_f32_16x16x32_bf16 a[72:75], v[192:195], v[4:7], a[72:75]\n"
    "ds_read_b64_tr_b16 v[184:185], v244 offset:8192\n"
    "v_accvgpr_read_b32 v113, a57\n"
    "v_accvgpr_read_b32 v114, a58\n"
    "v_accvgpr_read_b32 v115, a59\n"
    "v_mfma_f32_16x16x32_bf16 a[76:79], v[196:199], v[180:183], a[76:79]\n"
    "ds_read_b64_tr_b16 v[186:187], v244 offset:10240\n"
    "v_accvgpr_read_b32 v116, a60\n"
    "v_accvgpr_read_b32 v117, a61\n"
    "v_accvgpr_read_b32 v118, a62\n"
    "v_mfma_f32_16x16x32_bf16 a[64:67], v[192:195], v[0:3], a[64:67]\n"
    "ds_read_b64_tr_b16 v[188:189], v22 offset:8192\n"
    "v_accvgpr_read_b32 v119, a63\n"
    "v_cvt_pk_bf16_f32 v120, v104, v105\n"
    "v_cvt_pk_bf16_f32 v121, v106, v107\n"
    "v_mfma_f32_16x16x32_bf16 a[68:71], v[196:199], v[176:179], a[68:71]\n"
    "s_waitcnt lgkmcnt(3)\n"
    "ds_read_b64_tr_b16 v[190:191], v22 offset:10240\n"
    "v_cvt_pk_bf16_f32 v122, v108, v109\n"
    "v_cvt_pk_bf16_f32 v123, v110, v111\n"
    "v_cvt_pk_bf16_f32 v124, v112, v113\n"
    "v_mfma_f32_16x16x32_bf16 a[68:71], v[200:203], v[24:27], a[68:71]\n"
    "ds_read_b64_tr_b16 v[192:193], v10 offset:8192\n"
    "v_cvt_pk_bf16_f32 v125, v114, v115\n"
    "v_cvt_pk_bf16_f32 v126, v116, v117\n"
    "v_cvt_pk_bf16_f32 v127, v118, v119\n"
    "v_mfma_f32_16x16x32_bf16 a[68:71], v[204:207], v[0:3], a[68:71]\n"
    "ds_read_b64_tr_b16 v[194:195], v10 offset:10240\n"
    "global_store_dwordx4 v254, v[120:123], s[52:53] offset:2048 sc1\n"
    "global_store_dwordx4 v254, v[124:127], s[52:53] offset:3072 sc1\n"
    "v_mfma_f32_16x16x32_bf16 a[76:79], v[200:203], v[28:31], a[76:79]\n"
    "ds_read_b64_tr_b16 v[196:197], v245 offset:8192\n"
    "v_mfma_f32_16x16x32_bf16 a[76:79], v[204:207], v[4:7], a[76:79]\n"
    "s_waitcnt lgkmcnt(3)\n"
    "ds_read_b64_tr_b16 v[198:199], v245 offset:10240\n"
    "v_mfma_f32_16x16x32_bf16 a[88:91], v[184:187], v[180:183], a[88:91]\n"
    "ds_read_b64_tr_b16 v[200:201], v23 offset:8192\n"
    "s_add_u32 s52, s52, 0x1000\n"
    "s_addc_u32 s53, s53, 0\n"
    "v_mfma_f32_16x16x32_bf16 a[88:91], v[188:191], v[28:31], a[88:91]\n"
    "ds_read_b64_tr_b16 v[202:203], v23 offset:10240\n"
    "v_accvgpr_read_b32 v32, a64\n"
    "v_accvgpr_read_b32 v33, a65\n"
    "v_accvgpr_read_b32 v34, a66\n"
    "v_mfma_f32_16x16x32_bf16 a[80:83], v[184:187], v[176:179], a[80:83]\n"
    "ds_read_b64_tr_b16 v[204:205], v11 offset:8192\n"
    "v_accvgpr_read_b32 v35, a67\n"
    "v_accvgpr_read_b32 v36, a68\n"
    "v_accvgpr_read_b32 v37, a69\n"
    "v_mfma_f32_16x16x32_bf16 a[80:83], v[188:191], v[24:27], a[80:83]\n"
    "s_waitcnt lgkmcnt(3)\n"
    "ds_read_b64_tr_b16 v[206:207], v11 offset:10240\n"
    "v_accvgpr_read_b32 v38, a70\n"
    "v_accvgpr_read_b32 v39, a71\n"
    "v_accvgpr_read_b32 v40, a72\n"
    "v_mfma_f32_16x16x32_bf16 a[80:83], v[192:195], v[0:3], a[80:83]\n"
    "ds_read_b64_tr_b16 v[184:185], v242 offset:12288\n"
    "v_accvgpr_read_b32 v41, a73\n"
    "v_accvgpr_read_b32 v42, a74\n"
    "v_accvgpr_read_b32 v43, a75\n"
    "v_mfma_f32_16x16x32_bf16 a[84:87], v[196:199], v[176:179], a[84:87]\n"
    "ds_read_b64_tr_b16 v[186:187], v242 offset:14336\n"
    "v_accvgpr_read_b32 v44, a76\n"
    "v_accvgpr_read_b32 v45, a77\n"
    "v_accvgpr_read_b32 v46, a78\n"
    "v_mfma_f32_16x16x32_bf16 a[88:91], v[192:195], v[4:7], a[88:91]\n"
    "ds_read_b64_tr_b16 v[188:189], v20 offset:12288\n"
    "v_accvgpr_read_b32 v47, a79\n"
    "v_cvt_pk_bf16_f32 v48, v32, v33\n"
    "v_cvt_pk_bf16_f32 v49, v34, v35\n"
    "v_mfma_f32_16x16x32_bf16 a[92:95], v[196:199], v[180:183], a[92:95]\n"
    "s_waitcnt lgkmcnt(3)\n"
    "ds_read_b64_tr_b16 v[190:191], v20 offset:14336\n"
    "v_cvt_pk_bf16_f32 v50, v36, v37\n"
    "v_cvt_pk_bf16_f32 v51, v38, v39\n"
    "v_cvt_pk_bf16_f32 v52, v40, v41\n"
    "v_mfma_f32_16x16x32_bf16 a[92:95], v[200:203], v[28:31], a[92:95]\n"
    "ds_read_b64_tr_b16 v[192:193], v8 offset:12288\n"
    "v_cvt_pk_bf16_f32 v53, v42, v43\n"
    "v_cvt_pk_bf16_f32 v54, v44, v45\n"
    "v_cvt_pk_bf16_f32 v55, v46, v47\n"
    "v_mfma_f32_16x16x32_bf16 a[92:95], v[204:207], v[4:7], a[92:95]\n"
    "ds_read_b64_tr_b16 v[194:195], v8 offset:14336\n"
    "global_store_dwordx4 v254, v[48:51], s[52:53] offset:0 sc1\n"
    "global_store_dwordx4 v254, v[52:55], s[52:53] offset:1024 sc1\n"
    "v_mfma_f32_16x16x32_bf16 a[84:87], v[200:203], v[24:27], a[84:87]\n"
    "ds_read_b64_tr_b16 v[196:197], v243 offset:12288\n"
    "v_mfma_f32_16x16x32_bf16 a[84:87], v[204:207], v[0:3], a[84:87]\n"
    "s_waitcnt lgkmcnt(3)\n"
    "ds_read_b64_tr_b16 v[198:199], v243 offset:14336\n"
    "v_mfma_f32_16x16x32_bf16 a[96:99], v[184:187], v[176:179], a[96:99]\n"
    "ds_read_b64_tr_b16 v[200:201], v21 offset:12288\n"
    "v_mfma_f32_16x16x32_bf16 a[96:99], v[188:191], v[24:27], a[96:99]\n"
    "ds_read_b64_tr_b16 v[202:203], v21 offset:14336\n"
    "v_accvgpr_read_b32 v56, a80\n"
    "v_accvgpr_read_b32 v57, a81\n"
    "v_accvgpr_read_b32 v58, a82\n"
    "v_mfma_f32_16x16x32_bf16 a[104:107], v[184:187], v[180:183], a[104:107]\n"
    "ds_read_b64_tr_b16 v[204:205], v9 offset:12288\n"
    "v_accvgpr_read_b32 v59, a83\n"
    "v_accvgpr_read_b32 v60, a84\n"
    "v_accvgpr_read_b32 v61, a85\n"
    "v_mfma_f32_16x16x32_bf16 a[104:107], v[188:191], v[28:31], a[104:107]\n"
    "s_waitcnt lgkmcnt(3)\n"
    "ds_read_b64_tr_b16 v[206:207], v9 offset:14336\n"
    "v_accvgpr_read_b32 v62, a86\n"
    "v_accvgpr_read_b32 v63, a87\n"
    "v_accvgpr_read_b32 v64, a88\n"
    "v_mfma_f32_16x16x32_bf16 a[104:107], v[192:195], v[4:7], a[104:107]\n"
    "ds_read_b64_tr_b16 v[184:185], v244 offset:12288\n"
    "v_accvgpr_read_b32 v65, a89\n"
    "v_accvgpr_read_b32 v66, a90\n"
    "v_accvgpr_read_b32 v67, a91\n"
    "v_mfma_f32_16x16x32_bf16 a[108:111], v[196:199], v[180:183], a[108:111]\n"
    "ds_read_b64_tr_b16 v[186:187], v244 offset:14336\n"
    "v_accvgpr_read_b32 v68, a92\n"
    "v_accvgpr_read_b32 v69, a93\n"
    "v_accvgpr_read_b32 v70, a94\n"
    "v_mfma_f32_16x16x32_bf16 a[96:99], v[192:195], v[0:3], a[96:99]\n"
    "ds_read_b64_tr_b16 v[188:189], v22 offset:12288\n"
    "v_accvgpr_read_b32 v71, a95\n"
    "v_cvt_pk_bf16_f32 v72, v56, v57\n"
    "v_cvt_pk_bf16_f32 v73, v58, v59\n"
    "v_mfma_f32_16x16x32_bf16 a[100:103], v[196:199], v[176:179], a[100:103]\n"
    "s_waitcnt lgkmcnt(3)\n"
    "ds_read_b64_tr_b16 v[190:191], v22 offset:14336\n"
    "v_cvt_pk_bf16_f32 v74, v60, v61\n"
    "v_cvt_pk_bf16_f32 v75, v62, v63\n"
    "v_cvt_pk_bf16_f32 v76, v64, v65\n"
    "v_mfma_f32_16x16x32_bf16 a[100:103], v[200:203], v[24:27], a[100:103]\n"
    "ds_read_b64_tr_b16 v[192:193], v10 offset:12288\n"
    "v_cvt_pk_bf16_f32 v77, v66, v67\n"
    "v_cvt_pk_bf16_f32 v78, v68, v69\n"
    "v_cvt_pk_bf16_f32 v79, v70, v71\n"
    "v_mfma_f32_16x16x32_bf16 a[100:103], v[204:207], v[0:3], a[100:103]\n"
    "ds_read_b64_tr_b16 v[194:195], v10 offset:14336\n"
    "global_store_dwordx4 v254, v[72:75], s[52:53] offset:2048 sc1\n"
    "global_store_dwordx4 v254, v[76:79], s[52:53] offset:3072 sc1\n"
    "v_mfma_f32_16x16x32_bf16 a[108:111], v[200:203], v[28:31], a[108:111]\n"
    "ds_read_b64_tr_b16 v[196:197], v245 offset:12288\n"
    "v_mfma_f32_16x16x32_bf16 a[108:111], v[204:207], v[4:7], a[108:111]\n"
    "s_waitcnt lgkmcnt(3)\n"
    "ds_read_b64_tr_b16 v[198:199], v245 offset:14336\n"
    "v_mfma_f32_16x16x32_bf16 a[120:123], v[184:187], v[180:183], a[120:123]\n"
    "ds_read_b64_tr_b16 v[200:201], v23 offset:12288\n"
    "s_add_u32 s52, s52, 0x1000\n"
    "s_addc_u32 s53, s53, 0\n"
    "v_mfma_f32_16x16x32_bf16 a[120:123], v[188:191], v[28:31], a[120:123]\n"
    "ds_read_b64_tr_b16 v[202:203], v23 offset:14336\n"
    "v_accvgpr_read_b32 v80, a96\n"
    "v_accvgpr_read_b32 v81, a97\n"
    "v_accvgpr_read_b32 v82, a98\n"
    "v_mfma_f32_16x16x32_bf16 a[112:115], v[184:187], v[176:179], a[112:115]\n"
    "ds_read_b64_tr_b16 v[204:205], v11 offset:12288\n"
    "v_accvgpr_read_b32 v83, a99\n"
    "v_accvgpr_read_b32 v84, a100\n"
    "v_accvgpr_read_b32 v85, a101\n"
    "v_mfma_f32_16x16x32_bf16 a[112:115], v[188:191], v[24:27], a[112:115]\n"
    "s_waitcnt lgkmcnt(3)\n"
    "ds_read_b64_tr_b16 v[206:207], v11 offset:14336\n"
    "v_accvgpr_read_b32 v86, a102\n"
    "v_accvgpr_read_b32 v87, a103\n"
    "v_accvgpr_read_b32 v88, a104\n"
    "v_mfma_f32_16x16x32_bf16 a[112:115], v[192:195], v[0:3], a[112:115]\n"
    "ds_read_b64_tr_b16 v[184:185], v242 offset:16384\n"
    "v_accvgpr_read_b32 v89, a105\n"
    "v_accvgpr_read_b32 v90, a106\n"
    "v_accvgpr_read_b32 v91, a107\n"
    "v_mfma_f32_16x16x32_bf16 a[116:119], v[196:199], v[176:179], a[116:119]\n"
    "ds_read_b64_tr_b16 v[186:187], v242 offset:18432\n"
    "v_accvgpr_read_b32 v92, a108\n"
    "v_accvgpr_read_b32 v93, a109\n"
    "v_accvgpr_read_b32 v94, a110\n"
    "v_mfma_f32_16x16x32_bf16 a[120:123], v[192:195], v[4:7], a[120:123]\n"
    "ds_read_b64_tr_b16 v[188:189], v20 offset:16384\n"
    "v_accvgpr_read_b32 v95, a111\n"
    "v_cvt_pk_bf16_f32 v96, v80, v81\n"
    "v_cvt_pk_bf16_f32 v97, v82, v83\n"
    "v_mfma_f32_16x16x32_bf16 a[124:127], v[196:199], v[180:183], a[124:127]\n"
    "s_waitcnt lgkmcnt(3)\n"
    "ds_read_b64_tr_b16 v[190:191], v20 offset:18432\n"
    "v_cvt_pk_bf16_f32 v98, v84, v85\n"
    "v_cvt_pk_bf16_f32 v99, v86, v87\n"
    "v_cvt_pk_bf16_f32 v100, v88, v89\n"
    "v_mfma_f32_16x16x32_bf16 a[124:127], v[200:203], v[28:31], a[124:127]\n"
    "ds_read_b64_tr_b16 v[192:193], v8 offset:16384\n"
    "v_cvt_pk_bf16_f32 v101, v90, v91\n"
    "v_cvt_pk_bf16_f32 v102, v92, v93\n"
    "v_cvt_pk_bf16_f32 v103, v94, v95\n"
    "v_mfma_f32_16x16x32_bf16 a[124:127], v[204:207], v[4:7], a[124:127]\n"
    "ds_read_b64_tr_b16 v[194:195], v8 offset:18432\n"
    "global_store_dwordx4 v254, v[96:99], s[52:53] offset:0 sc1\n"
    "global_store_dwordx4 v254, v[100:103], s[52:53] offset:1024 sc1\n"
    "v_mfma_f32_16x16x32_bf16 a[116:119], v[200:203], v[24:27], a[116:119]\n"
    "ds_read_b64_tr_b16 v[196:197], v243 offset:16384\n"
    "v_mfma_f32_16x16x32_bf16 a[116:119], v[204:207], v[0:3], a[116:119]\n"
    "s_waitcnt lgkmcnt(3)\n"
    "ds_read_b64_tr_b16 v[198:199], v243 offset:18432\n"
    "v_mfma_f32_16x16x32_bf16 a[128:131], v[184:187], v[176:179], a[128:131]\n"
    "ds_read_b64_tr_b16 v[200:201], v21 offset:16384\n"
    "v_mfma_f32_16x16x32_bf16 a[128:131], v[188:191], v[24:27], a[128:131]\n"
    "ds_read_b64_tr_b16 v[202:203], v21 offset:18432\n"
    "v_accvgpr_read_b32 v104, a112\n"
    "v_accvgpr_read_b32 v105, a113\n"
    "v_accvgpr_read_b32 v106, a114\n"
    "v_mfma_f32_16x16x32_bf16 a[136:139], v[184:187], v[180:183], a[136:139]\n"
    "ds_read_b64_tr_b16 v[204:205], v9 offset:16384\n"
    "v_accvgpr_read_b32 v107, a115\n"
    "v_accvgpr_read_b32 v108, a116\n"
    "v_accvgpr_read_b32 v109, a117\n"
    "v_mfma_f32_16x16x32_bf16 a[136:139], v[188:191], v[28:31], a[136:139]\n"
    "s_waitcnt lgkmcnt(3)\n"
    "ds_read_b64_tr_b16 v[206:207], v9 offset:18432\n"
    "v_accvgpr_read_b32 v110, a118\n"
    "v_accvgpr_read_b32 v111, a119\n"
    "v_accvgpr_read_b32 v112, a120\n"
    "v_mfma_f32_16x16x32_bf16 a[136:139], v[192:195], v[4:7], a[136:139]\n"
    "ds_read_b64_tr_b16 v[184:185], v244 offset:16384\n"
    "v_accvgpr_read_b32 v113, a121\n"
    "v_accvgpr_read_b32 v114, a122\n"
    "v_accvgpr_read_b32 v115, a123\n"
    "v_mfma_f32_16x16x32_bf16 a[140:143], v[196:199], v[180:183], a[140:143]\n"
    "ds_read_b64_tr_b16 v[186:187], v244 offset:18432\n"
    "v_accvgpr_read_b32 v116, a124\n"
    "v_accvgpr_read_b32 v117, a125\n"
    "v_accvgpr_read_b32 v118, a126\n"
    "v_mfma_f32_16x16x32_bf16 a[128:131], v[192:195], v[0:3], a[128:131]\n"
    "ds_read_b64_tr_b16 v[188:189], v22 offset:16384\n"
    "v_accvgpr_read_b32 v119, a127\n"
    "v_cvt_pk_bf16_f32 v120, v104, v105\n"
    "v_cvt_pk_bf16_f32 v121, v106, v107\n"
    "v_mfma_f32_16x16x32_bf16 a[132:135], v[196:199], v[176:179], a[132:135]\n"
    "s_waitcnt lgkmcnt(3)\n"
    "ds_read_b64_tr_b16 v[190:191], v22 offset:18432\n"
    "v_cvt_pk_bf16_f32 v122, v108, v109\n"
    "v_cvt_pk_bf16_f32 v123, v110, v111\n"
    "v_cvt_pk_bf16_f32 v124, v112, v113\n"
    "v_mfma_f32_16x16x32_bf16 a[132:135], v[200:203], v[24:27], a[132:135]\n"
    "ds_read_b64_tr_b16 v[192:193], v10 offset:16384\n"
    "v_cvt_pk_bf16_f32 v125, v114, v115\n"
    "v_cvt_pk_bf16_f32 v126, v116, v117\n"
    "v_cvt_pk_bf16_f32 v127, v118, v119\n"
    "v_mfma_f32_16x16x32_bf16 a[132:135], v[204:207], v[0:3], a[132:135]\n"
    "ds_read_b64_tr_b16 v[194:195], v10 offset:18432\n"
    "global_store_dwordx4 v254, v[120:123], s[52:53] offset:2048 sc1\n"
    "global_store_dwordx4 v254, v[124:127], s[52:53] offset:3072 sc1\n"
    "v_mfma_f32_16x16x32_bf16 a[140:143], v[200:203], v[28:31], a[140:143]\n"
    "ds_read_b64_tr_b16 v[196:197], v245 offset:16384\n"
    "v_mfma_f32_16x16x32_bf16 a[140:143], v[204:207], v[4:7], a[140:143]\n"
    "s_waitcnt lgkmcnt(3)\n"
    "ds_read_b64_tr_b16 v[198:199], v245 offset:18432\n"
    "v_mfma_f32_16x16x32_bf16 a[152:155], v[184:187], v[180:183], a[152:155]\n"
    "ds_read_b64_tr_b16 v[200:201], v23 offset:16384\n"
    "s_add_u32 s52, s52, 0x1000\n"
    "s_addc_u32 s53, s53, 0\n"
    "v_mfma_f32_16x16x32_bf16 a[152:155], v[188:191], v[28:31], a[152:155]\n"
    "ds_read_b64_tr_b16 v[202:203], v23 offset:18432\n"
    "v_accvgpr_read_b32 v32, a128\n"
    "v_accvgpr_read_b32 v33, a129\n"
    "v_accvgpr_read_b32 v34, a130\n"
    "v_mfma_f32_16x16x32_bf16 a[144:147], v[184:187], v[176:179], a[144:147]\n"
    "ds_read_b64_tr_b16 v[204:205], v11 offset:16384\n"
    "v_accvgpr_read_b32 v35, a131\n"
    "v_accvgpr_read_b32 v36, a132\n"
    "v_accvgpr_read_b32 v37, a133\n"
    "v_mfma_f32_16x16x32_bf16 a[144:147], v[188:191], v[24:27], a[144:147]\n"
    "s_waitcnt lgkmcnt(3)\n"
    "ds_read_b64_tr_b16 v[206:207], v11 offset:18432\n"
    "v_accvgpr_read_b32 v38, a134\n"
    "v_accvgpr_read_b32 v39, a135\n"
    "v_accvgpr_read_b32 v40, a136\n"
    "v_mfma_f32_16x16x32_bf16 a[144:147], v[192:195], v[0:3], a[144:147]\n"
    "ds_read_b64_tr_b16 v[184:185], v242 offset:20480\n"
    "v_accvgpr_read_b32 v41, a137\n"
    "v_accvgpr_read_b32 v42, a138\n"
    "v_accvgpr_read_b32 v43, a139\n"
    "v_mfma_f32_16x16x32_bf16 a[148:151], v[196:199], v[176:179], a[148:151]\n"
    "ds_read_b64_tr_b16 v[186:187], v242 offset:22528\n"
    "v_accvgpr_read_b32 v44, a140\n"
    "v_accvgpr_read_b32 v45, a141\n"
    "v_accvgpr_read_b32 v46, a142\n"
    "v_mfma_f32_16x16x32_bf16 a[152:155], v[192:195], v[4:7], a[152:155]\n"
    "ds_read_b64_tr_b16 v[188:189], v20 offset:20480\n"
    "v_accvgpr_read_b32 v47, a143\n"
    "v_cvt_pk_bf16_f32 v48, v32, v33\n"
    "v_cvt_pk_bf16_f32 v49, v34, v35\n"
    "v_mfma_f32_16x16x32_bf16 a[156:159], v[196:199], v[180:183], a[156:159]\n"
    "s_waitcnt lgkmcnt(3)\n"
    "ds_read_b64_tr_b16 v[190:191], v20 offset:22528\n"
    "v_cvt_pk_bf16_f32 v50, v36, v37\n"
    "v_cvt_pk_bf16_f32 v51, v38, v39\n"
    "v_cvt_pk_bf16_f32 v52, v40, v41\n"
    "v_mfma_f32_16x16x32_bf16 a[156:159], v[200:203], v[28:31], a[156:159]\n"
    "ds_read_b64_tr_b16 v[192:193], v8 offset:20480\n"
    "v_cvt_pk_bf16_f32 v53, v42, v43\n"
    "v_cvt_pk_bf16_f32 v54, v44, v45\n"
    "v_cvt_pk_bf16_f32 v55, v46, v47\n"
    "v_mfma_f32_16x16x32_bf16 a[156:159], v[204:207], v[4:7], a[156:159]\n"
    "ds_read_b64_tr_b16 v[194:195], v8 offset:22528\n"
    "global_store_dwordx4 v254, v[48:51], s[52:53] offset:0 sc1\n"
    "global_store_dwordx4 v254, v[52:55], s[52:53] offset:1024 sc1\n"
    "v_mfma_f32_16x16x32_bf16 a[148:151], v[200:203], v[24:27], a[148:151]\n"
    "ds_read_b64_tr_b16 v[196:197], v243 offset:20480\n"
    "v_mfma_f32_16x16x32_bf16 a[148:151], v[204:207], v[0:3], a[148:151]\n"
    "s_waitcnt lgkmcnt(3)\n"
    "ds_read_b64_tr_b16 v[198:199], v243 offset:22528\n"
    "v_mfma_f32_16x16x32_bf16 a[160:163], v[184:187], v[176:179], a[160:163]\n"
    "ds_read_b64_tr_b16 v[200:201], v21 offset:20480\n"
    "v_mfma_f32_16x16x32_bf16 a[160:163], v[188:191], v[24:27], a[160:163]\n"
    "ds_read_b64_tr_b16 v[202:203], v21 offset:22528\n"
    "v_accvgpr_read_b32 v56, a144\n"
    "v_accvgpr_read_b32 v57, a145\n"
    "v_accvgpr_read_b32 v58, a146\n"
    "v_mfma_f32_16x16x32_bf16 a[168:171], v[184:187], v[180:183], a[168:171]\n"
    "ds_read_b64_tr_b16 v[204:205], v9 offset:20480\n"
    "v_accvgpr_read_b32 v59, a147\n"
    "v_accvgpr_read_b32 v60, a148\n"
    "v_accvgpr_read_b32 v61, a149\n"
    "v_mfma_f32_16x16x32_bf16 a[168:171], v[188:191], v[28:31], a[168:171]\n"
    "s_waitcnt lgkmcnt(3)\n"
    "ds_read_b64_tr_b16 v[206:207], v9 offset:22528\n"
    "v_accvgpr_read_b32 v62, a150\n"
    "v_accvgpr_read_b32 v63, a151\n"
    "v_accvgpr_read_b32 v64, a152\n"
    "v_mfma_f32_16x16x32_bf16 a[168:171], v[192:195], v[4:7], a[168:171]\n"
    "ds_read_b64_tr_b16 v[184:185], v244 offset:20480\n"
    "v_accvgpr_read_b32 v65, a153\n"
    "v_accvgpr_read_b32 v66, a154\n"
    "v_accvgpr_read_b32 v67, a155\n"
    "v_mfma_f32_16x16x32_bf16 a[172:175], v[196:199], v[180:183], a[172:175]\n"
    "ds_read_b64_tr_b16 v[186:187], v244 offset:22528\n"
    "v_accvgpr_read_b32 v68, a156\n"
    "v_accvgpr_read_b32 v69, a157\n"
    "v_accvgpr_read_b32 v70, a158\n"
    "v_mfma_f32_16x16x32_bf16 a[160:163], v[192:195], v[0:3], a[160:163]\n"
    "ds_read_b64_tr_b16 v[188:189], v22 offset:20480\n"
    "v_accvgpr_read_b32 v71, a159\n"
    "v_cvt_pk_bf16_f32 v72, v56, v57\n"
    "v_cvt_pk_bf16_f32 v73, v58, v59\n"
    "v_mfma_f32_16x16x32_bf16 a[164:167], v[196:199], v[176:179], a[164:167]\n"
    "s_waitcnt lgkmcnt(3)\n"
    "ds_read_b64_tr_b16 v[190:191], v22 offset:22528\n"
    "v_cvt_pk_bf16_f32 v74, v60, v61\n"
    "v_cvt_pk_bf16_f32 v75, v62, v63\n"
    "v_cvt_pk_bf16_f32 v76, v64, v65\n"
    "v_mfma_f32_16x16x32_bf16 a[164:167], v[200:203], v[24:27], a[164:167]\n"
    "ds_read_b64_tr_b16 v[192:193], v10 offset:20480\n"
    "v_cvt_pk_bf16_f32 v77, v66, v67\n"
    "v_cvt_pk_bf16_f32 v78, v68, v69\n"
    "v_cvt_pk_bf16_f32 v79, v70, v71\n"
    "v_mfma_f32_16x16x32_bf16 a[164:167], v[204:207], v[0:3], a[164:167]\n"
    "ds_read_b64_tr_b16 v[194:195], v10 offset:22528\n"
    "global_store_dwordx4 v254, v[72:75], s[52:53] offset:2048 sc1\n"
    "global_store_dwordx4 v254, v[76:79], s[52:53] offset:3072 sc1\n"
    "v_mfma_f32_16x16x32_bf16 a[172:175], v[200:203], v[28:31], a[172:175]\n"
    "ds_read_b64_tr_b16 v[196:197], v245 offset:20480\n"
    "v_mfma_f32_16x16x32_bf16 a[172:175], v[204:207], v[4:7], a[172:175]\n"
    "s_waitcnt lgkmcnt(3)\n"
    "ds_read_b64_tr_b16 v[198:199], v245 offset:22528\n"
    "v_mfma_f32_16x16x32_bf16 a[184:187], v[184:187], v[180:183], a[184:187]\n"
    "ds_read_b64_tr_b16 v[200:201], v23 offset:20480\n"
    "s_add_u32 s52, s52, 0x1000\n"
    "s_addc_u32 s53, s53, 0\n"
    "v_mfma_f32_16x16x32_bf16 a[184:187], v[188:191], v[28:31], a[184:187]\n"
    "ds_read_b64_tr_b16 v[202:203], v23 offset:22528\n"
    "v_accvgpr_read_b32 v80, a160\n"
    "v_accvgpr_read_b32 v81, a161\n"
    "v_accvgpr_read_b32 v82, a162\n"
    "v_mfma_f32_16x16x32_bf16 a[176:179], v[184:187], v[176:179], a[176:179]\n"
    "ds_read_b64_tr_b16 v[204:205], v11 offset:20480\n"
    "v_accvgpr_read_b32 v83, a163\n"
    "v_accvgpr_read_b32 v84, a164\n"
    "v_accvgpr_read_b32 v85, a165\n"
    "v_mfma_f32_16x16x32_bf16 a[176:179], v[188:191], v[24:27], a[176:179]\n"
    "s_waitcnt lgkmcnt(3)\n"
    "ds_read_b64_tr_b16 v[206:207], v11 offset:22528\n"
    "v_accvgpr_read_b32 v86, a166\n"
    "v_accvgpr_read_b32 v87, a167\n"
    "v_accvgpr_read_b32 v88, a168\n"
    "v_mfma_f32_16x16x32_bf16 a[176:179], v[192:195], v[0:3], a[176:179]\n"
    "ds_read_b64_tr_b16 v[184:185], v242 offset:24576\n"
    "v_accvgpr_read_b32 v89, a169\n"
    "v_accvgpr_read_b32 v90, a170\n"
    "v_accvgpr_read_b32 v91, a171\n"
    "v_mfma_f32_16x16x32_bf16 a[180:183], v[196:199], v[176:179], a[180:183]\n"
    "ds_read_b64_tr_b16 v[186:187], v242 offset:26624\n"
    "v_accvgpr_read_b32 v92, a172\n"
    "v_accvgpr_read_b32 v93, a173\n"
    "v_accvgpr_read_b32 v94, a174\n"
    "v_mfma_f32_16x16x32_bf16 a[184:187], v[192:195], v[4:7], a[184:187]\n"
    "ds_read_b64_tr_b16 v[188:189], v20 offset:24576\n"
    "v_accvgpr_read_b32 v95, a175\n"
    "v_cvt_pk_bf16_f32 v96, v80, v81\n"
    "v_cvt_pk_bf16_f32 v97, v82, v83\n"
    "v_mfma_f32_16x16x32_bf16 a[188:191], v[196:199], v[180:183], a[188:191]\n"
    "s_waitcnt lgkmcnt(3)\n"
    "ds_read_b64_tr_b16 v[190:191], v20 offset:26624\n"
    "v_cvt_pk_bf16_f32 v98, v84, v85\n"
    "v_cvt_pk_bf16_f32 v99, v86, v87\n"
    "v_cvt_pk_bf16_f32 v100, v88, v89\n"
    "v_mfma_f32_16x16x32_bf16 a[188:191], v[200:203], v[28:31], a[188:191]\n"
    "ds_read_b64_tr_b16 v[192:193], v8 offset:24576\n"
    "v_cvt_pk_bf16_f32 v101, v90, v91\n"
    "v_cvt_pk_bf16_f32 v102, v92, v93\n"
    "v_cvt_pk_bf16_f32 v103, v94, v95\n"
    "v_mfma_f32_16x16x32_bf16 a[188:191], v[204:207], v[4:7], a[188:191]\n"
    "ds_read_b64_tr_b16 v[194:195], v8 offset:26624\n"
    "global_store_dwordx4 v254, v[96:99], s[52:53] offset:0 sc1\n"
    "global_store_dwordx4 v254, v[100:103], s[52:53] offset:1024 sc1\n"
    "v_mfma_f32_16x16x32_bf16 a[180:183], v[200:203], v[24:27], a[180:183]\n"
    "ds_read_b64_tr_b16 v[196:197], v243 offset:24576\n"
    "v_mfma_f32_16x16x32_bf16 a[180:183], v[204:207], v[0:3], a[180:183]\n"
    "s_waitcnt lgkmcnt(3)\n"
    "ds_read_b64_tr_b16 v[198:199], v243 offset:26624\n"
    "v_mfma_f32_16x16x32_bf16 a[192:195], v[184:187], v[176:179], a[192:195]\n"
    "ds_read_b64_tr_b16 v[200:201], v21 offset:24576\n"
    "v_mfma_f32_16x16x32_bf16 a[192:195], v[188:191], v[24:27], a[192:195]\n"
    "ds_read_b64_tr_b16 v[202:203], v21 offset:26624\n"
    "v_accvgpr_read_b32 v104, a176\n"
    "v_accvgpr_read_b32 v105, a177\n"
    "v_accvgpr_read_b32 v106, a178\n"
    "v_mfma_f32_16x16x32_bf16 a[200:203], v[184:187], v[180:183], a[200:203]\n"
    "ds_read_b64_tr_b16 v[204:205], v9 offset:24576\n"
    "v_accvgpr_read_b32 v107, a179\n"
    "v_accvgpr_read_b32 v108, a180\n"
    "v_accvgpr_read_b32 v109, a181\n"
    "v_mfma_f32_16x16x32_bf16 a[200:203], v[188:191], v[28:31], a[200:203]\n"
    "s_waitcnt lgkmcnt(3)\n"
    "ds_read_b64_tr_b16 v[206:207], v9 offset:26624\n"
    "v_accvgpr_read_b32 v110, a182\n"
    "v_accvgpr_read_b32 v111, a183\n"
    "v_accvgpr_read_b32 v112, a184\n"
    "v_mfma_f32_16x16x32_bf16 a[200:203], v[192:195], v[4:7], a[200:203]\n"
    "ds_read_b64_tr_b16 v[184:185], v244 offset:24576\n"
    "v_accvgpr_read_b32 v113, a185\n"
    "v_accvgpr_read_b32 v114, a186\n"
    "v_accvgpr_read_b32 v115, a187\n"
    "v_mfma_f32_16x16x32_bf16 a[204:207], v[196:199], v[180:183], a[204:207]\n"
    "ds_read_b64_tr_b16 v[186:187], v244 offset:26624\n"
    "v_accvgpr_read_b32 v116, a188\n"
    "v_accvgpr_read_b32 v117, a189\n"
    "v_accvgpr_read_b32 v118, a190\n"
    "v_mfma_f32_16x16x32_bf16 a[192:195], v[192:195], v[0:3], a[192:195]\n"
    "ds_read_b64_tr_b16 v[188:189], v22 offset:24576\n"
    "v_accvgpr_read_b32 v119, a191\n"
    "v_cvt_pk_bf16_f32 v120, v104, v105\n"
    "v_cvt_pk_bf16_f32 v121, v106, v107\n"
    "v_mfma_f32_16x16x32_bf16 a[196:199], v[196:199], v[176:179], a[196:199]\n"
    "s_waitcnt lgkmcnt(3)\n"
    "ds_read_b64_tr_b16 v[190:191], v22 offset:26624\n"
    "v_cvt_pk_bf16_f32 v122, v108, v109\n"
    "v_cvt_pk_bf16_f32 v123, v110, v111\n"
    "v_cvt_pk_bf16_f32 v124, v112, v113\n"
    "v_mfma_f32_16x16x32_bf16 a[196:199], v[200:203], v[24:27], a[196:199]\n"
    "ds_read_b64_tr_b16 v[192:193], v10 offset:24576\n"
    "v_cvt_pk_bf16_f32 v125, v114, v115\n"
    "v_cvt_pk_bf16_f32 v126, v116, v117\n"
    "v_cvt_pk_bf16_f32 v127, v118, v119\n"
    "v_mfma_f32_16x16x32_bf16 a[196:199], v[204:207], v[0:3], a[196:199]\n"
    "ds_read_b64_tr_b16 v[194:195], v10 offset:26624\n"
    "global_store_dwordx4 v254, v[120:123], s[52:53] offset:2048 sc1\n"
    "global_store_dwordx4 v254, v[124:127], s[52:53] offset:3072 sc1\n"
    "v_mfma_f32_16x16x32_bf16 a[204:207], v[200:203], v[28:31], a[204:207]\n"
    "ds_read_b64_tr_b16 v[196:197], v245 offset:24576\n"
    "v_mfma_f32_16x16x32_bf16 a[204:207], v[204:207], v[4:7], a[204:207]\n"
    "s_waitcnt lgkmcnt(3)\n"
    "ds_read_b64_tr_b16 v[198:199], v245 offset:26624\n"
    "v_mfma_f32_16x16x32_bf16 a[216:219], v[184:187], v[180:183], a[216:219]\n"
    "ds_read_b64_tr_b16 v[200:201], v23 offset:24576\n"
    "s_add_u32 s52, s52, 0x1000\n"
    "s_addc_u32 s53, s53, 0\n"
    "v_mfma_f32_16x16x32_bf16 a[216:219], v[188:191], v[28:31], a[216:219]\n"
    "ds_read_b64_tr_b16 v[202:203], v23 offset:26624\n"
    "v_accvgpr_read_b32 v32, a192\n"
    "v_accvgpr_read_b32 v33, a193\n"
    "v_accvgpr_read_b32 v34, a194\n"
    "v_mfma_f32_16x16x32_bf16 a[208:211], v[184:187], v[176:179], a[208:211]\n"
    "ds_read_b64_tr_b16 v[204:205], v11 offset:24576\n"
    "v_accvgpr_read_b32 v35, a195\n"
    "v_accvgpr_read_b32 v36, a196\n"
    "v_accvgpr_read_b32 v37, a197\n"
    "v_mfma_f32_16x16x32_bf16 a[208:211], v[188:191], v[24:27], a[208:211]\n"
    "s_waitcnt lgkmcnt(3)\n"
    "ds_read_b64_tr_b16 v[206:207], v11 offset:26624\n"
    "v_accvgpr_read_b32 v38, a198\n"
    "v_accvgpr_read_b32 v39, a199\n"
    "v_accvgpr_read_b32 v40, a200\n"
    "v_mfma_f32_16x16x32_bf16 a[208:211], v[192:195], v[0:3], a[208:211]\n"
    "ds_read_b64_tr_b16 v[184:185], v242 offset:28672\n"
    "v_accvgpr_read_b32 v41, a201\n"
    "v_accvgpr_read_b32 v42, a202\n"
    "v_accvgpr_read_b32 v43, a203\n"
    "v_mfma_f32_16x16x32_bf16 a[212:215], v[196:199], v[176:179], a[212:215]\n"
    "ds_read_b64_tr_b16 v[186:187], v242 offset:30720\n"
    "v_accvgpr_read_b32 v44, a204\n"
    "v_accvgpr_read_b32 v45, a205\n"
    "v_accvgpr_read_b32 v46, a206\n"
    "v_mfma_f32_16x16x32_bf16 a[216:219], v[192:195], v[4:7], a[216:219]\n"
    "ds_read_b64_tr_b16 v[188:189], v20 offset:28672\n"
    "v_accvgpr_read_b32 v47, a207\n"
    "v_cvt_pk_bf16_f32 v48, v32, v33\n"
    "v_cvt_pk_bf16_f32 v49, v34, v35\n"
    "v_mfma_f32_16x16x32_bf16 a[220:223], v[196:199], v[180:183], a[220:223]\n"
    "s_waitcnt lgkmcnt(3)\n"
    "ds_read_b64_tr_b16 v[190:191], v20 offset:30720\n"
    "v_cvt_pk_bf16_f32 v50, v36, v37\n"
    "v_cvt_pk_bf16_f32 v51, v38, v39\n"
    "v_cvt_pk_bf16_f32 v52, v40, v41\n"
    "v_mfma_f32_16x16x32_bf16 a[220:223], v[200:203], v[28:31], a[220:223]\n"
    "ds_read_b64_tr_b16 v[192:193], v8 offset:28672\n"
    "v_cvt_pk_bf16_f32 v53, v42, v43\n"
    "v_cvt_pk_bf16_f32 v54, v44, v45\n"
    "v_cvt_pk_bf16_f32 v55, v46, v47\n"
    "v_mfma_f32_16x16x32_bf16 a[220:223], v[204:207], v[4:7], a[220:223]\n"
    "ds_read_b64_tr_b16 v[194:195], v8 offset:30720\n"
    "global_store_dwordx4 v254, v[48:51], s[52:53] offset:0 sc1\n"
    "global_store_dwordx4 v254, v[52:55], s[52:53] offset:1024 sc1\n"
    "v_mfma_f32_16x16x32_bf16 a[212:215], v[200:203], v[24:27], a[212:215]\n"
    "ds_read_b64_tr_b16 v[196:197], v243 offset:28672\n"
    "v_mfma_f32_16x16x32_bf16 a[212:215], v[204:207], v[0:3], a[212:215]\n"
    "s_waitcnt lgkmcnt(3)\n"
    "ds_read_b64_tr_b16 v[198:199], v243 offset:30720\n"
    "v_mfma_f32_16x16x32_bf16 a[224:227], v[184:187], v[176:179], a[224:227]\n"
    "ds_read_b64_tr_b16 v[200:201], v21 offset:28672\n"
    "v_mfma_f32_16x16x32_bf16 a[224:227], v[188:191], v[24:27], a[224:227]\n"
    "ds_read_b64_tr_b16 v[202:203], v21 offset:30720\n"
    "v_accvgpr_read_b32 v56, a208\n"
    "v_accvgpr_read_b32 v57, a209\n"
    "v_accvgpr_read_b32 v58, a210\n"
    "v_mfma_f32_16x16x32_bf16 a[232:235], v[184:187], v[180:183], a[232:235]\n"
    "ds_read_b64_tr_b16 v[204:205], v9 offset:28672\n"
    "v_accvgpr_read_b32 v59, a211\n"
    "v_accvgpr_read_b32 v60, a212\n"
    "v_accvgpr_read_b32 v61, a213\n"
    "v_mfma_f32_16x16x32_bf16 a[232:235], v[188:191], v[28:31], a[232:235]\n"
    "s_waitcnt lgkmcnt(3)\n"
    "ds_read_b64_tr_b16 v[206:207], v9 offset:30720\n"
    "v_accvgpr_read_b32 v62, a214\n"
    "v_accvgpr_read_b32 v63, a215\n"
    "v_accvgpr_read_b32 v64, a216\n"
    "v_mfma_f32_16x16x32_bf16 a[232:235], v[192:195], v[4:7], a[232:235]\n"
    "ds_read_b64_tr_b16 v[184:185], v244 offset:28672\n"
    "v_accvgpr_read_b32 v65, a217\n"
    "v_accvgpr_read_b32 v66, a218\n"
    "v_accvgpr_read_b32 v67, a219\n"
    "v_mfma_f32_16x16x32_bf16 a[236:239], v[196:199], v[180:183], a[236:239]\n"
    "ds_read_b64_tr_b16 v[186:187], v244 offset:30720\n"
    "v_accvgpr_read_b32 v68, a220\n"
    "v_accvgpr_read_b32 v69, a221\n"
    "v_accvgpr_read_b32 v70, a222\n"
    "v_mfma_f32_16x16x32_bf16 a[224:227], v[192:195], v[0:3], a[224:227]\n"
    "ds_read_b64_tr_b16 v[188:189], v22 offset:28672\n"
    "v_accvgpr_read_b32 v71, a223\n"
    "v_cvt_pk_bf16_f32 v72, v56, v57\n"
    "v_cvt_pk_bf16_f32 v73, v58, v59\n"
    "v_mfma_f32_16x16x32_bf16 a[228:231], v[196:199], v[176:179], a[228:231]\n"
    "s_waitcnt lgkmcnt(3)\n"
    "ds_read_b64_tr_b16 v[190:191], v22 offset:30720\n"
    "v_cvt_pk_bf16_f32 v74, v60, v61\n"
    "v_cvt_pk_bf16_f32 v75, v62, v63\n"
    "v_cvt_pk_bf16_f32 v76, v64, v65\n"
    "v_mfma_f32_16x16x32_bf16 a[228:231], v[200:203], v[24:27], a[228:231]\n"
    "ds_read_b64_tr_b16 v[192:193], v10 offset:28672\n"
    "v_cvt_pk_bf16_f32 v77, v66, v67\n"
    "v_cvt_pk_bf16_f32 v78, v68, v69\n"
    "v_cvt_pk_bf16_f32 v79, v70, v71\n"
    "v_mfma_f32_16x16x32_bf16 a[228:231], v[204:207], v[0:3], a[228:231]\n"
    "ds_read_b64_tr_b16 v[194:195], v10 offset:30720\n"
    "global_store_dwordx4 v254, v[72:75], s[52:53] offset:2048 sc1\n"
    "global_store_dwordx4 v254, v[76:79], s[52:53] offset:3072 sc1\n"
    "v_mfma_f32_16x16x32_bf16 a[236:239], v[200:203], v[28:31], a[236:239]\n"
    "ds_read_b64_tr_b16 v[196:197], v245 offset:28672\n"
    "v_mfma_f32_16x16x32_bf16 a[236:239], v[204:207], v[4:7], a[236:239]\n"
    "s_waitcnt lgkmcnt(3)\n"
    "ds_read_b64_tr_b16 v[198:199], v245 offset:30720\n"
    "v_mfma_f32_16x16x32_bf16 a[248:251], v[184:187], v[180:183], a[248:251]\n"
    "ds_read_b64_tr_b16 v[200:201], v23 offset:28672\n"
    "s_add_u32 s52, s52, 0x1000\n"
    "s_addc_u32 s53, s53, 0\n"
    "v_mfma_f32_16x16x32_bf16 a[248:251], v[188:191], v[28:31], a[248:251]\n"
    "ds_read_b64_tr_b16 v[202:203], v23 offset:30720\n"
    "v_accvgpr_read_b32 v80, a224\n"
    "v_accvgpr_read_b32 v81, a225\n"
    "v_accvgpr_read_b32 v82, a226\n"
    "v_mfma_f32_16x16x32_bf16 a[240:243], v[184:187], v[176:179], a[240:243]\n"
    "ds_read_b64_tr_b16 v[204:205], v11 offset:28672\n"
    "v_accvgpr_read_b32 v83, a227\n"
    "v_accvgpr_read_b32 v84, a228\n"
    "v_accvgpr_read_b32 v85, a229\n"
    "v_mfma_f32_16x16x32_bf16 a[240:243], v[188:191], v[24:27], a[240:243]\n"
    "s_waitcnt lgkmcnt(3)\n"
    "ds_read_b64_tr_b16 v[206:207], v11 offset:30720\n"
    "v_accvgpr_read_b32 v86, a230\n"
    "v_accvgpr_read_b32 v87, a231\n"
    "v_accvgpr_read_b32 v88, a232\n"
    "v_mfma_f32_16x16x32_bf16 a[240:243], v[192:195], v[0:3], a[240:243]\n"
    "ds_read_b64_tr_b16 v[184:185], v242 offset:32768\n"
    "v_accvgpr_read_b32 v89, a233\n"
    "v_accvgpr_read_b32 v90, a234\n"
    "v_accvgpr_read_b32 v91, a235\n"
    "v_mfma_f32_16x16x32_bf16 a[244:247], v[196:199], v[176:179], a[244:247]\n"
    "ds_read_b64_tr_b16 v[186:187], v242 offset:34816\n"
    "v_accvgpr_read_b32 v92, a236\n"
    "v_accvgpr_read_b32 v93, a237\n"
    "v_accvgpr_read_b32 v94, a238\n"
    "v_mfma_f32_16x16x32_bf16 a[248:251], v[192:195], v[4:7], a[248:251]\n"
    "ds_read_b64_tr_b16 v[188:189], v20 offset:32768\n"
    "v_accvgpr_read_b32 v95, a239\n"
    "v_cvt_pk_bf16_f32 v96, v80, v81\n"
    "v_cvt_pk_bf16_f32 v97, v82, v83\n"
    "v_mfma_f32_16x16x32_bf16 a[252:255], v[196:199], v[180:183], a[252:255]\n"
    "s_waitcnt lgkmcnt(3)\n"
    "ds_read_b64_tr_b16 v[190:191], v20 offset:34816\n"
    "v_cvt_pk_bf16_f32 v98, v84, v85\n"
    "v_cvt_pk_bf16_f32 v99, v86, v87\n"
    "v_cvt_pk_bf16_f32 v100, v88, v89\n"
    "v_mfma_f32_16x16x32_bf16 a[252:255], v[200:203], v[28:31], a[252:255]\n"
    "ds_read_b64_tr_b16 v[192:193], v8 offset:32768\n"
    "v_cvt_pk_bf16_f32 v101, v90, v91\n"
    "v_cvt_pk_bf16_f32 v102, v92, v93\n"
    "v_cvt_pk_bf16_f32 v103, v94, v95\n"
    "v_mfma_f32_16x16x32_bf16 a[252:255], v[204:207], v[4:7], a[252:255]\n"
    "ds_read_b64_tr_b16 v[194:195], v8 offset:34816\n"
    "global_store_dwordx4 v254, v[96:99], s[52:53] offset:0 sc1\n"
    "global_store_dwordx4 v254, v[100:103], s[52:53] offset:1024 sc1\n"
    "v_mfma_f32_16x16x32_bf16 a[244:247], v[200:203], v[24:27], a[244:247]\n"
    "ds_read_b64_tr_b16 v[196:197], v243 offset:32768\n"
    "v_mfma_f32_16x16x32_bf16 a[244:247], v[204:207], v[0:3], a[244:247]\n"
    "s_waitcnt lgkmcnt(3)\n"
    "ds_read_b64_tr_b16 v[198:199], v243 offset:34816\n"
    "v_mfma_f32_16x16x32_bf16 v[208:211], v[184:187], v[176:179], v[208:211]\n"
    "ds_read_b64_tr_b16 v[200:201], v21 offset:32768\n"
    "v_mfma_f32_16x16x32_bf16 v[208:211], v[188:191], v[24:27], v[208:211]\n"
    "ds_read_b64_tr_b16 v[202:203], v21 offset:34816\n"
    "v_accvgpr_read_b32 v104, a240\n"
    "v_accvgpr_read_b32 v105, a241\n"
    "v_accvgpr_read_b32 v106, a242\n"
    "v_mfma_f32_16x16x32_bf16 v[216:219], v[184:187], v[180:183], v[216:219]\n"
    "ds_read_b64_tr_b16 v[204:205], v9 offset:32768\n"
    "v_accvgpr_read_b32 v107, a243\n"
    "v_accvgpr_read_b32 v108, a244\n"
    "v_accvgpr_read_b32 v109, a245\n"
    "v_mfma_f32_16x16x32_bf16 v[216:219], v[188:191], v[28:31], v[216:219]\n"
    "s_waitcnt lgkmcnt(3)\n"
    "ds_read_b64_tr_b16 v[206:207], v9 offset:34816\n"
    "v_accvgpr_read_b32 v110, a246\n"
    "v_accvgpr_read_b32 v111, a247\n"
    "v_accvgpr_read_b32 v112, a248\n"
    "v_mfma_f32_16x16x32_bf16 v[216:219], v[192:195], v[4:7], v[216:219]\n"
    "ds_read_b64_tr_b16 v[184:185], v244 offset:32768\n"
    "v_accvgpr_read_b32 v113, a249\n"
    "v_accvgpr_read_b32 v114, a250\n"
    "v_accvgpr_read_b32 v115, a251\n"
    "v_mfma_f32_16x16x32_bf16 v[220:223], v[196:199], v[180:183], v[220:223]\n"
    "ds_read_b64_tr_b16 v[186:187], v244 offset:34816\n"
    "v_accvgpr_read_b32 v116, a252\n"
    "v_accvgpr_read_b32 v117, a253\n"
    "v_accvgpr_read_b32 v118, a254\n"
    "v_mfma_f32_16x16x32_bf16 v[208:211], v[192:195], v[0:3], v[208:211]\n"
    "ds_read_b64_tr_b16 v[188:189], v22 offset:32768\n"
    "v_accvgpr_read_b32 v119, a255\n"
    "v_cvt_pk_bf16_f32 v120, v104, v105\n"
    "v_cvt_pk_bf16_f32 v121, v106, v107\n"
    "v_mfma_f32_16x16x32_bf16 v[212:215], v[196:199], v[176:179], v[212:215]\n"
    "s_waitcnt lgkmcnt(3)\n"
    "ds_read_b64_tr_b16 v[190:191], v22 offset:34816\n"
    "v_cvt_pk_bf16_f32 v122, v108, v109\n"
    "v_cvt_pk_bf16_f32 v123, v110, v111\n"
    "v_cvt_pk_bf16_f32 v124, v112, v113\n"
    "v_mfma_f32_16x16x32_bf16 v[212:215], v[200:203], v[24:27], v[212:215]\n"
    "ds_read_b64_tr_b16 v[192:193], v10 offset:32768\n"
    "v_cvt_pk_bf16_f32 v125, v114, v115\n"
    "v_cvt_pk_bf16_f32 v126, v116, v117\n"
    "v_cvt_pk_bf16_f32 v127, v118, v119\n"
    "v_mfma_f32_16x16x32_bf16 v[212:215], v[204:207], v[0:3], v[212:215]\n"
    "ds_read_b64_tr_b16 v[194:195], v10 offset:34816\n"
    "global_store_dwordx4 v254, v[120:123], s[52:53] offset:2048 sc1\n"
    "global_store_dwordx4 v254, v[124:127], s[52:53] offset:3072 sc1\n"
    "v_mfma_f32_16x16x32_bf16 v[220:223], v[200:203], v[28:31], v[220:223]\n"
    "ds_read_b64_tr_b16 v[196:197], v245 offset:32768\n"
    "v_mfma_f32_16x16x32_bf16 v[220:223], v[204:207], v[4:7], v[220:223]\n"
    "s_waitcnt lgkmcnt(3)\n"
    "ds_read_b64_tr_b16 v[198:199], v245 offset:34816\n"
    "v_mfma_f32_16x16x32_bf16 v[232:235], v[184:187], v[180:183], v[232:235]\n"
    "ds_read_b64_tr_b16 v[200:201], v23 offset:32768\n"
    "s_add_u32 s52, s52, 0x1000\n"
    "s_addc_u32 s53, s53, 0\n"
    "v_mfma_f32_16x16x32_bf16 v[232:235], v[188:191], v[28:31], v[232:235]\n"
    "ds_read_b64_tr_b16 v[202:203], v23 offset:34816\n"
    "v_cvt_pk_bf16_f32 v48, v208, v209\n"
    "v_mfma_f32_16x16x32_bf16 v[224:227], v[184:187], v[176:179], v[224:227]\n"
    "ds_read_b64_tr_b16 v[204:205], v11 offset:32768\n"
    "v_cvt_pk_bf16_f32 v49, v210, v211\n"
    "v_mfma_f32_16x16x32_bf16 v[224:227], v[188:191], v[24:27], v[224:227]\n"
    "s_waitcnt lgkmcnt(3)\n"
    "ds_read_b64_tr_b16 v[206:207], v11 offset:34816\n"
    "v_cvt_pk_bf16_f32 v50, v212, v213\n"
    "v_mfma_f32_16x16x32_bf16 v[224:227], v[192:195], v[0:3], v[224:227]\n"
    "v_cvt_pk_bf16_f32 v51, v214, v215\n"
    "v_mfma_f32_16x16x32_bf16 v[228:231], v[196:199], v[176:179], v[228:231]\n"
    "v_cvt_pk_bf16_f32 v52, v216, v217\n"
    "v_mfma_f32_16x16x32_bf16 v[232:235], v[192:195], v[4:7], v[232:235]\n"
    "v_cvt_pk_bf16_f32 v53, v218, v219\n"
    "v_mfma_f32_16x16x32_bf16 v[236:239], v[196:199], v[180:183], v[236:239]\n"
    "s_waitcnt lgkmcnt(0)\n"
    "v_cvt_pk_bf16_f32 v54, v220, v221\n"
    "v_mfma_f32_16x16x32_bf16 v[236:239], v[200:203], v[28:31], v[236:239]\n"
    "v_cvt_pk_bf16_f32 v55, v222, v223\n"
    "v_mfma_f32_16x16x32_bf16 v[236:239], v[204:207], v[4:7], v[236:239]\n"
    "global_store_dwordx4 v254, v[48:51], s[52:53] offset:0 sc1\n"
    "v_mfma_f32_16x16x32_bf16 v[228:231], v[200:203], v[24:27], v[228:231]\n"
    "global_store_dwordx4 v254, v[52:55], s[52:53] offset:1024 sc1\n"
    "v_mfma_f32_16x16x32_bf16 v[228:231], v[204:207], v[0:3], v[228:231]\n"
    "s_nop 7\n"
    "v_cvt_pk_bf16_f32 v72, v224, v225\n"
    "v_cvt_pk_bf16_f32 v73, v226, v227\n"
    "v_cvt_pk_bf16_f32 v74, v228, v229\n"
    "v_cvt_pk_bf16_f32 v75, v230, v231\n"
    "v_cvt_pk_bf16_f32 v76, v232, v233\n"
    "v_cvt_pk_bf16_f32 v77, v234, v235\n"
    "v_cvt_pk_bf16_f32 v78, v236, v237\n"
    "v_cvt_pk_bf16_f32 v79, v238, v239\n"
    "global_store_dwordx4 v254, v[72:75], s[52:53] offset:2048 sc1\n"
    "global_store_dwordx4 v254, v[76:79], s[52:53] offset:3072 sc1\n"
    "s_add_u32 s52, s52, 0x1000\n"
    "s_addc_u32 s53, s53, 0\n"
    "s_mov_b64 s[72:73], s[40:41]\n"
    "s_mov_b64 s[74:75], s[42:43]\n"
    "s_add_u32 s76, s56, s60\n"
    "s_add_u32 s35, s55, s60\n"
    "s_lshl_b32 s68, s71, 6\n"
    "s_add_u32 s68, s68, s69\n"
    "s_mov_b32 s77, s68\n"
    "s_lshl_b32 s68, s79, 6\n"
    "s_add_u32 s68, s68, s69\n"
    "s_mov_b32 s34, s68\n"
    "s_add_u32 s58, s54, %8\n"
    "s_add_u32 s59, s56, %8\n"
    "s_add_u32 s60, s55, %8\n"
    "s_mul_i32 s61, %7, 3072\n"
    "s_add_u32 s61, s61, %8\n"
    "s_add_u32 s61, s61, 148480\n"
    "s_lshl_b32 s54, %13, 1\n"
    "s_add_u32 s55, s54, 1\n"
    "s_lshr_b32 s56, %13, 2\n"
    "s_add_u32 s56, s56, 16\n"
    "s_lshl_b32 s67, %12, 2\n"
    "s_add_u32 s67, s67, %7\n"
    "s_mul_i32 s68, s67, 36864\n"
    "s_add_u32 s69, s68, 33555456\n"
    "s_mov_b64 s[40:41], %2\n"
    "s_add_u32 s40, s40, s69\n"
    "s_addc_u32 s41, s41, 0\n"
    "s_and_b32 s41, s41, 0xffff\n"
    "s_mov_b32 s42, 36864\n"
    "s_mov_b32 s43, 0x20000\n"
    "s_add_u32 s69, s68, 1049600\n"
    "s_mov_b64 s[44:45], %2\n"
    "s_add_u32 s44, s44, s69\n"
    "s_addc_u32 s45, s45, 0\n"
    "s_and_b32 s45, s45, 0xffff\n"
    "s_mov_b32 s46, 16551936\n"
    "s_mov_b32 s47, 0x20000\n"
    "s_add_u32 s68, s68, 18432\n"
    "s_mov_b64 s[50:51], %4\n"
    "s_add_u32 s50, s50, s68\n"
    "s_addc_u32 s51, s51, 0\n"
    "s_and_b32 s57, %13, 3\n"
    "s_cmp_eq_u32 s57, %7\n"
    "s_cselect_b32 s57, 1, 0\n"
    "v_lshlrev_b32 v129, 4, v255\n"
    "v_and_b32 v126, 15, v255\n"
    "v_mul_u32_u24 v130, 0x480, v126\n"
    "v_lshrrev_b32 v127, 4, v255\n"
    "v_and_b32 v131, 1, v127\n"
    "v_lshl_add_u32 v130, v131, 5, v130\n"
    "v_lshrrev_b32 v131, 1, v127\n"
    "v_lshl_add_u32 v130, v131, 4, v130\n"
    "v_lshlrev_b32 v125, 6, v126\n"
    "v_lshl_add_u32 v125, v127, 3, v125\n"
    "v_bfe_u32 v131, v126, 2, 1\n"
    "v_lshl_add_u32 v125, v131, 5, v125\n"
    "v_add_u32 v125, s61, v125\n"
    "v_xor_b32 v135, 32, v125\n"
    "s_lshl_b32 s67, s54, 6\n"
    "v_add_u32 v122, s67, v130\n"
    "s_lshl_b32 s67, s55, 6\n"
    "v_add_u32 v123, s67, v130\n"
    "s_lshl_b32 s67, s56, 6\n"
    "v_add_u32 v124, s67, v130\n"
    "v_lshlrev_b32 v131, 2, v127\n"
    "v_sub_u32 v131, v126, v131\n"
    "v_mov_b32 v132, 0x3f80\n"
    "v_mov_b32 v133, 0x3f800000\n"
    "v_lshlrev_b32 v126, 2, v126\n"
    "s_mul_i32 s67, %7, 0\n"
    "v_add_u32 v126, s67, v126\n"
    "v_cmp_eq_u32 vcc, 0, v131\n"
    "v_cmp_eq_u32 s[52:53], 1, v131\n"
    "v_mov_b32 v108, 0\n"
    "v_cndmask_b32 v110, 0, v132, vcc\n"
    "v_mov_b32 v112, 0\n"
    "v_cndmask_b32 v110, v110, v133, s[52:53]\n"
    "v_cmp_eq_u32 vcc, 2, v131\n"
    "v_cmp_eq_u32 s[52:53], 3, v131\n"
    "v_mov_b32 v109, 0\n"
    "v_cndmask_b32 v111, 0, v132, vcc\n"
    "v_mov_b32 v113, 0\n"
    "v_cndmask_b32 v111, v111, v133, s[52:53]\n"
    "v_bfe_u32 v131, v255, 2, 2\n"
    "v_and_b32 v132, 3, v255\n"
    "v_lshlrev_b32 v132, 3, v132\n"
    "v_lshrrev_b32 v133, 1, v127\n"
    "v_mul_u32_u24 v133, 18, v133\n"
    "v_and_b32 v134, 1, v127\n"
    "v_lshl_add_u32 v133, v134, 2, v133\n"
    "v_add_u32 v133, v133, v131\n"
    "v_bfe_u32 v134, v133, 2, 1\n"
    "v_lshl_add_u32 v133, v133, 6, v132\n"
    "v_lshl_add_u32 v133, v134, 5, v133\n"
    "v_lshrrev_b32 v134, 1, v131\n"
    "v_lshl_add_u32 v134, v127, 2, v134\n"
    "v_mul_u32_u24 v134, 18, v134\n"
    "v_and_b32 v131, 1, v131\n"
    "v_add3_u32 v134, v134, v131, 16\n"
    "v_bfe_u32 v131, v134, 2, 1\n"
    "v_lshl_add_u32 v134, v134, 6, v132\n"
    "v_lshl_add_u32 v134, v131, 5, v134\n"
    "v_add_u32 v114, s58, v133\n"
    "v_add_u32 v117, s58, v134\n"
    "v_xor_b32 v180, 32, v114\n"
    "v_xor_b32 v183, 32, v117\n"
    "v_add_u32 v115, s59, v133\n"
    "v_add_u32 v118, s59, v134\n"
    "v_xor_b32 v181, 32, v115\n"
    "v_xor_b32 v184, 32, v118\n"
    "v_add_u32 v116, s60, v133\n"
    "v_add_u32 v119, s60, v134\n"
    "v_xor_b32 v182, 32, v116\n"
    "v_xor_b32 v185, 32, v119\n"
    "s_lshl_b32 s63, s54, 11\n"
    "s_lshl_b32 s64, s55, 11\n"
    "s_lshl_b32 s65, s56, 11\n"
    "s_waitcnt vmcnt(0)\n"
    "s_barrier\n"
    "s_cmp_lg_u32 %7, 0\n"
    "s_cbranch_scc1 30f\n"
    "s_lshl_b32 s67, %6, 2\n"
    "v_mov_b32 v1, s78\n"
    "v_mov_b32 v0, s67\n"
    "s_mov_b64 exec, 1\n"
    "global_store_dword v0, v1, s[30:31] offset:160 sc1\n"
    "s_mov_b64 exec, -1\n"
    "30:\n"
    "s_mov_b32 m0, s76\n"
    "s_nop 0\n"
    "buffer_load_dwordx4 v250, s[72:75], s77 offen lds\n"
    "s_add_u32 m0, m0, 0x400\n"
    "s_add_u32 s77, s77, 18432\n"
    "buffer_load_dwordx4 v250, s[72:75], s77 offen lds\n"
    "s_add_u32 m0, m0, 0x400\n"
    "s_add_u32 s77, s77, 18432\n"
    "buffer_load_dwordx4 v250, s[72:75], s77 offen lds\n"
    "s_add_u32 m0, m0, 0x400\n"
    "s_add_u32 s77, s77, 18432\n"
    "buffer_load_dwordx4 v250, s[72:75], s77 offen lds\n"
    "s_add_u32 m0, m0, 0x400\n"
    "s_add_u32 s77, s77, 18432\n"
    "buffer_load_dwordx4 v250, s[72:75], s77 offen lds\n"
    "s_add_u32 m0, m0, 0x400\n"
    "s_add_u32 s77, s77, 18432\n"
    "buffer_load_dwordx4 v250, s[72:75], s77 offen lds\n"
    "s_add_u32 m0, m0, 0x400\n"
    "s_add_u32 s77, s77, 18432\n"
    "buffer_load_dwordx4 v250, s[72:75], s77 offen lds\n"
    "s_add_u32 m0, m0, 0x400\n"
    "s_add_u32 s77, s77, 18432\n"
    "buffer_load_dwordx4 v250, s[72:75], s77 offen lds\n"
    "s_add_u32 m0, m0, 0x400\n"
    "s_add_u32 s77, s77, 18432\n"
    "buffer_load_dwordx4 v250, s[72:75], s77 offen lds\n"
    "s_mov_b32 m0, s35\n"
    "s_nop 0\n"
    "buffer_load_dwordx4 v250, s[72:75], s34 offen lds\n"
    "s_add_u32 m0, m0, 0x400\n"
    "s_add_u32 s34, s34, 18432\n"
    "buffer_load_dwordx4 v250, s[72:75], s34 offen lds\n"
    "s_add_u32 m0, m0, 0x400\n"
    "s_add_u32 s34, s34, 18432\n"
    "buffer_load_dwordx4 v250, s[72:75], s34 offen lds\n"
    "s_add_u32 m0, m0, 0x400\n"
    "s_add_u32 s34, s34, 18432\n"
    "buffer_load_dwordx4 v250, s[72:75], s34 offen lds\n"
    "s_add_u32 m0, m0, 0x400\n"
    "s_add_u32 s34, s34, 18432\n"
    "buffer_load_dwordx4 v250, s[72:75], s34 offen lds\n"
    "s_add_u32 m0, m0, 0x400\n"
    "s_add_u32 s34, s34, 18432\n"
    "buffer_load_dwordx4 v250, s[72:75], s34 offen lds\n"
    "s_add_u32 m0, m0, 0x400\n"
    "s_add_u32 s34, s34, 18432\n"
    "buffer_load_dwordx4 v250, s[72:75], s34 offen lds\n"
    "s_add_u32 m0, m0, 0x400\n"
    "s_add_u32 s34, s34, 18432\n"
    "buffer_load_dwordx4 v250, s[72:75], s34 offen lds\n"
    "s_add_u32 m0, m0, 0x400\n"
    "s_add_u32 s34, s34, 18432\n"
    "buffer_load_dwordx4 v250, s[72:75], s34 offen lds\n"
    "ds_read_b64 v[96:97], v125 offset:0\n"
    "ds_read_b64 v[98:99], v135 offset:0\n"
    "ds_read_b64 v[100:101], v125 offset:1024\n"
    "ds_read_b64 v[102:103], v135 offset:1024\n"
    "ds_read_b64 v[104:105], v125 offset:2048\n"
    "ds_read_b64 v[106:107], v135 offset:2048\n"
    "s_mov_b32 s70, 0\n"
    "s_mov_b32 s66, 0x80000\n"
    "s_mov_b32 s69, 0\n"
    "61:\n"
    "global_load_dword v128, v126, s[32:33] offset:128 sc1\n"
    "s_waitcnt vmcnt(0) lgkmcnt(0)\n"
    "s_bitcmp1_b32 s70, 5\n"
    "s_cbranch_scc1 60f\n"
    "s_bitset1_b32 s70, 5\n"
    "s_barrier\n"
    "60:\n"
    "v_cmp_eq_u32 vcc, s62, v128\n"
    "s_nop 1\n"
    "s_and_b32 s72, vcc_lo, 0xffff\n"
    "s_andn2_b32 s72, s72, s69\n"
    "s_or_b32 s67, s72, s69\n"
    "s_and_b32 s67, s67, 0xff\n"
    "s_cmp_eq_u32 s67, 0xff\n"
    "s_cbranch_scc1 59f\n"
    "s_and_b32 s71, s72, 0xff00\n"
    "s_and_b32 s72, s72, 0xff\n"
    "59:\n"
    "s_cmp_lg_u32 s72, 0\n"
    "s_cbranch_scc0 68f\n"
    "s_or_b32 s69, s69, s72\n"
    "s_cmp_eq_u32 s57, 0\n"
    "s_cbranch_scc1 62f\n"
    "s_bitcmp1_b32 s72, 8\n"
    "s_cbranch_scc0 65f\n"
    "s_add_u32 s67, s65, 0\n"
    "buffer_load_dwordx4 a[64:67], v129, s[44:47], s67 offen sc1\n"
    "65:\n"
    "s_bitcmp1_b32 s72, 9\n"
    "s_cbranch_scc0 65f\n"
    "s_add_u32 s67, s65, 2359296\n"
    "buffer_load_dwordx4 a[68:71], v129, s[44:47], s67 offen sc1\n"
    "65:\n"
    "s_bitcmp1_b32 s72, 10\n"
    "s_cbranch_scc0 65f\n"
    "s_add_u32 s67, s65, 4718592\n"
    "buffer_load_dwordx4 a[72:75], v129, s[44:47], s67 offen sc1\n"
    "65:\n"
    "s_bitcmp1_b32 s72, 11\n"
    "s_cbranch_scc0 65f\n"
    "s_add_u32 s67, s65, 7077888\n"
    "buffer_load_dwordx4 a[76:79], v129, s[44:47], s67 offen sc1\n"
    "65:\n"
    "s_bitcmp1_b32 s72, 12\n"
    "s_cbranch_scc0 65f\n"
    "s_add_u32 s67, s65, 9437184\n"
    "buffer_load_dwordx4 a[80:83], v129, s[44:47], s67 offen sc1\n"
    "65:\n"
    "s_bitcmp1_b32 s72, 13\n"
    "s_cbranch_scc0 65f\n"
    "s_add_u32 s67, s65, 11796480\n"
    "buffer_load_dwordx4 a[84:87], v129, s[44:47], s67 offen sc1\n"
    "65:\n"
    "s_bitcmp1_b32 s72, 14\n"
    "s_cbranch_scc0 65f\n"
    "s_add_u32 s67, s65, 14155776\n"
    "buffer_load_dwordx4 a[88:91], v129, s[44:47], s67 offen sc1\n"
    "65:\n"
    "s_bitcmp1_b32 s72, 15\n"
    "s_cbranch_scc0 65f\n"
    "s_add_u32 s67, s65, 16515072\n"
    "buffer_load_dwordx4 a[92:95], v129, s[44:47], s67 offen sc1\n"
    "65:\n"
    "62:\n"
    "s_bitcmp1_b32 s70, 0\n"
    "s_cbranch_scc1 63f\n"
    "s_bitcmp1_b32 s72, 0\n"
    "s_cbranch_scc0 65f\n"
    "s_mov_b32 s67, 0\n"
    "buffer_load_dwordx4 v[0:3], v129, s[40:43], s67 offen sc1\n"
    "buffer_load_dwordx4 v[4:7], v129, s[40:43], s67 offen offset:2048 sc1\n"
    "65:\n"
    "s_bitcmp1_b32 s72, 1\n"
    "s_cbranch_scc0 65f\n"
    "s_mov_b32 s67, 4096\n"
    "buffer_load_dwordx4 v[8:11], v129, s[40:43], s67 offen sc1\n"
    "buffer_load_dwordx4 v[12:15], v129, s[40:43], s67 offen offset:2048 sc1\n"
    "65:\n"
    "s_bitcmp1_b32 s72, 2\n"
    "s_cbranch_scc0 65f\n"
    "s_mov_b32 s67, 8192\n"
    "buffer_load_dwordx4 v[16:19], v129, s[40:43], s67 offen sc1\n"
    "buffer_load_dwordx4 v[20:23], v129, s[40:43], s67 offen offset:2048 sc1\n"
    "65:\n"
    "s_bitcmp1_b32 s72, 3\n"
    "s_cbranch_scc0 65f\n"
    "s_mov_b32 s67, 12288\n"
    "buffer_load_dwordx4 v[24:27], v129, s[40:43], s67 offen sc1\n"
    "buffer_load_dwordx4 v[28:31], v129, s[40:43], s67 offen offset:2048 sc1\n"
    "65:\n"
    "s_bitcmp1_b32 s72, 4\n"
    "s_cbranch_scc0 65f\n"
    "s_mov_b32 s67, 16384\n"
    "buffer_load_dwordx4 v[32:35], v129, s[40:43], s67 offen sc1\n"
    "buffer_load_dwordx4 v[36:39], v129, s[40:43], s67 offen offset:2048 sc1\n"
    "65:\n"
    "s_bitcmp1_b32 s72, 5\n"
    "s_cbranch_scc0 65f\n"
    "s_mov_b32 s67, 20480\n"
    "buffer_load_dwordx4 v[40:43], v129, s[40:43], s67 offen sc1\n"
    "buffer_load_dwordx4 v[44:47], v129, s[40:43], s67 offen offset:2048 sc1\n"
    "65:\n"
    "s_bitcmp1_b32 s72, 6\n"
    "s_cbranch_scc0 65f\n"
    "s_mov_b32 s67, 24576\n"
    "buffer_load_dwordx4 v[48:51], v129, s[40:43], s67 offen sc1\n"
    "buffer_load_dwordx4 v[52:55], v129, s[40:43], s67 offen offset:2048 sc1\n"
    "65:\n"
    "s_bitcmp1_b32 s72, 7\n"
    "s_cbranch_scc0 65f\n"
    "s_mov_b32 s67, 28672\n"
    "buffer_load_dwordx4 v[56:59], v129, s[40:43], s67 offen sc1\n"
    "buffer_load_dwordx4 v[60:63], v129, s[40:43], s67 offen offset:2048 sc1\n"
    "65:\n"
    "s_and_b32 s67, s69, 0xff\n"
    "s_cmp_eq_u32 s67, 0xff\n"
    "s_cbranch_scc0 63f\n"
    "s_bitset1_b32 s70, 0\n"
    "s_mov_b32 s67, 32768\n"
    "buffer_load_dwordx4 v[64:67], v129, s[40:43], s67 offen sc1\n"
    "buffer_load_dwordx4 v[68:71], v129, s[40:43], s67 offen offset:2048 sc1\n"
    "s_and_b32 s67, s72, 0xff\n"
    "s_cmp_eq_u32 s67, 0xff\n"
    "s_cbranch_scc1 64f\n"
    "s_waitcnt vmcnt(2)\n"
    "64:\n"
    "ds_read_b64_tr_b16 a[96:97], v114 offset:0\n"
    "ds_read_b64_tr_b16 a[98:99], v114 offset:512\n"
    "ds_read_b64_tr_b16 a[100:101], v180 offset:0\n"
    "ds_read_b64_tr_b16 a[102:103], v180 offset:512\n"
    "ds_read_b64_tr_b16 a[104:105], v115 offset:0\n"
    "ds_read_b64_tr_b16 a[106:107], v115 offset:512\n"
    "ds_read_b64_tr_b16 a[108:109], v181 offset:0\n"
    "ds_read_b64_tr_b16 a[110:111], v181 offset:512\n"
    "ds_read_b64_tr_b16 a[112:113], v180 offset:2304\n"
    "ds_read_b64_tr_b16 a[114:115], v180 offset:2816\n"
    "ds_read_b64_tr_b16 a[116:117], v114 offset:2304\n"
    "ds_read_b64_tr_b16 a[118:119], v114 offset:2816\n"
    "s_waitcnt lgkmcnt(8) vmcnt(17)\n"
    "v_mfma_f32_16x16x32_bf16 v[148:151], a[96:99], v[0:3], 0\n"
    "ds_read_b64_tr_b16 a[120:121], v181 offset:2304\n"
    "ds_read_b64_tr_b16 a[122:123], v181 offset:2816\n"
    "s_bitcmp1_b32 s72, 8\n"
    "s_cbranch_scc1 94f\n"
    "s_mov_b32 s46, 0\n"
    "buffer_load_dwordx4 a[128:131], v129, s[44:47], 0 offen\n"
    "buffer_load_dwordx4 a[128:131], v129, s[44:47], 0 offen\n"
    "s_mov_b32 s46, 16551936\n"
    "s_branch 95f\n"
    "94:\n"
    "s_add_u32 s67, s63, 0\n"
    "buffer_load_dwordx4 a[0:3], v129, s[44:47], s67 offen sc1\n"
    "s_add_u32 s67, s64, 0\n"
    "buffer_load_dwordx4 a[32:35], v129, s[44:47], s67 offen sc1\n"
    "95:\n"
    "v_mfma_f32_16x16x32_bf16 v[152:155], a[100:103], v[0:3], 0\n"
    "s_waitcnt lgkmcnt(6)\n"
    "ds_read_b64_tr_b16 a[124:125], v115 offset:2304\n"
    "ds_read_b64_tr_b16 a[126:127], v115 offset:2816\n"
    "v_mfma_f32_16x16x32_bf16 v[156:159], a[104:107], v[0:3], 0\n"
    "ds_read_b64_tr_b16 a[96:97], v114 offset:4608\n"
    "ds_read_b64_tr_b16 a[98:99], v114 offset:5120\n"
    "v_mfma_f32_16x16x32_bf16 v[160:163], a[108:111], v[0:3], 0\n"
    "s_waitcnt lgkmcnt(6) vmcnt(18)\n"
    "ds_read_b64_tr_b16 a[100:101], v180 offset:4608\n"
    "ds_read_b64_tr_b16 a[102:103], v180 offset:5120\n"
    "v_mfma_f32_16x16x32_bf16 v[148:151], a[112:115], v[4:7], v[148:151]\n"
    "ds_read_b64_tr_b16 a[104:105], v115 offset:4608\n"
    "ds_read_b64_tr_b16 a[106:107], v115 offset:5120\n"
    "v_mfma_f32_16x16x32_bf16 v[152:155], a[116:119], v[4:7], v[152:155]\n"
    "s_waitcnt lgkmcnt(6)\n"
    "ds_read_b64_tr_b16 a[108:109], v181 offset:4608\n"
    "ds_read_b64_tr_b16 a[110:111], v181 offset:5120\n"
    "v_mfma_f32_16x16x32_bf16 v[156:159], a[120:123], v[4:7], v[156:159]\n"
    "ds_read_b64_tr_b16 a[112:113], v180 offset:6912\n"
    "ds_read_b64_tr_b16 a[114:115], v180 offset:7424\n"
    "v_mfma_f32_16x16x32_bf16 v[160:163], a[124:127], v[4:7], v[160:163]\n"
    "s_waitcnt lgkmcnt(6) vmcnt(17)\n"
    "ds_read_b64_tr_b16 a[116:117], v114 offset:6912\n"
    "ds_read_b64_tr_b16 a[118:119], v114 offset:7424\n"
    "v_mfma_f32_16x16x32_bf16 v[148:151], a[96:99], v[8:11], v[148:151]\n"
    "ds_read_b64_tr_b16 a[120:121], v181 offset:6912\n"
    "ds_read_b64_tr_b16 a[122:123], v181 offset:7424\n"
    "s_bitcmp1_b32 s72, 9\n"
    "s_cbranch_scc1 94f\n"
    "s_mov_b32 s46, 0\n"
    "buffer_load_dwordx4 a[128:131], v129, s[44:47], 0 offen\n"
    "buffer_load_dwordx4 a[128:131], v129, s[44:47], 0 offen\n"
    "s_mov_b32 s46, 16551936\n"
    "s_branch 95f\n"
    "94:\n"
    "s_add_u32 s67, s63, 2359296\n"
    "buffer_load_dwordx4 a[4:7], v129, s[44:47], s67 offen sc1\n"
    "s_add_u32 s67, s64, 2359296\n"
    "buffer_load_dwordx4 a[36:39], v129, s[44:47], s67 offen sc1\n"
    "95:\n"
    "v_mfma_f32_16x16x32_bf16 v[152:155], a[100:103], v[8:11], v[152:155]\n"
    "s_waitcnt lgkmcnt(6)\n"
    "ds_read_b64_tr_b16 a[124:125], v115 offset:6912\n"
    "ds_read_b64_tr_b16 a[126:127], v115 offset:7424\n"
    "v_mfma_f32_16x16x32_bf16 v[156:159], a[104:107], v[8:11], v[156:159]\n"
    "ds_read_b64_tr_b16 a[96:97], v114 offset:9216\n"
    "ds_read_b64_tr_b16 a[98:99], v114 offset:9728\n"
    "v_mfma_f32_16x16x32_bf16 v[160:163], a[108:111], v[8:11], v[160:163]\n"
    "s_waitcnt lgkmcnt(6) vmcnt(18)\n"
    "ds_read_b64_tr_b16 a[100:101], v180 offset:9216\n"
    "ds_read_b64_tr_b16 a[102:103], v180 offset:9728\n"
    "v_mfma_f32_16x16x32_bf16 v[148:151], a[112:115], v[12:15], v[148:151]\n"
    "ds_read_b64_tr_b16 a[104:105], v115 offset:9216\n"
    "ds_read_b64_tr_b16 a[106:107], v115 offset:9728\n"
    "v_mfma_f32_16x16x32_bf16 v[152:155], a[116:119], v[12:15], v[152:155]\n"
    "s_waitcnt lgkmcnt(6)\n"
    "ds_read_b64_tr_b16 a[108:109], v181 offset:9216\n"
    "ds_read_b64_tr_b16 a[110:111], v181 offset:9728\n"
    "v_mfma_f32_16x16x32_bf16 v[156:159], a[120:123], v[12:15], v[156:159]\n"
    "ds_read_b64_tr_b16 a[112:113], v180 offset:11520\n"
    "ds_read_b64_tr_b16 a[114:115], v180 offset:12032\n"
    "v_mfma_f32_16x16x32_bf16 v[160:163], a[124:127], v[12:15], v[160:163]\n"
    "s_waitcnt lgkmcnt(6) vmcnt(17)\n"
    "ds_read_b64_tr_b16 a[116:117], v114 offset:11520\n"
    "ds_read_b64_tr_b16 a[118:119], v114 offset:12032\n"
    "v_mfma_f32_16x16x32_bf16 v[148:151], a[96:99], v[16:19], v[148:151]\n"
    "ds_read_b64_tr_b16 a[120:121], v181 offset:11520\n"
    "ds_read_b64_tr_b16 a[122:123], v181 offset:12032\n"
    "s_bitcmp1_b32 s72, 10\n"
    "s_cbranch_scc1 94f\n"
    "s_mov_b32 s46, 0\n"
    "buffer_load_dwordx4 a[128:131], v129, s[44:47], 0 offen\n"
    "buffer_load_dwordx4 a[128:131], v129, s[44:47], 0 offen\n"
    "s_mov_b32 s46, 16551936\n"
    "s_branch 95f\n"
    "94:\n"
    "s_add_u32 s67, s63, 4718592\n"
    "buffer_load_dwordx4 a[8:11], v129, s[44:47], s67 offen sc1\n"
    "s_add_u32 s67, s64, 4718592\n"
    "buffer_load_dwordx4 a[40:43], v129, s[44:47], s67 offen sc1\n"
    "95:\n"
    "v_mfma_f32_16x16x32_bf16 v[152:155], a[100:103], v[16:19], v[152:155]\n"
    "s_waitcnt lgkmcnt(6)\n"
    "ds_read_b64_tr_b16 a[124:125], v115 offset:11520\n"
    "ds_read_b64_tr_b16 a[126:127], v115 offset:12032\n"
    "v_mfma_f32_16x16x32_bf16 v[156:159], a[104:107], v[16:19], v[156:159]\n"
    "ds_read_b64_tr_b16 a[96:97], v114 offset:13824\n"
    "ds_read_b64_tr_b16 a[98:99], v114 offset:14336\n"
    "v_mfma_f32_16x16x32_bf16 v[160:163], a[108:111], v[16:19], v[160:163]\n"
    "s_waitcnt lgkmcnt(6) vmcnt(18)\n"
    "ds_read_b64_tr_b16 a[100:101], v180 offset:13824\n"
    "ds_read_b64_tr_b16 a[102:103], v180 offset:14336\n"
    "v_mfma_f32_16x16x32_bf16 v[148:151], a[112:115], v[20:23], v[148:151]\n"
    "ds_read_b64_tr_b16 a[104:105], v115 offset:13824\n"
    "ds_read_b64_tr_b16 a[106:107], v115 offset:14336\n"
    "v_mfma_f32_16x16x32_bf16 v[152:155], a[116:119], v[20:23], v[152:155]\n"
    "s_waitcnt lgkmcnt(6)\n"
    "ds_read_b64_tr_b16 a[108:109], v181 offset:13824\n"
    "ds_read_b64_tr_b16 a[110:111], v181 offset:14336\n"
    "v_mfma_f32_16x16x32_bf16 v[156:159], a[120:123], v[20:23], v[156:159]\n"
    "ds_read_b64_tr_b16 a[112:113], v180 offset:16128\n"
    "ds_read_b64_tr_b16 a[114:115], v180 offset:16640\n"
    "v_mfma_f32_16x16x32_bf16 v[160:163], a[124:127], v[20:23], v[160:163]\n"
    "s_waitcnt lgkmcnt(6) vmcnt(17)\n"
    "ds_read_b64_tr_b16 a[116:117], v114 offset:16128\n"
    "ds_read_b64_tr_b16 a[118:119], v114 offset:16640\n"
    "v_mfma_f32_16x16x32_bf16 v[148:151], a[96:99], v[24:27], v[148:151]\n"
    "ds_read_b64_tr_b16 a[120:121], v181 offset:16128\n"
    "ds_read_b64_tr_b16 a[122:123], v181 offset:16640\n"
    "s_bitcmp1_b32 s72, 11\n"
    "s_cbranch_scc1 94f\n"
    "s_mov_b32 s46, 0\n"
    "buffer_load_dwordx4 a[128:131], v129, s[44:47], 0 offen\n"
    "buffer_load_dwordx4 a[128:131], v129, s[44:47], 0 offen\n"
    "s_mov_b32 s46, 16551936\n"
    "s_branch 95f\n"
    "94:\n"
    "s_add_u32 s67, s63, 7077888\n"
    "buffer_load_dwordx4 a[12:15], v129, s[44:47], s67 offen sc1\n"
    "s_add_u32 s67, s64, 7077888\n"
    "buffer_load_dwordx4 a[44:47], v129, s[44:47], s67 offen sc1\n"
    "95:\n"
    "v_mfma_f32_16x16x32_bf16 v[152:155], a[100:103], v[24:27], v[152:155]\n"
    "s_waitcnt lgkmcnt(6)\n"
    "ds_read_b64_tr_b16 a[124:125], v115 offset:16128\n"
    "ds_read_b64_tr_b16 a[126:127], v115 offset:16640\n"
    "v_mfma_f32_16x16x32_bf16 v[156:159], a[104:107], v[24:27], v[156:159]\n"
    "ds_read_b64_tr_b16 a[96:97], v114 offset:18432\n"
    "ds_read_b64_tr_b16 a[98:99], v114 offset:18944\n"
    "v_mfma_f32_16x16x32_bf16 v[160:163], a[108:111], v[24:27], v[160:163]\n"
    "s_waitcnt lgkmcnt(6) vmcnt(18)\n"
    "ds_read_b64_tr_b16 a[100:101], v180 offset:18432\n"
    "ds_read_b64_tr_b16 a[102:103], v180 offset:18944\n"
    "v_mfma_f32_16x16x32_bf16 v[148:151], a[112:115], v[28:31], v[148:151]\n"
    "ds_read_b64_tr_b16 a[104:105], v115 offset:18432\n"
    "ds_read_b64_tr_b16 a[106:107], v115 offset:18944\n"
    "v_mfma_f32_16x16x32_bf16 v[152:155], a[116:119], v[28:31], v[152:155]\n"
    "s_waitcnt lgkmcnt(6)\n"
    "ds_read_b64_tr_b16 a[108:109], v181 offset:18432\n"
    "ds_read_b64_tr_b16 a[110:111], v181 offset:18944\n"
    "v_mfma_f32_16x16x32_bf16 v[156:159], a[120:123], v[28:31], v[156:159]\n"
    "ds_read_b64_tr_b16 a[112:113], v180 offset:20736\n"
    "ds_read_b64_tr_b16 a[114:115], v180 offset:21248\n"
    "v_mfma_f32_16x16x32_bf16 v[160:163], a[124:127], v[28:31], v[160:163]\n"
    "s_waitcnt lgkmcnt(6) vmcnt(17)\n"
    "ds_read_b64_tr_b16 a[116:117], v114 offset:20736\n"
    "ds_read_b64_tr_b16 a[118:119], v114 offset:21248\n"
    "v_mfma_f32_16x16x32_bf16 v[148:151], a[96:99], v[32:35], v[148:151]\n"
    "ds_read_b64_tr_b16 a[120:121], v181 offset:20736\n"
    "ds_read_b64_tr_b16 a[122:123], v181 offset:21248\n"
    "s_bitcmp1_b32 s72, 12\n"
    "s_cbranch_scc1 94f\n"
    "s_mov_b32 s46, 0\n"
    "buffer_load_dwordx4 a[128:131], v129, s[44:47], 0 offen\n"
    "buffer_load_dwordx4 a[128:131], v129, s[44:47], 0 offen\n"
    "s_mov_b32 s46, 16551936\n"
    "s_branch 95f\n"
    "94:\n"
    "s_add_u32 s67, s63, 9437184\n"
    "buffer_load_dwordx4 a[16:19], v129, s[44:47], s67 offen sc1\n"
    "s_add_u32 s67, s64, 9437184\n"
    "buffer_load_dwordx4 a[48:51], v129, s[44:47], s67 offen sc1\n"
    "95:\n"
    "v_mfma_f32_16x16x32_bf16 v[152:155], a[100:103], v[32:35], v[152:155]\n"
    "s_waitcnt lgkmcnt(6)\n"
    "ds_read_b64_tr_b16 a[124:125], v115 offset:20736\n"
    "ds_read_b64_tr_b16 a[126:127], v115 offset:21248\n"
    "v_mfma_f32_16x16x32_bf16 v[156:159], a[104:107], v[32:35], v[156:159]\n"
    "ds_read_b64_tr_b16 a[96:97], v114 offset:23040\n"
    "ds_read_b64_tr_b16 a[98:99], v114 offset:23552\n"
    "v_mfma_f32_16x16x32_bf16 v[160:163], a[108:111], v[32:35], v[160:163]\n"
    "s_waitcnt lgkmcnt(6) vmcnt(18)\n"
    "ds_read_b64_tr_b16 a[100:101], v180 offset:23040\n"
    "ds_read_b64_tr_b16 a[102:103], v180 offset:23552\n"
    "v_mfma_f32_16x16x32_bf16 v[148:151], a[112:115], v[36:39], v[148:151]\n"
    "ds_read_b64_tr_b16 a[104:105], v115 offset:23040\n"
    "ds_read_b64_tr_b16 a[106:107], v115 offset:23552\n"
    "v_mfma_f32_16x16x32_bf16 v[152:155], a[116:119], v[36:39], v[152:155]\n"
    "s_waitcnt lgkmcnt(6)\n"
    "ds_read_b64_tr_b16 a[108:109], v181 offset:23040\n"
    "ds_read_b64_tr_b16 a[110:111], v181 offset:23552\n"
    "v_mfma_f32_16x16x32_bf16 v[156:159], a[120:123], v[36:39], v[156:159]\n"
    "ds_read_b64_tr_b16 a[112:113], v180 offset:25344\n"
    "ds_read_b64_tr_b16 a[114:115], v180 offset:25856\n"
    "v_mfma_f32_16x16x32_bf16 v[160:163], a[124:127], v[36:39], v[160:163]\n"
    "s_waitcnt lgkmcnt(6) vmcnt(17)\n"
    "ds_read_b64_tr_b16 a[116:117], v114 offset:25344\n"
    "ds_read_b64_tr_b16 a[118:119], v114 offset:25856\n"
    "v_mfma_f32_16x16x32_bf16 v[148:151], a[96:99], v[40:43], v[148:151]\n"
    "ds_read_b64_tr_b16 a[120:121], v181 offset:25344\n"
    "ds_read_b64_tr_b16 a[122:123], v181 offset:25856\n"
    "s_bitcmp1_b32 s72, 13\n"
    "s_cbranch_scc1 94f\n"
    "s_mov_b32 s46, 0\n"
    "buffer_load_dwordx4 a[128:131], v129, s[44:47], 0 offen\n"
    "buffer_load_dwordx4 a[128:131], v129, s[44:47], 0 offen\n"
    "s_mov_b32 s46, 16551936\n"
    "s_branch 95f\n"
    "94:\n"
    "s_add_u32 s67, s63, 11796480\n"
    "buffer_load_dwordx4 a[20:23], v129, s[44:47], s67 offen sc1\n"
    "s_add_u32 s67, s64, 11796480\n"
    "buffer_load_dwordx4 a[52:55], v129, s[44:47], s67 offen sc1\n"
    "95:\n"
    "v_mfma_f32_16x16x32_bf16 v[152:155], a[100:103], v[40:43], v[152:155]\n"
    "s_waitcnt lgkmcnt(6)\n"
    "ds_read_b64_tr_b16 a[124:125], v115 offset:25344\n"
    "ds_read_b64_tr_b16 a[126:127], v115 offset:25856\n"
    "v_mfma_f32_16x16x32_bf16 v[156:159], a[104:107], v[40:43], v[156:159]\n"
    "ds_read_b64_tr_b16 a[96:97], v114 offset:27648\n"
    "ds_read_b64_tr_b16 a[98:99], v114 offset:28160\n"
    "v_mfma_f32_16x16x32_bf16 v[160:163], a[108:111], v[40:43], v[160:163]\n"
    "s_waitcnt lgkmcnt(6) vmcnt(18)\n"
    "ds_read_b64_tr_b16 a[100:101], v180 offset:27648\n"
    "ds_read_b64_tr_b16 a[102:103], v180 offset:28160\n"
    "v_mfma_f32_16x16x32_bf16 v[148:151], a[112:115], v[44:47], v[148:151]\n"
    "ds_read_b64_tr_b16 a[104:105], v115 offset:27648\n"
    "ds_read_b64_tr_b16 a[106:107], v115 offset:28160\n"
    "v_mfma_f32_16x16x32_bf16 v[152:155], a[116:119], v[44:47], v[152:155]\n"
    "s_waitcnt lgkmcnt(6)\n"
    "ds_read_b64_tr_b16 a[108:109], v181 offset:27648\n"
    "ds_read_b64_tr_b16 a[110:111], v181 offset:28160\n"
    "v_mfma_f32_16x16x32_bf16 v[156:159], a[120:123], v[44:47], v[156:159]\n"
    "ds_read_b64_tr_b16 a[112:113], v180 offset:29952\n"
    "ds_read_b64_tr_b16 a[114:115], v180 offset:30464\n"
    "v_mfma_f32_16x16x32_bf16 v[160:163], a[124:127], v[44:47], v[160:163]\n"
    "s_waitcnt lgkmcnt(6) vmcnt(17)\n"
    "ds_read_b64_tr_b16 a[116:117], v114 offset:29952\n"
    "ds_read_b64_tr_b16 a[118:119], v114 offset:30464\n"
    "v_mfma_f32_16x16x32_bf16 v[148:151], a[96:99], v[48:51], v[148:151]\n"
    "ds_read_b64_tr_b16 a[120:121], v181 offset:29952\n"
    "ds_read_b64_tr_b16 a[122:123], v181 offset:30464\n"
    "s_bitcmp1_b32 s72, 14\n"
    "s_cbranch_scc1 94f\n"
    "s_mov_b32 s46, 0\n"
    "buffer_load_dwordx4 a[128:131], v129, s[44:47], 0 offen\n"
    "buffer_load_dwordx4 a[128:131], v129, s[44:47], 0 offen\n"
    "s_mov_b32 s46, 16551936\n"
    "s_branch 95f\n"
    "94:\n"
    "s_add_u32 s67, s63, 14155776\n"
    "buffer_load_dwordx4 a[24:27], v129, s[44:47], s67 offen sc1\n"
    "s_add_u32 s67, s64, 14155776\n"
    "buffer_load_dwordx4 a[56:59], v129, s[44:47], s67 offen sc1\n"
    "95:\n"
    "v_mfma_f32_16x16x32_bf16 v[152:155], a[100:103], v[48:51], v[152:155]\n"
    "s_waitcnt lgkmcnt(6)\n"
    "ds_read_b64_tr_b16 a[124:125], v115 offset:29952\n"
    "ds_read_b64_tr_b16 a[126:127], v115 offset:30464\n"
    "v_mfma_f32_16x16x32_bf16 v[156:159], a[104:107], v[48:51], v[156:159]\n"
    "ds_read_b64_tr_b16 a[96:97], v114 offset:32256\n"
    "ds_read_b64_tr_b16 a[98:99], v114 offset:32768\n"
    "v_mfma_f32_16x16x32_bf16 v[160:163], a[108:111], v[48:51], v[160:163]\n"
    "s_waitcnt lgkmcnt(6) vmcnt(18)\n"
    "ds_read_b64_tr_b16 a[100:101], v180 offset:32256\n"
    "ds_read_b64_tr_b16 a[102:103], v180 offset:32768\n"
    "v_mfma_f32_16x16x32_bf16 v[148:151], a[112:115], v[52:55], v[148:151]\n"
    "ds_read_b64_tr_b16 a[104:105], v115 offset:32256\n"
    "ds_read_b64_tr_b16 a[106:107], v115 offset:32768\n"
    "v_mfma_f32_16x16x32_bf16 v[152:155], a[116:119], v[52:55], v[152:155]\n"
    "s_waitcnt lgkmcnt(6)\n"
    "ds_read_b64_tr_b16 a[108:109], v181 offset:32256\n"
    "ds_read_b64_tr_b16 a[110:111], v181 offset:32768\n"
    "v_mfma_f32_16x16x32_bf16 v[156:159], a[120:123], v[52:55], v[156:159]\n"
    "ds_read_b64_tr_b16 a[112:113], v180 offset:34560\n"
    "ds_read_b64_tr_b16 a[114:115], v180 offset:35072\n"
    "v_mfma_f32_16x16x32_bf16 v[160:163], a[124:127], v[52:55], v[160:163]\n"
    "s_waitcnt lgkmcnt(6) vmcnt(17)\n"
    "ds_read_b64_tr_b16 a[116:117], v114 offset:34560\n"
    "ds_read_b64_tr_b16 a[118:119], v114 offset:35072\n"
    "v_mfma_f32_16x16x32_bf16 v[148:151], a[96:99], v[56:59], v[148:151]\n"
    "ds_read_b64_tr_b16 a[120:121], v181 offset:34560\n"
    "ds_read_b64_tr_b16 a[122:123], v181 offset:35072\n"
    "s_bitcmp1_b32 s72, 15\n"
    "s_cbranch_scc1 94f\n"
    "s_mov_b32 s46, 0\n"
    "buffer_load_dwordx4 a[128:131], v129, s[44:47], 0 offen\n"
    "buffer_load_dwordx4 a[128:131], v129, s[44:47], 0 offen\n"
    "s_mov_b32 s46, 16551936\n"
    "s_branch 95f\n"
    "94:\n"
    "s_add_u32 s67, s63, 16515072\n"
    "buffer_load_dwordx4 a[28:31], v129, s[44:47], s67 offen sc1\n"
    "s_add_u32 s67, s64, 16515072\n"
    "buffer_load_dwordx4 a[60:63], v129, s[44:47], s67 offen sc1\n"
    "95:\n"
    "v_mfma_f32_16x16x32_bf16 v[152:155], a[100:103], v[56:59], v[152:155]\n"
    "s_waitcnt lgkmcnt(6)\n"
    "ds_read_b64_tr_b16 a[124:125], v115 offset:34560\n"
    "ds_read_b64_tr_b16 a[126:127], v115 offset:35072\n"
    "v_mfma_f32_16x16x32_bf16 v[156:159], a[104:107], v[56:59], v[156:159]\n"
    "ds_read_b64_tr_b16 a[96:97], v117 offset:0\n"
    "ds_read_b64_tr_b16 a[98:99], v183 offset:2304\n"
    "v_mfma_f32_16x16x32_bf16 v[160:163], a[108:111], v[56:59], v[160:163]\n"
    "s_waitcnt lgkmcnt(6) vmcnt(18)\n"
    "ds_read_b64_tr_b16 a[100:101], v183 offset:0\n"
    "ds_read_b64_tr_b16 a[102:103], v117 offset:2304\n"
    "v_mfma_f32_16x16x32_bf16 v[148:151], a[112:115], v[60:63], v[148:151]\n"
    "ds_read_b64_tr_b16 a[104:105], v118 offset:0\n"
    "ds_read_b64_tr_b16 a[106:107], v184 offset:2304\n"
    "v_mfma_f32_16x16x32_bf16 v[152:155], a[116:119], v[60:63], v[152:155]\n"
    "s_waitcnt lgkmcnt(6)\n"
    "ds_read_b64_tr_b16 a[108:109], v184 offset:0\n"
    "ds_read_b64_tr_b16 a[110:111], v118 offset:2304\n"
    "v_mfma_f32_16x16x32_bf16 v[156:159], a[120:123], v[60:63], v[156:159]\n"
    "ds_read_b64_tr_b16 a[112:113], v117 offset:18432\n"
    "ds_read_b64_tr_b16 a[114:115], v183 offset:20736\n"
    "v_mfma_f32_16x16x32_bf16 v[160:163], a[124:127], v[60:63], v[160:163]\n"
    "s_waitcnt lgkmcnt(6) vmcnt(17)\n"
    "ds_read_b64_tr_b16 a[116:117], v183 offset:18432\n"
    "ds_read_b64_tr_b16 a[118:119], v117 offset:20736\n"
    "v_mfma_f32_16x16x32_bf16 v[148:151], a[96:99], v[64:67], v[148:151]\n"
    "ds_read_b64_tr_b16 a[120:121], v118 offset:18432\n"
    "ds_read_b64_tr_b16 a[122:123], v184 offset:20736\n"
    "v_mfma_f32_16x16x32_bf16 v[152:155], a[100:103], v[64:67], v[152:155]\n"
    "s_waitcnt lgkmcnt(6)\n"
    "ds_read_b64_tr_b16 a[124:125], v184 offset:18432\n"
    "ds_read_b64_tr_b16 a[126:127], v118 offset:20736\n"
    "v_mfma_f32_16x16x32_bf16 v[156:159], a[104:107], v[64:67], v[156:159]\n"
    "v_mfma_f32_16x16x32_bf16 v[160:163], a[108:111], v[64:67], v[160:163]\n"
    "s_waitcnt lgkmcnt(4) vmcnt(16)\n"
    "v_mfma_f32_16x16x32_bf16 v[148:151], a[112:115], v[68:71], v[148:151]\n"
    "v_mfma_f32_16x16x32_bf16 v[152:155], a[116:119], v[68:71], v[152:155]\n"
    "s_waitcnt lgkmcnt(0)\n"
    "v_mfma_f32_16x16x32_bf16 v[156:159], a[120:123], v[68:71], v[156:159]\n"
    "v_mfma_f32_16x16x32_bf16 v[160:163], a[124:127], v[68:71], v[160:163]\n"
    "s_cmp_eq_u32 s57, 0\n"
    "s_cbranch_scc1 66f\n"
    "ds_read_b64_tr_b16 a[96:97], v116 offset:0\n"
    "ds_read_b64_tr_b16 a[98:99], v116 offset:512\n"
    "ds_read_b64_tr_b16 a[100:101], v182 offset:0\n"
    "ds_read_b64_tr_b16 a[102:103], v182 offset:512\n"
    "ds_read_b64_tr_b16 a[104:105], v182 offset:2304\n"
    "ds_read_b64_tr_b16 a[106:107], v182 offset:2816\n"
    "ds_read_b64_tr_b16 a[108:109], v116 offset:2304\n"
    "ds_read_b64_tr_b16 a[110:111], v116 offset:2816\n"
    "ds_read_b64_tr_b16 a[112:113], v116 offset:4608\n"
    "ds_read_b64_tr_b16 a[114:115], v116 offset:5120\n"
    "ds_read_b64_tr_b16 a[116:117], v182 offset:4608\n"
    "ds_read_b64_tr_b16 a[118:119], v182 offset:5120\n"
    "s_waitcnt lgkmcnt(8)\n"
    "v_mfma_f32_16x16x32_bf16 v[164:167], a[96:99], v[0:3], 0\n"
    "ds_read_b64_tr_b16 a[120:121], v182 offset:6912\n"
    "ds_read_b64_tr_b16 a[122:123], v182 offset:7424\n"
    "v_mfma_f32_16x16x32_bf16 v[168:171], a[100:103], v[0:3], 0\n"
    "s_waitcnt lgkmcnt(6)\n"
    "ds_read_b64_tr_b16 a[124:125], v116 offset:6912\n"
    "ds_read_b64_tr_b16 a[126:127], v116 offset:7424\n"
    "v_mfma_f32_16x16x32_bf16 v[164:167], a[104:107], v[4:7], v[164:167]\n"
    "ds_read_b64_tr_b16 a[96:97], v116 offset:9216\n"
    "ds_read_b64_tr_b16 a[98:99], v116 offset:9728\n"
    "v_mfma_f32_16x16x32_bf16 v[168:171], a[108:111], v[4:7], v[168:171]\n"
    "s_waitcnt lgkmcnt(6)\n"
    "ds_read_b64_tr_b16 a[100:101], v182 offset:9216\n"
    "ds_read_b64_tr_b16 a[102:103], v182 offset:9728\n"
    "v_mfma_f32_16x16x32_bf16 v[164:167], a[112:115], v[8:11], v[164:167]\n"
    "ds_read_b64_tr_b16 a[104:105], v182 offset:11520\n"
    "ds_read_b64_tr_b16 a[106:107], v182 offset:12032\n"
    "v_mfma_f32_16x16x32_bf16 v[168:171], a[116:119], v[8:11], v[168:171]\n"
    "s_waitcnt lgkmcnt(6)\n"
    "ds_read_b64_tr_b16 a[108:109], v116 offset:11520\n"
    "ds_read_b64_tr_b16 a[110:111], v116 offset:12032\n"
    "v_mfma_f32_16x16x32_bf16 v[164:167], a[120:123], v[12:15], v[164:167]\n"
    "ds_read_b64_tr_b16 a[112:113], v116 offset:13824\n"
    "ds_read_b64_tr_b16 a[114:115], v116 offset:14336\n"
    "v_mfma_f32_16x16x32_bf16 v[168:171], a[124:127], v[12:15], v[168:171]\n"
    "s_waitcnt lgkmcnt(6)\n"
    "ds_read_b64_tr_b16 a[116:117], v182 offset:13824\n"
    "ds_read_b64_tr_b16 a[118:119], v182 offset:14336\n"
    "v_mfma_f32_16x16x32_bf16 v[164:167], a[96:99], v[16:19], v[164:167]\n"
    "ds_read_b64_tr_b16 a[120:121], v182 offset:16128\n"
    "ds_read_b64_tr_b16 a[122:123], v182 offset:16640\n"
    "v_mfma_f32_16x16x32_bf16 v[168:171], a[100:103], v[16:19], v[168:171]\n"
    "s_waitcnt lgkmcnt(6)\n"
    "ds_read_b64_tr_b16 a[124:125], v116 offset:16128\n"
    "ds_read_b64_tr_b16 a[126:127], v116 offset:16640\n"
    "v_mfma_f32_16x16x32_bf16 v[164:167], a[104:107], v[20:23], v[164:167]\n"
    "ds_read_b64_tr_b16 a[96:97], v116 offset:18432\n"
    "ds_read_b64_tr_b16 a[98:99], v116 offset:18944\n"
    "v_mfma_f32_16x16x32_bf16 v[168:171], a[108:111], v[20:23], v[168:171]\n"
    "s_waitcnt lgkmcnt(6)\n"
    "ds_read_b64_tr_b16 a[100:101], v182 offset:18432\n"
    "ds_read_b64_tr_b16 a[102:103], v182 offset:18944\n"
    "v_mfma_f32_16x16x32_bf16 v[164:167], a[112:115], v[24:27], v[164:167]\n"
    "ds_read_b64_tr_b16 a[104:105], v182 offset:20736\n"
    "ds_read_b64_tr_b16 a[106:107], v182 offset:21248\n"
    "v_mfma_f32_16x16x32_bf16 v[168:171], a[116:119], v[24:27], v[168:171]\n"
    "s_waitcnt lgkmcnt(6)\n"
    "ds_read_b64_tr_b16 a[108:109], v116 offset:20736\n"
    "ds_read_b64_tr_b16 a[110:111], v116 offset:21248\n"
    "v_mfma_f32_16x16x32_bf16 v[164:167], a[120:123], v[28:31], v[164:167]\n"
    "ds_read_b64_tr_b16 a[112:113], v116 offset:23040\n"
    "ds_read_b64_tr_b16 a[114:115], v116 offset:23552\n"
    "v_mfma_f32_16x16x32_bf16 v[168:171], a[124:127], v[28:31], v[168:171]\n"
    "s_waitcnt lgkmcnt(6)\n"
    "ds_read_b64_tr_b16 a[116:117], v182 offset:23040\n"
    "ds_read_b64_tr_b16 a[118:119], v182 offset:23552\n"
    "v_mfma_f32_16x16x32_bf16 v[164:167], a[96:99], v[32:35], v[164:167]\n"
    "ds_read_b64_tr_b16 a[120:121], v182 offset:25344\n"
    "ds_read_b64_tr_b16 a[122:123], v182 offset:25856\n"
    "v_mfma_f32_16x16x32_bf16 v[168:171], a[100:103], v[32:35], v[168:171]\n"
    "s_waitcnt lgkmcnt(6)\n"
    "ds_read_b64_tr_b16 a[124:125], v116 offset:25344\n"
    "ds_read_b64_tr_b16 a[126:127], v116 offset:25856\n"
    "v_mfma_f32_16x16x32_bf16 v[164:167], a[104:107], v[36:39], v[164:167]\n"
    "ds_read_b64_tr_b16 a[96:97], v116 offset:27648\n"
    "ds_read_b64_tr_b16 a[98:99], v116 offset:28160\n"
    "v_mfma_f32_16x16x32_bf16 v[168:171], a[108:111], v[36:39], v[168:171]\n"
    "s_waitcnt lgkmcnt(6)\n"
    "ds_read_b64_tr_b16 a[100:101], v182 offset:27648\n"
    "ds_read_b64_tr_b16 a[102:103], v182 offset:28160\n"
    "v_mfma_f32_16x16x32_bf16 v[164:167], a[112:115], v[40:43], v[164:167]\n"
    "ds_read_b64_tr_b16 a[104:105], v182 offset:29952\n"
    "ds_read_b64_tr_b16 a[106:107], v182 offset:30464\n"
    "v_mfma_f32_16x16x32_bf16 v[168:171], a[116:119], v[40:43], v[168:171]\n"
    "s_waitcnt lgkmcnt(6)\n"
    "ds_read_b64_tr_b16 a[108:109], v116 offset:29952\n"
    "ds_read_b64_tr_b16 a[110:111], v116 offset:30464\n"
    "v_mfma_f32_16x16x32_bf16 v[164:167], a[120:123], v[44:47], v[164:167]\n"
    "ds_read_b64_tr_b16 a[112:113], v116 offset:32256\n"
    "ds_read_b64_tr_b16 a[114:115], v116 offset:32768\n"
    "v_mfma_f32_16x16x32_bf16 v[168:171], a[124:127], v[44:47], v[168:171]\n"
    "s_waitcnt lgkmcnt(6)\n"
    "ds_read_b64_tr_b16 a[116:117], v182 offset:32256\n"
    "ds_read_b64_tr_b16 a[118:119], v182 offset:32768\n"
    "v_mfma_f32_16x16x32_bf16 v[164:167], a[96:99], v[48:51], v[164:167]\n"
    "ds_read_b64_tr_b16 a[120:121], v182 offset:34560\n"
    "ds_read_b64_tr_b16 a[122:123], v182 offset:35072\n"
    "v_mfma_f32_16x16x32_bf16 v[168:171], a[100:103], v[48:51], v[168:171]\n"
    "s_waitcnt lgkmcnt(6)\n"
    "ds_read_b64_tr_b16 a[124:125], v116 offset:34560\n"
    "ds_read_b64_tr_b16 a[126:127], v116 offset:35072\n"
    "v_mfma_f32_16x16x32_bf16 v[164:167], a[104:107], v[52:55], v[164:167]\n"
    "ds_read_b64_tr_b16 a[96:97], v119 offset:0\n"
    "ds_read_b64_tr_b16 a[98:99], v185 offset:2304\n"
    "v_mfma_f32_16x16x32_bf16 v[168:171], a[108:111], v[52:55], v[168:171]\n"
    "s_waitcnt lgkmcnt(6)\n"
    "ds_read_b64_tr_b16 a[100:101], v185 offset:0\n"
    "ds_read_b64_tr_b16 a[102:103], v119 offset:2304\n"
    "v_mfma_f32_16x16x32_bf16 v[164:167], a[112:115], v[56:59], v[164:167]\n"
    "ds_read_b64_tr_b16 a[104:105], v119 offset:18432\n"
    "ds_read_b64_tr_b16 a[106:107], v185 offset:20736\n"
    "v_mfma_f32_16x16x32_bf16 v[168:171], a[116:119], v[56:59], v[168:171]\n"
    "s_waitcnt lgkmcnt(6)\n"
    "ds_read_b64_tr_b16 a[108:109], v185 offset:18432\n"
    "ds_read_b64_tr_b16 a[110:111], v119 offset:20736\n"
    "v_mfma_f32_16x16x32_bf16 v[164:167], a[120:123], v[60:63], v[164:167]\n"
    "v_mfma_f32_16x16x32_bf16 v[168:171], a[124:127], v[60:63], v[168:171]\n"
    "s_waitcnt lgkmcnt(4)\n"
    "v_mfma_f32_16x16x32_bf16 v[164:167], a[96:99], v[64:67], v[164:167]\n"
    "v_mfma_f32_16x16x32_bf16 v[168:171], a[100:103], v[64:67], v[168:171]\n"
    "s_waitcnt lgkmcnt(0)\n"
    "v_mfma_f32_16x16x32_bf16 v[164:167], a[104:107], v[68:71], v[164:167]\n"
    "v_mfma_f32_16x16x32_bf16 v[168:171], a[108:111], v[68:71], v[168:171]\n"
    "s_branch 66f\n"
    "63:\n"
    "s_bitcmp1_b32 s72, 8\n"
    "s_cbranch_scc0 65f\n"
    "s_add_u32 s67, s63, 0\n"
    "buffer_load_dwordx4 a[0:3], v129, s[44:47], s67 offen sc1\n"
    "s_add_u32 s67, s64, 0\n"
    "buffer_load_dwordx4 a[32:35], v129, s[44:47], s67 offen sc1\n"
    "65:\n"
    "s_bitcmp1_b32 s72, 9\n"
    "s_cbranch_scc0 65f\n"
    "s_add_u32 s67, s63, 2359296\n"
    "buffer_load_dwordx4 a[4:7], v129, s[44:47], s67 offen sc1\n"
    "s_add_u32 s67, s64, 2359296\n"
    "buffer_load_dwordx4 a[36:39], v129, s[44:47], s67 offen sc1\n"
    "65:\n"
    "s_bitcmp1_b32 s72, 10\n"
    "s_cbranch_scc0 65f\n"
    "s_add_u32 s67, s63, 4718592\n"
    "buffer_load_dwordx4 a[8:11], v129, s[44:47], s67 offen sc1\n"
    "s_add_u32 s67, s64, 4718592\n"
    "buffer_load_dwordx4 a[40:43], v129, s[44:47], s67 offen sc1\n"
    "65:\n"
    "s_bitcmp1_b32 s72, 11\n"
    "s_cbranch_scc0 65f\n"
    "s_add_u32 s67, s63, 7077888\n"
    "buffer_load_dwordx4 a[12:15], v129, s[44:47], s67 offen sc1\n"
    "s_add_u32 s67, s64, 7077888\n"
    "buffer_load_dwordx4 a[44:47], v129, s[44:47], s67 offen sc1\n"
    "65:\n"
    "s_bitcmp1_b32 s72, 12\n"
    "s_cbranch_scc0 65f\n"
    "s_add_u32 s67, s63, 9437184\n"
    "buffer_load_dwordx4 a[16:19], v129, s[44:47], s67 offen sc1\n"
    "s_add_u32 s67, s64, 9437184\n"
    "buffer_load_dwordx4 a[48:51], v129, s[44:47], s67 offen sc1\n"
    "65:\n"
    "s_bitcmp1_b32 s72, 13\n"
    "s_cbranch_scc0 65f\n"
    "s_add_u32 s67, s63, 11796480\n"
    "buffer_load_dwordx4 a[20:23], v129, s[44:47], s67 offen sc1\n"
    "s_add_u32 s67, s64, 11796480\n"
    "buffer_load_dwordx4 a[52:55], v129, s[44:47], s67 offen sc1\n"
    "65:\n"
    "s_bitcmp1_b32 s72, 14\n"
    "s_cbranch_scc0 65f\n"
    "s_add_u32 s67, s63, 14155776\n"
    "buffer_load_dwordx4 a[24:27], v129, s[44:47], s67 offen sc1\n"
    "s_add_u32 s67, s64, 14155776\n"
    "buffer_load_dwordx4 a[56:59], v129, s[44:47], s67 offen sc1\n"
    "65:\n"
    "s_bitcmp1_b32 s72, 15\n"
    "s_cbranch_scc0 65f\n"
    "s_add_u32 s67, s63, 16515072\n"
    "buffer_load_dwordx4 a[28:31], v129, s[44:47], s67 offen sc1\n"
    "s_add_u32 s67, s64, 16515072\n"
    "buffer_load_dwordx4 a[60:63], v129, s[44:47], s67 offen sc1\n"
    "65:\n"
    "66:\n"
    "s_cmp_eq_u32 s69, 0xffff\n"
    "s_cbranch_scc1 86f\n"
    "s_branch 61b\n"
    "68:\n"
    "s_bitcmp1_b32 s70, 1\n"
    "s_cbranch_scc1 69f\n"
    "s_cmp_lt_u32 s69, 0xff00\n"
    "s_cbranch_scc1 69f\n"
    "s_bitset1_b32 s70, 1\n"
    "s_branch 84f\n"
    "69:\n"
    "s_sleep 1\n"
    "s_sub_u32 s66, s66, 1\n"
    "s_cmp_lg_u32 s66, 0\n"
    "s_cbranch_scc1 61b\n"
    "86:\n"
    "s_mov_b32 s69, 0xffff\n"
    "s_waitcnt vmcnt(0)\n"
    "s_bitcmp1_b32 s70, 1\n"
    "s_cbranch_scc1 88f\n"
    "84:\n"
    "v_mfma_f32_16x16x32_bf16 v[72:75], v[110:113], v[96:99], 0\n"
    "v_mfma_f32_16x16x32_bf16 v[76:79], v[108:111], v[96:99], 0\n"
    "v_mfma_f32_16x16x32_bf16 v[80:83], v[110:113], v[100:103], 0\n"
    "v_mfma_f32_16x16x32_bf16 v[84:87], v[108:111], v[100:103], 0\n"
    "v_mfma_f32_16x16x32_bf16 v[72:75], v[110:113], a[0:3], v[72:75]\n"
    "v_mfma_f32_16x16x32_bf16 v[76:79], v[108:111], a[0:3], v[76:79]\n"
    "v_mfma_f32_16x16x32_bf16 v[80:83], v[110:113], a[32:35], v[80:83]\n"
    "v_mfma_f32_16x16x32_bf16 v[84:87], v[108:111], a[32:35], v[84:87]\n"
    "v_mfma_f32_16x16x32_bf16 v[72:75], v[110:113], a[4:7], v[72:75]\n"
    "v_mfma_f32_16x16x32_bf16 v[76:79], v[108:111], a[4:7], v[76:79]\n"
    "v_mfma_f32_16x16x32_bf16 v[80:83], v[110:113], a[36:39], v[80:83]\n"
    "v_mfma_f32_16x16x32_bf16 v[84:87], v[108:111], a[36:39], v[84:87]\n"
    "v_mfma_f32_16x16x32_bf16 v[72:75], v[110:113], a[8:11], v[72:75]\n"
    "v_mfma_f32_16x16x32_bf16 v[76:79], v[108:111], a[8:11], v[76:79]\n"
    "v_mfma_f32_16x16x32_bf16 v[80:83], v[110:113], a[40:43], v[80:83]\n"
    "v_mfma_f32_16x16x32_bf16 v[84:87], v[108:111], a[40:43], v[84:87]\n"
    "v_mfma_f32_16x16x32_bf16 v[72:75], v[110:113], a[12:15], v[72:75]\n"
    "v_mfma_f32_16x16x32_bf16 v[76:79], v[108:111], a[12:15], v[76:79]\n"
    "v_mfma_f32_16x16x32_bf16 v[80:83], v[110:113], a[44:47], v[80:83]\n"
    "v_mfma_f32_16x16x32_bf16 v[84:87], v[108:111], a[44:47], v[84:87]\n"
    "v_mfma_f32_16x16x32_bf16 v[72:75], v[110:113], a[16:19], v[72:75]\n"
    "v_mfma_f32_16x16x32_bf16 v[76:79], v[108:111], a[16:19], v[76:79]\n"
    "v_mfma_f32_16x16x32_bf16 v[80:83], v[110:113], a[48:51], v[80:83]\n"
    "v_mfma_f32_16x16x32_bf16 v[84:87], v[108:111], a[48:51], v[84:87]\n"
    "v_mfma_f32_16x16x32_bf16 v[72:75], v[110:113], a[20:23], v[72:75]\n"
    "v_mfma_f32_16x16x32_bf16 v[76:79], v[108:111], a[20:23], v[76:79]\n"
    "v_mfma_f32_16x16x32_bf16 v[80:83], v[110:113], a[52:55], v[80:83]\n"
    "v_mfma_f32_16x16x32_bf16 v[84:87], v[108:111], a[52:55], v[84:87]\n"
    "v_mfma_f32_16x16x32_bf16 v[72:75], v[110:113], a[24:27], v[72:75]\n"
    "v_mfma_f32_16x16x32_bf16 v[76:79], v[108:111], a[24:27], v[76:79]\n"
    "v_mfma_f32_16x16x32_bf16 v[80:83], v[110:113], a[56:59], v[80:83]\n"
    "v_mfma_f32_16x16x32_bf16 v[84:87], v[108:111], a[56:59], v[84:87]\n"
    "v_mfma_f32_16x16x32_bf16 v[72:75], v[110:113], a[28:31], v[72:75]\n"
    "v_mfma_f32_16x16x32_bf16 v[76:79], v[108:111], a[28:31], v[76:79]\n"
    "v_mfma_f32_16x16x32_bf16 v[80:83], v[110:113], a[60:63], v[80:83]\n"
    "v_mfma_f32_16x16x32_bf16 v[84:87], v[108:111], a[60:63], v[84:87]\n"
    "s_cmp_eq_u32 s57, 0\n"
    "s_cbranch_scc1 85f\n"
    "v_mfma_f32_16x16x32_bf16 v[88:91], v[110:113], v[104:107], 0\n"
    "v_mfma_f32_16x16x32_bf16 v[92:95], v[108:111], v[104:107], 0\n"
    "v_mfma_f32_16x16x32_bf16 v[88:91], v[110:113], a[64:67], v[88:91]\n"
    "v_mfma_f32_16x16x32_bf16 v[92:95], v[108:111], a[64:67], v[92:95]\n"
    "v_mfma_f32_16x16x32_bf16 v[88:91], v[110:113], a[68:71], v[88:91]\n"
    "v_mfma_f32_16x16x32_bf16 v[92:95], v[108:111], a[68:71], v[92:95]\n"
    "v_mfma_f32_16x16x32_bf16 v[88:91], v[110:113], a[72:75], v[88:91]\n"
    "v_mfma_f32_16x16x32_bf16 v[92:95], v[108:111], a[72:75], v[92:95]\n"
    "v_mfma_f32_16x16x32_bf16 v[88:91], v[110:113], a[76:79], v[88:91]\n"
    "v_mfma_f32_16x16x32_bf16 v[92:95], v[108:111], a[76:79], v[92:95]\n"
    "v_mfma_f32_16x16x32_bf16 v[88:91], v[110:113], a[80:83], v[88:91]\n"
    "v_mfma_f32_16x16x32_bf16 v[92:95], v[108:111], a[80:83], v[92:95]\n"
    "v_mfma_f32_16x16x32_bf16 v[88:91], v[110:113], a[84:87], v[88:91]\n"
    "v_mfma_f32_16x16x32_bf16 v[92:95], v[108:111], a[84:87], v[92:95]\n"
    "v_mfma_f32_16x16x32_bf16 v[88:91], v[110:113], a[88:91], v[88:91]\n"
    "v_mfma_f32_16x16x32_bf16 v[92:95], v[108:111], a[88:91], v[92:95]\n"
    "v_mfma_f32_16x16x32_bf16 v[88:91], v[110:113], a[92:95], v[88:91]\n"
    "v_mfma_f32_16x16x32_bf16 v[92:95], v[108:111], a[92:95], v[92:95]\n"
    "85:\n"
    "s_cmp_lg_u32 s69, 0xffff\n"
    "s_cbranch_scc1 61b\n"
    "88:\n"
    "s_nop 7\n"
    "s_cmp_eq_u32 s57, 0\n"
    "s_cbranch_scc1 87f\n"
    "v_add_f32 v88, v88, v164\n"
    "v_add_f32 v89, v89, v165\n"
    "v_add_f32 v90, v90, v166\n"
    "v_add_f32 v91, v91, v167\n"
    "v_add_f32 v92, v92, v168\n"
    "v_add_f32 v93, v93, v169\n"
    "v_add_f32 v94, v94, v170\n"
    "v_add_f32 v95, v95, v171\n"
    "v_cvt_pk_bf16_f32 v144, v88, v89\n"
    "v_cvt_pk_bf16_f32 v145, v90, v91\n"
    "v_cvt_pk_bf16_f32 v146, v92, v93\n"
    "v_cvt_pk_bf16_f32 v147, v94, v95\n"
    "s_nop 1\n"
    "v_permlane16_swap_b32 v144, v146\n"
    "v_permlane16_swap_b32 v145, v147\n"
    "global_store_dwordx4 v124, v[144:147], s[50:51] sc1\n"
    "87:\n"
    "v_add_f32 v72, v72, v148\n"
    "v_add_f32 v73, v73, v149\n"
    "v_add_f32 v74, v74, v150\n"
    "v_add_f32 v75, v75, v151\n"
    "v_add_f32 v76, v76, v152\n"
    "v_add_f32 v77, v77, v153\n"
    "v_add_f32 v78, v78, v154\n"
    "v_add_f32 v79, v79, v155\n"
    "v_cvt_pk_bf16_f32 v136, v72, v73\n"
    "v_cvt_pk_bf16_f32 v137, v74, v75\n"
    "v_cvt_pk_bf16_f32 v138, v76, v77\n"
    "v_cvt_pk_bf16_f32 v139, v78, v79\n"
    "s_nop 1\n"
    "v_permlane16_swap_b32 v136, v138\n"
    "v_permlane16_swap_b32 v137, v139\n"
    "global_store_dwordx4 v122, v[136:139], s[50:51] sc1\n"
    "v_add_f32 v80, v80, v156\n"
    "v_add_f32 v81, v81, v157\n"
    "v_add_f32 v82, v82, v158\n"
    "v_add_f32 v83, v83, v159\n"
    "v_add_f32 v84, v84, v160\n"
    "v_add_f32 v85, v85, v161\n"
    "v_add_f32 v86, v86, v162\n"
    "v_add_f32 v87, v87, v163\n"
    "v_cvt_pk_bf16_f32 v140, v80, v81\n"
    "v_cvt_pk_bf16_f32 v141, v82, v83\n"
    "v_cvt_pk_bf16_f32 v142, v84, v85\n"
    "v_cvt_pk_bf16_f32 v143, v86, v87\n"
    "s_nop 1\n"
    "v_permlane16_swap_b32 v140, v142\n"
    "v_permlane16_swap_b32 v141, v143\n"
    "global_store_dwordx4 v123, v[140:143], s[50:51] sc1\n"
    "71:\n"
    "99:\n"
    :: "s"(x), "s"(wqkv), "s"(ws), "s"(wo), "s"(out), "s"(t), "s"(s), "s"(wid), "s"(ldsb), "s"(wg), "s"(wu), "s"(wd), "s"(tf), "s"(sf), "s"(b), "s"(rot) : "v0","v1","v2","v3","v4","v5","v6","v7","v8","v9","v10","v11","v12","v13","v14","v15","v16","v17","v18","v19","v20","v21","v22","v23","v24","v25","v26","v27","v28","v29","v30","v31","v32","v33","v34","v35","v36","v37","v38","v39","v40","v41","v42","v43","v44","v45","v46","v47","v48","v49","v50","v51","v52","v53","v54","v55","v56","v57","v58","v59","v60","v61","v62","v63","v64","v65","v66","v67","v68","v69","v70","v71","v72","v73","v74","v75","v76","v77","v78","v79","v80","v81","v82","v83","v84","v85","v86","v87","v88","v89","v90","v91","v92","v93","v94","v95","v96","v97","v98","v99","v100","v101","v102","v103","v104","v105","v106","v107","v108","v109","v110","v111","v112","v113","v114","v115","v116","v117","v118","v119","v120","v121","v122","v123","v124","v125","v126","v127","v128","v129","v130","v131","v132","v133","v134","v135","v136","v137","v138","v139","v140","v141","v142","v143","v144","v145","v146","v147","v148","v149","v150","v151","v152","v153","v154","v155","v156","v157","v158","v159","v160","v161","v162","v163","v164","v165","v166","v167","v168","v169","v170","v171","v172","v173","v174","v175","v176","v177","v178","v179","v180","v181","v182","v183","v184","v185","v186","v187","v188","v189","v190","v191","v192","v193","v194","v195","v196","v197","v198","v199","v200","v201","v202","v203","v204","v205","v206","v207","v208","v209","v210","v211","v212","v213","v214","v215","v216","v217","v218","v219","v220","v221","v222","v223","v224","v225","v226","v227","v228","v229","v230","v231","v232","v233","v234","v235","v236","v237","v238","v239","v240","v241","v242","v243","v244","v245","v246","v247","v248","v249","v250","v251","v252","v253","v254","v255","a0","a1","a2","a3","a4","a5","a6","a7","a8","a9","a10","a11","a12","a13","a14","a15","a16","a17","a18","a19","a20","a21","a22","a23","a24","a25","a26","a27","a28","a29","a30","a31","a32","a33","a34","a35","a36","a37","a38","a39","a40","a41","a42","a43","a44","a45","a46","a47","a48","a49","a50","a51","a52","a53","a54","a55","a56","a57","a58","a59","a60","a61","a62","a63","a64","a65","a66","a67","a68","a69","a70","a71","a72","a73","a74","a75","a76","a77","a78","a79","a80","a81","a82","a83","a84","a85","a86","a87","a88","a89","a90","a91","a92","a93","a94","a95","a96","a97","a98","a99","a100","a101","a102","a103","a104","a105","a106","a107","a108","a109","a110","a111","a112","a113","a114","a115","a116","a117","a118","a119","a120","a121","a122","a123","a124","a125","a126","a127","a128","a129","a130","a131","a132","a133","a134","a135","a136","a137","a138","a139","a140","a141","a142","a143","a144","a145","a146","a147","a148","a149","a150","a151","a152","a153","a154","a155","a156","a157","a158","a159","a160","a161","a162","a163","a164","a165","a166","a167","a168","a169","a170","a171","a172","a173","a174","a175","a176","a177","a178","a179","a180","a181","a182","a183","a184","a185","a186","a187","a188","a189","a190","a191","a192","a193","a194","a195","a196","a197","a198","a199","a200","a201","a202","a203","a204","a205","a206","a207","a208","a209","a210","a211","a212","a213","a214","a215","a216","a217","a218","a219","a220","a221","a222","a223","a224","a225","a226","a227","a228","a229","a230","a231","a232","a233","a234","a235","a236","a237","a238","a239","a240","a241","a242","a243","a244","a245","a246","a247","a248","a249","a250","a251","a252","a253","a254","a255","s30","s31","s32","s33","s34","s35","s36","s37","s38","s39","s40","s41","s42","s43","s44","s45","s46","s47","s48","s49","s50","s51","s52","s53","s54","s55","s56","s57","s58","s59","s60","s61","s62","s63","s64","s65","s66","s67","s68","s69","s70","s71","s72","s73","s74","s75","s76","s77","s78","s79","s80","s81","s82","s83","s84","s85","s86","s87","s88","s89","s90","s91","s92","s93","s94","s95","s96","s97","s98","s99","vcc","scc","memory");
  }
}

extern "C" void kernel_launch(void* const* d_in, const int* in_sizes, int n_in, void* d_out, int out_size, void* d_ws, size_t ws_size, hipStream_t st) {
  const u16* x = (const u16*)d_in[0]; const u16* wqkv = (const u16*)d_in[1]; const u16* wo = (const u16*)d_in[2];
  const u16* wg = (const u16*)d_in[4]; const u16* wu = (const u16*)d_in[5]; const u16* wd = (const u16*)d_in[6];
  (void)hipFuncSetAttribute((const void*)k_main, hipFuncAttributeMaxDynamicSharedMemorySize, 160 * 1024);
  k_main<<<256, 256, 160 * 1024, st>>>(x, wqkv, wo, (u16*)d_out, wg, wu, wd, (char*)d_ws + 0);
}
```
